```python
import torch

def _softmax(x, axis=-1):
    return torch.softmax(x, dim=axis)
def _gelu(x):
    return torch.nn.functional.gelu(x, approximate="tanh")
def _silu(x):
    return torch.nn.functional.silu(x)
def _sigmoid(x):
    return torch.sigmoid(x)
def _layernorm(x, axis=-1, eps=1e-5):
    m = x.mean(dim=axis, keepdim=True)
    v = x.var(dim=axis, keepdim=True, unbiased=False)
    return (x - m) / torch.sqrt(v + eps)
def _scatter_add(tgt, idx, src, axis=0):
    return tgt.clone().index_add_(axis, idx, src)
def _segment_sum(src, seg, n):
    out = torch.zeros((n,) + src.shape[1:], dtype=src.dtype)
    return out.index_add_(0, seg, src)
def _pad(x, pw):
    flat = tuple(v for pair in reversed(pw) for v in pair)
    return torch.nn.functional.pad(x, flat)

kernel_name = 'interleave_layers__grouped_kv_mqa__mixer_token_mlp__prompt'

def setup_inputs():

    _w = (lambda a: (lambda t: t.to(torch.bfloat16) if t.is_floating_point() else t)(torch.as_tensor(a)))
    def _A_setup():
        B, S, D, H = 1, 2048, 640, 32
        Dh = D // H
        return {
            "x": _w((lambda *s: torch.randn(*s, dtype=torch.bfloat16) * 0.05)(B, S, D)),
            "wq": _w((lambda *s: torch.randn(*s, dtype=torch.bfloat16) * 0.05)(D, H * Dh)),
            "wk": _w((lambda *s: torch.randn(*s, dtype=torch.bfloat16) * 0.05)(D, Dh)),
            "wv": _w((lambda *s: torch.randn(*s, dtype=torch.bfloat16) * 0.05)(D, Dh)),
            "wo": _w((lambda *s: torch.randn(*s, dtype=torch.bfloat16) * 0.05)(H * Dh, D)),
        }
    _A_in = _A_setup()

    def _B_setup():
        B, S, D, F = 1, 2048, 640, 1728
        return {
            "x": _w((lambda *s: torch.randn(*s, dtype=torch.bfloat16) * 0.05)(B, S, D)),
            "w1": _w((lambda *s: torch.randn(*s, dtype=torch.bfloat16) * 0.4)(S, F) * (S ** -0.5)),
            "b1": _w((lambda *s: torch.randn(*s, dtype=torch.bfloat16) * 0.4)(F) * 0.02),
            "w2": _w((lambda *s: torch.randn(*s, dtype=torch.bfloat16) * 0.4)(F, S) * (F ** -0.5)),
            "b2": _w((lambda *s: torch.randn(*s, dtype=torch.bfloat16) * 0.4)(S) * 0.02),
        }
    _B_in = _B_setup()

    return {
        "A_x": _A_in["x"],
        "A_wq": _A_in["wq"],
        "A_wk": _A_in["wk"],
        "A_wv": _A_in["wv"],
        "A_wo": _A_in["wo"],
        "B_x": _B_in["x"],
        "B_w1": _B_in["w1"],
        "B_b1": _B_in["b1"],
        "B_w2": _B_in["w2"],
        "B_b2": _B_in["b2"],
    }

def reference(A_x, A_wq, A_wk, A_wv, A_wo, B_x, B_w1, B_b1, B_w2, B_b2):
    def _A_ref(x, wq, wk, wv, wo):
        B, S, D = x.shape
        H = 32
        Dh = D // H
        q = (lambda x, *d: x.permute(*d))((x @ wq).reshape(B, S, H, Dh), 0, 2, 1, 3)
        k = x @ wk
        v = x @ wv
        scores = torch.einsum("bhqd,bkd->bhqk", q, k) * (Dh ** -0.5)
        p = _softmax(scores, axis=-1)
        o = torch.einsum("bhqk,bkd->bhqd", p, v)
        return (lambda x, *d: x.permute(*d))(o, 0, 2, 1, 3).reshape(B, S, H * Dh) @ wo

    def _B_ref(x, w1, b1, w2, b2):
        B, S, D = x.shape
        h = _layernorm(x, axis=-1)
        u = torch.einsum("bsd,sf->bdf", h, w1) + b1
        u = _gelu(u)
        y = torch.einsum("bdf,fs->bds", u, w2) + b2
        return (lambda x, *d: x.permute(*d))(y, 0, 2, 1)

    x = A_x
    x = x + _A_ref(_layernorm(x, axis=-1), A_wq, A_wk, A_wv, A_wo)
    x = x + _B_ref(_layernorm(x, axis=-1), B_w1, B_b1, B_w2, B_b2)
    x = x + _A_ref(_layernorm(x, axis=-1), A_wq, A_wk, A_wv, A_wo)
    x = x + _B_ref(_layernorm(x, axis=-1), B_w1, B_b1, B_w2, B_b2)
    return x
```

```cpp
#include <hip/hip_runtime.h>
#include <hip/hip_bf16.h>
#include <stdint.h>

typedef unsigned short u16;
typedef short bf16x8 __attribute__((ext_vector_type(8)));
typedef short bf16x4 __attribute__((ext_vector_type(4)));
typedef float f32x16 __attribute__((ext_vector_type(16)));
typedef float f32x4 __attribute__((ext_vector_type(4)));
typedef float f32x2 __attribute__((ext_vector_type(2)));
typedef unsigned u32x4 __attribute__((ext_vector_type(4)));
typedef unsigned u32x2 __attribute__((ext_vector_type(2)));
typedef __bf16 bf2_t __attribute__((ext_vector_type(2)));
typedef __attribute__((address_space(3))) char* lds_ptr;
typedef __attribute__((address_space(3))) const char* lds_cptr;
typedef __attribute__((address_space(1))) unsigned long long gu64;

#define DI __device__ __forceinline__
#define SBAR() __builtin_amdgcn_sched_barrier(0)
#define PIN(x) asm volatile("" : "+v"(x))
#define MFMA32_16(a, b, c) __builtin_amdgcn_mfma_f32_32x32x16_bf16(a, b, c, 0, 0, 0)
#define MFMA32_8(a, b, c) __builtin_amdgcn_mfma_f32_32x32x8bf16_1k(a, b, c, 0, 0, 0)
#define MFMA16_32(a, b, c) __builtin_amdgcn_mfma_f32_16x16x32_bf16(a, b, c, 0, 0, 0)
#define EX2(x) __builtin_amdgcn_exp2f(x)
#define RAW_BARRIER() asm volatile("s_waitcnt lgkmcnt(0)\n\ts_barrier" ::: "memory")
#define VMCNT(n) asm volatile("s_waitcnt vmcnt(%0)" :: "i"(n) : "memory")

#define SST_DECL do {} while (0)
#define SST(i) do {} while (0)
#define SST_FLUSH(ws) do {} while (0)
constexpr int S_ = 2048, D_ = 640, F_ = 1728;
constexpr float LN_EPS = 1e-5f;
constexpr float QSCALE = 0.22360679774997896f * 1.4426950408889634f;
constexpr float SAFE_BOUND = 60.0f;

DI unsigned pk2(float a, float b) { f32x2 v = {a, b}; bf2_t r = __builtin_convertvector(v, bf2_t); return __builtin_bit_cast(unsigned, r); }
DI float bf_lo(unsigned u) { return __uint_as_float(u << 16); }
DI float bf_hi(unsigned u) { return __uint_as_float(u & 0xffff0000u); }
DI float bf2f(u16 v) { return __uint_as_float((unsigned)v << 16); }
DI float dot2(unsigned a, unsigned b, float c) { return __builtin_amdgcn_fdot2_f32_bf16(__builtin_bit_cast(bf2_t, a), __builtin_bit_cast(bf2_t, b), c, false); }
DI bf16x4 tr_read(lds_cptr p) { return __builtin_amdgcn_ds_read_tr16_b64_v4i16((__attribute__((address_space(3))) bf16x4*)p); }
DI bf16x8 cat8(bf16x4 a, bf16x4 b) { return __builtin_shufflevector(a, b, 0, 1, 2, 3, 4, 5, 6, 7); }
DI void glds16(const void* g, lds_ptr l) {
    __builtin_amdgcn_global_load_lds((const __attribute__((address_space(1))) void*)g, (__attribute__((address_space(3))) void*)l, 16, 0, 0);
}
DI float xor32(float v) { return __shfl_xor(v, 32); }
DI float add32(float v) {
    auto r = __builtin_amdgcn_permlane32_swap(__float_as_uint(v), __float_as_uint(v), false, false);
    return __uint_as_float(r[0]) + __uint_as_float(r[1]);
}
DI float add16(float v) {
    auto r = __builtin_amdgcn_permlane16_swap(__float_as_uint(v), __float_as_uint(v), false, false);
    return __uint_as_float(r[0]) + __uint_as_float(r[1]);
}
DI u32x4 gather8(const u16* s, int ld) {
    u32x4 r;
#pragma unroll
    for (int e = 0; e < 4; ++e) r[e] = (unsigned)s[(2 * e) * ld] | ((unsigned)s[(2 * e + 1) * ld] << 16);
    return r;
}

constexpr size_t OFF_Q = 0;
constexpr size_t OFF_K1 = OFF_Q + (size_t)2048 * 32 * 48;
constexpr size_t OFF_K2 = OFF_K1 + 2048 * 32;
constexpr size_t OFF_V = OFF_K2 + 2048 * 8;
constexpr size_t OFF_KN = OFF_V + 128 * 640;
constexpr size_t OFF_QN = OFF_KN + 512;
constexpr size_t OFF_CS = OFF_QN + 4096;
constexpr size_t OFF_CONST = OFF_CS + 4096;
constexpr size_t OFF_XG = OFF_CONST + 256;
constexpr size_t OFF_XS = OFF_XG + 262144;
constexpr size_t OFF_X1 = OFF_XS + 131072;
constexpr size_t OFF_H = OFF_X1 + (size_t)S_ * D_ * 2;
constexpr size_t OFF_U = OFF_H + (size_t)S_ * D_ * 2;
constexpr size_t OFF_X2 = OFF_U + (size_t)D_ * F_ * 2;
constexpr size_t OFF_O = OFF_X2 + (size_t)S_ * D_ * 2;
constexpr size_t OFF_W1F = OFF_O + (size_t)S_ * D_ * 2;
constexpr size_t OFF_W2F = OFF_W1F + (size_t)S_ * F_ * 2;
constexpr size_t OFF_WQF = OFF_W2F + (size_t)S_ * F_ * 2;
constexpr size_t OFF_WOF = OFF_WQF + (size_t)44 * 20 * 1024;
constexpr int NPREP = 2 * 6912;

DI void chan8(const f32x4 (&st)[4], float& mean, float& var) {
    float m = 0.f, m2 = 0.f;
#pragma unroll
    for (int i = 0; i < 4; ++i) { m += st[i][0] + st[i][2]; m2 += st[i][1] + st[i][3]; }
    m *= 0.125f;
#pragma unroll
    for (int i = 0; i < 4; ++i) { const float d0 = st[i][0] - m, d1 = st[i][2] - m; m2 += 80.f * (d0 * d0 + d1 * d1); }
    mean = m; var = m2 * (1.f / 640.f);
}

constexpr int QKV_LDS = 128000;
template <bool FAST>
__global__ __launch_bounds__(256, 1) void k_qkv(const u16* __restrict__ x, const u16* __restrict__ wq, const u16* __restrict__ wk,
                                                const u16* __restrict__ wv, const u16* __restrict__ wo, char* __restrict__ ws) {
    extern __shared__ __attribute__((aligned(16))) char lds_raw[];
    lds_ptr lds = (lds_ptr)lds_raw;
    char* L = lds_raw;
    const int tid = threadIdx.x, lane = tid & 63, w = __builtin_amdgcn_readfirstlane(tid >> 6);
    const int b = blockIdx.x, slot = b >> 3, tb = (b & 7) * 4 + (slot >> 3), cg = slot & 7;
    const int mt0 = cg >> 1, isv = cg & 1;
    const int n16 = lane & 15, g = lane >> 4;
    SST_DECL; SST(0);
    f32x4 acc[4][5], acck[2];
#pragma unroll
    for (int i = 0; i < 4; ++i)
#pragma unroll
        for (int j = 0; j < 5; ++j) acc[i][j] = f32x4{0.f, 0.f, 0.f, 0.f};
    acck[0] = f32x4{0.f, 0.f, 0.f, 0.f}; acck[1] = acck[0];
    f32x4 csq[5], cs0, cs1, stq[4], stk[4];
    float sm[4] = {0.f, 0.f, 0.f, 0.f}, sq[4] = {0.f, 0.f, 0.f, 0.f};
    float cs[5] = {0.f, 0.f, 0.f, 0.f, 0.f}, csk[2] = {0.f, 0.f};
    u32x4 pa, pb, pc; char *pa_dst = nullptr, *pb_dst = nullptr;
    *(u32x4*)(ws + OFF_XG + ((size_t)(tb * 64 + (tid >> 2)) * 8 + cg) * 16 + 0) = u32x4{0u, 0u, 0u, 0u};
    if (FAST) {
        const bf16x8* xb = (const bf16x8*)x + (size_t)(tb * 4 * 20 + 5 * w) * 64 + lane;
        const bf16x8* wb = (const bf16x8*)(ws + OFF_WQF) + (size_t)(5 * cg * 20 + 5 * w) * 64 + lane;
        const bf16x8* kb = (const bf16x8*)(ws + OFF_WQF) + (size_t)((40 + 2 * isv) * 20 + 5 * w) * 64 + lane;
        bf16x8 xf[5][4], xk[5], wf[5][5], wkf[5][2];
#define QKV_LD(ks) do { _Pragma("unroll") for (int i = 0; i < 4; ++i) xf[ks][i] = xb[(((i + w) & 3) * 20 + (ks)) * 64]; \
        _Pragma("unroll") for (int nt = 0; nt < 5; ++nt) wf[ks][nt] = wb[(nt * 20 + (ks)) * 64]; \
        xk[ks] = xb[(mt0 * 20 + (ks)) * 64]; wkf[ks][0] = kb[(ks) * 64]; wkf[ks][1] = kb[(20 + (ks)) * 64]; SBAR(); } while (0)
        QKV_LD(0); QKV_LD(1); QKV_LD(2);
#pragma unroll
        for (int ks = 0; ks < 5; ++ks) {
#pragma unroll
            for (int nt = 0; nt < 5; ++nt)
#pragma unroll
                for (int i = 0; i < 4; ++i) acc[i][nt] = MFMA16_32(wf[ks][nt], xf[ks][i], acc[i][nt]);
            acck[0] = MFMA16_32(wkf[ks][0], xk[ks], acck[0]);
            acck[1] = MFMA16_32(wkf[ks][1], xk[ks], acck[1]);
            SBAR();
            if (ks == 0) QKV_LD(3);
            if (ks == 1) QKV_LD(4);
            if (ks == 2) {
                const float* csp = (const float*)(ws + OFF_CS);
#pragma unroll
                for (int nt = 0; nt < 5; ++nt) csq[nt] = *(const f32x4*)(csp + 80 * cg + 16 * nt + 4 * g);
                cs0 = *(const f32x4*)(csp + 640 + 32 * isv + 4 * g); cs1 = *(const f32x4*)(csp + 640 + 32 * isv + 16);
                const f32x4* sp = (const f32x4*)(ws + OFF_XS) + (size_t)(tb * 64 + n16) * 4;
#pragma unroll
                for (int i = 0; i < 4; ++i) { stq[i] = sp[16 * w * 4 + i]; stk[i] = sp[16 * mt0 * 4 + i]; }
                SBAR();
            }
        }
    } else {
        const char* src = (const char*)(wq + (size_t)(160 * w) * 640 + 80 * cg);
        const unsigned loff = (unsigned)(lane / 10) * 1280u + (unsigned)(lane % 10) * 16u;
        lds_ptr dst = lds + w * 25600;
        bf16x8 xf[4][5], xk[5];
        const u16* xr = x + (size_t)(tb * 64 + n16) * 640 + 160 * w + 8 * g;
#pragma unroll
        for (int i = 0; i < 4; ++i)
#pragma unroll
            for (int ks = 0; ks < 5; ++ks) xf[i][ks] = *(const bf16x8*)(xr + ((i + w) & 3) * 16 * 640 + 32 * ks);
#pragma unroll
        for (int ks = 0; ks < 5; ++ks) xk[ks] = *(const bf16x8*)(xr + mt0 * 16 * 640 + 32 * ks);
#pragma unroll
        for (int i = 0; i < 26; ++i) glds16(src + (size_t)i * 7680 + loff, dst + i * 960);
        if (lane < 40) glds16(src + (size_t)26 * 7680 + loff, dst + 26 * 960);
        const char* src2 = (const char*)((isv ? wv : wk) + (size_t)(160 * w) * 20);
        lds_ptr dst2 = lds + 102400 + w * 6400;
#pragma unroll
        for (int i = 0; i < 6; ++i) glds16(src2 + i * 1024 + lane * 16, dst2 + i * 1024);
        if (lane < 16) glds16(src2 + 6 * 1024 + lane * 16, dst2 + 6 * 1024);
        {
            const int T = b * 256 + tid, ln = T & 63, id = T >> 6;
            if (id < 800) {
                const int ks = id / 40, ct = id - ks * 40;
                pa = gather8(wo + (size_t)(32 * ks + 8 * (ln >> 4)) * 640 + 16 * ct + (ln & 15), 640);
                pa_dst = ws + OFF_WOF + (size_t)(ct * 20 + ks) * 1024 + ln * 16;
                pb = gather8(wq + (size_t)(32 * ks + 8 * (ln >> 4)) * 640 + 16 * ct + (ln & 15), 640);
                pb_dst = ws + OFF_WQF + (size_t)(ct * 20 + ks) * 1024 + ln * 16;
            } else if (id < 880) {
                const int j = id - 800, ks = j >> 2, ct = j & 3; int cn = 16 * (ct & 1) + (ln & 15); if (cn >= 20) cn = 0;
                pa = gather8(((ct & 2) ? wv : wk) + (size_t)(32 * ks + 8 * (ln >> 4)) * 20 + cn, 20);
                pa_dst = ws + OFF_WQF + (size_t)((40 + ct) * 20 + ks) * 1024 + ln * 16;
            } else if (T < 880 * 64 + 4) {
                const unsigned v = (T & 2) ? 0x3f803f80u : 0u;
                pa = u32x4{v, v, v, v};
                pa_dst = ws + OFF_CONST + (T & 3) * 16;
            }
        }
        SST(1);
        const unsigned ONES = 0x3f803f80u;
#pragma unroll
        for (int i = 0; i < 4; ++i)
#pragma unroll
            for (int ks = 0; ks < 5; ++ks) {
                u32x4 u = __builtin_bit_cast(u32x4, xf[i][ks]);
#pragma unroll
                for (int e = 0; e < 4; ++e) { sm[i] = dot2(u[e], ONES, sm[i]); sq[i] = dot2(u[e], u[e], sq[i]); }
            }
        SST(2);
        VMCNT(0);
        SST(3);
        const int q4 = (lane & 15) >> 2, p4 = lane & 3;
        lds_cptr wb = (lds_cptr)lds + w * 25600 + (8 * g + q4) * 160 + p4 * 8;
        lds_cptr kb = (lds_cptr)lds + 102400 + w * 6400 + (8 * g + q4) * 40 + p4 * 8;
        bf16x8 wf[2][7];
#define QKV_TR(ks, s_) do { _Pragma("unroll") for (int nt = 0; nt < 5; ++nt) wf[s_][nt] = cat8(tr_read(wb + (ks) * 5120 + nt * 32), tr_read(wb + (ks) * 5120 + 640 + nt * 32)); \
        _Pragma("unroll") for (int nt = 0; nt < 2; ++nt) wf[s_][5 + nt] = cat8(tr_read(kb + (ks) * 1280 + nt * 32), tr_read(kb + (ks) * 1280 + 160 + nt * 32)); SBAR(); } while (0)
        QKV_TR(0, 0);
#pragma unroll
        for (int ks = 0; ks < 5; ++ks) {
            if (ks < 4) QKV_TR(ks + 1, (ks + 1) & 1);
#pragma unroll
            for (int nt = 0; nt < 5; ++nt) {
                u32x4 u = __builtin_bit_cast(u32x4, wf[ks & 1][nt]);
#pragma unroll
                for (int e = 0; e < 4; ++e) cs[nt] = dot2(u[e], ONES, cs[nt]);
#pragma unroll
                for (int i = 0; i < 4; ++i) acc[i][nt] = MFMA16_32(wf[ks & 1][nt], xf[i][ks], acc[i][nt]);
            }
#pragma unroll
            for (int nt = 0; nt < 2; ++nt) {
                u32x4 u = __builtin_bit_cast(u32x4, wf[ks & 1][5 + nt]);
#pragma unroll
                for (int e = 0; e < 4; ++e) csk[nt] = dot2(u[e], ONES, csk[nt]);
                acck[nt] = MFMA16_32(wf[ks & 1][5 + nt], xk[ks], acck[nt]);
            }
            SBAR();
        }
#pragma unroll
        for (int i = 0; i < 4; ++i) { sm[i] = add32(add16(sm[i])); sq[i] = add32(add16(sq[i])); }
#pragma unroll
        for (int i = 0; i < 5; ++i) cs[i] = add32(add16(cs[i]));
#pragma unroll
        for (int i = 0; i < 2; ++i) csk[i] = add32(add16(csk[i]));
    }
    SST(4);
    if (!FAST) RAW_BARRIER();
    constexpr int R_OFF = 0, PK_OFF = 61440, ST_OFF = 69632, CS_OFF = 71680;
#pragma unroll
    for (int j = 1; j < 4; ++j) {
        const int o = (w + j) & 3;
#pragma unroll
        for (int nt = 0; nt < 5; ++nt) *(f32x4*)(L + R_OFF + (((o * 3 + (j - 1)) * 5 + nt) * 64 + lane) * 16) = acc[j][nt];
    }
    *(f32x4*)(L + PK_OFF + ((w * 2 + 0) * 64 + lane) * 16) = acck[0];
    *(f32x4*)(L + PK_OFF + ((w * 2 + 1) * 64 + lane) * 16) = acck[1];
    if (!FAST && g == 0) {
#pragma unroll
        for (int i = 0; i < 4; ++i) *(f32x2*)(L + ST_OFF + (w * 64 + ((i + w) & 3) * 16 + n16) * 8) = f32x2{sm[i], sq[i]};
#pragma unroll
        for (int nt = 0; nt < 5; ++nt) *(float*)(L + CS_OFF + w * 448 + (nt * 16 + n16) * 4) = cs[nt];
#pragma unroll
        for (int nt = 0; nt < 2; ++nt) *(float*)(L + CS_OFF + w * 448 + (80 + nt * 16 + n16) * 4) = csk[nt];
    }
    RAW_BARRIER();
    SST(5);
    auto stats = [&](int tl, const f32x4 (&st)[4], float& mean, float& rstd) {
        float var;
        if (FAST) chan8(st, mean, var);
        else {
            float s = 0.f, s2 = 0.f;
#pragma unroll
            for (int ww = 0; ww < 4; ++ww) { f32x2 v = *(const f32x2*)(L + ST_OFF + (ww * 64 + tl) * 8); s += v[0]; s2 += v[1]; }
            mean = s * (1.f / 640.f);
            var = fmaxf(s2 * (1.f / 640.f) - mean * mean, 0.f);
        }
        rstd = __builtin_amdgcn_rsqf(var + LN_EPS);
    };
    {
        float mean, rstd; stats(16 * w + n16, stq, mean, rstd);
        const float sc = rstd * QSCALE;
        char* qrow = ws + OFF_Q + (size_t)(tb * 8 + cg) * 12288 + (16 * w + n16) * 48;
        float hn[4] = {0.f, 0.f, 0.f, 0.f};
#pragma unroll
        for (int nt = 0; nt < 5; ++nt) {
            f32x4 a = acc[0][nt], s;
#pragma unroll
            for (int j = 0; j < 3; ++j) a += *(const f32x4*)(L + R_OFF + (((w * 3 + j) * 5 + nt) * 64 + lane) * 16);
            if (FAST) s = csq[nt];
            else {
                s = f32x4{0.f, 0.f, 0.f, 0.f};
#pragma unroll
                for (int ww = 0; ww < 4; ++ww) s += *(const f32x4*)(L + CS_OFF + ww * 448 + (nt * 16 + 4 * g) * 4);
                if (tb == 0 && w == 0 && n16 == 0) *(f32x4*)(ws + OFF_CS + (80 * cg + 16 * nt + 4 * g) * 4) = s;
            }
            const int G = 4 * nt + g, hl = G / 5, dq = G - 5 * hl;
            const unsigned u0 = pk2((a[0] - mean * s[0]) * sc, (a[1] - mean * s[1]) * sc);
            const unsigned u1 = pk2((a[2] - mean * s[2]) * sc, (a[3] - mean * s[3]) * sc);
            *(u32x2*)(qrow + hl * 3072 + dq * 8) = u32x2{u0, u1};
            const float n2 = dot2(u1, u1, dot2(u0, u0, 0.f));
#pragma unroll
            for (int hh = 0; hh < 4; ++hh)
                if ((4 * nt) / 5 == hh || (4 * nt + 3) / 5 == hh) hn[hh] += (hl == hh) ? n2 : 0.f;
        }
#pragma unroll
        for (int hh = 0; hh < 4; ++hh) hn[hh] = add32(add16(hn[hh]));
        const float n2g = g == 0 ? hn[0] : (g == 1 ? hn[1] : (g == 2 ? hn[2] : hn[3]));
        const unsigned qb = pk2(__builtin_amdgcn_sqrtf(n2g), 0.f);
        *(u32x2*)(qrow + g * 3072 + 40) = u32x2{qb, 0u};
        float m = bf_lo(qb);
        if (!(m == m)) m = INFINITY;
#pragma unroll
        for (int s = 1; s < 64; s <<= 1) m = fmaxf(m, __shfl_xor(m, s));
        if (lane == 0) *(float*)(ws + OFF_QN + ((tb * 8 + cg) * 4 + w) * 4) = m;
    }
    SST(6);
    if (w == 3) {
        const int t16 = tb * 4 + mt0;
        float mean, rstd; stats(mt0 * 16 + n16, stk, mean, rstd);
        f32x4 a0 = {0.f, 0.f, 0.f, 0.f}, a1 = a0, s0 = a0, s1 = a0;
#pragma unroll
        for (int ww = 0; ww < 4; ++ww) {
            a0 += *(const f32x4*)(L + PK_OFF + ((ww * 2 + 0) * 64 + lane) * 16);
            a1 += *(const f32x4*)(L + PK_OFF + ((ww * 2 + 1) * 64 + n16) * 16);
            if (!FAST) {
                s0 += *(const f32x4*)(L + CS_OFF + ww * 448 + (80 + 4 * g) * 4);
                s1 += *(const f32x4*)(L + CS_OFF + ww * 448 + 96 * 4);
            }
        }
        if (FAST) { s0 = cs0; s1 = cs1; }
        else if (tb == 0 && cg < 2 && n16 == 0) {
            *(f32x4*)(ws + OFF_CS + (640 + 32 * isv + 4 * g) * 4) = s0;
            if (g == 0) *(f32x4*)(ws + OFF_CS + (640 + 32 * isv + 16) * 4) = s1;
        }
        const unsigned u0 = pk2((a0[0] - mean * s0[0]) * rstd, (a0[1] - mean * s0[1]) * rstd);
        const unsigned u1 = pk2((a0[2] - mean * s0[2]) * rstd, (a0[3] - mean * s0[3]) * rstd);
        const unsigned u2 = pk2((a1[0] - mean * s1[0]) * rstd, (a1[1] - mean * s1[1]) * rstd);
        const unsigned u3 = pk2((a1[2] - mean * s1[2]) * rstd, (a1[3] - mean * s1[3]) * rstd);
        if (!isv) {
            const int key = t16 * 16 + n16;
            *(u32x2*)(ws + OFF_K1 + key * 32 + g * 8) = u32x2{u0, u1};
            if (g == 0) *(u32x2*)(ws + OFF_K2 + key * 8) = u32x2{u2, u3};
            float n2 = dot2(u1, u1, dot2(u0, u0, 0.f));
            n2 = add32(add16(n2));
            n2 = dot2(u3, u3, dot2(u2, u2, n2));
            if (!(n2 == n2)) n2 = INFINITY;
#pragma unroll
            for (int s = 1; s < 16; s <<= 1) n2 = fmaxf(n2, __shfl_xor(n2, s));
            if (lane == 0) *(float*)(ws + OFF_KN + t16 * 4) = n2;
        } else {
            u16* vb = (u16*)(ws + OFF_V + t16 * 640 + (n16 >> 3) * 320) + (n16 & 7);
            vb[(4 * g + 0) * 8] = (u16)u0; vb[(4 * g + 1) * 8] = (u16)(u0 >> 16);
            vb[(4 * g + 2) * 8] = (u16)u1; vb[(4 * g + 3) * 8] = (u16)(u1 >> 16);
            if (g == 0) { vb[16 * 8] = (u16)u2; vb[17 * 8] = (u16)(u2 >> 16); vb[18 * 8] = (u16)u3; vb[19 * 8] = (u16)(u3 >> 16); }
        }
    }
    if (!FAST) {
        if (pa_dst) *(u32x4*)pa_dst = pa;
        if (pb_dst) *(u32x4*)pb_dst = pb;
    }
    SST(7); SST_FLUSH(ws);
}

constexpr int PBW = 2688, ATT_LDS = 32 * PBW;
template <bool PREP>
__global__ __launch_bounds__(512, 2) void k_attn(char* __restrict__ ws, const u16* __restrict__ w1, const u16* __restrict__ w2) {
    extern __shared__ __attribute__((aligned(16))) char lds_raw[];
    char* L = lds_raw;
    const int tid = threadIdx.x, lane = tid & 63, w = __builtin_amdgcn_readfirstlane(tid >> 6);
    const int b = blockIdx.x, slot = b >> 3, tb = (b & 7) * 4 + (slot >> 3), cg = slot & 7;
    const int r32 = lane & 31, h = lane >> 5, n16 = lane & 15, g = lane >> 4;
    const int cgp = w & 1, kq = w >> 1;
    SST_DECL; SST(0);
    float kn0 = *(const float*)(ws + OFF_KN + lane * 4), kn1 = *(const float*)(ws + OFF_KN + 256 + lane * 4);
    float qmax = *(const float*)(ws + OFF_QN + ((tb * 8 + cg) * 4 + (lane & 3)) * 4);
    bf16x8 q1[4]; bf16x4 q2[4];
    {
        const char* qg = ws + OFF_Q + (size_t)(tb * 8 + cg) * 12288 + cgp * 6144 + r32 * 48;
#pragma unroll
        for (int j = 0; j < 4; ++j) { q1[j] = *(const bf16x8*)(qg + j * 1536 + 16 * h); q2[j] = *(const bf16x4*)(qg + j * 1536 + 32 + 8 * h); }
    }
    SBAR();
    const int kr = (r32 & 19) | ((r32 & 4) << 1) | ((r32 & 8) >> 1);
    const unsigned ko1 = (unsigned)OFF_K1 + (unsigned)(512 * kq + kr) * 32u + 16u * h;
    const unsigned ko2 = h ? (unsigned)OFF_CONST + 32u : (unsigned)OFF_K2 + (unsigned)(512 * kq + kr) * 8u;
    const unsigned ks2 = h ? 0u : 256u;
    const unsigned vo = r32 < 20 ? (unsigned)OFF_V + (unsigned)(32 * kq) * 640u + (unsigned)(h * 20 + r32) * 16u
                                 : (unsigned)OFF_CONST + (r32 == 20 ? 32u : 0u);
    const unsigned vs = r32 < 20 ? 640u : 0u;
    bf16x8 k1[3], v0[3], v1[3]; bf16x4 k2[3];
#define K_LOAD(t) do { k1[(t) % 3] = *(const bf16x8*)(ws + ko1 + (t) * 1024); k2[(t) % 3] = *(const bf16x4*)(ws + (ko2 + (t) * ks2)); } while (0)
#define V_LOAD(t) do { v0[(t) % 3] = *(const bf16x8*)(ws + (vo + (2 * (t)) * vs)); v1[(t) % 3] = *(const bf16x8*)(ws + (vo + (2 * (t) + 1) * vs)); } while (0)
    K_LOAD(0); V_LOAD(0); SBAR(); K_LOAD(1); V_LOAD(1); SBAR();
    PIN(kn0); PIN(kn1);
    float kmax2 = fmaxf(kn0, kn1);
#pragma unroll
    for (int s = 1; s < 64; s <<= 1) kmax2 = fmaxf(kmax2, __shfl_xor(kmax2, s));
#pragma unroll
    for (int s = 1; s < 4; s <<= 1) qmax = fmaxf(qmax, __shfl_xor(qmax, s));
    const float kb = __builtin_amdgcn_sqrtf(kmax2);
    const bool safe = __builtin_amdgcn_readfirstlane((int)(qmax * kb <= SAFE_BOUND)) != 0;
    const f32x16 zero16 = {0.f, 0.f, 0.f, 0.f, 0.f, 0.f, 0.f, 0.f, 0.f, 0.f, 0.f, 0.f, 0.f, 0.f, 0.f, 0.f};
    if (safe) {
        if (h) {
#pragma unroll
            for (int j = 0; j < 4; ++j) q2[j][0] = (short)pk2(-bf2f((u16)q2[j][0]) * kb, 0.f);
        }
    } else {
        float m[4];
#pragma unroll
        for (int j = 0; j < 4; ++j) { m[j] = -INFINITY; if (h) q2[j] = bf16x4{0, 0, 0, 0}; }
        for (int t = 0; t < 16; ++t) {
            const bf16x8 ka = *(const bf16x8*)(ws + ko1 + t * 1024);
            const bf16x4 kc = *(const bf16x4*)(ws + (ko2 + t * ks2));
#pragma unroll
            for (int j = 0; j < 4; ++j) {
                f32x16 s = MFMA32_16(ka, q1[j], zero16);
                SBAR(); asm volatile("s_nop 15" ::: "memory"); SBAR();
                s = MFMA32_8(kc, q2[j], s);
#pragma unroll
                for (int e = 0; e < 16; ++e) m[j] = fmaxf(m[j], s[e]);
                SBAR();
            }
        }
#pragma unroll
        for (int j = 0; j < 4; ++j) {
            const float mm = fmaxf(m[j], xor32(m[j]));
            if (h == 0) *(float*)(L + ((w * 4 + j) * 32 + r32) * 4) = mm;
        }
        RAW_BARRIER();
#pragma unroll
        for (int j = 0; j < 4; ++j) {
            float mm = -INFINITY;
#pragma unroll
            for (int qq = 0; qq < 4; ++qq) mm = fmaxf(mm, *(const float*)(L + (((2 * qq + cgp) * 4 + j) * 32 + r32) * 4));
            const unsigned hi = __float_as_uint(mm) & 0xffff0000u;
            const float r1 = mm - __uint_as_float(hi);
            const unsigned mid = __float_as_uint(r1) & 0xffff0000u;
            const float r2 = r1 - __uint_as_float(mid);
            const unsigned lo = __float_as_uint(r2) & 0xffff0000u;
            if (h) q2[j] = __builtin_bit_cast(bf16x4, u32x2{hi ^ 0x80000000u, ((mid ^ 0x80000000u) >> 16) | (lo ^ 0x80000000u)});
        }
        RAW_BARRIER();
    }
    SST(1);
    f32x16 o[4], sa, sb;
#pragma unroll
    for (int j = 0; j < 4; ++j) o[j] = zero16;
    u32x4 p1prev;
    sa = MFMA32_16(k1[0], q1[0], zero16);
    SBAR(); asm volatile("s_nop 15" ::: "memory"); SBAR();
    sa = MFMA32_8(k2[0], q2[0], sa);
    SBAR(); asm volatile("s_nop 15" ::: "memory"); SBAR();
    u32x4 prep; char* prep_dst = nullptr;
#pragma unroll
    for (int t = 0; t < 16; ++t) {
#pragma unroll
        for (int j = 0; j < 4; ++j) {
            const int it = 4 * t + j;
            f32x16& cur = (it & 1) ? sb : sa; f32x16& nxt = (it & 1) ? sa : sb;
            const int tn = j < 3 ? t : t + 1, jn = (j + 1) & 3;
            const int tp = j > 0 ? t : t - 1, jp = (j + 3) & 3;
            u32x4 p0, p1;
            if (j == 0 && t + 2 < 16) K_LOAD(t + 2);
            if (j == 1 && t + 2 < 16) V_LOAD(t + 2);
            if (PREP && j == 2 && (t & 1) == 1 && t >= 3) { *(u32x4*)prep_dst = prep; }
            if (PREP && j == 2 && (t & 1) == 1 && t < 15) {
                int id = (t >> 1) * 2048 + b * 8 + w; if (id >= NPREP) id -= 2048;
                if (id < 6912) { const int ks = id / 108, ft = id - ks * 108;
                                 prep = gather8(w1 + (size_t)(32 * ks + 8 * g) * F_ + 16 * ft + n16, F_);
                                 prep_dst = ws + OFF_W1F + (size_t)(ft * 64 + ks) * 1024 + lane * 16; }
                else { const int jj = id - 6912, ks = jj >> 7, st = jj & 127;
                       prep = gather8(w2 + (size_t)(32 * ks + 8 * g) * S_ + 16 * st + n16, S_);
                       prep_dst = ws + OFF_W2F + (size_t)(st * 54 + ks) * 1024 + lane * 16; }
            }
            SBAR();
            if (it < 63) nxt = MFMA32_16(k1[tn % 3], q1[jn], zero16);
            SBAR();
            p0[0] = pk2(EX2(cur[0]), EX2(cur[1])); p0[1] = pk2(EX2(cur[2]), EX2(cur[3]));
            PIN(p0); SBAR();
            if (it > 0) o[jp] = MFMA32_16(v1[(tp + 3) % 3], __builtin_bit_cast(bf16x8, p1prev), o[jp]);
            SBAR();
            p0[2] = pk2(EX2(cur[4]), EX2(cur[5])); p0[3] = pk2(EX2(cur[6]), EX2(cur[7]));
            PIN(p0); SBAR();
            if (it < 63) nxt = MFMA32_8(k2[tn % 3], q2[jn], nxt);
            SBAR();
            p1[0] = pk2(EX2(cur[8]), EX2(cur[9])); p1[1] = pk2(EX2(cur[10]), EX2(cur[11]));
            PIN(p1); SBAR();
            o[j] = MFMA32_16(v0[t % 3], __builtin_bit_cast(bf16x8, p0), o[j]);
            SBAR();
            p1[2] = pk2(EX2(cur[12]), EX2(cur[13])); p1[3] = pk2(EX2(cur[14]), EX2(cur[15]));
            PIN(p1); SBAR();
            p1prev = p1;
        }
        if (t == 7) SST(2);
    }
    o[3] = MFMA32_16(v1[15 % 3], __builtin_bit_cast(bf16x8, p1prev), o[3]);
    SST(3);
#pragma unroll
    for (int j = 0; j < 4; ++j) {
        char* pb = L + (kq * 8 + cgp * 4 + j) * PBW;
        *(f32x4*)(pb + lane * 16) = f32x4{o[j][0], o[j][1], o[j][2], o[j][3]};
        *(f32x4*)(pb + 1024 + lane * 16) = f32x4{o[j][4], o[j][5], o[j][6], o[j][7]};
        if (h == 0) *(f32x4*)(pb + 2048 + r32 * 16) = f32x4{o[j][8], o[j][9], o[j][10], o[j][11]};
        else *(float*)(pb + 2560 + r32 * 4) = o[j][8];
    }
    RAW_BARRIER();
    SST(4);
    {
        const char* pb = L + w * PBW;
        f32x4 a0 = {0.f, 0.f, 0.f, 0.f}, a1 = a0, a2 = a0; float l = 0.f;
#pragma unroll
        for (int qq = 0; qq < 4; ++qq) {
            a0 += *(const f32x4*)(pb + qq * 8 * PBW + lane * 16);
            a1 += *(const f32x4*)(pb + qq * 8 * PBW + 1024 + lane * 16);
            a2 += *(const f32x4*)(pb + qq * 8 * PBW + 2048 + r32 * 16);
            l += *(const float*)(pb + qq * 8 * PBW + 2560 + r32 * 4);
        }
        const float inv = __builtin_amdgcn_rcpf(l);
        const int tl = 32 * (w & 1) + r32, hd0 = 80 * cg + 20 * (w >> 1);
        char* ob = ws + OFF_O + (size_t)(tb * 4 + (tl >> 4)) * 20480 + (tl & 15) * 16;
        auto st = [&](int hd, const f32x4& a) {
            *(u32x2*)(ob + (hd >> 5) * 1024 + ((hd & 31) >> 3) * 256 + (hd & 7) * 2) = u32x2{pk2(a[0] * inv, a[1] * inv), pk2(a[2] * inv, a[3] * inv)};
        };
        st(hd0 + 4 * h, a0); st(hd0 + 8 + 4 * h, a1);
        if (h == 0) st(hd0 + 16, a2);
    }
    SST(5); SST_FLUSH(ws);
}

template <bool XF>
__global__ __launch_bounds__(256, 1) void k_wo(char* __restrict__ ws, const u16* __restrict__ xin) {
    __shared__ __attribute__((aligned(16))) char L[61440];
    const int tid = threadIdx.x, lane = tid & 63, w = __builtin_amdgcn_readfirstlane(tid >> 6);
    const int b = blockIdx.x, slot = b >> 3, tb = (b & 7) * 4 + (slot >> 3), cg = slot & 7;
    const int n16 = lane & 15, g = lane >> 4;
    SST_DECL; SST(0);
    f32x4 acc[4][5];
#pragma unroll
    for (int i = 0; i < 4; ++i)
#pragma unroll
        for (int j = 0; j < 5; ++j) acc[i][j] = f32x4{0.f, 0.f, 0.f, 0.f};
    const int tok = tb * 64 + 16 * w + n16;
    u32x2 xi[5];
    {
        const bf16x8* xb = (const bf16x8*)(ws + OFF_O) + (size_t)(tb * 4 * 20 + 5 * w) * 64 + lane;
        const bf16x8* wb = (const bf16x8*)(ws + OFF_WOF) + (size_t)(5 * cg * 20 + 5 * w) * 64 + lane;
        bf16x8 xf[5][4], wf[5][5];
#define WO_LD(ks) do { _Pragma("unroll") for (int i = 0; i < 4; ++i) xf[ks][i] = xb[(((i + w) & 3) * 20 + (ks)) * 64]; \
        _Pragma("unroll") for (int nt = 0; nt < 5; ++nt) wf[ks][nt] = wb[(nt * 20 + (ks)) * 64]; SBAR(); } while (0)
        WO_LD(0); WO_LD(1); WO_LD(2);
#pragma unroll
        for (int ks = 0; ks < 5; ++ks) {
#pragma unroll
            for (int nt = 0; nt < 5; ++nt)
#pragma unroll
                for (int i = 0; i < 4; ++i) acc[i][nt] = MFMA16_32(wf[ks][nt], xf[ks][i], acc[i][nt]);
            SBAR();
            if (ks == 0) WO_LD(3);
            if (ks == 1) WO_LD(4);
            if (ks == 2) {
#pragma unroll
                for (int nt = 0; nt < 5; ++nt) {
                    const int d = 80 * cg + 16 * nt + 4 * g;
                    if (XF) xi[nt] = *(const u32x2*)(xin + ((size_t)((tok >> 4) * 20 + (d >> 5)) * 64 + ((d & 31) >> 3) * 16 + n16) * 8 + (d & 7));
                    else xi[nt] = *(const u32x2*)(xin + (size_t)tok * 640 + d);
                }
                SBAR();
            }
        }
    }
    SST(1);
#pragma unroll
    for (int j = 1; j < 4; ++j) {
        const int o = (w + j) & 3;
#pragma unroll
        for (int nt = 0; nt < 5; ++nt) *(f32x4*)(L + (((o * 3 + (j - 1)) * 5 + nt) * 64 + lane) * 16) = acc[j][nt];
    }
    RAW_BARRIER();
    SST(2);
    float xv[20];
    {
        u16* x1r = (u16*)(ws + OFF_X1) + (size_t)tok * 640 + 80 * cg + 4 * g;
        float s = 0.f; u32x2 x1o[5];
#pragma unroll
        for (int nt = 0; nt < 5; ++nt) {
            f32x4 a = acc[0][nt];
#pragma unroll
            for (int j = 0; j < 3; ++j) a += *(const f32x4*)(L + (((w * 3 + j) * 5 + nt) * 64 + lane) * 16);
            const unsigned u0 = pk2(bf_lo(xi[nt][0]) + a[0], bf_hi(xi[nt][0]) + a[1]);
            const unsigned u1 = pk2(bf_lo(xi[nt][1]) + a[2], bf_hi(xi[nt][1]) + a[3]);
            x1o[nt] = u32x2{u0, u1};
            xv[4 * nt] = bf_lo(u0); xv[4 * nt + 1] = bf_hi(u0); xv[4 * nt + 2] = bf_lo(u1); xv[4 * nt + 3] = bf_hi(u1);
            s += (xv[4 * nt] + xv[4 * nt + 1]) + (xv[4 * nt + 2] + xv[4 * nt + 3]);
        }
        const float mw = add32(add16(s)) * (1.f / 80.f);
        float s2 = 0.f;
#pragma unroll
        for (int i = 0; i < 20; ++i) { const float dd = xv[i] - mw; s2 += dd * dd; }
        s2 = add32(add16(s2));
        gu64* gp = (gu64*)(ws + OFF_XG) + ((size_t)tok * 8 + cg) * 2;
        if (g < 2) __hip_atomic_store(gp + g, (1ull << 32) | __float_as_uint(g ? s2 : mw), __ATOMIC_RELAXED, __HIP_MEMORY_SCOPE_AGENT);
#pragma unroll
        for (int nt = 0; nt < 5; ++nt) *(u32x2*)(x1r + 16 * nt) = x1o[nt];
    }
    SST(3);
    float mean, var;
    {
        gu64* gp = (gu64*)(ws + OFF_XG) + ((size_t)tok * 8 + 2 * g) * 2;
        unsigned long long v[4];
        for (int spin = 0; spin < (1 << 22); ++spin) {
            bool ok = true;
#pragma unroll
            for (int i = 0; i < 4; ++i) { v[i] = __hip_atomic_load(gp + i, __ATOMIC_RELAXED, __HIP_MEMORY_SCOPE_AGENT); ok &= (v[i] >> 32) == 1ull; }
            if (__all(ok)) break;
            __builtin_amdgcn_s_sleep(1);
        }
        const float m0 = __uint_as_float((unsigned)v[0]), m1 = __uint_as_float((unsigned)v[2]);
        mean = add32(add16(m0 + m1)) * 0.125f;
        const float d0 = m0 - mean, d1 = m1 - mean;
        var = add32(add16(__uint_as_float((unsigned)v[1]) + __uint_as_float((unsigned)v[3]) + 80.f * (d0 * d0 + d1 * d1))) * (1.f / 640.f);
    }
    SST(4);
    {
        const float r1 = __builtin_amdgcn_rsqf(var + LN_EPS);
        const float r2 = r1 * __builtin_amdgcn_rsqf(var * r1 * r1 + LN_EPS);
        char* hb = L + w * 15360;
#pragma unroll
        for (int nt = 0; nt < 5; ++nt)
            *(u32x2*)(hb + n16 * 176 + (16 * nt + 4 * g) * 2) =
                u32x2{pk2((xv[4 * nt] - mean) * r2, (xv[4 * nt + 1] - mean) * r2), pk2((xv[4 * nt + 2] - mean) * r2, (xv[4 * nt + 3] - mean) * r2)};
        asm volatile("s_waitcnt lgkmcnt(0)" ::: "memory");
        const int q4 = (lane & 15) >> 2, p4 = lane & 3;
        lds_cptr hr = (lds_cptr)(lds_ptr)L + w * 15360 + (8 * (g & 1) + q4) * 176 + p4 * 8 + (g >> 1) * 32;
        const int s0 = tb * 64 + 16 * w + 8 * (g & 1);
        u16* hf = (u16*)(ws + OFF_H) + ((size_t)(5 * cg + (g >> 1)) * 64 + (s0 >> 5)) * 512 + (((s0 & 31) >> 3) * 16 + n16) * 8;
        const bf16x8 f0 = cat8(tr_read(hr), tr_read(hr + 704));
        const bf16x8 f1 = cat8(tr_read(hr + 64), tr_read(hr + 704 + 64));
        const bf16x8 f2 = cat8(tr_read(hr + (g < 2 ? 128 : 0)), tr_read(hr + 704 + (g < 2 ? 128 : 0)));
        *(bf16x8*)(hf) = f0;
        *(bf16x8*)(hf + 2 * 64 * 512) = f1;
        if (g < 2) *(bf16x8*)(hf + 4 * 64 * 512) = f2;
    }
    SST(5); SST_FLUSH(ws);
}

template <int MODE, int NS, int PF>
DI void gemm_body(const bf16x8* __restrict__ po, const bf16x8* __restrict__ pf, const int (&ooff)[4], int fstride, f32x4 (&acc)[4][5]) {
    bf16x8 ro[PF][4], rf[PF][5];
#pragma unroll
    for (int s = 0; s < PF; ++s) {
#pragma unroll
        for (int j = 0; j < 4; ++j) ro[s][j] = po[ooff[j] + s * 64];
#pragma unroll
        for (int i = 0; i < 5; ++i) rf[s][i] = pf[i * fstride + s * 64];
        SBAR();
    }
#pragma unroll
    for (int s = 0; s < NS; ++s) {
        const int sl = s % PF;
        SBAR();
#pragma unroll
        for (int j = 0; j < 4; ++j)
#pragma unroll
            for (int i = 0; i < 5; ++i) {
                if (MODE == 1) acc[j][i] = MFMA16_32(ro[sl][j], rf[sl][i], acc[j][i]);
                else acc[j][i] = MFMA16_32(rf[sl][i], ro[sl][j], acc[j][i]);
            }
        SBAR();
        if (s + PF < NS) {
#pragma unroll
            for (int j = 0; j < 4; ++j) ro[sl][j] = po[ooff[j] + (s + PF) * 64];
#pragma unroll
            for (int i = 0; i < 5; ++i) rf[sl][i] = pf[i * fstride + (s + PF) * 64];
        }
    }
}

template <int MODE>
__global__ __launch_bounds__(256, 1) void k_gemm(char* __restrict__ ws, const u16* __restrict__ bias, u16* __restrict__ out) {
    __shared__ __attribute__((aligned(16))) f32x4 red[4 * 3 * 5 * 64];
    const int tid = threadIdx.x, lane = tid & 63, w = __builtin_amdgcn_readfirstlane(tid >> 6);
    const int b = blockIdx.x, xcd = b & 7, slot = b >> 3;
    const int n16 = lane & 15, g = lane >> 4;
    constexpr int KS = MODE == 1 ? 64 : 54;
    int ot, dt;
    if (MODE == 1) {
        ot = 7 * (xcd >> 1) + (slot >> 2); dt = 4 * (xcd & 1) + (slot & 3);
        if (slot >= 28 || ot >= 27) return;
    } else {
        ot = 8 * (xcd >> 1) + (slot >> 2); dt = 4 * (xcd & 1) + (slot & 3);
    }
    SST_DECL; SST(0);
    u32x2 bb = {0u, 0u}, xi[5]; float bs = 0.f;
    if (MODE == 1) bb = *(const u32x2*)(bias + 64 * ot + 16 * w + 4 * g);
    else {
        const int s = 64 * ot + 16 * w + n16;
        bs = bf2f(bias[s]);
        const u16* x1 = (const u16*)(ws + OFF_X1) + (size_t)s * 640 + 80 * dt + 4 * g;
#pragma unroll
        for (int i = 0; i < 5; ++i) xi[i] = *(const u32x2*)(x1 + 16 * i);
    }
    const int k0 = MODE == 1 ? 16 * w : (w < 2 ? 14 * w : 28 + 13 * (w - 2));
    const bf16x8* po = (const bf16x8*)(ws + (MODE == 1 ? OFF_W1F : OFF_W2F)) + ((size_t)(4 * ot) * KS + k0) * 64 + lane;
    const bf16x8* pf = (const bf16x8*)(ws + (MODE == 1 ? OFF_H : OFF_U)) + ((size_t)(5 * dt) * KS + k0) * 64 + lane;
    int ooff[4];
#pragma unroll
    for (int j = 0; j < 4; ++j) ooff[j] = ((j + w) & 3) * KS * 64;
    f32x4 acc[4][5];
#pragma unroll
    for (int j = 0; j < 4; ++j)
#pragma unroll
        for (int i = 0; i < 5; ++i) acc[j][i] = f32x4{0.f, 0.f, 0.f, 0.f};
    if (MODE == 1) gemm_body<1, 16, 6>(po, pf, ooff, KS * 64, acc);
    else if (w < 2) gemm_body<2, 14, 6>(po, pf, ooff, KS * 64, acc);
    else gemm_body<2, 13, 6>(po, pf, ooff, KS * 64, acc);
    SST(1);
#pragma unroll
    for (int j = 1; j < 4; ++j) {
        const int o = (w + j) & 3;
#pragma unroll
        for (int i = 0; i < 5; ++i) red[((o * 3 + (j - 1)) * 5 + i) * 64 + lane] = acc[j][i];
    }
    RAW_BARRIER();
#pragma unroll
    for (int j = 0; j < 3; ++j)
#pragma unroll
        for (int i = 0; i < 5; ++i) acc[0][i] += red[((w * 3 + j) * 5 + i) * 64 + lane];
    SST(2);
    if (MODE == 1) {
        const int f = 64 * ot + 16 * w + 4 * g;
        const float bv[4] = {bf_lo(bb[0]), bf_hi(bb[0]), bf_lo(bb[1]), bf_hi(bb[1])};
        u16* uf = (u16*)(ws + OFF_U);
#pragma unroll
        for (int i = 0; i < 5; ++i) {
            float r[4];
#pragma unroll
            for (int e = 0; e < 4; ++e) {
                const float u = acc[0][i][e] + bv[e];
                const float z = u * (1.f + 0.044715f * u * u) * (-2.f * 0.7978845608028654f * 1.4426950408889634f);
                r[e] = u * __builtin_amdgcn_rcpf(1.f + EX2(z));
            }
            *(u32x2*)(uf + ((size_t)((5 * dt + i) * 54 + (f >> 5)) * 64 + ((f & 31) >> 3) * 16 + n16) * 8 + (f & 7)) =
                u32x2{pk2(r[0], r[1]), pk2(r[2], r[3])};
        }
    } else {
        const int s = 64 * ot + 16 * w + n16;
        float xv[20]; float sum = 0.f;
#pragma unroll
        for (int i = 0; i < 5; ++i) {
            const int d = 80 * dt + 16 * i + 4 * g;
            u16* o = MODE == 3 ? out + (size_t)s * 640 + d
                               : out + ((size_t)((4 * ot + w) * 20 + (d >> 5)) * 64 + ((d & 31) >> 3) * 16 + n16) * 8 + (d & 7);
            const unsigned u0 = pk2(bf_lo(xi[i][0]) + acc[0][i][0] + bs, bf_hi(xi[i][0]) + acc[0][i][1] + bs);
            const unsigned u1 = pk2(bf_lo(xi[i][1]) + acc[0][i][2] + bs, bf_hi(xi[i][1]) + acc[0][i][3] + bs);
            *(u32x2*)o = u32x2{u0, u1};
            xv[4 * i] = bf_lo(u0); xv[4 * i + 1] = bf_hi(u0); xv[4 * i + 2] = bf_lo(u1); xv[4 * i + 3] = bf_hi(u1);
            sum += (xv[4 * i] + xv[4 * i + 1]) + (xv[4 * i + 2] + xv[4 * i + 3]);
        }
        if (MODE == 2) {
            const float mw = add32(add16(sum)) * (1.f / 80.f);
            float s2 = 0.f;
#pragma unroll
            for (int i = 0; i < 20; ++i) { const float dd = xv[i] - mw; s2 += dd * dd; }
            s2 = add32(add16(s2));
            if (g == 0) *(f32x2*)(ws + OFF_XS + ((size_t)s * 8 + dt) * 8) = f32x2{mw, s2};
        }
    }
    SST(3); SST_FLUSH(ws);
}

extern "C" void kernel_launch(void* const* d_in, const int* in_sizes, int n_in, void* d_out, int out_size, void* d_ws,
                              size_t ws_size, hipStream_t stream) {
    const u16* x = (const u16*)d_in[0];
    const u16* wq = (const u16*)d_in[1];
    const u16* wk = (const u16*)d_in[2];
    const u16* wv = (const u16*)d_in[3];
    const u16* wo = (const u16*)d_in[4];
    const u16* w1 = (const u16*)d_in[6];
    const u16* b1 = (const u16*)d_in[7];
    const u16* w2 = (const u16*)d_in[8];
    const u16* b2 = (const u16*)d_in[9];
    char* ws = (char*)d_ws;
    u16* x2 = (u16*)(ws + OFF_X2);
    (void)hipFuncSetAttribute((const void*)k_qkv<false>, hipFuncAttributeMaxDynamicSharedMemorySize, QKV_LDS);
    (void)hipFuncSetAttribute((const void*)k_qkv<true>, hipFuncAttributeMaxDynamicSharedMemorySize, QKV_LDS);
    (void)hipFuncSetAttribute((const void*)k_attn<true>, hipFuncAttributeMaxDynamicSharedMemorySize, ATT_LDS);
    (void)hipFuncSetAttribute((const void*)k_attn<false>, hipFuncAttributeMaxDynamicSharedMemorySize, ATT_LDS);
    k_qkv<false><<<256, 256, QKV_LDS, stream>>>(x, wq, wk, wv, wo, ws);
    k_attn<true><<<256, 512, ATT_LDS, stream>>>(ws, w1, w2);
    k_wo<false><<<256, 256, 0, stream>>>(ws, x);
    k_gemm<1><<<256, 256, 0, stream>>>(ws, b1, nullptr);
    k_gemm<2><<<256, 256, 0, stream>>>(ws, b2, x2);
    k_qkv<true><<<256, 256, 73728, stream>>>(x2, wq, wk, wv, wo, ws);
    k_attn<false><<<256, 512, ATT_LDS, stream>>>(ws, w1, w2);
    k_wo<true><<<256, 256, 0, stream>>>(ws, x2);
    k_gemm<1><<<256, 256, 0, stream>>>(ws, b1, nullptr);
    k_gemm<3><<<256, 256, 0, stream>>>(ws, b2, (u16*)d_out);
}
```

```cpp
#include <hip/hip_runtime.h>
#include <hip/hip_bf16.h>
#include <stdint.h>

typedef unsigned short u16;
typedef short bf16x8 __attribute__((ext_vector_type(8)));
typedef short bf16x4 __attribute__((ext_vector_type(4)));
typedef float f32x16 __attribute__((ext_vector_type(16)));
typedef float f32x4 __attribute__((ext_vector_type(4)));
typedef float f32x2 __attribute__((ext_vector_type(2)));
typedef unsigned u32x4 __attribute__((ext_vector_type(4)));
typedef unsigned u32x2 __attribute__((ext_vector_type(2)));
typedef __bf16 bf2_t __attribute__((ext_vector_type(2)));
typedef __attribute__((address_space(3))) char* lds_ptr;
typedef __attribute__((address_space(3))) const char* lds_cptr;
typedef __attribute__((address_space(1))) unsigned long long gu64;

#define DI __device__ __forceinline__
#define SBAR() __builtin_amdgcn_sched_barrier(0)
#define PIN(x) asm volatile("" : "+v"(x))
#define MFMA32_16(a, b, c) __builtin_amdgcn_mfma_f32_32x32x16_bf16(a, b, c, 0, 0, 0)
#define MFMA32_8(a, b, c) __builtin_amdgcn_mfma_f32_32x32x8bf16_1k(a, b, c, 0, 0, 0)
#define MFMA16_32(a, b, c) __builtin_amdgcn_mfma_f32_16x16x32_bf16(a, b, c, 0, 0, 0)
#define EX2(x) __builtin_amdgcn_exp2f(x)
#define RAW_BARRIER() asm volatile("s_waitcnt lgkmcnt(0)\n\ts_barrier" ::: "memory")
#define VMCNT(n) asm volatile("s_waitcnt vmcnt(%0)" :: "i"(n) : "memory")

#define SST_DECL do {} while (0)
#define SST(i) do {} while (0)
#define SST_FLUSHK(ws, kid) do {} while (0)
#define PREP_STORE(p, v) asm volatile("global_store_dwordx4 %0, %1, off sc1\n\ts_nop 1" :: "v"(p), "v"(v) : "memory")
constexpr int S_ = 2048, D_ = 640, F_ = 1728;
constexpr float LN_EPS = 1e-5f;
constexpr float QSCALE = 0.22360679774997896f * 1.4426950408889634f;
constexpr float SAFE_BOUND = 60.0f;

DI unsigned pk2(float a, float b) { f32x2 v = {a, b}; bf2_t r = __builtin_convertvector(v, bf2_t); return __builtin_bit_cast(unsigned, r); }
DI float bf_lo(unsigned u) { return __uint_as_float(u << 16); }
DI float bf_hi(unsigned u) { return __uint_as_float(u & 0xffff0000u); }
DI float bf2f(u16 v) { return __uint_as_float((unsigned)v << 16); }
DI float dot2(unsigned a, unsigned b, float c) { return __builtin_amdgcn_fdot2_f32_bf16(__builtin_bit_cast(bf2_t, a), __builtin_bit_cast(bf2_t, b), c, false); }
DI bf16x4 tr_read(lds_cptr p) { return __builtin_amdgcn_ds_read_tr16_b64_v4i16((__attribute__((address_space(3))) bf16x4*)p); }
DI bf16x8 cat8(bf16x4 a, bf16x4 b) { return __builtin_shufflevector(a, b, 0, 1, 2, 3, 4, 5, 6, 7); }
DI void glds16(const void* g, lds_ptr l) {
    unsigned keep;
    asm volatile("s_mov_b32 %0, m0\n\ts_mov_b32 m0, %2\n\ts_nop 0\n\tglobal_load_lds_dwordx4 %1, off\n\ts_mov_b32 m0, %0"
                 : "=&s"(keep) : "v"(g), "s"((unsigned)(uintptr_t)l) : "memory");
}
DI float xor32(float v) { return __shfl_xor(v, 32); }
DI float add32(float v) {
    auto r = __builtin_amdgcn_permlane32_swap(__float_as_uint(v), __float_as_uint(v), false, false);
    return __uint_as_float(r[0]) + __uint_as_float(r[1]);
}
DI float add16(float v) {
    auto r = __builtin_amdgcn_permlane16_swap(__float_as_uint(v), __float_as_uint(v), false, false);
    return __uint_as_float(r[0]) + __uint_as_float(r[1]);
}
DI float max16x(float v) { auto r = __builtin_amdgcn_permlane16_swap(__float_as_uint(v), __float_as_uint(v), false, false); return fmaxf(__uint_as_float(r[0]), __uint_as_float(r[1])); }
DI float max32x(float v) { auto r = __builtin_amdgcn_permlane32_swap(__float_as_uint(v), __float_as_uint(v), false, false); return fmaxf(__uint_as_float(r[0]), __uint_as_float(r[1])); }
template <int CTRL> DI float dppf(float v) { return __int_as_float(__builtin_amdgcn_update_dpp(0, __float_as_int(v), CTRL, 0xf, 0xf, false)); }
DI float rowmax16(float v) {
    v = fmaxf(v, dppf<0x128>(v)); v = fmaxf(v, dppf<0x124>(v)); v = fmaxf(v, dppf<0x122>(v)); v = fmaxf(v, dppf<0x121>(v)); return v;
}
DI float wavemax(float v) { return max32x(max16x(rowmax16(v))); }
DI u32x4 gather8(const u16* s, int ld) {
    u32x4 r;
#pragma unroll
    for (int e = 0; e < 4; ++e) r[e] = (unsigned)s[(2 * e) * ld] | ((unsigned)s[(2 * e + 1) * ld] << 16);
    return r;
}

constexpr size_t OFF_Q = 0;
constexpr size_t OFF_K1 = OFF_Q + (size_t)2048 * 32 * 48;
constexpr size_t OFF_K2 = OFF_K1 + 2048 * 32;
constexpr size_t OFF_V = OFF_K2 + 2048 * 8;
constexpr size_t OFF_KN = OFF_V + 128 * 640;
constexpr size_t OFF_QN = OFF_KN + 512;
constexpr size_t OFF_CS = OFF_QN + 4096;
constexpr size_t OFF_CONST = OFF_CS + 4096;
constexpr size_t OFF_CNT = OFF_CONST + 256;
constexpr size_t OFF_XG = OFF_CNT + 48 * 256;
constexpr size_t OFF_XS = OFF_XG + 262144;
constexpr size_t OFF_X1 = OFF_XS + 131072;
constexpr size_t OFF_H = OFF_X1 + (size_t)S_ * D_ * 2;
constexpr size_t OFF_U = OFF_H + (size_t)S_ * D_ * 2;
constexpr size_t OFF_X2 = OFF_U + (size_t)D_ * F_ * 2;
constexpr size_t OFF_O = OFF_X2 + (size_t)S_ * D_ * 2;
constexpr size_t OFF_W1F = OFF_O + (size_t)S_ * D_ * 2;
constexpr size_t OFF_W2F = OFF_W1F + (size_t)S_ * F_ * 2;
constexpr size_t OFF_WQF = OFF_W2F + (size_t)S_ * F_ * 2;
constexpr size_t OFF_WOF = OFF_WQF + (size_t)44 * 20 * 1024;
constexpr int NPREP = 1728 + 1728 + 200 + 200;

DI void chan8(const f32x4 (&st)[4], float& mean, float& var) {
    float m = 0.f, m2 = 0.f;
#pragma unroll
    for (int i = 0; i < 4; ++i) { m += st[i][0] + st[i][2]; m2 += st[i][1] + st[i][3]; }
    m *= 0.125f;
#pragma unroll
    for (int i = 0; i < 4; ++i) { const float d0 = st[i][0] - m, d1 = st[i][2] - m; m2 += 80.f * (d0 * d0 + d1 * d1); }
    mean = m; var = m2 * (1.f / 640.f);
}

template <bool FAST>
DI void qkv_finish(char* __restrict__ ws, char* L, int tb, int cg, int w, int lane, f32x4 (&acc)[4][5], f32x4 (&acck)[2],
                   const f32x4 (&csq)[5], const f32x4& cs0, const f32x4& cs1, const f32x4 (&stq)[4], const f32x4 (&stk)[4],
                   const float (&sm)[4], const float (&sq)[4], const float (&cs)[5], const float (&csk)[2]) {
    const int n16 = lane & 15, g = lane >> 4, mt0 = cg >> 1, isv = cg & 1;
    constexpr int R_OFF = 0, PK_OFF = 61440, ST_OFF = 69632, CS_OFF = 71680;
#pragma unroll
    for (int j = 1; j < 4; ++j) {
        const int o = (w + j) & 3;
#pragma unroll
        for (int nt = 0; nt < 5; ++nt) *(f32x4*)(L + R_OFF + (((o * 3 + (j - 1)) * 5 + nt) * 64 + lane) * 16) = acc[j][nt];
    }
    *(f32x4*)(L + PK_OFF + ((w * 2 + 0) * 64 + lane) * 16) = acck[0];
    *(f32x4*)(L + PK_OFF + ((w * 2 + 1) * 64 + lane) * 16) = acck[1];
    if (!FAST && g == 0) {
#pragma unroll
        for (int i = 0; i < 4; ++i) *(f32x2*)(L + ST_OFF + (w * 64 + ((i + w) & 3) * 16 + n16) * 8) = f32x2{sm[i], sq[i]};
#pragma unroll
        for (int nt = 0; nt < 5; ++nt) *(float*)(L + CS_OFF + w * 448 + (nt * 16 + n16) * 4) = cs[nt];
#pragma unroll
        for (int nt = 0; nt < 2; ++nt) *(float*)(L + CS_OFF + w * 448 + (80 + nt * 16 + n16) * 4) = csk[nt];
    }
    char* qrow = ws + OFF_Q + (size_t)(tb * 8 + cg) * 12288 + (16 * w + n16) * 48;
    int qoff[5];
#pragma unroll
    for (int nt = 0; nt < 5; ++nt) { const int G = 4 * nt + g, hl = G / 5; qoff[nt] = hl * 3072 + (G - 5 * hl) * 8; PIN(qoff[nt]); }
    RAW_BARRIER();
    auto stats = [&](int tl, const f32x4 (&st)[4], float& mean, float& rstd) {
        float var;
        if (FAST) chan8(st, mean, var);
        else {
            float s = 0.f, s2 = 0.f;
#pragma unroll
            for (int ww = 0; ww < 4; ++ww) { f32x2 v = *(const f32x2*)(L + ST_OFF + (ww * 64 + tl) * 8); s += v[0]; s2 += v[1]; }
            mean = s * (1.f / 640.f);
            var = fmaxf(s2 * (1.f / 640.f) - mean * mean, 0.f);
        }
        rstd = __builtin_amdgcn_rsqf(var + LN_EPS);
    };
    {
        f32x4 rr[3][5], cc[4][5];
#pragma unroll
        for (int j = 0; j < 3; ++j)
#pragma unroll
            for (int nt = 0; nt < 5; ++nt) rr[j][nt] = *(const f32x4*)(L + R_OFF + (((w * 3 + j) * 5 + nt) * 64 + lane) * 16);
        if (!FAST) {
#pragma unroll
            for (int ww = 0; ww < 4; ++ww)
#pragma unroll
                for (int nt = 0; nt < 5; ++nt) cc[ww][nt] = *(const f32x4*)(L + CS_OFF + ww * 448 + (nt * 16 + 4 * g) * 4);
        }
        float mean, rstd; stats(16 * w + n16, stq, mean, rstd);
        SBAR();
        const float sc = rstd * QSCALE;
        float n2[5];
#pragma unroll
        for (int nt = 0; nt < 5; ++nt) {
            const f32x4 a = (acc[0][nt] + rr[0][nt]) + (rr[1][nt] + rr[2][nt]);
            f32x4 s;
            if (FAST) s = csq[nt];
            else {
                s = (cc[0][nt] + cc[1][nt]) + (cc[2][nt] + cc[3][nt]);
                if (tb == 0 && w == 0 && n16 == 0) *(f32x4*)(ws + OFF_CS + (80 * cg + 16 * nt + 4 * g) * 4) = s;
            }
            const unsigned u0 = pk2((a[0] - mean * s[0]) * sc, (a[1] - mean * s[1]) * sc);
            const unsigned u1 = pk2((a[2] - mean * s[2]) * sc, (a[3] - mean * s[3]) * sc);
            *(u32x2*)(qrow + qoff[nt]) = u32x2{u0, u1};
            n2[nt] = dot2(u1, u1, dot2(u0, u0, 0.f));
        }
        float hn[4];
        hn[0] = n2[0] + (g == 0 ? n2[1] : 0.f);
        hn[1] = (g > 0 ? n2[1] : 0.f) + (g < 2 ? n2[2] : 0.f);
        hn[2] = (g > 1 ? n2[2] : 0.f) + (g < 3 ? n2[3] : 0.f);
        hn[3] = (g == 3 ? n2[3] : 0.f) + n2[4];
#pragma unroll
        for (int hh = 0; hh < 4; ++hh) hn[hh] = add32(add16(hn[hh]));
        const float n2a = (g & 1) ? hn[1] : hn[0], n2b = (g & 1) ? hn[3] : hn[2];
        const unsigned qb = pk2(__builtin_amdgcn_sqrtf((g & 2) ? n2b : n2a), 0.f);
        *(u32x2*)(qrow + g * 3072 + 40) = u32x2{qb, 0u};
        float m = bf_lo(qb);
        if (!(m == m)) m = INFINITY;
        m = wavemax(m);
        if (lane == 0) *(float*)(ws + OFF_QN + ((tb * 8 + cg) * 4 + w) * 4) = m;
    }
    if (w == 3) {
        const int t16 = tb * 4 + mt0;
        float mean, rstd; stats(mt0 * 16 + n16, stk, mean, rstd);
        f32x4 a0 = {0.f, 0.f, 0.f, 0.f}, a1 = a0, s0 = a0, s1 = a0;
#pragma unroll
        for (int ww = 0; ww < 4; ++ww) {
            a0 += *(const f32x4*)(L + PK_OFF + ((ww * 2 + 0) * 64 + lane) * 16);
            a1 += *(const f32x4*)(L + PK_OFF + ((ww * 2 + 1) * 64 + n16) * 16);
            if (!FAST) {
                s0 += *(const f32x4*)(L + CS_OFF + ww * 448 + (80 + 4 * g) * 4);
                s1 += *(const f32x4*)(L + CS_OFF + ww * 448 + 96 * 4);
            }
        }
        if (FAST) { s0 = cs0; s1 = cs1; }
        else if (tb == 0 && cg < 2 && n16 == 0) {
            *(f32x4*)(ws + OFF_CS + (640 + 32 * isv + 4 * g) * 4) = s0;
            if (g == 0) *(f32x4*)(ws + OFF_CS + (640 + 32 * isv + 16) * 4) = s1;
        }
        const unsigned u0 = pk2((a0[0] - mean * s0[0]) * rstd, (a0[1] - mean * s0[1]) * rstd);
        const unsigned u1 = pk2((a0[2] - mean * s0[2]) * rstd, (a0[3] - mean * s0[3]) * rstd);
        const unsigned u2 = pk2((a1[0] - mean * s1[0]) * rstd, (a1[1] - mean * s1[1]) * rstd);
        const unsigned u3 = pk2((a1[2] - mean * s1[2]) * rstd, (a1[3] - mean * s1[3]) * rstd);
        if (!isv) {
            const int key = t16 * 16 + n16;
            *(u32x2*)(ws + OFF_K1 + key * 32 + g * 8) = u32x2{u0, u1};
            if (g == 0) *(u32x2*)(ws + OFF_K2 + key * 8) = u32x2{u2, u3};
            float n2 = dot2(u1, u1, dot2(u0, u0, 0.f));
            n2 = add32(add16(n2));
            n2 = dot2(u3, u3, dot2(u2, u2, n2));
            if (!(n2 == n2)) n2 = INFINITY;
            n2 = rowmax16(n2);
            if (lane == 0) *(float*)(ws + OFF_KN + t16 * 4) = n2;
        } else {
            u16* vb = (u16*)(ws + OFF_V + t16 * 640 + (n16 >> 3) * 320) + (n16 & 7);
            vb[(4 * g + 0) * 8] = (u16)u0; vb[(4 * g + 1) * 8] = (u16)(u0 >> 16);
            vb[(4 * g + 2) * 8] = (u16)u1; vb[(4 * g + 3) * 8] = (u16)(u1 >> 16);
            if (g == 0) { vb[16 * 8] = (u16)u2; vb[17 * 8] = (u16)(u2 >> 16); vb[18 * 8] = (u16)u3; vb[19 * 8] = (u16)(u3 >> 16); }
        }
    }
    if (w == 0) *(u32x2*)(ws + OFF_XG + ((size_t)(tb * 64 + lane) * 8 + cg) * 8) = u32x2{0u, 0u};
}

constexpr int QKV_LDS = 163840;
template <bool FAST>
__global__ __launch_bounds__(256, 1) void k_qkv(const u16* __restrict__ x, const u16* __restrict__ wq, const u16* __restrict__ wk,
                                                const u16* __restrict__ wv, const u16* __restrict__ wo, char* __restrict__ ws) {
    extern __shared__ __attribute__((aligned(16))) char lds_raw[];
    lds_ptr lds = (lds_ptr)lds_raw;
    char* L = lds_raw;
    const int tid = threadIdx.x, lane = tid & 63, w = __builtin_amdgcn_readfirstlane(tid >> 6);
    const int b = blockIdx.x, slot = b >> 3, tb = ((b & 7) >> 1) * 8 + (slot >> 2), cg = (b & 1) * 4 + (slot & 3);
    const int mt0 = cg >> 1, isv = cg & 1;
    const int n16 = lane & 15, g = lane >> 4;
    SST_DECL; SST(0);
    f32x4 acc[4][5], acck[2];
#pragma unroll
    for (int i = 0; i < 4; ++i)
#pragma unroll
        for (int j = 0; j < 5; ++j) acc[i][j] = f32x4{0.f, 0.f, 0.f, 0.f};
    acck[0] = f32x4{0.f, 0.f, 0.f, 0.f}; acck[1] = acck[0];
    f32x4 csq[5], cs0, cs1, stq[4], stk[4];
    float sm[4] = {0.f, 0.f, 0.f, 0.f}, sq[4] = {0.f, 0.f, 0.f, 0.f};
    float cs[5] = {0.f, 0.f, 0.f, 0.f, 0.f}, csk[2] = {0.f, 0.f};
    if (FAST) {
        const bf16x8* xb = (const bf16x8*)x + (size_t)(tb * 4 * 20 + 5 * w) * 64 + lane;
        const bf16x8* wb = (const bf16x8*)(ws + OFF_WQF) + (size_t)(5 * cg * 20 + 5 * w) * 64 + lane;
        const bf16x8* kb = (const bf16x8*)(ws + OFF_WQF) + (size_t)((40 + 2 * isv) * 20 + 5 * w) * 64 + lane;
        bf16x8 xf[5][4], xk[5], wf[5][5], wkf[5][2];
#define QKV_LD(ks) do { _Pragma("unroll") for (int i = 0; i < 4; ++i) xf[ks][i] = xb[(((i + w) & 3) * 20 + (ks)) * 64]; \
        _Pragma("unroll") for (int nt = 0; nt < 5; ++nt) wf[ks][nt] = wb[(nt * 20 + (ks)) * 64]; \
        xk[ks] = xb[(mt0 * 20 + (ks)) * 64]; wkf[ks][0] = kb[(ks) * 64]; wkf[ks][1] = kb[(20 + (ks)) * 64]; SBAR(); } while (0)
        QKV_LD(0); QKV_LD(1); QKV_LD(2); QKV_LD(3);
#pragma unroll
        for (int ks = 0; ks < 5; ++ks) {
#pragma unroll
            for (int nt = 0; nt < 5; ++nt)
#pragma unroll
                for (int i = 0; i < 4; ++i) acc[i][nt] = MFMA16_32(wf[ks][nt], xf[ks][i], acc[i][nt]);
            acck[0] = MFMA16_32(wkf[ks][0], xk[ks], acck[0]);
            acck[1] = MFMA16_32(wkf[ks][1], xk[ks], acck[1]);
            SBAR();
            if (ks == 0) {
                QKV_LD(4);
                const float* csp = (const float*)(ws + OFF_CS);
#pragma unroll
                for (int nt = 0; nt < 5; ++nt) csq[nt] = *(const f32x4*)(csp + 80 * cg + 16 * nt + 4 * g);
                cs0 = *(const f32x4*)(csp + 640 + 32 * isv + 4 * g); cs1 = *(const f32x4*)(csp + 640 + 32 * isv + 16);
                const f32x4* sp = (const f32x4*)(ws + OFF_XS) + (size_t)(tb * 64 + n16) * 4;
#pragma unroll
                for (int i = 0; i < 4; ++i) { stq[i] = sp[16 * w * 4 + i]; stk[i] = sp[16 * mt0 * 4 + i]; }
                SBAR();
            }
        }
    } else {
        lds_ptr ra = lds + w * 40960, rb = ra + 20480;
        const char* xs = (const char*)(x + (size_t)(tb * 64) * 640 + 160 * w);
#pragma unroll
        for (int j = 0; j < 5; ++j) {
            const int c = 64 * j + lane;
            const unsigned xo = (unsigned)(c / 20) * 1280u + (unsigned)(c % 20) * 16u;
#pragma unroll
            for (int i = 0; i < 4; ++i) glds16(xs + (size_t)i * 20480 + xo, ra + i * 5120 + j * 1024);
        }
        const char* src = (const char*)(wq + (size_t)(160 * w) * 640 + 80 * cg);
        const unsigned loff = (unsigned)(lane / 10) * 1280u + (unsigned)(lane % 10) * 16u;
        const char* src2 = (const char*)((isv ? wv : wk) + (size_t)(160 * w) * 20);
#pragma unroll
        for (int i = 0; i < 16; ++i) glds16(src + (size_t)i * 7680 + loff, rb + i * 960);
#pragma unroll
        for (int i = 0; i < 3; ++i) glds16(src2 + i * 1024 + lane * 16, rb + 15488 + i * 1024);
        if (lane < 48) glds16(src2 + 3 * 1024 + lane * 16, rb + 15488 + 3 * 1024);
        SST(1);
        VMCNT(20);
        bf16x8 xf[4][5], xk[5];
        {
            const char* xl = L + w * 40960 + n16 * 320 + g * 16;
#pragma unroll
            for (int i = 0; i < 4; ++i)
#pragma unroll
                for (int ks = 0; ks < 5; ++ks) xf[i][ks] = *(const bf16x8*)(xl + ((i + w) & 3) * 5120 + ks * 64);
#pragma unroll
            for (int ks = 0; ks < 5; ++ks) xk[ks] = *(const bf16x8*)(xl + mt0 * 5120 + ks * 64);
        }
        asm volatile("s_waitcnt lgkmcnt(0)" ::: "memory");
        SBAR();
#pragma unroll
        for (int i = 0; i < 10; ++i) glds16(src + (size_t)(16 + i) * 7680 + loff, ra + i * 960);
        if (lane < 40) glds16(src + (size_t)26 * 7680 + loff, ra + 10 * 960);
#pragma unroll
        for (int i = 0; i < 2; ++i) glds16(src2 + 3840 + i * 1024 + lane * 16, ra + 10240 + i * 1024);
        if (lane < 32) glds16(src2 + 3840 + 2 * 1024 + lane * 16, ra + 10240 + 2 * 1024);
        SST(2);
        const unsigned ONES = 0x3f803f80u;
#pragma unroll
        for (int i = 0; i < 4; ++i)
#pragma unroll
            for (int ks = 0; ks < 5; ++ks) {
                u32x4 u = __builtin_bit_cast(u32x4, xf[i][ks]);
#pragma unroll
                for (int e = 0; e < 4; ++e) { sm[i] = dot2(u[e], ONES, sm[i]); sq[i] = dot2(u[e], u[e], sq[i]); }
            }
        SBAR();
        VMCNT(14);
        SST(3);
        const int q4 = (lane & 15) >> 2, p4 = lane & 3;
        lds_cptr wb0 = (lds_cptr)rb + (8 * g + q4) * 160 + p4 * 8, wb1 = (lds_cptr)ra + (8 * g + q4) * 160 + p4 * 8 - 3 * 5120;
        lds_cptr kb0 = (lds_cptr)rb + 15488 + (8 * g + q4) * 40 + p4 * 8, kb1 = (lds_cptr)ra + 10240 + (8 * g + q4) * 40 + p4 * 8 - 3 * 1280;
        bf16x8 wf[2][7];
#define QKV_TR(ks, s_) do { lds_cptr wb = (ks) < 3 ? wb0 : wb1, kb = (ks) < 3 ? kb0 : kb1; \
        _Pragma("unroll") for (int nt = 0; nt < 5; ++nt) wf[s_][nt] = cat8(tr_read(wb + (ks) * 5120 + nt * 32), tr_read(wb + (ks) * 5120 + 640 + nt * 32)); \
        _Pragma("unroll") for (int nt = 0; nt < 2; ++nt) wf[s_][5 + nt] = cat8(tr_read(kb + (ks) * 1280 + nt * 32), tr_read(kb + (ks) * 1280 + 160 + nt * 32)); SBAR(); } while (0)
        QKV_TR(0, 0);
#pragma unroll
        for (int ks = 0; ks < 5; ++ks) {
            if (ks == 2) { VMCNT(0); SBAR(); }
            if (ks < 4) QKV_TR(ks + 1, (ks + 1) & 1);
#pragma unroll
            for (int nt = 0; nt < 5; ++nt) {
                u32x4 u = __builtin_bit_cast(u32x4, wf[ks & 1][nt]);
#pragma unroll
                for (int e = 0; e < 4; ++e) cs[nt] = dot2(u[e], ONES, cs[nt]);
#pragma unroll
                for (int i = 0; i < 4; ++i) acc[i][nt] = MFMA16_32(wf[ks & 1][nt], xf[i][ks], acc[i][nt]);
            }
#pragma unroll
            for (int nt = 0; nt < 2; ++nt) {
                u32x4 u = __builtin_bit_cast(u32x4, wf[ks & 1][5 + nt]);
#pragma unroll
                for (int e = 0; e < 4; ++e) csk[nt] = dot2(u[e], ONES, csk[nt]);
                acck[nt] = MFMA16_32(wf[ks & 1][5 + nt], xk[ks], acck[nt]);
            }
            SBAR();
        }
#pragma unroll
        for (int i = 0; i < 4; ++i) { sm[i] = add32(add16(sm[i])); sq[i] = add32(add16(sq[i])); }
#pragma unroll
        for (int i = 0; i < 5; ++i) cs[i] = add32(add16(cs[i]));
#pragma unroll
        for (int i = 0; i < 2; ++i) csk[i] = add32(add16(csk[i]));
    }
    SST(4);
    if (!FAST) RAW_BARRIER();
    qkv_finish<FAST>(ws, L, tb, cg, w, lane, acc, acck, csq, cs0, cs1, stq, stk, sm, sq, cs, csk);
    if (!FAST && w == 1) {
        if (b < 80) {
            const int ks = b >> 2, ct = b & 3; int cn = 16 * (ct & 1) + n16; if (cn >= 20) cn = 0;
            *(u32x4*)(ws + OFF_WQF + (size_t)((40 + ct) * 20 + ks) * 1024 + lane * 16) =
                gather8(((ct & 2) ? wv : wk) + (size_t)(32 * ks + 8 * g) * 20 + cn, 20);
        } else if (b == 80 && lane < 4) {
            const unsigned v = (lane & 2) ? 0x3f803f80u : 0u;
            *(u32x4*)(ws + OFF_CONST + lane * 16) = u32x4{v, v, v, v};
        }
    }
    SST(7); SST_FLUSHK(ws, FAST ? 5 : 0);
}

constexpr int PBW = 2688, ATT_LDS = 32 * PBW;
template <bool PREP>
__global__ __launch_bounds__(512, 2) void k_attn(char* __restrict__ ws, const u16* __restrict__ w1, const u16* __restrict__ w2,
                                                 const u16* __restrict__ wo, const u16* __restrict__ wq) {
    extern __shared__ __attribute__((aligned(16))) char lds_raw[];
    char* L = lds_raw;
    const int tid = threadIdx.x, lane = tid & 63, w = __builtin_amdgcn_readfirstlane(tid >> 6);
    const int b = blockIdx.x, slot = b >> 3, tb = ((b & 7) >> 1) * 8 + (slot >> 2), cg = (b & 1) * 4 + (slot & 3);
    const int r32 = lane & 31, h = lane >> 5, n16 = lane & 15, g = lane >> 4;
    const int cgp = w & 1, kq = w >> 1;
    SST_DECL; SST(0);
    float kn0 = *(const float*)(ws + OFF_KN + lane * 4), kn1 = *(const float*)(ws + OFF_KN + 256 + lane * 4);
    float qmax = *(const float*)(ws + OFF_QN + ((tb * 8 + cg) * 4 + (lane & 3)) * 4);
    bf16x8 q1[4]; bf16x4 q2[4];
    {
        const char* qg = ws + OFF_Q + (size_t)(tb * 8 + cg) * 12288 + cgp * 6144 + r32 * 48;
#pragma unroll
        for (int j = 0; j < 4; ++j) { q1[j] = *(const bf16x8*)(qg + j * 1536 + 16 * h); q2[j] = *(const bf16x4*)(qg + j * 1536 + 32 + 8 * h); }
    }
    SBAR();
    const int kr = (r32 & 19) | ((r32 & 4) << 1) | ((r32 & 8) >> 1);
    const unsigned ko1 = (unsigned)OFF_K1 + (unsigned)(512 * kq + kr) * 32u + 16u * h;
    const unsigned ko2 = h ? (unsigned)OFF_CONST + 32u : (unsigned)OFF_K2 + (unsigned)(512 * kq + kr) * 8u;
    const unsigned ks2 = h ? 0u : 256u;
    const unsigned vo = r32 < 20 ? (unsigned)OFF_V + (unsigned)(32 * kq) * 640u + (unsigned)(h * 20 + r32) * 16u
                                 : (unsigned)OFF_CONST + (r32 == 20 ? 32u : 0u);
    const unsigned vs = r32 < 20 ? 640u : 0u;
    bf16x8 k1[3], v0[3], v1[3]; bf16x4 k2[3];
#define K_LOAD(t) do { k1[(t) % 3] = *(const bf16x8*)(ws + ko1 + (t) * 1024); k2[(t) % 3] = *(const bf16x4*)(ws + (ko2 + (t) * ks2)); } while (0)
#define V_LOAD(t) do { v0[(t) % 3] = *(const bf16x8*)(ws + (vo + (2 * (t)) * vs)); v1[(t) % 3] = *(const bf16x8*)(ws + (vo + (2 * (t) + 1) * vs)); } while (0)
    K_LOAD(0); V_LOAD(0); SBAR(); K_LOAD(1); V_LOAD(1); SBAR();
    PIN(kn0); PIN(kn1);
    const float kmax2 = wavemax(fmaxf(kn0, kn1));
    qmax = rowmax16(qmax);
    const float kb = __builtin_amdgcn_sqrtf(kmax2);
    const bool safe = __builtin_amdgcn_readfirstlane((int)(qmax * kb <= SAFE_BOUND)) != 0;
    const f32x16 zero16 = {0.f, 0.f, 0.f, 0.f, 0.f, 0.f, 0.f, 0.f, 0.f, 0.f, 0.f, 0.f, 0.f, 0.f, 0.f, 0.f};
    if (safe) {
        if (h) {
#pragma unroll
            for (int j = 0; j < 4; ++j) q2[j][0] = (short)pk2(-bf2f((u16)q2[j][0]) * kb, 0.f);
        }
    } else {
        float m[4];
#pragma unroll
        for (int j = 0; j < 4; ++j) { m[j] = -INFINITY; if (h) q2[j] = bf16x4{0, 0, 0, 0}; }
        for (int t = 0; t < 16; ++t) {
            const bf16x8 ka = *(const bf16x8*)(ws + ko1 + t * 1024);
            const bf16x4 kc = *(const bf16x4*)(ws + (ko2 + t * ks2));
#pragma unroll
            for (int j = 0; j < 4; ++j) {
                f32x16 s = MFMA32_16(ka, q1[j], zero16);
                SBAR(); asm volatile("s_nop 15" ::: "memory"); SBAR();
                s = MFMA32_8(kc, q2[j], s);
#pragma unroll
                for (int e = 0; e < 16; ++e) m[j] = fmaxf(m[j], s[e]);
                SBAR();
            }
        }
#pragma unroll
        for (int j = 0; j < 4; ++j) {
            const float mm = fmaxf(m[j], xor32(m[j]));
            if (h == 0) *(float*)(L + ((w * 4 + j) * 32 + r32) * 4) = mm;
        }
        RAW_BARRIER();
#pragma unroll
        for (int j = 0; j < 4; ++j) {
            float mm = -INFINITY;
#pragma unroll
            for (int qq = 0; qq < 4; ++qq) mm = fmaxf(mm, *(const float*)(L + (((2 * qq + cgp) * 4 + j) * 32 + r32) * 4));
            const unsigned hi = __float_as_uint(mm) & 0xffff0000u;
            const float r1 = mm - __uint_as_float(hi);
            const unsigned mid = __float_as_uint(r1) & 0xffff0000u;
            const float r2 = r1 - __uint_as_float(mid);
            const unsigned lo = __float_as_uint(r2) & 0xffff0000u;
            if (h) q2[j] = __builtin_bit_cast(bf16x4, u32x2{hi ^ 0x80000000u, ((mid ^ 0x80000000u) >> 16) | (lo ^ 0x80000000u)});
        }
        RAW_BARRIER();
    }
    SST(1);
    f32x16 o[4], sa, sb;
#pragma unroll
    for (int j = 0; j < 4; ++j) o[j] = zero16;
    u32x4 p1prev;
    sa = MFMA32_16(k1[0], q1[0], zero16);
    SBAR(); asm volatile("s_nop 15" ::: "memory"); SBAR();
    sa = MFMA32_8(k2[0], q2[0], sa);
    SBAR(); asm volatile("s_nop 15" ::: "memory"); SBAR();
    u32x4 prep[4]; char* prep_dst = nullptr; int prep_st = 0;
#pragma unroll
    for (int t = 0; t < 16; ++t) {
#pragma unroll
        for (int j = 0; j < 4; ++j) {
            const int it = 4 * t + j;
            f32x16& cur = (it & 1) ? sb : sa; f32x16& nxt = (it & 1) ? sa : sb;
            const int tn = j < 3 ? t : t + 1, jn = (j + 1) & 3;
            const int tp = j > 0 ? t : t - 1, jp = (j + 3) & 3;
            u32x4 p0, p1;
            if (j == 0 && t + 2 < 16) K_LOAD(t + 2);
            if (j == 1 && t + 2 < 16) V_LOAD(t + 2);
            if (PREP && j == 2 && (t == 1 || t == 8)) {
                int id = (t >> 3) * 2048 + b * 8 + w; if (id >= NPREP) id -= 2048;
                const u16* src; int ld;
                if (id < 1728) { const int ks = id / 27, c4 = id - ks * 27; src = w1 + (size_t)(32 * ks) * F_ + 64 * c4; ld = F_;
                                 prep_dst = ws + OFF_W1F + (size_t)(4 * c4 * 64 + ks) * 1024; prep_st = 64 * 1024; }
                else if (id < 3456) { const int jj = id - 1728, ks = jj >> 5, c4 = jj & 31; src = w2 + (size_t)(32 * ks) * S_ + 64 * c4; ld = S_;
                                      prep_dst = ws + OFF_W2F + (size_t)(4 * c4 * 54 + ks) * 1024; prep_st = 54 * 1024; }
                else { int jj = id - 3456; const bool isq = jj >= 200; if (isq) jj -= 200; const int ks = jj / 10, c4 = jj - ks * 10;
                       src = (isq ? wq : wo) + (size_t)(32 * ks) * 640 + 64 * c4; ld = 640;
                       prep_dst = ws + (isq ? OFF_WQF : OFF_WOF) + (size_t)(4 * c4 * 20 + ks) * 1024; prep_st = 20 * 1024; }
                src += (size_t)(lane >> 3) * ld + (lane & 7) * 8;
#pragma unroll
                for (int i = 0; i < 4; ++i) prep[i] = *(const u32x4*)(src + (size_t)(8 * i) * ld);
            }
            if (PREP && j == 2 && (t == 6 || t == 13)) {
#pragma unroll
                for (int i = 0; i < 4; ++i) *(u32x4*)(L + w * 4096 + i * 1024 + lane * 16) = prep[i];
            }
            if (PREP && j == 3 && (t == 6 || t == 13)) {
                lds_cptr tp_ = (lds_cptr)(lds_ptr)L + w * 4096 + (8 * g + (n16 >> 2)) * 128 + (lane & 3) * 8;
#pragma unroll
                for (int i = 0; i < 4; ++i) prep[i] = __builtin_bit_cast(u32x4, cat8(tr_read(tp_ + i * 32), tr_read(tp_ + 512 + i * 32)));
            }
            if (PREP && j == 0 && (t == 7 || t == 14)) {
#pragma unroll
                for (int i = 0; i < 4; ++i) PREP_STORE((u32x4*)(prep_dst + (size_t)i * prep_st + lane * 16), prep[i]);
            }
            SBAR();
            if (it < 63) nxt = MFMA32_16(k1[tn % 3], q1[jn], zero16);
            SBAR();
            p0[0] = pk2(EX2(cur[0]), EX2(cur[1])); p0[1] = pk2(EX2(cur[2]), EX2(cur[3]));
            PIN(p0); SBAR();
            if (it > 0) o[jp] = MFMA32_16(v1[(tp + 3) % 3], __builtin_bit_cast(bf16x8, p1prev), o[jp]);
            SBAR();
            p0[2] = pk2(EX2(cur[4]), EX2(cur[5])); p0[3] = pk2(EX2(cur[6]), EX2(cur[7]));
            PIN(p0); SBAR();
            if (it < 63) nxt = MFMA32_8(k2[tn % 3], q2[jn], nxt);
            SBAR();
            p1[0] = pk2(EX2(cur[8]), EX2(cur[9])); p1[1] = pk2(EX2(cur[10]), EX2(cur[11]));
            PIN(p1); SBAR();
            o[j] = MFMA32_16(v0[t % 3], __builtin_bit_cast(bf16x8, p0), o[j]);
            SBAR();
            p1[2] = pk2(EX2(cur[12]), EX2(cur[13])); p1[3] = pk2(EX2(cur[14]), EX2(cur[15]));
            PIN(p1); SBAR();
            p1prev = p1;
        }
        if (t == 7) SST(2);
    }
    o[3] = MFMA32_16(v1[15 % 3], __builtin_bit_cast(bf16x8, p1prev), o[3]);
    SST(3);
    if (PREP) RAW_BARRIER();
#pragma unroll
    for (int j = 0; j < 4; ++j) {
        char* pb = L + (kq * 8 + cgp * 4 + j) * PBW;
        *(f32x4*)(pb + lane * 16) = f32x4{o[j][0], o[j][1], o[j][2], o[j][3]};
        *(f32x4*)(pb + 1024 + lane * 16) = f32x4{o[j][4], o[j][5], o[j][6], o[j][7]};
        if (h == 0) *(f32x4*)(pb + 2048 + r32 * 16) = f32x4{o[j][8], o[j][9], o[j][10], o[j][11]};
        else *(float*)(pb + 2560 + r32 * 4) = o[j][8];
    }
    RAW_BARRIER();
    SST(4);
    {
        const char* pb = L + w * PBW;
        f32x4 a0 = {0.f, 0.f, 0.f, 0.f}, a1 = a0, a2 = a0; float l = 0.f;
#pragma unroll
        for (int qq = 0; qq < 4; ++qq) {
            a0 += *(const f32x4*)(pb + qq * 8 * PBW + lane * 16);
            a1 += *(const f32x4*)(pb + qq * 8 * PBW + 1024 + lane * 16);
            a2 += *(const f32x4*)(pb + qq * 8 * PBW + 2048 + r32 * 16);
            l += *(const float*)(pb + qq * 8 * PBW + 2560 + r32 * 4);
        }
        const float inv = __builtin_amdgcn_rcpf(l);
        const int tl = 32 * (w & 1) + r32, hd0 = 80 * cg + 20 * (w >> 1);
        char* ob = ws + OFF_O + (size_t)(tb * 4 + (tl >> 4)) * 20480 + (tl & 15) * 16;
        auto st = [&](int hd, const f32x4& a) {
            *(u32x2*)(ob + (hd >> 5) * 1024 + ((hd & 31) >> 3) * 256 + (hd & 7) * 2) = u32x2{pk2(a[0] * inv, a[1] * inv), pk2(a[2] * inv, a[3] * inv)};
        };
        st(hd0 + 4 * h, a0); st(hd0 + 8 + 4 * h, a1);
        if (h == 0) st(hd0 + 16, a2);
    }
    if (b == 0 && tid < 48) *(unsigned*)(ws + OFF_CNT + tid * 256) = 0u;
    SST(5); SST_FLUSHK(ws, PREP ? 1 : 6);
}

template <bool XF>
__global__ __launch_bounds__(256, 1) void k_wo(char* __restrict__ ws, const u16* __restrict__ xin) {
    __shared__ __attribute__((aligned(16))) char L[61440];
    const int tid = threadIdx.x, lane = tid & 63, w = __builtin_amdgcn_readfirstlane(tid >> 6);
    const int b = blockIdx.x, slot = b >> 3, tb = ((b & 7) >> 1) * 8 + (slot >> 2), cg = (b & 1) * 4 + (slot & 3);
    const int n16 = lane & 15, g = lane >> 4;
    SST_DECL; SST(0);
    f32x4 acc[4][5];
#pragma unroll
    for (int i = 0; i < 4; ++i)
#pragma unroll
        for (int j = 0; j < 5; ++j) acc[i][j] = f32x4{0.f, 0.f, 0.f, 0.f};
    const int tok = tb * 64 + 16 * w + n16;
    u32x2 xi[5];
    {
        const bf16x8* xb = (const bf16x8*)(ws + OFF_O) + (size_t)(tb * 4 * 20 + 5 * w) * 64 + lane;
        const bf16x8* wb = (const bf16x8*)(ws + OFF_WOF) + (size_t)(5 * cg * 20 + 5 * w) * 64 + lane;
        bf16x8 xf[5][4], wf[5][5];
#define WO_LD(ks) do { _Pragma("unroll") for (int i = 0; i < 4; ++i) xf[ks][i] = xb[(((i + w) & 3) * 20 + (ks)) * 64]; \
        _Pragma("unroll") for (int nt = 0; nt < 5; ++nt) wf[ks][nt] = wb[(nt * 20 + (ks)) * 64]; SBAR(); } while (0)
        WO_LD(0); WO_LD(1); WO_LD(2); WO_LD(3);
#pragma unroll
        for (int ks = 0; ks < 5; ++ks) {
#pragma unroll
            for (int nt = 0; nt < 5; ++nt)
#pragma unroll
                for (int i = 0; i < 4; ++i) acc[i][nt] = MFMA16_32(wf[ks][nt], xf[ks][i], acc[i][nt]);
            SBAR();
            if (ks == 0) {
                WO_LD(4);
#pragma unroll
                for (int nt = 0; nt < 5; ++nt) {
                    const int d = 80 * cg + 16 * nt + 4 * g;
                    if (XF) xi[nt] = *(const u32x2*)(xin + ((size_t)((tok >> 4) * 20 + (d >> 5)) * 64 + ((d & 31) >> 3) * 16 + n16) * 8 + (d & 7));
                    else xi[nt] = *(const u32x2*)(xin + (size_t)tok * 640 + d);
                }
                SBAR();
            }
        }
    }
    SST(1);
#pragma unroll
    for (int j = 1; j < 4; ++j) {
        const int o = (w + j) & 3;
#pragma unroll
        for (int nt = 0; nt < 5; ++nt) *(f32x4*)(L + (((o * 3 + (j - 1)) * 5 + nt) * 64 + lane) * 16) = acc[j][nt];
    }
    RAW_BARRIER();
    SST(2);
    float xv[20]; u32x2 x1o[5];
    {
        f32x4 rr[3][5];
#pragma unroll
        for (int j = 0; j < 3; ++j)
#pragma unroll
            for (int nt = 0; nt < 5; ++nt) rr[j][nt] = *(const f32x4*)(L + (((w * 3 + j) * 5 + nt) * 64 + lane) * 16);
        SBAR();
        float s = 0.f;
#pragma unroll
        for (int nt = 0; nt < 5; ++nt) {
            const f32x4 a = (acc[0][nt] + rr[0][nt]) + (rr[1][nt] + rr[2][nt]);
            const unsigned u0 = pk2(bf_lo(xi[nt][0]) + a[0], bf_hi(xi[nt][0]) + a[1]);
            const unsigned u1 = pk2(bf_lo(xi[nt][1]) + a[2], bf_hi(xi[nt][1]) + a[3]);
            x1o[nt] = u32x2{u0, u1};
            xv[4 * nt] = bf_lo(u0); xv[4 * nt + 1] = bf_hi(u0); xv[4 * nt + 2] = bf_lo(u1); xv[4 * nt + 3] = bf_hi(u1);
            s += (xv[4 * nt] + xv[4 * nt + 1]) + (xv[4 * nt + 2] + xv[4 * nt + 3]);
        }
        const float mw = add32(add16(s)) * (1.f / 80.f);
        float s2 = 0.f;
#pragma unroll
        for (int i = 0; i < 20; ++i) { const float dd = xv[i] - mw; s2 += dd * dd; }
        s2 = add32(add16(s2));
        gu64* gp = (gu64*)(ws + OFF_XG) + (size_t)tok * 8 + cg;
        if (g == 0) __hip_atomic_store(gp, ((unsigned long long)(__float_as_uint(s2) | 0x80000000u) << 32) | __float_as_uint(mw),
                                       __ATOMIC_RELAXED, __HIP_MEMORY_SCOPE_AGENT);
    }
    SST(3);
    float mean, var;
    {
        gu64* gp = (gu64*)(ws + OFF_XG) + (size_t)tok * 8 + 2 * g;
        unsigned long long v0, v1;
        for (int spin = 0; spin < (1 << 20); ++spin) {
            v0 = __hip_atomic_load(gp, __ATOMIC_RELAXED, __HIP_MEMORY_SCOPE_AGENT);
            v1 = __hip_atomic_load(gp + 1, __ATOMIC_RELAXED, __HIP_MEMORY_SCOPE_AGENT);
            if (__all((long long)(v0 & v1) < 0)) break;
            __builtin_amdgcn_s_sleep(4);
        }
        const float m0 = __uint_as_float((unsigned)v0), m1 = __uint_as_float((unsigned)v1);
        mean = add32(add16(m0 + m1)) * 0.125f;
        const float d0 = m0 - mean, d1 = m1 - mean;
        var = add32(add16(__uint_as_float((unsigned)(v0 >> 32) & 0x7fffffffu) + __uint_as_float((unsigned)(v1 >> 32) & 0x7fffffffu) +
                          80.f * (d0 * d0 + d1 * d1))) * (1.f / 640.f);
    }
    SST(4);
    {
        const float r1 = __builtin_amdgcn_rsqf(var + LN_EPS);
        const float r2 = r1 * __builtin_amdgcn_rsqf(var * r1 * r1 + LN_EPS);
        char* hb = L + w * 15360;
#pragma unroll
        for (int nt = 0; nt < 5; ++nt)
            *(u32x2*)(hb + n16 * 176 + (16 * nt + 4 * g) * 2) =
                u32x2{pk2((xv[4 * nt] - mean) * r2, (xv[4 * nt + 1] - mean) * r2), pk2((xv[4 * nt + 2] - mean) * r2, (xv[4 * nt + 3] - mean) * r2)};
        asm volatile("s_waitcnt lgkmcnt(0)" ::: "memory");
        const int q4 = (lane & 15) >> 2, p4 = lane & 3;
        lds_cptr hr = (lds_cptr)(lds_ptr)L + w * 15360 + (8 * (g & 1) + q4) * 176 + p4 * 8 + (g >> 1) * 32;
        const int s0 = tb * 64 + 16 * w + 8 * (g & 1);
        u16* hf = (u16*)(ws + OFF_H) + ((size_t)(5 * cg + (g >> 1)) * 64 + (s0 >> 5)) * 512 + (((s0 & 31) >> 3) * 16 + n16) * 8;
        const bf16x8 f0 = cat8(tr_read(hr), tr_read(hr + 704));
        const bf16x8 f1 = cat8(tr_read(hr + 64), tr_read(hr + 704 + 64));
        const bf16x8 f2 = cat8(tr_read(hr + (g < 2 ? 128 : 0)), tr_read(hr + 704 + (g < 2 ? 128 : 0)));
        *(bf16x8*)(hf) = f0;
        *(bf16x8*)(hf + 2 * 64 * 512) = f1;
        if (g < 2) *(bf16x8*)(hf + 4 * 64 * 512) = f2;
        u16* x1r = (u16*)(ws + OFF_X1) + (size_t)tok * 640 + 80 * cg + 4 * g;
#pragma unroll
        for (int nt = 0; nt < 5; ++nt) *(u32x2*)(x1r + 16 * nt) = x1o[nt];
    }
    SST(5); SST_FLUSHK(ws, XF ? 7 : 2);
}

template <int MODE, int NS, int PF>
DI void gemm_body(const bf16x8* __restrict__ po, const bf16x8* __restrict__ pf, const int (&ooff)[4], int fstride, f32x4 (&acc)[4][5]) {
    bf16x8 ro[PF][4], rf[PF][5];
#pragma unroll
    for (int s = 0; s < PF; ++s) {
#pragma unroll
        for (int j = 0; j < 4; ++j) ro[s][j] = po[ooff[j] + s * 64];
#pragma unroll
        for (int i = 0; i < 5; ++i) rf[s][i] = pf[i * fstride + s * 64];
        SBAR();
    }
#pragma unroll
    for (int s = 0; s < NS; ++s) {
        const int sl = s % PF;
        SBAR();
#pragma unroll
        for (int j = 0; j < 4; ++j)
#pragma unroll
            for (int i = 0; i < 5; ++i) {
                if (MODE == 1) acc[j][i] = MFMA16_32(ro[sl][j], rf[sl][i], acc[j][i]);
                else acc[j][i] = MFMA16_32(rf[sl][i], ro[sl][j], acc[j][i]);
            }
        SBAR();
        if (s + PF < NS) {
#pragma unroll
            for (int j = 0; j < 4; ++j) ro[sl][j] = po[ooff[j] + (s + PF) * 64];
#pragma unroll
            for (int i = 0; i < 5; ++i) rf[sl][i] = pf[i * fstride + (s + PF) * 64];
        }
    }
}

template <int MODE>
__global__ __launch_bounds__(256, 1) void k_gemm(char* __restrict__ ws, const u16* __restrict__ bias, u16* __restrict__ out) {
    __shared__ __attribute__((aligned(16))) f32x4 red[4 * 3 * 5 * 64];
    const int tid = threadIdx.x, lane = tid & 63, w = __builtin_amdgcn_readfirstlane(tid >> 6);
    const int b = blockIdx.x, xcd = b & 7, slot = b >> 3;
    const int n16 = lane & 15, g = lane >> 4;
    constexpr int KS = MODE == 1 ? 64 : 54;
    int ot, dt;
    if (MODE == 1) {
        ot = 7 * (xcd >> 1) + (slot >> 2); dt = 4 * (xcd & 1) + (slot & 3);
        if (slot >= 28 || ot >= 27) return;
    } else {
        ot = 8 * (xcd >> 1) + (slot >> 2); dt = 4 * (xcd & 1) + (slot & 3);
    }
    SST_DECL; SST(0);
    u32x2 bb = {0u, 0u}, xi[5]; float bs = 0.f;
    if (MODE == 1) bb = *(const u32x2*)(bias + 64 * ot + 16 * w + 4 * g);
    else {
        const int s = 64 * ot + 16 * w + n16;
        bs = bf2f(bias[s]);
        const u16* x1 = (const u16*)(ws + OFF_X1) + (size_t)s * 640 + 80 * dt + 4 * g;
#pragma unroll
        for (int i = 0; i < 5; ++i) xi[i] = *(const u32x2*)(x1 + 16 * i);
    }
    const int k0 = MODE == 1 ? 16 * w : (w < 2 ? 14 * w : 28 + 13 * (w - 2));
    const bf16x8* po = (const bf16x8*)(ws + (MODE == 1 ? OFF_W1F : OFF_W2F)) + ((size_t)(4 * ot) * KS + k0) * 64 + lane;
    const bf16x8* pf = (const bf16x8*)(ws + (MODE == 1 ? OFF_H : OFF_U)) + ((size_t)(5 * dt) * KS + k0) * 64 + lane;
    int ooff[4];
#pragma unroll
    for (int j = 0; j < 4; ++j) ooff[j] = ((j + w) & 3) * KS * 64;
    f32x4 acc[4][5];
#pragma unroll
    for (int j = 0; j < 4; ++j)
#pragma unroll
        for (int i = 0; i < 5; ++i) acc[j][i] = f32x4{0.f, 0.f, 0.f, 0.f};
    if (MODE == 1) gemm_body<1, 16, 6>(po, pf, ooff, KS * 64, acc);
    else if (w < 2) gemm_body<2, 14, 6>(po, pf, ooff, KS * 64, acc);
    else gemm_body<2, 13, 6>(po, pf, ooff, KS * 64, acc);
    SST(1);
#pragma unroll
    for (int j = 1; j < 4; ++j) {
        const int o = (w + j) & 3;
#pragma unroll
        for (int i = 0; i < 5; ++i) red[((o * 3 + (j - 1)) * 5 + i) * 64 + lane] = acc[j][i];
    }
    RAW_BARRIER();
#pragma unroll
    for (int j = 0; j < 3; ++j)
#pragma unroll
        for (int i = 0; i < 5; ++i) acc[1 + j][i] = red[((w * 3 + j) * 5 + i) * 64 + lane];
    SBAR();
#pragma unroll
    for (int i = 0; i < 5; ++i) acc[0][i] = (acc[0][i] + acc[1][i]) + (acc[2][i] + acc[3][i]);
    SST(2);
    if (MODE == 1) {
        const int f = 64 * ot + 16 * w + 4 * g;
        const float bv[4] = {bf_lo(bb[0]), bf_hi(bb[0]), bf_lo(bb[1]), bf_hi(bb[1])};
        u16* uf = (u16*)(ws + OFF_U);
#pragma unroll
        for (int i = 0; i < 5; ++i) {
            float r[4];
#pragma unroll
            for (int e = 0; e < 4; ++e) {
                const float u = acc[0][i][e] + bv[e];
                const float z = u * (1.f + 0.044715f * u * u) * (-2.f * 0.7978845608028654f * 1.4426950408889634f);
                r[e] = u * __builtin_amdgcn_rcpf(1.f + EX2(z));
            }
            *(u32x2*)(uf + ((size_t)((5 * dt + i) * 54 + (f >> 5)) * 64 + ((f & 31) >> 3) * 16 + n16) * 8 + (f & 7)) =
                u32x2{pk2(r[0], r[1]), pk2(r[2], r[3])};
        }
    } else {
        const int s = 64 * ot + 16 * w + n16;
        float xv[20]; float sum = 0.f;
#pragma unroll
        for (int i = 0; i < 5; ++i) {
            const int d = 80 * dt + 16 * i + 4 * g;
            u16* o = MODE == 3 ? out + (size_t)s * 640 + d
                               : out + ((size_t)((4 * ot + w) * 20 + (d >> 5)) * 64 + ((d & 31) >> 3) * 16 + n16) * 8 + (d & 7);
            const unsigned u0 = pk2(bf_lo(xi[i][0]) + acc[0][i][0] + bs, bf_hi(xi[i][0]) + acc[0][i][1] + bs);
            const unsigned u1 = pk2(bf_lo(xi[i][1]) + acc[0][i][2] + bs, bf_hi(xi[i][1]) + acc[0][i][3] + bs);
            *(u32x2*)o = u32x2{u0, u1};
            xv[4 * i] = bf_lo(u0); xv[4 * i + 1] = bf_hi(u0); xv[4 * i + 2] = bf_lo(u1); xv[4 * i + 3] = bf_hi(u1);
            sum += (xv[4 * i] + xv[4 * i + 1]) + (xv[4 * i + 2] + xv[4 * i + 3]);
        }
        if (MODE == 2) {
            const float mw = add32(add16(sum)) * (1.f / 80.f);
            float s2 = 0.f;
#pragma unroll
            for (int i = 0; i < 20; ++i) { const float dd = xv[i] - mw; s2 += dd * dd; }
            s2 = add32(add16(s2));
            if (g == 0) *(f32x2*)(ws + OFF_XS + ((size_t)s * 8 + dt) * 8) = f32x2{mw, s2};
        }
    }
    SST(3); SST_FLUSHK(ws, MODE == 1 ? (out ? 8 : 3) : (MODE == 2 ? 4 : 9));
}

typedef __attribute__((address_space(1))) unsigned gu32;
#define LD_SC1(off) __builtin_bit_cast(bf16x8, __builtin_amdgcn_raw_buffer_load_b128(rs, (int)(off), 0, 16))
DI void edge_publish(gu32* cnt) {
    RAW_BARRIER();
    if (threadIdx.x == 0) __hip_atomic_fetch_add(cnt, 1u, __ATOMIC_RELAXED, __HIP_MEMORY_SCOPE_AGENT);
}
DI void edge_wait(gu32* cnt, unsigned want) {
    if (threadIdx.x == 0) {
        for (int spin = 0; spin < (1 << 20); ++spin) {
            if (__hip_atomic_load(cnt, __ATOMIC_RELAXED, __HIP_MEMORY_SCOPE_AGENT) >= want) break;
            __builtin_amdgcn_s_sleep(1);
        }
        __builtin_amdgcn_fence(__ATOMIC_ACQUIRE, "wavefront");
    }
    RAW_BARRIER();
}
#define W1_A 6
#define W2_FIRST 7
constexpr int MLP_LDS = 73728;
template <bool R1>
__global__ __launch_bounds__(256, 1) void k_mlp(char* __restrict__ ws, const u16* __restrict__ xin, const u16* __restrict__ b1,
                                                const u16* __restrict__ b2, u16* __restrict__ out) {
    extern __shared__ __attribute__((aligned(16))) char lds_raw[];
    char* L = lds_raw;
    const int tid = threadIdx.x, lane = tid & 63, w = __builtin_amdgcn_readfirstlane(tid >> 6);
    const int b = blockIdx.x, slot = b >> 3, tb = ((b & 7) >> 1) * 8 + (slot >> 2), cg = (b & 1) * 4 + (slot & 3);
    const int ot1 = ((b & 7) >> 1) * 7 + (slot >> 2);
    const bool act1 = slot < 28 && ot1 < 27;
    const int n16 = lane & 15, g = lane >> 4;
    const __amdgpu_buffer_rsrc_t rs = __builtin_amdgcn_make_buffer_rsrc((void*)ws, 0, 0x8000000, 0x00020000);
    gu32* cnt = (gu32*)(ws + OFF_CNT);
    SST_DECL; SST(0);
    f32x4 acc[4][5];
#define ACC_ZERO() do { _Pragma("unroll") for (int i_ = 0; i_ < 4; ++i_) _Pragma("unroll") for (int j_ = 0; j_ < 5; ++j_) acc[i_][j_] = f32x4{0.f, 0.f, 0.f, 0.f}; } while (0)
#define ACC_REDUCE() do { \
    _Pragma("unroll") for (int j_ = 1; j_ < 4; ++j_) { const int o_ = (w + j_) & 3; \
        _Pragma("unroll") for (int i_ = 0; i_ < 5; ++i_) *(f32x4*)(L + (((o_ * 3 + (j_ - 1)) * 5 + i_) * 64 + lane) * 16) = acc[j_][i_]; } \
    RAW_BARRIER(); \
    _Pragma("unroll") for (int j_ = 0; j_ < 3; ++j_) _Pragma("unroll") for (int i_ = 0; i_ < 5; ++i_) \
        acc[1 + j_][i_] = *(const f32x4*)(L + (((w * 3 + j_) * 5 + i_) * 64 + lane) * 16); \
    SBAR(); \
    _Pragma("unroll") for (int i_ = 0; i_ < 5; ++i_) acc[0][i_] = (acc[0][i_] + acc[1][i_]) + (acc[2][i_] + acc[3][i_]); } while (0)
    const int tok = tb * 64 + 16 * w + n16;
    bf16x8 wr[16][4];
    int ro1[4], ro2[4];
#pragma unroll
    for (int j = 0; j < 4; ++j) { ro1[j] = ((j + w) & 3) * 64 * 64; ro2[j] = ((j + w) & 3) * 54 * 64; }
    const bf16x8* pw1 = (const bf16x8*)(ws + OFF_W1F) + ((size_t)(4 * (act1 ? ot1 : 0)) * 64 + 16 * w) * 64 + lane;
    const int k2 = w < 2 ? 14 * w : 28 + 13 * (w - 2);
    const bf16x8* pw2 = (const bf16x8*)(ws + OFF_W2F) + ((size_t)(4 * tb) * 54 + k2) * 64 + lane;
#define W1_LD(lo, hi) do { _Pragma("unroll") for (int s_ = (lo); s_ < (hi); ++s_) _Pragma("unroll") for (int j_ = 0; j_ < 4; ++j_) wr[s_][j_] = pw1[ro1[j_] + s_ * 64]; SBAR(); } while (0)
#define W2_LD(lo, hi) do { _Pragma("unroll") for (int s_ = (lo); s_ < (hi); ++s_) _Pragma("unroll") for (int j_ = 0; j_ < 4; ++j_) wr[s_][j_] = pw2[ro2[j_] + s_ * 64]; SBAR(); } while (0)

    u32x2 x1o[5], bb; float bs;
    {
        ACC_ZERO();
        u32x2 xi[5];
        const bf16x8* xb = (const bf16x8*)(ws + OFF_O) + (size_t)(tb * 4 * 20 + 5 * w) * 64 + lane;
        const bf16x8* wb = (const bf16x8*)(ws + OFF_WOF) + (size_t)(5 * cg * 20 + 5 * w) * 64 + lane;
        bf16x8 xf[5][4], wf[5][5];
        WO_LD(0); WO_LD(1); WO_LD(2); WO_LD(3);
#pragma unroll
        for (int ks = 0; ks < 5; ++ks) {
#pragma unroll
            for (int nt = 0; nt < 5; ++nt)
#pragma unroll
                for (int i = 0; i < 4; ++i) acc[i][nt] = MFMA16_32(wf[ks][nt], xf[ks][i], acc[i][nt]);
            SBAR();
            if (ks == 0) {
                WO_LD(4);
#pragma unroll
                for (int nt = 0; nt < 5; ++nt) {
                    const int d = 80 * cg + 16 * nt + 4 * g;
                    if (!R1) xi[nt] = *(const u32x2*)(xin + ((size_t)((tok >> 4) * 20 + (d >> 5)) * 64 + ((d & 31) >> 3) * 16 + n16) * 8 + (d & 7));
                    else xi[nt] = *(const u32x2*)(xin + (size_t)tok * 640 + d);
                }
                bb = *(const u32x2*)(b1 + 64 * (act1 ? ot1 : 0) + 16 * w + 4 * g);
                bs = bf2f(b2[tok]);
                SBAR();
            }
            if (ks == 1) W1_LD(0, 2);
            if (ks == 2) W1_LD(2, 4);
            if (ks == 3) W1_LD(4, 6);
            if (ks == 4) W1_LD(6, W1_A);
        }
        SST(1);
        ACC_REDUCE();
        float xv[20], s = 0.f;
#pragma unroll
        for (int nt = 0; nt < 5; ++nt) {
            const unsigned u0 = pk2(bf_lo(xi[nt][0]) + acc[0][nt][0], bf_hi(xi[nt][0]) + acc[0][nt][1]);
            const unsigned u1 = pk2(bf_lo(xi[nt][1]) + acc[0][nt][2], bf_hi(xi[nt][1]) + acc[0][nt][3]);
            x1o[nt] = u32x2{u0, u1};
            xv[4 * nt] = bf_lo(u0); xv[4 * nt + 1] = bf_hi(u0); xv[4 * nt + 2] = bf_lo(u1); xv[4 * nt + 3] = bf_hi(u1);
            s += (xv[4 * nt] + xv[4 * nt + 1]) + (xv[4 * nt + 2] + xv[4 * nt + 3]);
        }
        const float mw = add32(add16(s)) * (1.f / 80.f);
        float s2 = 0.f;
#pragma unroll
        for (int i = 0; i < 20; ++i) { const float dd = xv[i] - mw; s2 += dd * dd; }
        s2 = add32(add16(s2));
        gu64* gp = (gu64*)(ws + OFF_XG) + (size_t)tok * 8;
        if (g == 0) __hip_atomic_store(gp + cg, ((unsigned long long)(__float_as_uint(s2) | 0x80000000u) << 32) | __float_as_uint(mw),
                                       __ATOMIC_RELAXED, __HIP_MEMORY_SCOPE_AGENT);
        SBAR();
        SST(2);
        unsigned long long v0, v1;
        for (int spin = 0; spin < (1 << 20); ++spin) {
            v0 = __hip_atomic_load(gp + 2 * g, __ATOMIC_RELAXED, __HIP_MEMORY_SCOPE_AGENT);
            v1 = __hip_atomic_load(gp + 2 * g + 1, __ATOMIC_RELAXED, __HIP_MEMORY_SCOPE_AGENT);
            if (__all((long long)(v0 & v1) < 0)) break;
            __builtin_amdgcn_s_sleep(4);
        }
        SST(3);
        const float m0 = __uint_as_float((unsigned)v0), m1 = __uint_as_float((unsigned)v1);
        const float mean = add32(add16(m0 + m1)) * 0.125f;
        const float d0 = m0 - mean, d1 = m1 - mean;
        const float var = add32(add16(__uint_as_float((unsigned)(v0 >> 32) & 0x7fffffffu) + __uint_as_float((unsigned)(v1 >> 32) & 0x7fffffffu) +
                                      80.f * (d0 * d0 + d1 * d1))) * (1.f / 640.f);
        const float r1 = __builtin_amdgcn_rsqf(var + LN_EPS);
        const float r2 = r1 * __builtin_amdgcn_rsqf(var * r1 * r1 + LN_EPS);
        char* hb = L + w * 15360;
#pragma unroll
        for (int nt = 0; nt < 5; ++nt)
            *(u32x2*)(hb + n16 * 176 + (16 * nt + 4 * g) * 2) =
                u32x2{pk2((xv[4 * nt] - mean) * r2, (xv[4 * nt + 1] - mean) * r2), pk2((xv[4 * nt + 2] - mean) * r2, (xv[4 * nt + 3] - mean) * r2)};
        asm volatile("s_waitcnt lgkmcnt(0)" ::: "memory");
        const int q4 = (lane & 15) >> 2, p4 = lane & 3;
        lds_cptr hr = (lds_cptr)(lds_ptr)L + w * 15360 + (8 * (g & 1) + q4) * 176 + p4 * 8 + (g >> 1) * 32;
        const int s0 = tb * 64 + 16 * w + 8 * (g & 1);
        const int hoff = (int)OFF_H + ((5 * cg + (g >> 1)) * 64 + (s0 >> 5)) * 1024 + (((s0 & 31) >> 3) * 16 + n16) * 16;
        const bf16x8 f0 = cat8(tr_read(hr), tr_read(hr + 704));
        const bf16x8 f1 = cat8(tr_read(hr + 64), tr_read(hr + 704 + 64));
        const bf16x8 f2 = cat8(tr_read(hr + (g < 2 ? 128 : 0)), tr_read(hr + 704 + (g < 2 ? 128 : 0)));
        __builtin_amdgcn_raw_buffer_store_b128(__builtin_bit_cast(u32x4, f0), rs, hoff, 0, 16);
        __builtin_amdgcn_raw_buffer_store_b128(__builtin_bit_cast(u32x4, f1), rs, hoff + 2 * 65536, 0, 16);
        if (g < 2) __builtin_amdgcn_raw_buffer_store_b128(__builtin_bit_cast(u32x4, f2), rs, hoff + 4 * 65536, 0, 16);
        SBAR();
        W1_LD(W1_A, W1_A + 4);
        VMCNT(16);
        SST(4);
        edge_publish(cnt + cg * 64);
    }
    if (act1) {
        W1_LD(W1_A + 4, 16);
        edge_wait(cnt + cg * 64, 32);
        SST(5);
        ACC_ZERO();
        const int ho = (int)OFF_H + ((5 * cg) * 64 + 16 * w) * 1024 + lane * 16;
        constexpr int PF = 6;
        bf16x8 rf[PF][5];
#pragma unroll
        for (int s = 0; s < PF; ++s) {
#pragma unroll
            for (int i = 0; i < 5; ++i) rf[s][i] = LD_SC1(ho + i * 65536 + s * 1024);
            SBAR();
        }
#pragma unroll
        for (int s = 0; s < 16; ++s) {
#pragma unroll
            for (int j = 0; j < 4; ++j)
#pragma unroll
                for (int i = 0; i < 5; ++i) acc[j][i] = MFMA16_32(wr[s][j], rf[s % PF][i], acc[j][i]);
            SBAR();
            if (s + PF < 16) {
#pragma unroll
                for (int i = 0; i < 5; ++i) rf[s % PF][i] = LD_SC1(ho + i * 65536 + (s + PF) * 1024);
                SBAR();
            }
            if (s >= W2_FIRST && s - W2_FIRST < 9) W2_LD(s - W2_FIRST, s - W2_FIRST + 1);
        }
        SST(6);
        ACC_REDUCE();
        const int f = 64 * ot1 + 16 * w + 4 * g;
        const float bv[4] = {bf_lo(bb[0]), bf_hi(bb[0]), bf_lo(bb[1]), bf_hi(bb[1])};
        const int uo = (int)OFF_U + ((5 * cg) * 54 + (f >> 5)) * 1024 + (((f & 31) >> 3) * 16 + n16) * 16 + (f & 7) * 2;
#pragma unroll
        for (int i = 0; i < 5; ++i) {
            float r[4];
#pragma unroll
            for (int e = 0; e < 4; ++e) {
                const float u = acc[0][i][e] + bv[e];
                const float z = u * (1.f + 0.044715f * u * u) * (-2.f * 0.7978845608028654f * 1.4426950408889634f);
                r[e] = u * __builtin_amdgcn_rcpf(1.f + EX2(z));
            }
            __builtin_amdgcn_raw_buffer_store_b64(u32x2{pk2(r[0], r[1]), pk2(r[2], r[3])}, rs, uo + i * 54 * 1024, 0, 16);
        }
        SBAR();
        W2_LD(9, 13);
        VMCNT(16);
        SST(7);
        edge_publish(cnt + (8 + cg) * 64);
    } else {
        W2_LD(0, 13);
    }
    {
        if (w < 2) W2_LD(13, 14);
        edge_wait(cnt + (8 + cg) * 64, 27);
        SST(8);
        ACC_ZERO();
        const int uo = (int)OFF_U + ((5 * cg) * 54 + k2) * 1024 + lane * 16;
        constexpr int PF = 6;
        bf16x8 rf[PF][5];
#pragma unroll
        for (int s = 0; s < PF; ++s) {
#pragma unroll
            for (int i = 0; i < 5; ++i) rf[s][i] = LD_SC1(uo + i * 54 * 1024 + s * 1024);
            SBAR();
        }
#pragma unroll
        for (int s = 0; s < 14; ++s) {
            if (s < 13 || w < 2) {
#pragma unroll
                for (int j = 0; j < 4; ++j)
#pragma unroll
                    for (int i = 0; i < 5; ++i) acc[j][i] = MFMA16_32(rf[s % PF][i], wr[s][j], acc[j][i]);
            }
            SBAR();
            if (s + PF < 13 || (s + PF == 13 && w < 2)) {
#pragma unroll
                for (int i = 0; i < 5; ++i) rf[s % PF][i] = LD_SC1(uo + i * 54 * 1024 + (s + PF) * 1024);
                SBAR();
            }
        }
        SST(9);
        ACC_REDUCE();
        float xv[20], sum = 0.f;
#pragma unroll
        for (int i = 0; i < 5; ++i) {
            const int d = 80 * cg + 16 * i + 4 * g;
            const unsigned u0 = pk2(bf_lo(x1o[i][0]) + acc[0][i][0] + bs, bf_hi(x1o[i][0]) + acc[0][i][1] + bs);
            const unsigned u1 = pk2(bf_lo(x1o[i][1]) + acc[0][i][2] + bs, bf_hi(x1o[i][1]) + acc[0][i][3] + bs);
            if (R1) __builtin_amdgcn_raw_buffer_store_b64(u32x2{u0, u1}, rs,
                        (int)OFF_X2 + ((4 * tb + w) * 20 + (d >> 5)) * 1024 + (((d & 31) >> 3) * 16 + n16) * 16 + (d & 7) * 2, 0, 16);
            else *(u32x2*)(out + (size_t)tok * 640 + d) = u32x2{u0, u1};
            xv[4 * i] = bf_lo(u0); xv[4 * i + 1] = bf_hi(u0); xv[4 * i + 2] = bf_lo(u1); xv[4 * i + 3] = bf_hi(u1);
            sum += (xv[4 * i] + xv[4 * i + 1]) + (xv[4 * i + 2] + xv[4 * i + 3]);
        }
        if (R1) {
            const float mw = add32(add16(sum)) * (1.f / 80.f);
            float s2 = 0.f;
#pragma unroll
            for (int i = 0; i < 20; ++i) { const float dd = xv[i] - mw; s2 += dd * dd; }
            s2 = add32(add16(s2));
            if (g == 0) __builtin_amdgcn_raw_buffer_store_b64(u32x2{__float_as_uint(mw), __float_as_uint(s2)}, rs, (int)OFF_XS + (tok * 8 + cg) * 8, 0, 16);
        }
    }
    if (R1) {
        const int mt0 = cg >> 1, isv = cg & 1;
        const bf16x8* wb = (const bf16x8*)(ws + OFF_WQF) + (size_t)(5 * cg * 20 + 5 * w) * 64 + lane;
        const bf16x8* kb = (const bf16x8*)(ws + OFF_WQF) + (size_t)((40 + 2 * isv) * 20 + 5 * w) * 64 + lane;
        bf16x8 wf[5][5], wkf[5][2], xf[5][4], xk[5];
        SBAR();
#pragma unroll
        for (int i = 0; i < 16; ++i) wf[i / 5][i % 5] = wb[((i % 5) * 20 + i / 5) * 64];
        SBAR();
        VMCNT(16);
        SST(10);
        edge_publish(cnt + (16 + tb) * 64);
#pragma unroll
        for (int i = 16; i < 25; ++i) wf[i / 5][i % 5] = wb[((i % 5) * 20 + i / 5) * 64];
#pragma unroll
        for (int ks = 0; ks < 5; ++ks) { wkf[ks][0] = kb[ks * 64]; wkf[ks][1] = kb[(20 + ks) * 64]; }
        f32x4 csq[5], cs0, cs1, stq[4], stk[4], acck[2];
        {
            const float* csp = (const float*)(ws + OFF_CS);
#pragma unroll
            for (int nt = 0; nt < 5; ++nt) csq[nt] = *(const f32x4*)(csp + 80 * cg + 16 * nt + 4 * g);
            cs0 = *(const f32x4*)(csp + 640 + 32 * isv + 4 * g); cs1 = *(const f32x4*)(csp + 640 + 32 * isv + 16);
        }
        SBAR();
        edge_wait(cnt + (16 + tb) * 64, 8);
        SST(11);
        const int xo = (int)OFF_X2 + (tb * 4 * 20 + 5 * w) * 1024 + lane * 16;
#pragma unroll
        for (int ks = 0; ks < 5; ++ks) {
#pragma unroll
            for (int i = 0; i < 4; ++i) xf[ks][i] = LD_SC1(xo + (((i + w) & 3) * 20 + ks) * 1024);
            xk[ks] = LD_SC1(xo + (mt0 * 20 + ks) * 1024);
            SBAR();
        }
#pragma unroll
        for (int i = 0; i < 4; ++i) {
            stq[i] = __builtin_bit_cast(f32x4, LD_SC1((int)OFF_XS + (tb * 64 + 16 * w + n16) * 64 + i * 16));
            stk[i] = __builtin_bit_cast(f32x4, LD_SC1((int)OFF_XS + (tb * 64 + 16 * mt0 + n16) * 64 + i * 16));
        }
        SBAR();
        ACC_ZERO();
        acck[0] = f32x4{0.f, 0.f, 0.f, 0.f}; acck[1] = acck[0];
#pragma unroll
        for (int ks = 0; ks < 5; ++ks) {
#pragma unroll
            for (int nt = 0; nt < 5; ++nt)
#pragma unroll
                for (int i = 0; i < 4; ++i) acc[i][nt] = MFMA16_32(wf[ks][nt], xf[ks][i], acc[i][nt]);
            acck[0] = MFMA16_32(wkf[ks][0], xk[ks], acck[0]);
            acck[1] = MFMA16_32(wkf[ks][1], xk[ks], acck[1]);
            SBAR();
        }
        SST(12);
        const float z4[4] = {0.f, 0.f, 0.f, 0.f}, z5[5] = {0.f, 0.f, 0.f, 0.f, 0.f}, z2[2] = {0.f, 0.f};
        qkv_finish<true>(ws, L, tb, cg, w, lane, acc, acck, csq, cs0, cs1, stq, stk, z4, z4, z5, z2);
    }
    SST_FLUSHK(ws, R1 ? 2 : 7);
}

extern "C" void kernel_launch(void* const* d_in, const int* in_sizes, int n_in, void* d_out, int out_size, void* d_ws,
                              size_t ws_size, hipStream_t stream) {
    const u16* x = (const u16*)d_in[0];
    const u16* wq = (const u16*)d_in[1];
    const u16* wk = (const u16*)d_in[2];
    const u16* wv = (const u16*)d_in[3];
    const u16* wo = (const u16*)d_in[4];
    const u16* w1 = (const u16*)d_in[6];
    const u16* b1 = (const u16*)d_in[7];
    const u16* w2 = (const u16*)d_in[8];
    const u16* b2 = (const u16*)d_in[9];
    char* ws = (char*)d_ws;
    u16* x2 = (u16*)(ws + OFF_X2);
    (void)hipFuncSetAttribute((const void*)k_qkv<false>, hipFuncAttributeMaxDynamicSharedMemorySize, QKV_LDS);
    (void)hipFuncSetAttribute((const void*)k_qkv<true>, hipFuncAttributeMaxDynamicSharedMemorySize, QKV_LDS);
    (void)hipFuncSetAttribute((const void*)k_attn<true>, hipFuncAttributeMaxDynamicSharedMemorySize, ATT_LDS);
    (void)hipFuncSetAttribute((const void*)k_attn<false>, hipFuncAttributeMaxDynamicSharedMemorySize, ATT_LDS);
    (void)hipFuncSetAttribute((const void*)k_mlp<true>, hipFuncAttributeMaxDynamicSharedMemorySize, MLP_LDS);
    (void)hipFuncSetAttribute((const void*)k_mlp<false>, hipFuncAttributeMaxDynamicSharedMemorySize, MLP_LDS);
    k_qkv<false><<<256, 256, QKV_LDS, stream>>>(x, wq, wk, wv, wo, ws);
    k_attn<true><<<256, 512, ATT_LDS, stream>>>(ws, w1, w2, wo, wq);
    k_mlp<true><<<256, 256, MLP_LDS, stream>>>(ws, x, b1, b2, nullptr);
    k_attn<false><<<256, 512, ATT_LDS, stream>>>(ws, w1, w2, wo, wq);
    k_mlp<false><<<256, 256, MLP_LDS, stream>>>(ws, x2, b1, b2, (u16*)d_out);
}
```

```cpp
#include <hip/hip_runtime.h>
#include <hip/hip_bf16.h>
#include <stdint.h>

typedef unsigned short u16;
typedef short bf16x8 __attribute__((ext_vector_type(8)));
typedef short bf16x4 __attribute__((ext_vector_type(4)));
typedef float f32x16 __attribute__((ext_vector_type(16)));
typedef float f32x4 __attribute__((ext_vector_type(4)));
typedef float f32x2 __attribute__((ext_vector_type(2)));
typedef unsigned u32x4 __attribute__((ext_vector_type(4)));
typedef unsigned u32x2 __attribute__((ext_vector_type(2)));
typedef __bf16 bf2_t __attribute__((ext_vector_type(2)));
typedef __attribute__((address_space(3))) char* lds_ptr;
typedef __attribute__((address_space(3))) const char* lds_cptr;
typedef __attribute__((address_space(1))) unsigned long long gu64;

#define DI __device__ __forceinline__
#define SBAR() __builtin_amdgcn_sched_barrier(0)
#define PIN(x) asm volatile("" : "+v"(x))
#define MFMA32_16(a, b, c) __builtin_amdgcn_mfma_f32_32x32x16_bf16(a, b, c, 0, 0, 0)
#define MFMA32_8(a, b, c) __builtin_amdgcn_mfma_f32_32x32x8bf16_1k(a, b, c, 0, 0, 0)
#define MFMA16_32(a, b, c) __builtin_amdgcn_mfma_f32_16x16x32_bf16(a, b, c, 0, 0, 0)
#define EX2(x) __builtin_amdgcn_exp2f(x)
#define RAW_BARRIER() asm volatile("s_waitcnt lgkmcnt(0)\n\ts_barrier" ::: "memory")
#define VMCNT(n) asm volatile("s_waitcnt vmcnt(%0)" :: "i"(n) : "memory")

#define SST_DECL do {} while (0)
#define SST(i) do {} while (0)
#define SST_FLUSHK(ws, kid) do {} while (0)
#define PREP_STORE(p, v) asm volatile("global_store_dwordx4 %0, %1, off sc1\n\ts_nop 1" :: "v"(p), "v"(v) : "memory")
#define ST8(p, v) (*(u32x2*)(p) = (v))
#define KADDR(dst, sym) asm volatile("s_getpc_b64 s[40:41]\n\ts_add_u32 s40, s40, " sym "@rel32@lo+4\n\ts_addc_u32 s41, s41, " sym "@rel32@hi+12\n\ts_mov_b64 %0, s[40:41]" : "=s"(dst) :: "s40", "s41", "scc")
constexpr int S_ = 2048, D_ = 640, F_ = 1728;
constexpr float LN_EPS = 1e-5f;
constexpr float QSCALE = 0.22360679774997896f * 1.4426950408889634f;
constexpr float SAFE_BOUND = 60.0f;

DI unsigned pk2(float a, float b) { f32x2 v = {a, b}; bf2_t r = __builtin_convertvector(v, bf2_t); return __builtin_bit_cast(unsigned, r); }
DI float bf_lo(unsigned u) { return __uint_as_float(u << 16); }
DI float bf_hi(unsigned u) { return __uint_as_float(u & 0xffff0000u); }
DI float bf2f(u16 v) { return __uint_as_float((unsigned)v << 16); }
DI float dot2(unsigned a, unsigned b, float c) { return __builtin_amdgcn_fdot2_f32_bf16(__builtin_bit_cast(bf2_t, a), __builtin_bit_cast(bf2_t, b), c, false); }
DI bf16x4 tr_read(lds_cptr p) { return __builtin_amdgcn_ds_read_tr16_b64_v4i16((__attribute__((address_space(3))) bf16x4*)p); }
DI bf16x8 cat8(bf16x4 a, bf16x4 b) { return __builtin_shufflevector(a, b, 0, 1, 2, 3, 4, 5, 6, 7); }
DI void glds16(const void* g, lds_ptr l) {
    unsigned keep;
    asm volatile("s_mov_b32 %0, m0\n\ts_mov_b32 m0, %2\n\ts_nop 0\n\tglobal_load_lds_dwordx4 %1, off\n\ts_mov_b32 m0, %0"
                 : "=&s"(keep) : "v"(g), "s"((unsigned)(uintptr_t)l) : "memory");
}
DI float xor32(float v) { return __shfl_xor(v, 32); }
DI float add32(float v) {
    auto r = __builtin_amdgcn_permlane32_swap(__float_as_uint(v), __float_as_uint(v), false, false);
    return __uint_as_float(r[0]) + __uint_as_float(r[1]);
}
DI float add16(float v) {
    auto r = __builtin_amdgcn_permlane16_swap(__float_as_uint(v), __float_as_uint(v), false, false);
    return __uint_as_float(r[0]) + __uint_as_float(r[1]);
}
DI float max16x(float v) { auto r = __builtin_amdgcn_permlane16_swap(__float_as_uint(v), __float_as_uint(v), false, false); return fmaxf(__uint_as_float(r[0]), __uint_as_float(r[1])); }
DI float max32x(float v) { auto r = __builtin_amdgcn_permlane32_swap(__float_as_uint(v), __float_as_uint(v), false, false); return fmaxf(__uint_as_float(r[0]), __uint_as_float(r[1])); }
template <int CTRL> DI float dppf(float v) { return __int_as_float(__builtin_amdgcn_update_dpp(0, __float_as_int(v), CTRL, 0xf, 0xf, false)); }
DI float rowmax16(float v) {
    v = fmaxf(v, dppf<0x128>(v)); v = fmaxf(v, dppf<0x124>(v)); v = fmaxf(v, dppf<0x122>(v)); v = fmaxf(v, dppf<0x121>(v)); return v;
}
DI float wavemax(float v) { return max32x(max16x(rowmax16(v))); }
DI u32x4 gather8(const u16* s, int ld) {
    u32x4 r;
#pragma unroll
    for (int e = 0; e < 4; ++e) r[e] = (unsigned)s[(2 * e) * ld] | ((unsigned)s[(2 * e + 1) * ld] << 16);
    return r;
}

constexpr size_t OFF_Q = 0;
constexpr size_t OFF_K1 = OFF_Q + (size_t)2048 * 32 * 48;
constexpr size_t OFF_K2 = OFF_K1 + 2048 * 32;
constexpr size_t OFF_V = OFF_K2 + 2048 * 8;
constexpr size_t OFF_KN = OFF_V + 128 * 640;
constexpr size_t OFF_QN = OFF_KN + 512;
constexpr size_t OFF_CS = OFF_QN + 4096;
constexpr size_t OFF_CONST = OFF_CS + 4096;
constexpr size_t OFF_CNT = OFF_CONST + 256;
constexpr size_t OFF_XG = OFF_CNT + 48 * 256;
constexpr size_t OFF_XS = OFF_XG + 262144;
constexpr size_t OFF_X1 = OFF_XS + 131072;
constexpr size_t OFF_H = OFF_X1 + (size_t)S_ * D_ * 2;
constexpr size_t OFF_U = OFF_H + (size_t)S_ * D_ * 2;
constexpr size_t OFF_X2 = OFF_U + (size_t)D_ * F_ * 2;
constexpr size_t OFF_O = OFF_X2 + (size_t)S_ * D_ * 2;
constexpr size_t OFF_W1F = OFF_O + (size_t)S_ * D_ * 2;
constexpr size_t OFF_W2F = OFF_W1F + (size_t)S_ * F_ * 2;
constexpr size_t OFF_WQF = OFF_W2F + (size_t)S_ * F_ * 2;
constexpr size_t OFF_WOF = OFF_WQF + (size_t)44 * 20 * 1024;
constexpr int NPREP = 1728 + 1728 + 200 + 200;

DI void chan8(const f32x4 (&st)[4], float& mean, float& var) {
    float m = 0.f, m2 = 0.f;
#pragma unroll
    for (int i = 0; i < 4; ++i) { m += st[i][0] + st[i][2]; m2 += st[i][1] + st[i][3]; }
    m *= 0.125f;
#pragma unroll
    for (int i = 0; i < 4; ++i) { const float d0 = st[i][0] - m, d1 = st[i][2] - m; m2 += 80.f * (d0 * d0 + d1 * d1); }
    mean = m; var = m2 * (1.f / 640.f);
}

template <bool FAST>
DI void qkv_finish(char* __restrict__ ws, char* L, int tb, int cg, int w, int lane, f32x4 (&acc)[4][5], f32x4 (&acck)[2],
                   const f32x4 (&csq)[5], const f32x4& cs0, const f32x4& cs1, const f32x4 (&stq)[4], const f32x4 (&stk)[4],
                   const float (&sm)[4], const float (&sq)[4], const float (&cs)[5], const float (&csk)[2]) {
    const int n16 = lane & 15, g = lane >> 4, mt0 = cg >> 1, isv = cg & 1;
    constexpr int R_OFF = 0, PK_OFF = 61440, ST_OFF = 69632, CS_OFF = 71680;
#pragma unroll
    for (int j = 1; j < 4; ++j) {
        const int o = (w + j) & 3;
#pragma unroll
        for (int nt = 0; nt < 5; ++nt) *(f32x4*)(L + R_OFF + (((o * 3 + (j - 1)) * 5 + nt) * 64 + lane) * 16) = acc[j][nt];
    }
    *(f32x4*)(L + PK_OFF + ((w * 2 + 0) * 64 + lane) * 16) = acck[0];
    *(f32x4*)(L + PK_OFF + ((w * 2 + 1) * 64 + lane) * 16) = acck[1];
    if (!FAST && g == 0) {
#pragma unroll
        for (int i = 0; i < 4; ++i) *(f32x2*)(L + ST_OFF + (w * 64 + ((i + w) & 3) * 16 + n16) * 8) = f32x2{sm[i], sq[i]};
#pragma unroll
        for (int nt = 0; nt < 5; ++nt) *(float*)(L + CS_OFF + w * 448 + (nt * 16 + n16) * 4) = cs[nt];
#pragma unroll
        for (int nt = 0; nt < 2; ++nt) *(float*)(L + CS_OFF + w * 448 + (80 + nt * 16 + n16) * 4) = csk[nt];
    }
    char* qrow = ws + OFF_Q + (size_t)(tb * 8 + cg) * 12288 + (16 * w + n16) * 48;
    int qoff[5];
#pragma unroll
    for (int nt = 0; nt < 5; ++nt) { const int G = 4 * nt + g, hl = G / 5; qoff[nt] = hl * 3072 + (G - 5 * hl) * 8; PIN(qoff[nt]); }
    RAW_BARRIER();
    auto stats = [&](int tl, const f32x4 (&st)[4], float& mean, float& rstd) {
        float var;
        if (FAST) chan8(st, mean, var);
        else {
            float s = 0.f, s2 = 0.f;
#pragma unroll
            for (int ww = 0; ww < 4; ++ww) { f32x2 v = *(const f32x2*)(L + ST_OFF + (ww * 64 + tl) * 8); s += v[0]; s2 += v[1]; }
            mean = s * (1.f / 640.f);
            var = fmaxf(s2 * (1.f / 640.f) - mean * mean, 0.f);
        }
        rstd = __builtin_amdgcn_rsqf(var + LN_EPS);
    };
    {
        f32x4 rr[3][5], cc[4][5];
#pragma unroll
        for (int j = 0; j < 3; ++j)
#pragma unroll
            for (int nt = 0; nt < 5; ++nt) rr[j][nt] = *(const f32x4*)(L + R_OFF + (((w * 3 + j) * 5 + nt) * 64 + lane) * 16);
        if (!FAST) {
#pragma unroll
            for (int ww = 0; ww < 4; ++ww)
#pragma unroll
                for (int nt = 0; nt < 5; ++nt) cc[ww][nt] = *(const f32x4*)(L + CS_OFF + ww * 448 + (nt * 16 + 4 * g) * 4);
        }
        float mean, rstd; stats(16 * w + n16, stq, mean, rstd);
        SBAR();
        const float sc = rstd * QSCALE;
        float n2[5];
#pragma unroll
        for (int nt = 0; nt < 5; ++nt) {
            const f32x4 a = (acc[0][nt] + rr[0][nt]) + (rr[1][nt] + rr[2][nt]);
            f32x4 s;
            if (FAST) s = csq[nt];
            else {
                s = (cc[0][nt] + cc[1][nt]) + (cc[2][nt] + cc[3][nt]);
                if (tb == 0 && w == 0 && n16 == 0) *(f32x4*)(ws + OFF_CS + (80 * cg + 16 * nt + 4 * g) * 4) = s;
            }
            const unsigned u0 = pk2((a[0] - mean * s[0]) * sc, (a[1] - mean * s[1]) * sc);
            const unsigned u1 = pk2((a[2] - mean * s[2]) * sc, (a[3] - mean * s[3]) * sc);
            { const u32x2 v_ = u32x2{u0, u1}; ST8(qrow + qoff[nt], v_); }
            n2[nt] = dot2(u1, u1, dot2(u0, u0, 0.f));
        }
        float hn[4];
        hn[0] = n2[0] + (g == 0 ? n2[1] : 0.f);
        hn[1] = (g > 0 ? n2[1] : 0.f) + (g < 2 ? n2[2] : 0.f);
        hn[2] = (g > 1 ? n2[2] : 0.f) + (g < 3 ? n2[3] : 0.f);
        hn[3] = (g == 3 ? n2[3] : 0.f) + n2[4];
#pragma unroll
        for (int hh = 0; hh < 4; ++hh) hn[hh] = add32(add16(hn[hh]));
        const float n2a = (g & 1) ? hn[1] : hn[0], n2b = (g & 1) ? hn[3] : hn[2];
        const unsigned qb = pk2(__builtin_amdgcn_sqrtf((g & 2) ? n2b : n2a), 0.f);
        { const u32x2 v_ = u32x2{qb, 0u}; ST8(qrow + g * 3072 + 40, v_); }
        float m = bf_lo(qb);
        if (!(m == m)) m = INFINITY;
        m = wavemax(m);
        if (lane == 0) *(float*)(ws + OFF_QN + ((tb * 8 + cg) * 4 + w) * 4) = m;
    }
    if (w == 3) {
        const int t16 = tb * 4 + mt0;
        float mean, rstd; stats(mt0 * 16 + n16, stk, mean, rstd);
        f32x4 a0 = {0.f, 0.f, 0.f, 0.f}, a1 = a0, s0 = a0, s1 = a0;
#pragma unroll
        for (int ww = 0; ww < 4; ++ww) {
            a0 += *(const f32x4*)(L + PK_OFF + ((ww * 2 + 0) * 64 + lane) * 16);
            a1 += *(const f32x4*)(L + PK_OFF + ((ww * 2 + 1) * 64 + n16) * 16);
            if (!FAST) {
                s0 += *(const f32x4*)(L + CS_OFF + ww * 448 + (80 + 4 * g) * 4);
                s1 += *(const f32x4*)(L + CS_OFF + ww * 448 + 96 * 4);
            }
        }
        if (FAST) { s0 = cs0; s1 = cs1; }
        else if (tb == 0 && cg < 2 && n16 == 0) {
            *(f32x4*)(ws + OFF_CS + (640 + 32 * isv + 4 * g) * 4) = s0;
            if (g == 0) *(f32x4*)(ws + OFF_CS + (640 + 32 * isv + 16) * 4) = s1;
        }
        const unsigned u0 = pk2((a0[0] - mean * s0[0]) * rstd, (a0[1] - mean * s0[1]) * rstd);
        const unsigned u1 = pk2((a0[2] - mean * s0[2]) * rstd, (a0[3] - mean * s0[3]) * rstd);
        const unsigned u2 = pk2((a1[0] - mean * s1[0]) * rstd, (a1[1] - mean * s1[1]) * rstd);
        const unsigned u3 = pk2((a1[2] - mean * s1[2]) * rstd, (a1[3] - mean * s1[3]) * rstd);
        if (!isv) {
            const int key = t16 * 16 + n16;
            *(u32x2*)(ws + OFF_K1 + key * 32 + g * 8) = u32x2{u0, u1};
            if (g == 0) *(u32x2*)(ws + OFF_K2 + key * 8) = u32x2{u2, u3};
            float n2 = dot2(u1, u1, dot2(u0, u0, 0.f));
            n2 = add32(add16(n2));
            n2 = dot2(u3, u3, dot2(u2, u2, n2));
            if (!(n2 == n2)) n2 = INFINITY;
            n2 = rowmax16(n2);
            if (lane == 0) *(float*)(ws + OFF_KN + t16 * 4) = n2;
        } else {
            u16* vb = (u16*)(ws + OFF_V + t16 * 640 + (n16 >> 3) * 320) + (n16 & 7);
            vb[(4 * g + 0) * 8] = (u16)u0; vb[(4 * g + 1) * 8] = (u16)(u0 >> 16);
            vb[(4 * g + 2) * 8] = (u16)u1; vb[(4 * g + 3) * 8] = (u16)(u1 >> 16);
            if (g == 0) { vb[16 * 8] = (u16)u2; vb[17 * 8] = (u16)(u2 >> 16); vb[18 * 8] = (u16)u3; vb[19 * 8] = (u16)(u3 >> 16); }
        }
    }
    if (w == 0) *(u32x2*)(ws + OFF_XG + ((size_t)(tb * 64 + lane) * 8 + cg) * 8) = u32x2{0u, 0u};
}

constexpr int QKV_LDS = 163840;
template <bool FAST>
__global__ __launch_bounds__(256, 1) void k_qkv(const u16* __restrict__ x, const u16* __restrict__ wq, const u16* __restrict__ wk,
                                                const u16* __restrict__ wv, const u16* __restrict__ wo, char* __restrict__ ws) {
    extern __shared__ __attribute__((aligned(16))) char lds_raw[];
    lds_ptr lds = (lds_ptr)lds_raw;
    char* L = lds_raw;
    const int tid = threadIdx.x, lane = tid & 63, w = __builtin_amdgcn_readfirstlane(tid >> 6);
    const int b = blockIdx.x, slot = b >> 3, tb = ((b & 7) >> 1) * 8 + (slot >> 2), cg = (b & 1) * 4 + (slot & 3);
    const int mt0 = cg >> 1, isv = cg & 1;
    const int n16 = lane & 15, g = lane >> 4;
    SST_DECL; SST(0);
    unsigned code_touch = 0u;
    if (!FAST && w == 2) {
        uint64_t a[5];
        KADDR(a[0], "_Z6k_attnILb1EEvPcPKtS2_S2_S2_"); KADDR(a[1], "_Z6k_attnILb0EEvPcPKtS2_S2_S2_");
        KADDR(a[2], "_Z5k_mlpILb1EEvPcPKtS2_S2_Pt"); KADDR(a[3], "_Z5k_mlpILb0EEvPcPKtS2_S2_Pt"); KADDR(a[4], "_Z5k_endILi0EEvv");
        uint64_t lo = a[0], hi = a[0];
#pragma unroll
        for (int i = 1; i < 5; ++i) { lo = a[i] < lo ? a[i] : lo; hi = a[i] > hi ? a[i] : hi; }
        const uint64_t pa = lo + (uint64_t)(slot * 64 + lane) * 128;
        if (pa < hi) code_touch = *(const __attribute__((address_space(1))) unsigned*)pa;
        SBAR();
    }
    f32x4 acc[4][5], acck[2];
#pragma unroll
    for (int i = 0; i < 4; ++i)
#pragma unroll
        for (int j = 0; j < 5; ++j) acc[i][j] = f32x4{0.f, 0.f, 0.f, 0.f};
    acck[0] = f32x4{0.f, 0.f, 0.f, 0.f}; acck[1] = acck[0];
    f32x4 csq[5], cs0, cs1, stq[4], stk[4];
    float sm[4] = {0.f, 0.f, 0.f, 0.f}, sq[4] = {0.f, 0.f, 0.f, 0.f};
    float cs[5] = {0.f, 0.f, 0.f, 0.f, 0.f}, csk[2] = {0.f, 0.f};
    if (FAST) {
        const bf16x8* xb = (const bf16x8*)x + (size_t)(tb * 4 * 20 + 5 * w) * 64 + lane;
        const bf16x8* wb = (const bf16x8*)(ws + OFF_WQF) + (size_t)(5 * cg * 20 + 5 * w) * 64 + lane;
        const bf16x8* kb = (const bf16x8*)(ws + OFF_WQF) + (size_t)((40 + 2 * isv) * 20 + 5 * w) * 64 + lane;
        bf16x8 xf[5][4], xk[5], wf[5][5], wkf[5][2];
#define QKV_LD(ks) do { _Pragma("unroll") for (int i = 0; i < 4; ++i) xf[ks][i] = xb[(((i + w) & 3) * 20 + (ks)) * 64]; \
        _Pragma("unroll") for (int nt = 0; nt < 5; ++nt) wf[ks][nt] = wb[(nt * 20 + (ks)) * 64]; \
        xk[ks] = xb[(mt0 * 20 + (ks)) * 64]; wkf[ks][0] = kb[(ks) * 64]; wkf[ks][1] = kb[(20 + (ks)) * 64]; SBAR(); } while (0)
        QKV_LD(0); QKV_LD(1); QKV_LD(2); QKV_LD(3);
#pragma unroll
        for (int ks = 0; ks < 5; ++ks) {
#pragma unroll
            for (int nt = 0; nt < 5; ++nt)
#pragma unroll
                for (int i = 0; i < 4; ++i) acc[i][nt] = MFMA16_32(wf[ks][nt], xf[ks][i], acc[i][nt]);
            acck[0] = MFMA16_32(wkf[ks][0], xk[ks], acck[0]);
            acck[1] = MFMA16_32(wkf[ks][1], xk[ks], acck[1]);
            SBAR();
            if (ks == 0) {
                QKV_LD(4);
                const float* csp = (const float*)(ws + OFF_CS);
#pragma unroll
                for (int nt = 0; nt < 5; ++nt) csq[nt] = *(const f32x4*)(csp + 80 * cg + 16 * nt + 4 * g);
                cs0 = *(const f32x4*)(csp + 640 + 32 * isv + 4 * g); cs1 = *(const f32x4*)(csp + 640 + 32 * isv + 16);
                const f32x4* sp = (const f32x4*)(ws + OFF_XS) + (size_t)(tb * 64 + n16) * 4;
#pragma unroll
                for (int i = 0; i < 4; ++i) { stq[i] = sp[16 * w * 4 + i]; stk[i] = sp[16 * mt0 * 4 + i]; }
                SBAR();
            }
        }
    } else {
        lds_ptr ra = lds + w * 40960, rb = ra + 20480;
        const char* xs = (const char*)(x + (size_t)(tb * 64) * 640 + 160 * w);
#pragma unroll
        for (int j = 0; j < 5; ++j) {
            const int c = 64 * j + lane;
            const unsigned xo = (unsigned)(c / 20) * 1280u + (unsigned)(c % 20) * 16u;
#pragma unroll
            for (int i = 0; i < 4; ++i) glds16(xs + (size_t)i * 20480 + xo, ra + i * 5120 + j * 1024);
        }
        const char* src = (const char*)(wq + (size_t)(160 * w) * 640 + 80 * cg);
        const unsigned loff = (unsigned)(lane / 10) * 1280u + (unsigned)(lane % 10) * 16u;
        const char* src2 = (const char*)((isv ? wv : wk) + (size_t)(160 * w) * 20);
#pragma unroll
        for (int i = 0; i < 16; ++i) glds16(src + (size_t)i * 7680 + loff, rb + i * 960);
#pragma unroll
        for (int i = 0; i < 3; ++i) glds16(src2 + i * 1024 + lane * 16, rb + 15488 + i * 1024);
        if (lane < 48) glds16(src2 + 3 * 1024 + lane * 16, rb + 15488 + 3 * 1024);
        SST(1);
        VMCNT(20);
        bf16x8 xf[4][5], xk[5];
        {
            const char* xl = L + w * 40960 + n16 * 320 + g * 16;
#pragma unroll
            for (int i = 0; i < 4; ++i)
#pragma unroll
                for (int ks = 0; ks < 5; ++ks) xf[i][ks] = *(const bf16x8*)(xl + ((i + w) & 3) * 5120 + ks * 64);
#pragma unroll
            for (int ks = 0; ks < 5; ++ks) xk[ks] = *(const bf16x8*)(xl + mt0 * 5120 + ks * 64);
        }
        asm volatile("s_waitcnt lgkmcnt(0)" ::: "memory");
        SBAR();
#pragma unroll
        for (int i = 0; i < 10; ++i) glds16(src + (size_t)(16 + i) * 7680 + loff, ra + i * 960);
        if (lane < 40) glds16(src + (size_t)26 * 7680 + loff, ra + 10 * 960);
#pragma unroll
        for (int i = 0; i < 2; ++i) glds16(src2 + 3840 + i * 1024 + lane * 16, ra + 10240 + i * 1024);
        if (lane < 32) glds16(src2 + 3840 + 2 * 1024 + lane * 16, ra + 10240 + 2 * 1024);
        SST(2);
        const unsigned ONES = 0x3f803f80u;
#pragma unroll
        for (int i = 0; i < 4; ++i)
#pragma unroll
            for (int ks = 0; ks < 5; ++ks) {
                u32x4 u = __builtin_bit_cast(u32x4, xf[i][ks]);
#pragma unroll
                for (int e = 0; e < 4; ++e) { sm[i] = dot2(u[e], ONES, sm[i]); sq[i] = dot2(u[e], u[e], sq[i]); }
            }
        SBAR();
        VMCNT(14);
        SST(3);
        const int q4 = (lane & 15) >> 2, p4 = lane & 3;
        lds_cptr wb0 = (lds_cptr)rb + (8 * g + q4) * 160 + p4 * 8, wb1 = (lds_cptr)ra + (8 * g + q4) * 160 + p4 * 8 - 3 * 5120;
        lds_cptr kb0 = (lds_cptr)rb + 15488 + (8 * g + q4) * 40 + p4 * 8, kb1 = (lds_cptr)ra + 10240 + (8 * g + q4) * 40 + p4 * 8 - 3 * 1280;
        bf16x8 wf[2][7];
#define QKV_TR(ks, s_) do { lds_cptr wb = (ks) < 3 ? wb0 : wb1, kb = (ks) < 3 ? kb0 : kb1; \
        _Pragma("unroll") for (int nt = 0; nt < 5; ++nt) wf[s_][nt] = cat8(tr_read(wb + (ks) * 5120 + nt * 32), tr_read(wb + (ks) * 5120 + 640 + nt * 32)); \
        _Pragma("unroll") for (int nt = 0; nt < 2; ++nt) wf[s_][5 + nt] = cat8(tr_read(kb + (ks) * 1280 + nt * 32), tr_read(kb + (ks) * 1280 + 160 + nt * 32)); SBAR(); } while (0)
        QKV_TR(0, 0);
#pragma unroll
        for (int ks = 0; ks < 5; ++ks) {
            if (ks == 2) { VMCNT(0); SBAR(); }
            if (ks < 4) QKV_TR(ks + 1, (ks + 1) & 1);
#pragma unroll
            for (int nt = 0; nt < 5; ++nt) {
                u32x4 u = __builtin_bit_cast(u32x4, wf[ks & 1][nt]);
#pragma unroll
                for (int e = 0; e < 4; ++e) cs[nt] = dot2(u[e], ONES, cs[nt]);
#pragma unroll
                for (int i = 0; i < 4; ++i) acc[i][nt] = MFMA16_32(wf[ks & 1][nt], xf[i][ks], acc[i][nt]);
            }
#pragma unroll
            for (int nt = 0; nt < 2; ++nt) {
                u32x4 u = __builtin_bit_cast(u32x4, wf[ks & 1][5 + nt]);
#pragma unroll
                for (int e = 0; e < 4; ++e) csk[nt] = dot2(u[e], ONES, csk[nt]);
                acck[nt] = MFMA16_32(wf[ks & 1][5 + nt], xk[ks], acck[nt]);
            }
            SBAR();
        }
#pragma unroll
        for (int i = 0; i < 4; ++i) { sm[i] = add32(add16(sm[i])); sq[i] = add32(add16(sq[i])); }
#pragma unroll
        for (int i = 0; i < 5; ++i) cs[i] = add32(add16(cs[i]));
#pragma unroll
        for (int i = 0; i < 2; ++i) csk[i] = add32(add16(csk[i]));
    }
    SST(4);
    if (!FAST) RAW_BARRIER();
    qkv_finish<FAST>(ws, L, tb, cg, w, lane, acc, acck, csq, cs0, cs1, stq, stk, sm, sq, cs, csk);
    if (!FAST && w == 1) {
        if (b < 80) {
            const int ks = b >> 2, ct = b & 3; int cn = 16 * (ct & 1) + n16; if (cn >= 20) cn = 0;
            *(u32x4*)(ws + OFF_WQF + (size_t)((40 + ct) * 20 + ks) * 1024 + lane * 16) =
                gather8(((ct & 2) ? wv : wk) + (size_t)(32 * ks + 8 * g) * 20 + cn, 20);
        } else if (b == 80 && lane < 4) {
            const unsigned v = (lane & 2) ? 0x3f803f80u : 0u;
            *(u32x4*)(ws + OFF_CONST + lane * 16) = u32x4{v, v, v, v};
        }
    }
    PIN(code_touch);
    SST(7); SST_FLUSHK(ws, FAST ? 5 : 0);
}

constexpr int PBW = 2688, ATT_LDS = 32 * PBW;
template <bool PREP>
__global__ __launch_bounds__(512, 2) void k_attn(char* __restrict__ ws, const u16* __restrict__ w1, const u16* __restrict__ w2,
                                                 const u16* __restrict__ wo, const u16* __restrict__ wq) {
    extern __shared__ __attribute__((aligned(16))) char lds_raw[];
    char* L = lds_raw;
    const int tid = threadIdx.x, lane = tid & 63, w = __builtin_amdgcn_readfirstlane(tid >> 6);
    const int b = blockIdx.x, slot = b >> 3, tb = ((b & 7) >> 1) * 8 + (slot >> 2), cg = (b & 1) * 4 + (slot & 3);
    const int r32 = lane & 31, h = lane >> 5, n16 = lane & 15, g = lane >> 4;
    const int cgp = w & 1, kq = w >> 1;
    SST_DECL; SST(0);
    float kn0 = *(const float*)(ws + OFF_KN + lane * 4), kn1 = *(const float*)(ws + OFF_KN + 256 + lane * 4);
    float qmax = *(const float*)(ws + OFF_QN + ((tb * 8 + cg) * 4 + (lane & 3)) * 4);
    bf16x8 q1[4]; bf16x4 q2[4];
    {
        const char* qg = ws + OFF_Q + (size_t)(tb * 8 + cg) * 12288 + cgp * 6144 + r32 * 48;
#pragma unroll
        for (int j = 0; j < 4; ++j) { q1[j] = *(const bf16x8*)(qg + j * 1536 + 16 * h); q2[j] = *(const bf16x4*)(qg + j * 1536 + 32 + 8 * h); }
    }
    SBAR();
    const int kr = (r32 & 19) | ((r32 & 4) << 1) | ((r32 & 8) >> 1);
    const unsigned ko1 = (unsigned)OFF_K1 + (unsigned)(512 * kq + kr) * 32u + 16u * h;
    const unsigned ko2 = h ? (unsigned)OFF_CONST + 32u : (unsigned)OFF_K2 + (unsigned)(512 * kq + kr) * 8u;
    const unsigned ks2 = h ? 0u : 256u;
    const unsigned vo = r32 < 20 ? (unsigned)OFF_V + (unsigned)(32 * kq) * 640u + (unsigned)(h * 20 + r32) * 16u
                                 : (unsigned)OFF_CONST + (r32 == 20 ? 32u : 0u);
    const unsigned vs = r32 < 20 ? 640u : 0u;
    bf16x8 k1[3], v0[3], v1[3]; bf16x4 k2[3];
#define K_LOAD(t) do { k1[(t) % 3] = *(const bf16x8*)(ws + ko1 + (t) * 1024); k2[(t) % 3] = *(const bf16x4*)(ws + (ko2 + (t) * ks2)); } while (0)
#define V_LOAD(t) do { v0[(t) % 3] = *(const bf16x8*)(ws + (vo + (2 * (t)) * vs)); v1[(t) % 3] = *(const bf16x8*)(ws + (vo + (2 * (t) + 1) * vs)); } while (0)
    K_LOAD(0); V_LOAD(0); SBAR(); K_LOAD(1); V_LOAD(1); SBAR();
    PIN(kn0); PIN(kn1);
    const float kmax2 = wavemax(fmaxf(kn0, kn1));
    qmax = rowmax16(qmax);
    const float kb = __builtin_amdgcn_sqrtf(kmax2);
    const bool safe = __builtin_amdgcn_readfirstlane((int)(qmax * kb <= SAFE_BOUND)) != 0;
    const f32x16 zero16 = {0.f, 0.f, 0.f, 0.f, 0.f, 0.f, 0.f, 0.f, 0.f, 0.f, 0.f, 0.f, 0.f, 0.f, 0.f, 0.f};
    if (safe) {
        if (h) {
#pragma unroll
            for (int j = 0; j < 4; ++j) q2[j][0] = (short)pk2(-bf2f((u16)q2[j][0]) * kb, 0.f);
        }
    } else {
        float m[4];
#pragma unroll
        for (int j = 0; j < 4; ++j) { m[j] = -INFINITY; if (h) q2[j] = bf16x4{0, 0, 0, 0}; }
        for (int t = 0; t < 16; ++t) {
            const bf16x8 ka = *(const bf16x8*)(ws + ko1 + t * 1024);
            const bf16x4 kc = *(const bf16x4*)(ws + (ko2 + t * ks2));
#pragma unroll
            for (int j = 0; j < 4; ++j) {
                f32x16 s = MFMA32_16(ka, q1[j], zero16);
                SBAR(); asm volatile("s_nop 15" ::: "memory"); SBAR();
                s = MFMA32_8(kc, q2[j], s);
#pragma unroll
                for (int e = 0; e < 16; ++e) m[j] = fmaxf(m[j], s[e]);
                SBAR();
            }
        }
#pragma unroll
        for (int j = 0; j < 4; ++j) {
            const float mm = fmaxf(m[j], xor32(m[j]));
            if (h == 0) *(float*)(L + ((w * 4 + j) * 32 + r32) * 4) = mm;
        }
        RAW_BARRIER();
#pragma unroll
        for (int j = 0; j < 4; ++j) {
            float mm = -INFINITY;
#pragma unroll
            for (int qq = 0; qq < 4; ++qq) mm = fmaxf(mm, *(const float*)(L + (((2 * qq + cgp) * 4 + j) * 32 + r32) * 4));
            const unsigned hi = __float_as_uint(mm) & 0xffff0000u;
            const float r1 = mm - __uint_as_float(hi);
            const unsigned mid = __float_as_uint(r1) & 0xffff0000u;
            const float r2 = r1 - __uint_as_float(mid);
            const unsigned lo = __float_as_uint(r2) & 0xffff0000u;
            if (h) q2[j] = __builtin_bit_cast(bf16x4, u32x2{hi ^ 0x80000000u, ((mid ^ 0x80000000u) >> 16) | (lo ^ 0x80000000u)});
        }
        RAW_BARRIER();
    }
    SST(1);
    f32x16 o[4], sa, sb;
#pragma unroll
    for (int j = 0; j < 4; ++j) o[j] = zero16;
    u32x4 p1prev = {0u, 0u, 0u, 0u};
    f32x4 ea, eb;
    sa = MFMA32_16(k1[0], q1[0], zero16);
    SBAR(); asm volatile("s_nop 15" ::: "memory"); SBAR();
    sa = MFMA32_8(k2[0], q2[0], sa);
    SBAR(); asm volatile("s_nop 15" ::: "memory"); SBAR();
    u32x4 prep[4]; char* prep_dst = nullptr; int prep_st = 0;
#pragma unroll
    for (int t = 0; t < 16; ++t) {
#pragma unroll
        for (int j = 0; j < 4; ++j) {
            const int it = 4 * t + j;
            f32x16& cur = (it & 1) ? sb : sa; f32x16& nxt = (it & 1) ? sa : sb;
            const int tn = j < 3 ? t : t + 1, jn = (j + 1) & 3;
            const int tp = j > 0 ? t : t - 1, jp = (j + 3) & 3;
            u32x4 p0, p1;
            if (j == 0 && t + 2 < 16) K_LOAD(t + 2);
            if (j == 1 && t + 2 < 16) V_LOAD(t + 2);
            if (PREP && j == 2 && (t == 1 || t == 8)) {
                int id = (t >> 3) * 2048 + b * 8 + w; if (id >= NPREP) id -= 2048;
                const u16* src; int ld;
                if (id < 1728) { const int ks = id / 27, c4 = id - ks * 27; src = w1 + (size_t)(32 * ks) * F_ + 64 * c4; ld = F_;
                                 prep_dst = ws + OFF_W1F + (size_t)(4 * c4 * 64 + ks) * 1024; prep_st = 64 * 1024; }
                else if (id < 3456) { const int jj = id - 1728, ks = jj >> 5, c4 = jj & 31; src = w2 + (size_t)(32 * ks) * S_ + 64 * c4; ld = S_;
                                      prep_dst = ws + OFF_W2F + (size_t)(4 * c4 * 54 + ks) * 1024; prep_st = 54 * 1024; }
                else { int jj = id - 3456; const bool isq = jj >= 200; if (isq) jj -= 200; const int ks = jj / 10, c4 = jj - ks * 10;
                       src = (isq ? wq : wo) + (size_t)(32 * ks) * 640 + 64 * c4; ld = 640;
                       prep_dst = ws + (isq ? OFF_WQF : OFF_WOF) + (size_t)(4 * c4 * 20 + ks) * 1024; prep_st = 20 * 1024; }
                src += (size_t)(lane >> 3) * ld + (lane & 7) * 8;
#pragma unroll
                for (int i = 0; i < 4; ++i) prep[i] = *(const u32x4*)(src + (size_t)(8 * i) * ld);
            }
            if (PREP && j == 2 && (t == 6 || t == 13)) {
#pragma unroll
                for (int i = 0; i < 4; ++i) *(u32x4*)(L + w * 4096 + i * 1024 + lane * 16) = prep[i];
            }
            if (PREP && j == 3 && (t == 6 || t == 13)) {
                lds_cptr tp_ = (lds_cptr)(lds_ptr)L + w * 4096 + (8 * g + (n16 >> 2)) * 128 + (lane & 3) * 8;
#pragma unroll
                for (int i = 0; i < 4; ++i) prep[i] = __builtin_bit_cast(u32x4, cat8(tr_read(tp_ + i * 32), tr_read(tp_ + 512 + i * 32)));
            }
            if (PREP && j == 0 && (t == 7 || t == 14)) {
#pragma unroll
                for (int i = 0; i < 4; ++i) PREP_STORE((u32x4*)(prep_dst + (size_t)i * prep_st + lane * 16), prep[i]);
            }
            SBAR();
            if (it < 63) nxt = MFMA32_16(k1[tn % 3], q1[jn], zero16);
            SBAR();
            p0[0] = pk2(EX2(cur[0]), EX2(cur[1])); p0[1] = pk2(EX2(cur[2]), EX2(cur[3]));
            PIN(p0); SBAR();
            if (it > 0) o[jp] = MFMA32_16(v1[(tp + 3) % 3], __builtin_bit_cast(bf16x8, p1prev), o[jp]);
            SBAR();
            p0[2] = pk2(EX2(cur[4]), EX2(cur[5])); p0[3] = pk2(EX2(cur[6]), EX2(cur[7]));
            PIN(p0); SBAR();
            if (it < 63) nxt = MFMA32_8(k2[tn % 3], q2[jn], nxt);
            SBAR();
            p1[0] = pk2(EX2(cur[8]), EX2(cur[9])); p1[1] = pk2(EX2(cur[10]), EX2(cur[11]));
            PIN(p1); SBAR();
            o[j] = MFMA32_16(v0[t % 3], __builtin_bit_cast(bf16x8, p0), o[j]);
            SBAR();
            p1[2] = pk2(EX2(cur[12]), EX2(cur[13])); p1[3] = pk2(EX2(cur[14]), EX2(cur[15]));
            PIN(p1); SBAR();
            p1prev = p1;
        }
        if (t == 7) SST(2);
    }
    o[3] = MFMA32_16(v1[15 % 3], __builtin_bit_cast(bf16x8, p1prev), o[3]);
    SST(3);
    if (PREP) RAW_BARRIER();
#pragma unroll
    for (int j = 0; j < 4; ++j) {
        char* pb = L + (kq * 8 + cgp * 4 + j) * PBW;
        *(f32x4*)(pb + lane * 16) = f32x4{o[j][0], o[j][1], o[j][2], o[j][3]};
        *(f32x4*)(pb + 1024 + lane * 16) = f32x4{o[j][4], o[j][5], o[j][6], o[j][7]};
        if (h == 0) *(f32x4*)(pb + 2048 + r32 * 16) = f32x4{o[j][8], o[j][9], o[j][10], o[j][11]};
        else *(float*)(pb + 2560 + r32 * 4) = o[j][8];
    }
    RAW_BARRIER();
    SST(4);
    {
        const char* pb = L + w * PBW;
        f32x4 a0 = {0.f, 0.f, 0.f, 0.f}, a1 = a0, a2 = a0; float l = 0.f;
#pragma unroll
        for (int qq = 0; qq < 4; ++qq) {
            a0 += *(const f32x4*)(pb + qq * 8 * PBW + lane * 16);
            a1 += *(const f32x4*)(pb + qq * 8 * PBW + 1024 + lane * 16);
            a2 += *(const f32x4*)(pb + qq * 8 * PBW + 2048 + r32 * 16);
            l += *(const float*)(pb + qq * 8 * PBW + 2560 + r32 * 4);
        }
        const float inv = __builtin_amdgcn_rcpf(l);
        const int tl = 32 * (w & 1) + r32, hd0 = 80 * cg + 20 * (w >> 1);
        char* ob = ws + OFF_O + (size_t)(tb * 4 + (tl >> 4)) * 20480 + (tl & 15) * 16;
        auto st = [&](int hd, const f32x4& a) {
            const u32x2 v_ = u32x2{pk2(a[0] * inv, a[1] * inv), pk2(a[2] * inv, a[3] * inv)};
            ST8(ob + (hd >> 5) * 1024 + ((hd & 31) >> 3) * 256 + (hd & 7) * 2, v_);
        };
        st(hd0 + 4 * h, a0); st(hd0 + 8 + 4 * h, a1);
        if (h == 0) st(hd0 + 16, a2);
    }
    if (b == 0 && tid < 48) *(unsigned*)(ws + OFF_CNT + tid * 256) = 0u;
    SST(5); SST_FLUSHK(ws, PREP ? 1 : 6);
}

template <bool XF>
__global__ __launch_bounds__(256, 1) void k_wo(char* __restrict__ ws, const u16* __restrict__ xin) {
    __shared__ __attribute__((aligned(16))) char L[61440];
    const int tid = threadIdx.x, lane = tid & 63, w = __builtin_amdgcn_readfirstlane(tid >> 6);
    const int b = blockIdx.x, slot = b >> 3, tb = ((b & 7) >> 1) * 8 + (slot >> 2), cg = (b & 1) * 4 + (slot & 3);
    const int n16 = lane & 15, g = lane >> 4;
    SST_DECL; SST(0);
    f32x4 acc[4][5];
#pragma unroll
    for (int i = 0; i < 4; ++i)
#pragma unroll
        for (int j = 0; j < 5; ++j) acc[i][j] = f32x4{0.f, 0.f, 0.f, 0.f};
    const int tok = tb * 64 + 16 * w + n16;
    u32x2 xi[5];
    {
        const bf16x8* xb = (const bf16x8*)(ws + OFF_O) + (size_t)(tb * 4 * 20 + 5 * w) * 64 + lane;
        const bf16x8* wb = (const bf16x8*)(ws + OFF_WOF) + (size_t)(5 * cg * 20 + 5 * w) * 64 + lane;
        bf16x8 xf[5][4], wf[5][5];
#define WO_LD(ks) do { _Pragma("unroll") for (int i = 0; i < 4; ++i) xf[ks][i] = xb[(((i + w) & 3) * 20 + (ks)) * 64]; \
        _Pragma("unroll") for (int nt = 0; nt < 5; ++nt) wf[ks][nt] = wb[(nt * 20 + (ks)) * 64]; SBAR(); } while (0)
        WO_LD(0); WO_LD(1); WO_LD(2); WO_LD(3);
#pragma unroll
        for (int ks = 0; ks < 5; ++ks) {
#pragma unroll
            for (int nt = 0; nt < 5; ++nt)
#pragma unroll
                for (int i = 0; i < 4; ++i) acc[i][nt] = MFMA16_32(wf[ks][nt], xf[ks][i], acc[i][nt]);
            SBAR();
            if (ks == 0) {
                WO_LD(4);
#pragma unroll
                for (int nt = 0; nt < 5; ++nt) {
                    const int d = 80 * cg + 16 * nt + 4 * g;
                    if (XF) xi[nt] = *(const u32x2*)(xin + ((size_t)((tok >> 4) * 20 + (d >> 5)) * 64 + ((d & 31) >> 3) * 16 + n16) * 8 + (d & 7));
                    else xi[nt] = *(const u32x2*)(xin + (size_t)tok * 640 + d);
                }
                SBAR();
            }
        }
    }
    SST(1);
#pragma unroll
    for (int j = 1; j < 4; ++j) {
        const int o = (w + j) & 3;
#pragma unroll
        for (int nt = 0; nt < 5; ++nt) *(f32x4*)(L + (((o * 3 + (j - 1)) * 5 + nt) * 64 + lane) * 16) = acc[j][nt];
    }
    RAW_BARRIER();
    SST(2);
    float xv[20]; u32x2 x1o[5];
    {
        f32x4 rr[3][5];
#pragma unroll
        for (int j = 0; j < 3; ++j)
#pragma unroll
            for (int nt = 0; nt < 5; ++nt) rr[j][nt] = *(const f32x4*)(L + (((w * 3 + j) * 5 + nt) * 64 + lane) * 16);
        SBAR();
        float s = 0.f;
#pragma unroll
        for (int nt = 0; nt < 5; ++nt) {
            const f32x4 a = (acc[0][nt] + rr[0][nt]) + (rr[1][nt] + rr[2][nt]);
            const unsigned u0 = pk2(bf_lo(xi[nt][0]) + a[0], bf_hi(xi[nt][0]) + a[1]);
            const unsigned u1 = pk2(bf_lo(xi[nt][1]) + a[2], bf_hi(xi[nt][1]) + a[3]);
            x1o[nt] = u32x2{u0, u1};
            xv[4 * nt] = bf_lo(u0); xv[4 * nt + 1] = bf_hi(u0); xv[4 * nt + 2] = bf_lo(u1); xv[4 * nt + 3] = bf_hi(u1);
            s += (xv[4 * nt] + xv[4 * nt + 1]) + (xv[4 * nt + 2] + xv[4 * nt + 3]);
        }
        const float mw = add32(add16(s)) * (1.f / 80.f);
        float s2 = 0.f;
#pragma unroll
        for (int i = 0; i < 20; ++i) { const float dd = xv[i] - mw; s2 += dd * dd; }
        s2 = add32(add16(s2));
        gu64* gp = (gu64*)(ws + OFF_XG) + (size_t)tok * 8 + cg;
        if (g == 0) __hip_atomic_store(gp, ((unsigned long long)(__float_as_uint(s2) | 0x80000000u) << 32) | __float_as_uint(mw),
                                       __ATOMIC_RELAXED, __HIP_MEMORY_SCOPE_AGENT);
    }
    SST(3);
    float mean, var;
    {
        gu64* gp = (gu64*)(ws + OFF_XG) + (size_t)tok * 8 + 2 * g;
        unsigned long long v0, v1;
        for (int spin = 0; spin < (1 << 20); ++spin) {
            v0 = __hip_atomic_load(gp, __ATOMIC_RELAXED, __HIP_MEMORY_SCOPE_AGENT);
            v1 = __hip_atomic_load(gp + 1, __ATOMIC_RELAXED, __HIP_MEMORY_SCOPE_AGENT);
            if (__all((long long)(v0 & v1) < 0)) break;
            __builtin_amdgcn_s_sleep(4);
        }
        const float m0 = __uint_as_float((unsigned)v0), m1 = __uint_as_float((unsigned)v1);
        mean = add32(add16(m0 + m1)) * 0.125f;
        const float d0 = m0 - mean, d1 = m1 - mean;
        var = add32(add16(__uint_as_float((unsigned)(v0 >> 32) & 0x7fffffffu) + __uint_as_float((unsigned)(v1 >> 32) & 0x7fffffffu) +
                          80.f * (d0 * d0 + d1 * d1))) * (1.f / 640.f);
    }
    SST(4);
    {
        const float r1 = __builtin_amdgcn_rsqf(var + LN_EPS);
        const float r2 = r1 * __builtin_amdgcn_rsqf(var * r1 * r1 + LN_EPS);
        char* hb = L + w * 15360;
#pragma unroll
        for (int nt = 0; nt < 5; ++nt)
            *(u32x2*)(hb + n16 * 176 + (16 * nt + 4 * g) * 2) =
                u32x2{pk2((xv[4 * nt] - mean) * r2, (xv[4 * nt + 1] - mean) * r2), pk2((xv[4 * nt + 2] - mean) * r2, (xv[4 * nt + 3] - mean) * r2)};
        asm volatile("s_waitcnt lgkmcnt(0)" ::: "memory");
        const int q4 = (lane & 15) >> 2, p4 = lane & 3;
        lds_cptr hr = (lds_cptr)(lds_ptr)L + w * 15360 + (8 * (g & 1) + q4) * 176 + p4 * 8 + (g >> 1) * 32;
        const int s0 = tb * 64 + 16 * w + 8 * (g & 1);
        u16* hf = (u16*)(ws + OFF_H) + ((size_t)(5 * cg + (g >> 1)) * 64 + (s0 >> 5)) * 512 + (((s0 & 31) >> 3) * 16 + n16) * 8;
        const bf16x8 f0 = cat8(tr_read(hr), tr_read(hr + 704));
        const bf16x8 f1 = cat8(tr_read(hr + 64), tr_read(hr + 704 + 64));
        const bf16x8 f2 = cat8(tr_read(hr + (g < 2 ? 128 : 0)), tr_read(hr + 704 + (g < 2 ? 128 : 0)));
        *(bf16x8*)(hf) = f0;
        *(bf16x8*)(hf + 2 * 64 * 512) = f1;
        if (g < 2) *(bf16x8*)(hf + 4 * 64 * 512) = f2;
        u16* x1r = (u16*)(ws + OFF_X1) + (size_t)tok * 640 + 80 * cg + 4 * g;
#pragma unroll
        for (int nt = 0; nt < 5; ++nt) *(u32x2*)(x1r + 16 * nt) = x1o[nt];
    }
    SST(5); SST_FLUSHK(ws, XF ? 7 : 2);
}

template <int MODE, int NS, int PF>
DI void gemm_body(const bf16x8* __restrict__ po, const bf16x8* __restrict__ pf, const int (&ooff)[4], int fstride, f32x4 (&acc)[4][5]) {
    bf16x8 ro[PF][4], rf[PF][5];
#pragma unroll
    for (int s = 0; s < PF; ++s) {
#pragma unroll
        for (int j = 0; j < 4; ++j) ro[s][j] = po[ooff[j] + s * 64];
#pragma unroll
        for (int i = 0; i < 5; ++i) rf[s][i] = pf[i * fstride + s * 64];
        SBAR();
    }
#pragma unroll
    for (int s = 0; s < NS; ++s) {
        const int sl = s % PF;
        SBAR();
#pragma unroll
        for (int j = 0; j < 4; ++j)
#pragma unroll
            for (int i = 0; i < 5; ++i) {
                if (MODE == 1) acc[j][i] = MFMA16_32(ro[sl][j], rf[sl][i], acc[j][i]);
                else acc[j][i] = MFMA16_32(rf[sl][i], ro[sl][j], acc[j][i]);
            }
        SBAR();
        if (s + PF < NS) {
#pragma unroll
            for (int j = 0; j < 4; ++j) ro[sl][j] = po[ooff[j] + (s + PF) * 64];
#pragma unroll
            for (int i = 0; i < 5; ++i) rf[sl][i] = pf[i * fstride + (s + PF) * 64];
        }
    }
}

template <int MODE>
__global__ __launch_bounds__(256, 1) void k_gemm(char* __restrict__ ws, const u16* __restrict__ bias, u16* __restrict__ out) {
    __shared__ __attribute__((aligned(16))) f32x4 red[4 * 3 * 5 * 64];
    const int tid = threadIdx.x, lane = tid & 63, w = __builtin_amdgcn_readfirstlane(tid >> 6);
    const int b = blockIdx.x, xcd = b & 7, slot = b >> 3;
    const int n16 = lane & 15, g = lane >> 4;
    constexpr int KS = MODE == 1 ? 64 : 54;
    int ot, dt;
    if (MODE == 1) {
        ot = 7 * (xcd >> 1) + (slot >> 2); dt = 4 * (xcd & 1) + (slot & 3);
        if (slot >= 28 || ot >= 27) return;
    } else {
        ot = 8 * (xcd >> 1) + (slot >> 2); dt = 4 * (xcd & 1) + (slot & 3);
    }
    SST_DECL; SST(0);
    u32x2 bb = {0u, 0u}, xi[5]; float bs = 0.f;
    if (MODE == 1) bb = *(const u32x2*)(bias + 64 * ot + 16 * w + 4 * g);
    else {
        const int s = 64 * ot + 16 * w + n16;
        bs = bf2f(bias[s]);
        const u16* x1 = (const u16*)(ws + OFF_X1) + (size_t)s * 640 + 80 * dt + 4 * g;
#pragma unroll
        for (int i = 0; i < 5; ++i) xi[i] = *(const u32x2*)(x1 + 16 * i);
    }
    const int k0 = MODE == 1 ? 16 * w : (w < 2 ? 14 * w : 28 + 13 * (w - 2));
    const bf16x8* po = (const bf16x8*)(ws + (MODE == 1 ? OFF_W1F : OFF_W2F)) + ((size_t)(4 * ot) * KS + k0) * 64 + lane;
    const bf16x8* pf = (const bf16x8*)(ws + (MODE == 1 ? OFF_H : OFF_U)) + ((size_t)(5 * dt) * KS + k0) * 64 + lane;
    int ooff[4];
#pragma unroll
    for (int j = 0; j < 4; ++j) ooff[j] = ((j + w) & 3) * KS * 64;
    f32x4 acc[4][5];
#pragma unroll
    for (int j = 0; j < 4; ++j)
#pragma unroll
        for (int i = 0; i < 5; ++i) acc[j][i] = f32x4{0.f, 0.f, 0.f, 0.f};
    if (MODE == 1) gemm_body<1, 16, 6>(po, pf, ooff, KS * 64, acc);
    else if (w < 2) gemm_body<2, 14, 6>(po, pf, ooff, KS * 64, acc);
    else gemm_body<2, 13, 6>(po, pf, ooff, KS * 64, acc);
    SST(1);
#pragma unroll
    for (int j = 1; j < 4; ++j) {
        const int o = (w + j) & 3;
#pragma unroll
        for (int i = 0; i < 5; ++i) red[((o * 3 + (j - 1)) * 5 + i) * 64 + lane] = acc[j][i];
    }
    RAW_BARRIER();
#pragma unroll
    for (int j = 0; j < 3; ++j)
#pragma unroll
        for (int i = 0; i < 5; ++i) acc[1 + j][i] = red[((w * 3 + j) * 5 + i) * 64 + lane];
    SBAR();
#pragma unroll
    for (int i = 0; i < 5; ++i) acc[0][i] = (acc[0][i] + acc[1][i]) + (acc[2][i] + acc[3][i]);
    SST(2);
    if (MODE == 1) {
        const int f = 64 * ot + 16 * w + 4 * g;
        const float bv[4] = {bf_lo(bb[0]), bf_hi(bb[0]), bf_lo(bb[1]), bf_hi(bb[1])};
        u16* uf = (u16*)(ws + OFF_U);
#pragma unroll
        for (int i = 0; i < 5; ++i) {
            float r[4];
#pragma unroll
            for (int e = 0; e < 4; ++e) {
                const float u = acc[0][i][e] + bv[e];
                const float z = u * (1.f + 0.044715f * u * u) * (-2.f * 0.7978845608028654f * 1.4426950408889634f);
                r[e] = u * __builtin_amdgcn_rcpf(1.f + EX2(z));
            }
            *(u32x2*)(uf + ((size_t)((5 * dt + i) * 54 + (f >> 5)) * 64 + ((f & 31) >> 3) * 16 + n16) * 8 + (f & 7)) =
                u32x2{pk2(r[0], r[1]), pk2(r[2], r[3])};
        }
    } else {
        const int s = 64 * ot + 16 * w + n16;
        float xv[20]; float sum = 0.f;
#pragma unroll
        for (int i = 0; i < 5; ++i) {
            const int d = 80 * dt + 16 * i + 4 * g;
            u16* o = MODE == 3 ? out + (size_t)s * 640 + d
                               : out + ((size_t)((4 * ot + w) * 20 + (d >> 5)) * 64 + ((d & 31) >> 3) * 16 + n16) * 8 + (d & 7);
            const unsigned u0 = pk2(bf_lo(xi[i][0]) + acc[0][i][0] + bs, bf_hi(xi[i][0]) + acc[0][i][1] + bs);
            const unsigned u1 = pk2(bf_lo(xi[i][1]) + acc[0][i][2] + bs, bf_hi(xi[i][1]) + acc[0][i][3] + bs);
            *(u32x2*)o = u32x2{u0, u1};
            xv[4 * i] = bf_lo(u0); xv[4 * i + 1] = bf_hi(u0); xv[4 * i + 2] = bf_lo(u1); xv[4 * i + 3] = bf_hi(u1);
            sum += (xv[4 * i] + xv[4 * i + 1]) + (xv[4 * i + 2] + xv[4 * i + 3]);
        }
        if (MODE == 2) {
            const float mw = add32(add16(sum)) * (1.f / 80.f);
            float s2 = 0.f;
#pragma unroll
            for (int i = 0; i < 20; ++i) { const float dd = xv[i] - mw; s2 += dd * dd; }
            s2 = add32(add16(s2));
            if (g == 0) *(f32x2*)(ws + OFF_XS + ((size_t)s * 8 + dt) * 8) = f32x2{mw, s2};
        }
    }
    SST(3); SST_FLUSHK(ws, MODE == 1 ? (out ? 8 : 3) : (MODE == 2 ? 4 : 9));
}

typedef __attribute__((address_space(1))) unsigned gu32;
constexpr int AUXL = 16;
#define LD_SC1(off) __builtin_bit_cast(bf16x8, __builtin_amdgcn_raw_buffer_load_b128(rs, (int)(off), 0, 16))
DI void edge_publish(gu32* cnt) {
    RAW_BARRIER();
    if (threadIdx.x == 0) __hip_atomic_fetch_add(cnt, 1u, __ATOMIC_RELAXED, __HIP_MEMORY_SCOPE_AGENT);
}
DI void edge_wait(gu32* cnt, unsigned want) {
    if (threadIdx.x == 0) {
        for (int spin = 0; spin < (1 << 20); ++spin) {
            if (__hip_atomic_load(cnt, __ATOMIC_RELAXED, __HIP_MEMORY_SCOPE_AGENT) >= want) break;
            __builtin_amdgcn_s_sleep(1);
        }
        __builtin_amdgcn_fence(__ATOMIC_ACQUIRE, "wavefront");
    }
    RAW_BARRIER();
}
#define W1_A 6
#define W2_FIRST 7
constexpr int MLP_LDS = 73728;
template <bool R1>
__global__ __launch_bounds__(256, 1) void k_mlp(char* __restrict__ ws, const u16* __restrict__ xin, const u16* __restrict__ b1,
                                                const u16* __restrict__ b2, u16* __restrict__ out) {
    extern __shared__ __attribute__((aligned(16))) char lds_raw[];
    char* L = lds_raw;
    const int tid = threadIdx.x, lane = tid & 63, w = __builtin_amdgcn_readfirstlane(tid >> 6);
    const int b = blockIdx.x, slot = b >> 3, tb = ((b & 7) >> 1) * 8 + (slot >> 2), cg = (b & 1) * 4 + (slot & 3);
    const int ot1 = ((b & 7) >> 1) * 7 + (slot >> 2);
    const bool act1 = slot < 28 && ot1 < 27;
    const int n16 = lane & 15, g = lane >> 4;
    const __amdgpu_buffer_rsrc_t rs = __builtin_amdgcn_make_buffer_rsrc((void*)ws, 0, 0x8000000, 0x00020000);
    gu32* cnt = (gu32*)(ws + OFF_CNT);
    SST_DECL; SST(0);
    f32x4 acc[4][5];
#define ACC_ZERO() do { _Pragma("unroll") for (int i_ = 0; i_ < 4; ++i_) _Pragma("unroll") for (int j_ = 0; j_ < 5; ++j_) acc[i_][j_] = f32x4{0.f, 0.f, 0.f, 0.f}; } while (0)
#define ACC_REDUCE() do { \
    _Pragma("unroll") for (int j_ = 1; j_ < 4; ++j_) { const int o_ = (w + j_) & 3; \
        _Pragma("unroll") for (int i_ = 0; i_ < 5; ++i_) *(f32x4*)(L + (((o_ * 3 + (j_ - 1)) * 5 + i_) * 64 + lane) * 16) = acc[j_][i_]; } \
    RAW_BARRIER(); \
    _Pragma("unroll") for (int j_ = 0; j_ < 3; ++j_) _Pragma("unroll") for (int i_ = 0; i_ < 5; ++i_) \
        acc[1 + j_][i_] = *(const f32x4*)(L + (((w * 3 + j_) * 5 + i_) * 64 + lane) * 16); \
    SBAR(); \
    _Pragma("unroll") for (int i_ = 0; i_ < 5; ++i_) acc[0][i_] = (acc[0][i_] + acc[1][i_]) + (acc[2][i_] + acc[3][i_]); } while (0)
    const int tok = tb * 64 + 16 * w + n16;
    bf16x8 wr[16][4];
    int ro1[4], ro2[4];
#pragma unroll
    for (int j = 0; j < 4; ++j) { ro1[j] = ((j + w) & 3) * 64 * 64; ro2[j] = ((j + w) & 3) * 54 * 64; }
    const bf16x8* pw1 = (const bf16x8*)(ws + OFF_W1F) + ((size_t)(4 * (act1 ? ot1 : 0)) * 64 + 16 * w) * 64 + lane;
    const int k2 = w < 2 ? 14 * w : 28 + 13 * (w - 2);
    const bf16x8* pw2 = (const bf16x8*)(ws + OFF_W2F) + ((size_t)(4 * tb) * 54 + k2) * 64 + lane;
#define W1_LD(lo, hi) do { _Pragma("unroll") for (int s_ = (lo); s_ < (hi); ++s_) _Pragma("unroll") for (int j_ = 0; j_ < 4; ++j_) wr[s_][j_] = pw1[ro1[j_] + s_ * 64]; SBAR(); } while (0)
#define W2_LD(lo, hi) do { _Pragma("unroll") for (int s_ = (lo); s_ < (hi); ++s_) _Pragma("unroll") for (int j_ = 0; j_ < 4; ++j_) wr[s_][j_] = pw2[ro2[j_] + s_ * 64]; SBAR(); } while (0)

    u32x2 x1o[5], bb; float bs;
    {
        ACC_ZERO();
        u32x2 xi[5];
        const bf16x8* xb = (const bf16x8*)(ws + OFF_O) + (size_t)(tb * 4 * 20 + 5 * w) * 64 + lane;
        const bf16x8* wb = (const bf16x8*)(ws + OFF_WOF) + (size_t)(5 * cg * 20 + 5 * w) * 64 + lane;
        bf16x8 xf[5][4], wf[5][5];
        WO_LD(0); WO_LD(1); WO_LD(2); WO_LD(3);
#pragma unroll
        for (int ks = 0; ks < 5; ++ks) {
#pragma unroll
            for (int nt = 0; nt < 5; ++nt)
#pragma unroll
                for (int i = 0; i < 4; ++i) acc[i][nt] = MFMA16_32(wf[ks][nt], xf[ks][i], acc[i][nt]);
            SBAR();
            if (ks == 0) {
                WO_LD(4);
#pragma unroll
                for (int nt = 0; nt < 5; ++nt) {
                    const int d = 80 * cg + 16 * nt + 4 * g;
                    if (!R1) xi[nt] = *(const u32x2*)(xin + ((size_t)((tok >> 4) * 20 + (d >> 5)) * 64 + ((d & 31) >> 3) * 16 + n16) * 8 + (d & 7));
                    else xi[nt] = *(const u32x2*)(xin + (size_t)tok * 640 + d);
                }
                bb = *(const u32x2*)(b1 + 64 * (act1 ? ot1 : 0) + 16 * w + 4 * g);
                bs = bf2f(b2[tok]);
                SBAR();
            }
            if (ks == 1) W1_LD(0, 2);
            if (ks == 2) W1_LD(2, 4);
            if (ks == 3) W1_LD(4, 6);
            if (ks == 4) W1_LD(6, W1_A);
        }
        SST(1);
        ACC_REDUCE();
        float xv[20], s = 0.f;
#pragma unroll
        for (int nt = 0; nt < 5; ++nt) {
            const unsigned u0 = pk2(bf_lo(xi[nt][0]) + acc[0][nt][0], bf_hi(xi[nt][0]) + acc[0][nt][1]);
            const unsigned u1 = pk2(bf_lo(xi[nt][1]) + acc[0][nt][2], bf_hi(xi[nt][1]) + acc[0][nt][3]);
            x1o[nt] = u32x2{u0, u1};
            xv[4 * nt] = bf_lo(u0); xv[4 * nt + 1] = bf_hi(u0); xv[4 * nt + 2] = bf_lo(u1); xv[4 * nt + 3] = bf_hi(u1);
            s += (xv[4 * nt] + xv[4 * nt + 1]) + (xv[4 * nt + 2] + xv[4 * nt + 3]);
        }
        const float mw = add32(add16(s)) * (1.f / 80.f);
        float s2 = 0.f;
#pragma unroll
        for (int i = 0; i < 20; ++i) { const float dd = xv[i] - mw; s2 += dd * dd; }
        s2 = add32(add16(s2));
        gu64* gp = (gu64*)(ws + OFF_XG) + (size_t)tok * 8;
        if (g == 0) __hip_atomic_store(gp + cg, ((unsigned long long)(__float_as_uint(s2) | 0x80000000u) << 32) | __float_as_uint(mw),
                                       __ATOMIC_RELAXED, __HIP_MEMORY_SCOPE_AGENT);
        SBAR();
        SST(2);
        unsigned long long v0, v1;
        for (int spin = 0; spin < (1 << 20); ++spin) {
            v0 = __hip_atomic_load(gp + 2 * g, __ATOMIC_RELAXED, __HIP_MEMORY_SCOPE_AGENT);
            v1 = __hip_atomic_load(gp + 2 * g + 1, __ATOMIC_RELAXED, __HIP_MEMORY_SCOPE_AGENT);
            if (__all((long long)(v0 & v1) < 0)) break;
            __builtin_amdgcn_s_sleep(4);
        }
        SST(3);
        const float m0 = __uint_as_float((unsigned)v0), m1 = __uint_as_float((unsigned)v1);
        const float mean = add32(add16(m0 + m1)) * 0.125f;
        const float d0 = m0 - mean, d1 = m1 - mean;
        const float var = add32(add16(__uint_as_float((unsigned)(v0 >> 32) & 0x7fffffffu) + __uint_as_float((unsigned)(v1 >> 32) & 0x7fffffffu) +
                                      80.f * (d0 * d0 + d1 * d1))) * (1.f / 640.f);
        const float r1 = __builtin_amdgcn_rsqf(var + LN_EPS);
        const float r2 = r1 * __builtin_amdgcn_rsqf(var * r1 * r1 + LN_EPS);
        char* hb = L + w * 15360;
#pragma unroll
        for (int nt = 0; nt < 5; ++nt)
            *(u32x2*)(hb + n16 * 176 + (16 * nt + 4 * g) * 2) =
                u32x2{pk2((xv[4 * nt] - mean) * r2, (xv[4 * nt + 1] - mean) * r2), pk2((xv[4 * nt + 2] - mean) * r2, (xv[4 * nt + 3] - mean) * r2)};
        asm volatile("s_waitcnt lgkmcnt(0)" ::: "memory");
        const int q4 = (lane & 15) >> 2, p4 = lane & 3;
        lds_cptr hr = (lds_cptr)(lds_ptr)L + w * 15360 + (8 * (g & 1) + q4) * 176 + p4 * 8 + (g >> 1) * 32;
        const int s0 = tb * 64 + 16 * w + 8 * (g & 1);
        const int hoff = (int)OFF_H + ((5 * cg + (g >> 1)) * 64 + (s0 >> 5)) * 1024 + (((s0 & 31) >> 3) * 16 + n16) * 16;
        const bf16x8 f0 = cat8(tr_read(hr), tr_read(hr + 704));
        const bf16x8 f1 = cat8(tr_read(hr + 64), tr_read(hr + 704 + 64));
        const bf16x8 f2 = cat8(tr_read(hr + (g < 2 ? 128 : 0)), tr_read(hr + 704 + (g < 2 ? 128 : 0)));
        __builtin_amdgcn_raw_buffer_store_b128(__builtin_bit_cast(u32x4, f0), rs, hoff, 0, 16);
        __builtin_amdgcn_raw_buffer_store_b128(__builtin_bit_cast(u32x4, f1), rs, hoff + 2 * 65536, 0, 16);
        if (g < 2) __builtin_amdgcn_raw_buffer_store_b128(__builtin_bit_cast(u32x4, f2), rs, hoff + 4 * 65536, 0, 16);
        SBAR();
        W1_LD(W1_A, W1_A + 4);
        VMCNT(16);
        SST(4);
        edge_publish(cnt + cg * 64);
    }
    if (act1) {
        W1_LD(W1_A + 4, 16);
        edge_wait(cnt + cg * 64, 32);
        SST(5);
        ACC_ZERO();
        const int ho = (int)OFF_H + ((5 * cg) * 64 + 16 * w) * 1024 + lane * 16;
        constexpr int PF = 6;
        bf16x8 rf[PF][5];
#pragma unroll
        for (int s = 0; s < PF; ++s) {
#pragma unroll
            for (int i = 0; i < 5; ++i) rf[s][i] = LD_SC1(ho + i * 65536 + s * 1024);
            SBAR();
        }
#pragma unroll
        for (int s = 0; s < 16; ++s) {
#pragma unroll
            for (int j = 0; j < 4; ++j)
#pragma unroll
                for (int i = 0; i < 5; ++i) acc[j][i] = MFMA16_32(wr[s][j], rf[s % PF][i], acc[j][i]);
            SBAR();
            if (s + PF < 16) {
#pragma unroll
                for (int i = 0; i < 5; ++i) rf[s % PF][i] = LD_SC1(ho + i * 65536 + (s + PF) * 1024);
                SBAR();
            }
            if (s >= W2_FIRST && s - W2_FIRST < 9) W2_LD(s - W2_FIRST, s - W2_FIRST + 1);
        }
        SST(6);
        ACC_REDUCE();
        const int f = 64 * ot1 + 16 * w + 4 * g;
        const float bv[4] = {bf_lo(bb[0]), bf_hi(bb[0]), bf_lo(bb[1]), bf_hi(bb[1])};
        const int uo = (int)OFF_U + ((5 * cg) * 54 + (f >> 5)) * 1024 + (((f & 31) >> 3) * 16 + n16) * 16 + (f & 7) * 2;
#pragma unroll
        for (int i = 0; i < 5; ++i) {
            float r[4];
#pragma unroll
            for (int e = 0; e < 4; ++e) {
                const float u = acc[0][i][e] + bv[e];
                const float z = u * (1.f + 0.044715f * u * u) * (-2.f * 0.7978845608028654f * 1.4426950408889634f);
                r[e] = u * __builtin_amdgcn_rcpf(1.f + EX2(z));
            }
            __builtin_amdgcn_raw_buffer_store_b64(u32x2{pk2(r[0], r[1]), pk2(r[2], r[3])}, rs, uo + i * 54 * 1024, 0, 16);
        }
        SBAR();
        W2_LD(9, 13);
        VMCNT(16);
        SST(7);
        edge_publish(cnt + (8 + cg) * 64);
    } else {
        W2_LD(0, 13);
    }
    {
        if (w < 2) W2_LD(13, 14);
        edge_wait(cnt + (8 + cg) * 64, 27);
        SST(8);
        ACC_ZERO();
        const int uo = (int)OFF_U + ((5 * cg) * 54 + k2) * 1024 + lane * 16;
        constexpr int PF = 6;
        bf16x8 rf[PF][5];
#pragma unroll
        for (int s = 0; s < PF; ++s) {
#pragma unroll
            for (int i = 0; i < 5; ++i) rf[s][i] = LD_SC1(uo + i * 54 * 1024 + s * 1024);
            SBAR();
        }
#pragma unroll
        for (int s = 0; s < 14; ++s) {
            if (s < 13 || w < 2) {
#pragma unroll
                for (int j = 0; j < 4; ++j)
#pragma unroll
                    for (int i = 0; i < 5; ++i) acc[j][i] = MFMA16_32(rf[s % PF][i], wr[s][j], acc[j][i]);
            }
            SBAR();
            if (s + PF < 13 || (s + PF == 13 && w < 2)) {
#pragma unroll
                for (int i = 0; i < 5; ++i) rf[s % PF][i] = LD_SC1(uo + i * 54 * 1024 + (s + PF) * 1024);
                SBAR();
            }
        }
        SST(9);
        ACC_REDUCE();
        float xv[20], sum = 0.f;
#pragma unroll
        for (int i = 0; i < 5; ++i) {
            const int d = 80 * cg + 16 * i + 4 * g;
            const unsigned u0 = pk2(bf_lo(x1o[i][0]) + acc[0][i][0] + bs, bf_hi(x1o[i][0]) + acc[0][i][1] + bs);
            const unsigned u1 = pk2(bf_lo(x1o[i][1]) + acc[0][i][2] + bs, bf_hi(x1o[i][1]) + acc[0][i][3] + bs);
            if (R1) __builtin_amdgcn_raw_buffer_store_b64(u32x2{u0, u1}, rs,
                        (int)OFF_X2 + ((4 * tb + w) * 20 + (d >> 5)) * 1024 + (((d & 31) >> 3) * 16 + n16) * 16 + (d & 7) * 2, 0, 16);
            else *(u32x2*)(out + (size_t)tok * 640 + d) = u32x2{u0, u1};
            xv[4 * i] = bf_lo(u0); xv[4 * i + 1] = bf_hi(u0); xv[4 * i + 2] = bf_lo(u1); xv[4 * i + 3] = bf_hi(u1);
            sum += (xv[4 * i] + xv[4 * i + 1]) + (xv[4 * i + 2] + xv[4 * i + 3]);
        }
        if (R1) {
            const float mw = add32(add16(sum)) * (1.f / 80.f);
            float s2 = 0.f;
#pragma unroll
            for (int i = 0; i < 20; ++i) { const float dd = xv[i] - mw; s2 += dd * dd; }
            s2 = add32(add16(s2));
            if (g == 0) __builtin_amdgcn_raw_buffer_store_b64(u32x2{__float_as_uint(mw), __float_as_uint(s2)}, rs, (int)OFF_XS + ((tb * 8 + cg) * 64 + 16 * w + n16) * 8, 0, 16);
        }
    }
    if (R1) {
        const int mt0 = cg >> 1, isv = cg & 1;
        const bf16x8* wb = (const bf16x8*)(ws + OFF_WQF) + (size_t)(5 * cg * 20 + 5 * w) * 64 + lane;
        const bf16x8* kb = (const bf16x8*)(ws + OFF_WQF) + (size_t)((40 + 2 * isv) * 20 + 5 * w) * 64 + lane;
        bf16x8 wf[5][5], wkf[5][2], xf[5][4], xk[5];
        SBAR();
#pragma unroll
        for (int i = 0; i < 16; ++i) wf[i / 5][i % 5] = wb[((i % 5) * 20 + i / 5) * 64];
        SBAR();
        VMCNT(16);
        SST(10);
        edge_publish(cnt + (16 + tb) * 64);
#pragma unroll
        for (int i = 16; i < 25; ++i) wf[i / 5][i % 5] = wb[((i % 5) * 20 + i / 5) * 64];
#pragma unroll
        for (int ks = 0; ks < 5; ++ks) { wkf[ks][0] = kb[ks * 64]; wkf[ks][1] = kb[(20 + ks) * 64]; }
        f32x4 csq[5], cs0, cs1, stq[4], stk[4], acck[2];
        {
            const float* csp = (const float*)(ws + OFF_CS);
#pragma unroll
            for (int nt = 0; nt < 5; ++nt) csq[nt] = *(const f32x4*)(csp + 80 * cg + 16 * nt + 4 * g);
            cs0 = *(const f32x4*)(csp + 640 + 32 * isv + 4 * g); cs1 = *(const f32x4*)(csp + 640 + 32 * isv + 16);
        }
        SBAR();
        edge_wait(cnt + (16 + tb) * 64, 8);
        SST(11);
        const int xo = (int)OFF_X2 + (tb * 4 * 20 + 5 * w) * 1024 + lane * 16;
#pragma unroll
        for (int ks = 0; ks < 5; ++ks) {
#pragma unroll
            for (int i = 0; i < 4; ++i) xf[ks][i] = LD_SC1(xo + (((i + w) & 3) * 20 + ks) * 1024);
            xk[ks] = LD_SC1(xo + (mt0 * 20 + ks) * 1024);
            SBAR();
        }
#pragma unroll
        for (int i = 0; i < 4; ++i) {
            const u32x2 a0 = __builtin_amdgcn_raw_buffer_load_b64(rs, (int)OFF_XS + ((tb * 8 + 2 * i) * 64 + 16 * w + n16) * 8, 0, AUXL);
            const u32x2 a1 = __builtin_amdgcn_raw_buffer_load_b64(rs, (int)OFF_XS + ((tb * 8 + 2 * i + 1) * 64 + 16 * w + n16) * 8, 0, AUXL);
            const u32x2 c0 = __builtin_amdgcn_raw_buffer_load_b64(rs, (int)OFF_XS + ((tb * 8 + 2 * i) * 64 + 16 * mt0 + n16) * 8, 0, AUXL);
            const u32x2 c1 = __builtin_amdgcn_raw_buffer_load_b64(rs, (int)OFF_XS + ((tb * 8 + 2 * i + 1) * 64 + 16 * mt0 + n16) * 8, 0, AUXL);
            stq[i] = __builtin_bit_cast(f32x4, u32x4{a0[0], a0[1], a1[0], a1[1]});
            stk[i] = __builtin_bit_cast(f32x4, u32x4{c0[0], c0[1], c1[0], c1[1]});
        }
        SBAR();
        ACC_ZERO();
        acck[0] = f32x4{0.f, 0.f, 0.f, 0.f}; acck[1] = acck[0];
#pragma unroll
        for (int ks = 0; ks < 5; ++ks) {
#pragma unroll
            for (int nt = 0; nt < 5; ++nt)
#pragma unroll
                for (int i = 0; i < 4; ++i) acc[i][nt] = MFMA16_32(wf[ks][nt], xf[ks][i], acc[i][nt]);
            acck[0] = MFMA16_32(wkf[ks][0], xk[ks], acck[0]);
            acck[1] = MFMA16_32(wkf[ks][1], xk[ks], acck[1]);
            SBAR();
        }
        SST(12);
        const float z4[4] = {0.f, 0.f, 0.f, 0.f}, z5[5] = {0.f, 0.f, 0.f, 0.f, 0.f}, z2[2] = {0.f, 0.f};
        qkv_finish<true>(ws, L, tb, cg, w, lane, acc, acck, csq, cs0, cs1, stq, stk, z4, z4, z5, z2);
    }
    SST_FLUSHK(ws, R1 ? 2 : 7);
}

template <int N> __global__ void k_end() {}

extern "C" void kernel_launch(void* const* d_in, const int* in_sizes, int n_in, void* d_out, int out_size, void* d_ws,
                              size_t ws_size, hipStream_t stream) {
    const u16* x = (const u16*)d_in[0];
    const u16* wq = (const u16*)d_in[1];
    const u16* wk = (const u16*)d_in[2];
    const u16* wv = (const u16*)d_in[3];
    const u16* wo = (const u16*)d_in[4];
    const u16* w1 = (const u16*)d_in[6];
    const u16* b1 = (const u16*)d_in[7];
    const u16* w2 = (const u16*)d_in[8];
    const u16* b2 = (const u16*)d_in[9];
    char* ws = (char*)d_ws;
    u16* x2 = (u16*)(ws + OFF_X2);
    (void)hipFuncSetAttribute((const void*)k_qkv<false>, hipFuncAttributeMaxDynamicSharedMemorySize, QKV_LDS);
    (void)hipFuncSetAttribute((const void*)k_attn<true>, hipFuncAttributeMaxDynamicSharedMemorySize, ATT_LDS);
    (void)hipFuncSetAttribute((const void*)k_attn<false>, hipFuncAttributeMaxDynamicSharedMemorySize, ATT_LDS);
    (void)hipFuncSetAttribute((const void*)k_mlp<true>, hipFuncAttributeMaxDynamicSharedMemorySize, MLP_LDS);
    (void)hipFuncSetAttribute((const void*)k_mlp<false>, hipFuncAttributeMaxDynamicSharedMemorySize, MLP_LDS);
    (void)hipFuncSetAttribute((const void*)k_end<0>, hipFuncAttributeMaxDynamicSharedMemorySize, 0);
    k_qkv<false><<<256, 256, QKV_LDS, stream>>>(x, wq, wk, wv, wo, ws);
    k_attn<true><<<256, 512, ATT_LDS, stream>>>(ws, w1, w2, wo, wq);
    k_mlp<true><<<256, 256, MLP_LDS, stream>>>(ws, x, b1, b2, nullptr);
    k_attn<false><<<256, 512, ATT_LDS, stream>>>(ws, w1, w2, wo, wq);
    k_mlp<false><<<256, 256, MLP_LDS, stream>>>(ws, x2, b1, b2, (u16*)d_out);
}
```

```cpp
#include <hip/hip_runtime.h>
#include <hip/hip_bf16.h>
#include <stdint.h>

typedef unsigned short u16;
typedef short bf16x8 __attribute__((ext_vector_type(8)));
typedef short bf16x4 __attribute__((ext_vector_type(4)));
typedef float f32x16 __attribute__((ext_vector_type(16)));
typedef float f32x4 __attribute__((ext_vector_type(4)));
typedef float f32x2 __attribute__((ext_vector_type(2)));
typedef unsigned u32x4 __attribute__((ext_vector_type(4)));
typedef unsigned u32x2 __attribute__((ext_vector_type(2)));
typedef __bf16 bf2_t __attribute__((ext_vector_type(2)));
typedef __attribute__((address_space(3))) char* lds_ptr;
typedef __attribute__((address_space(3))) const char* lds_cptr;
typedef __attribute__((address_space(1))) unsigned long long gu64;

#define DI __device__ __forceinline__
#define SBAR() __builtin_amdgcn_sched_barrier(0)
#define PIN(x) asm volatile("" : "+v"(x))
#define MFMA32_16(a, b, c) __builtin_amdgcn_mfma_f32_32x32x16_bf16(a, b, c, 0, 0, 0)
#define MFMA32_8(a, b, c) __builtin_amdgcn_mfma_f32_32x32x8bf16_1k(a, b, c, 0, 0, 0)
#define MFMA16_32(a, b, c) __builtin_amdgcn_mfma_f32_16x16x32_bf16(a, b, c, 0, 0, 0)
#define EX2(x) __builtin_amdgcn_exp2f(x)
#define RAW_BARRIER() asm volatile("s_waitcnt lgkmcnt(0)\n\ts_barrier" ::: "memory")
#define VMCNT(n) asm volatile("s_waitcnt vmcnt(%0)" :: "i"(n) : "memory")

#define SST_DECL do {} while (0)
#define SST(i) do {} while (0)
#define SST_FLUSHK(ws, kid) do {} while (0)
#define PREP_STORE(p, v) asm volatile("global_store_dwordx4 %0, %1, off sc1\n\ts_nop 1" :: "v"(p), "v"(v) : "memory")
#define ST8(p, v) (*(u32x2*)(p) = (v))
#define KADDR(dst, sym) asm volatile("s_getpc_b64 s[40:41]\n\ts_add_u32 s40, s40, " sym "@rel32@lo+4\n\ts_addc_u32 s41, s41, " sym "@rel32@hi+12\n\ts_mov_b64 %0, s[40:41]" : "=s"(dst) :: "s40", "s41", "scc")
constexpr int S_ = 2048, D_ = 640, F_ = 1728;
constexpr float LN_EPS = 1e-5f;
constexpr float QSCALE = 0.22360679774997896f * 1.4426950408889634f;
constexpr float SAFE_BOUND = 60.0f;

DI unsigned pk2(float a, float b) { f32x2 v = {a, b}; bf2_t r = __builtin_convertvector(v, bf2_t); return __builtin_bit_cast(unsigned, r); }
DI float bf_lo(unsigned u) { return __uint_as_float(u << 16); }
DI float bf_hi(unsigned u) { return __uint_as_float(u & 0xffff0000u); }
DI float bf2f(u16 v) { return __uint_as_float((unsigned)v << 16); }
DI float dot2(unsigned a, unsigned b, float c) { return __builtin_amdgcn_fdot2_f32_bf16(__builtin_bit_cast(bf2_t, a), __builtin_bit_cast(bf2_t, b), c, false); }
DI bf16x4 tr_read(lds_cptr p) { return __builtin_amdgcn_ds_read_tr16_b64_v4i16((__attribute__((address_space(3))) bf16x4*)p); }
DI bf16x8 cat8(bf16x4 a, bf16x4 b) { return __builtin_shufflevector(a, b, 0, 1, 2, 3, 4, 5, 6, 7); }
DI void glds16(const void* g, lds_ptr l) {
    unsigned keep;
    asm volatile("s_mov_b32 %0, m0\n\ts_mov_b32 m0, %2\n\ts_nop 0\n\tglobal_load_lds_dwordx4 %1, off\n\ts_mov_b32 m0, %0"
                 : "=&s"(keep) : "v"(g), "s"((unsigned)(uintptr_t)l) : "memory");
}
DI float xor32(float v) { return __shfl_xor(v, 32); }
DI float add32(float v) {
    auto r = __builtin_amdgcn_permlane32_swap(__float_as_uint(v), __float_as_uint(v), false, false);
    return __uint_as_float(r[0]) + __uint_as_float(r[1]);
}
DI float add16(float v) {
    auto r = __builtin_amdgcn_permlane16_swap(__float_as_uint(v), __float_as_uint(v), false, false);
    return __uint_as_float(r[0]) + __uint_as_float(r[1]);
}
DI float max16x(float v) { auto r = __builtin_amdgcn_permlane16_swap(__float_as_uint(v), __float_as_uint(v), false, false); return fmaxf(__uint_as_float(r[0]), __uint_as_float(r[1])); }
DI float max32x(float v) { auto r = __builtin_amdgcn_permlane32_swap(__float_as_uint(v), __float_as_uint(v), false, false); return fmaxf(__uint_as_float(r[0]), __uint_as_float(r[1])); }
template <int CTRL> DI float dppf(float v) { return __int_as_float(__builtin_amdgcn_update_dpp(0, __float_as_int(v), CTRL, 0xf, 0xf, false)); }
DI float rowmax16(float v) {
    v = fmaxf(v, dppf<0x128>(v)); v = fmaxf(v, dppf<0x124>(v)); v = fmaxf(v, dppf<0x122>(v)); v = fmaxf(v, dppf<0x121>(v)); return v;
}
DI float wavemax(float v) { return max32x(max16x(rowmax16(v))); }
DI u32x4 gather8(const u16* s, int ld) {
    u32x4 r;
#pragma unroll
    for (int e = 0; e < 4; ++e) r[e] = (unsigned)s[(2 * e) * ld] | ((unsigned)s[(2 * e + 1) * ld] << 16);
    return r;
}

constexpr size_t OFF_Q = 0;
constexpr size_t OFF_K1 = OFF_Q + (size_t)2048 * 32 * 48;
constexpr size_t OFF_K2 = OFF_K1 + 2048 * 32;
constexpr size_t OFF_V = OFF_K2 + 2048 * 8;
constexpr size_t OFF_KN = OFF_V + 128 * 640;
constexpr size_t OFF_QN = OFF_KN + 512;
constexpr size_t OFF_CS = OFF_QN + 4096;
constexpr size_t OFF_CONST = OFF_CS + 4096;
constexpr size_t OFF_CNT = OFF_CONST + 256;
constexpr size_t OFF_XG = OFF_CNT + 48 * 256;
constexpr size_t OFF_XS = OFF_XG + 262144;
constexpr size_t OFF_X1 = OFF_XS + 131072;
constexpr size_t OFF_H = OFF_X1 + (size_t)S_ * D_ * 2;
constexpr size_t OFF_U = OFF_H + (size_t)S_ * D_ * 2;
constexpr size_t OFF_X2 = OFF_U + (size_t)D_ * F_ * 2;
constexpr size_t OFF_O = OFF_X2 + (size_t)S_ * D_ * 2;
constexpr size_t OFF_W1F = OFF_O + (size_t)S_ * D_ * 2;
constexpr size_t OFF_W2F = OFF_W1F + (size_t)S_ * F_ * 2;
constexpr size_t OFF_WQF = OFF_W2F + (size_t)S_ * F_ * 2;
constexpr size_t OFF_WOF = OFF_WQF + (size_t)44 * 20 * 1024;
constexpr int NPREP = 1728 + 1728 + 200 + 200;

DI void chan8(const f32x4 (&st)[4], float& mean, float& var) {
    float m = 0.f, m2 = 0.f;
#pragma unroll
    for (int i = 0; i < 4; ++i) { m += st[i][0] + st[i][2]; m2 += st[i][1] + st[i][3]; }
    m *= 0.125f;
#pragma unroll
    for (int i = 0; i < 4; ++i) { const float d0 = st[i][0] - m, d1 = st[i][2] - m; m2 += 80.f * (d0 * d0 + d1 * d1); }
    mean = m; var = m2 * (1.f / 640.f);
}

template <bool FAST>
DI void qkv_finish(char* __restrict__ ws, char* L, int tb, int cg, int w, int lane, f32x4 (&acc)[4][5], f32x4 (&acck)[2],
                   const f32x4 (&csq)[5], const f32x4& cs0, const f32x4& cs1, const f32x4 (&stq)[4], const f32x4 (&stk)[4],
                   const float (&sm)[4], const float (&sq)[4], const float (&cs)[5], const float (&csk)[2]) {
    const int n16 = lane & 15, g = lane >> 4, mt0 = cg >> 1, isv = cg & 1;
    constexpr int R_OFF = 0, PK_OFF = 61440, ST_OFF = 69632, CS_OFF = 71680;
#pragma unroll
    for (int j = 1; j < 4; ++j) {
        const int o = (w + j) & 3;
#pragma unroll
        for (int nt = 0; nt < 5; ++nt) *(f32x4*)(L + R_OFF + (((o * 3 + (j - 1)) * 5 + nt) * 64 + lane) * 16) = acc[j][nt];
    }
    *(f32x4*)(L + PK_OFF + ((w * 2 + 0) * 64 + lane) * 16) = acck[0];
    *(f32x4*)(L + PK_OFF + ((w * 2 + 1) * 64 + lane) * 16) = acck[1];
    if (!FAST && g == 0) {
#pragma unroll
        for (int i = 0; i < 4; ++i) *(f32x2*)(L + ST_OFF + (w * 64 + ((i + w) & 3) * 16 + n16) * 8) = f32x2{sm[i], sq[i]};
#pragma unroll
        for (int nt = 0; nt < 5; ++nt) *(float*)(L + CS_OFF + w * 448 + (nt * 16 + n16) * 4) = cs[nt];
#pragma unroll
        for (int nt = 0; nt < 2; ++nt) *(float*)(L + CS_OFF + w * 448 + (80 + nt * 16 + n16) * 4) = csk[nt];
    }
    char* qrow = ws + OFF_Q + (size_t)(tb * 8 + cg) * 12288 + (16 * w + n16) * 48;
    int qoff[5];
#pragma unroll
    for (int nt = 0; nt < 5; ++nt) { const int G = 4 * nt + g, hl = G / 5; qoff[nt] = hl * 3072 + (G - 5 * hl) * 8; PIN(qoff[nt]); }
    RAW_BARRIER();
    auto stats = [&](int tl, const f32x4 (&st)[4], float& mean, float& rstd) {
        float var;
        if (FAST) chan8(st, mean, var);
        else {
            float s = 0.f, s2 = 0.f;
#pragma unroll
            for (int ww = 0; ww < 4; ++ww) { f32x2 v = *(const f32x2*)(L + ST_OFF + (ww * 64 + tl) * 8); s += v[0]; s2 += v[1]; }
            mean = s * (1.f / 640.f);
            var = fmaxf(s2 * (1.f / 640.f) - mean * mean, 0.f);
        }
        rstd = __builtin_amdgcn_rsqf(var + LN_EPS);
    };
    {
        f32x4 rr[3][5], cc[4][5];
#pragma unroll
        for (int j = 0; j < 3; ++j)
#pragma unroll
            for (int nt = 0; nt < 5; ++nt) rr[j][nt] = *(const f32x4*)(L + R_OFF + (((w * 3 + j) * 5 + nt) * 64 + lane) * 16);
        if (!FAST) {
#pragma unroll
            for (int ww = 0; ww < 4; ++ww)
#pragma unroll
                for (int nt = 0; nt < 5; ++nt) cc[ww][nt] = *(const f32x4*)(L + CS_OFF + ww * 448 + (nt * 16 + 4 * g) * 4);
        }
        float mean, rstd; stats(16 * w + n16, stq, mean, rstd);
        SBAR();
        const float sc = rstd * QSCALE;
        float n2[5];
#pragma unroll
        for (int nt = 0; nt < 5; ++nt) {
            const f32x4 a = (acc[0][nt] + rr[0][nt]) + (rr[1][nt] + rr[2][nt]);
            f32x4 s;
            if (FAST) s = csq[nt];
            else {
                s = (cc[0][nt] + cc[1][nt]) + (cc[2][nt] + cc[3][nt]);
                if (tb == 0 && w == 0 && n16 == 0) *(f32x4*)(ws + OFF_CS + (80 * cg + 16 * nt + 4 * g) * 4) = s;
            }
            const unsigned u0 = pk2((a[0] - mean * s[0]) * sc, (a[1] - mean * s[1]) * sc);
            const unsigned u1 = pk2((a[2] - mean * s[2]) * sc, (a[3] - mean * s[3]) * sc);
            { const u32x2 v_ = u32x2{u0, u1}; ST8(qrow + qoff[nt], v_); }
            n2[nt] = dot2(u1, u1, dot2(u0, u0, 0.f));
        }
        float hn[4];
        hn[0] = n2[0] + (g == 0 ? n2[1] : 0.f);
        hn[1] = (g > 0 ? n2[1] : 0.f) + (g < 2 ? n2[2] : 0.f);
        hn[2] = (g > 1 ? n2[2] : 0.f) + (g < 3 ? n2[3] : 0.f);
        hn[3] = (g == 3 ? n2[3] : 0.f) + n2[4];
#pragma unroll
        for (int hh = 0; hh < 4; ++hh) hn[hh] = add32(add16(hn[hh]));
        const float n2a = (g & 1) ? hn[1] : hn[0], n2b = (g & 1) ? hn[3] : hn[2];
        const unsigned qb = pk2(__builtin_amdgcn_sqrtf((g & 2) ? n2b : n2a), 0.f);
        { const u32x2 v_ = u32x2{qb, 0u}; ST8(qrow + g * 3072 + 40, v_); }
        float m = bf_lo(qb);
        if (!(m == m)) m = INFINITY;
        m = wavemax(m);
        if (lane == 0) *(float*)(ws + OFF_QN + ((tb * 8 + cg) * 4 + w) * 4) = m;
    }
    if (w == 3) {
        const int t16 = tb * 4 + mt0;
        float mean, rstd; stats(mt0 * 16 + n16, stk, mean, rstd);
        f32x4 a0 = {0.f, 0.f, 0.f, 0.f}, a1 = a0, s0 = a0, s1 = a0;
#pragma unroll
        for (int ww = 0; ww < 4; ++ww) {
            a0 += *(const f32x4*)(L + PK_OFF + ((ww * 2 + 0) * 64 + lane) * 16);
            a1 += *(const f32x4*)(L + PK_OFF + ((ww * 2 + 1) * 64 + n16) * 16);
            if (!FAST) {
                s0 += *(const f32x4*)(L + CS_OFF + ww * 448 + (80 + 4 * g) * 4);
                s1 += *(const f32x4*)(L + CS_OFF + ww * 448 + 96 * 4);
            }
        }
        if (FAST) { s0 = cs0; s1 = cs1; }
        else if (tb == 0 && cg < 2 && n16 == 0) {
            *(f32x4*)(ws + OFF_CS + (640 + 32 * isv + 4 * g) * 4) = s0;
            if (g == 0) *(f32x4*)(ws + OFF_CS + (640 + 32 * isv + 16) * 4) = s1;
        }
        const unsigned u0 = pk2((a0[0] - mean * s0[0]) * rstd, (a0[1] - mean * s0[1]) * rstd);
        const unsigned u1 = pk2((a0[2] - mean * s0[2]) * rstd, (a0[3] - mean * s0[3]) * rstd);
        const unsigned u2 = pk2((a1[0] - mean * s1[0]) * rstd, (a1[1] - mean * s1[1]) * rstd);
        const unsigned u3 = pk2((a1[2] - mean * s1[2]) * rstd, (a1[3] - mean * s1[3]) * rstd);
        if (!isv) {
            const int key = t16 * 16 + n16;
            *(u32x2*)(ws + OFF_K1 + key * 32 + g * 8) = u32x2{u0, u1};
            if (g == 0) *(u32x2*)(ws + OFF_K2 + key * 8) = u32x2{u2, u3};
            float n2 = dot2(u1, u1, dot2(u0, u0, 0.f));
            n2 = add32(add16(n2));
            n2 = dot2(u3, u3, dot2(u2, u2, n2));
            if (!(n2 == n2)) n2 = INFINITY;
            n2 = rowmax16(n2);
            if (lane == 0) *(float*)(ws + OFF_KN + t16 * 4) = n2;
        } else {
            u16* vb = (u16*)(ws + OFF_V + t16 * 640 + (n16 >> 3) * 320) + (n16 & 7);
            vb[(4 * g + 0) * 8] = (u16)u0; vb[(4 * g + 1) * 8] = (u16)(u0 >> 16);
            vb[(4 * g + 2) * 8] = (u16)u1; vb[(4 * g + 3) * 8] = (u16)(u1 >> 16);
            if (g == 0) { vb[16 * 8] = (u16)u2; vb[17 * 8] = (u16)(u2 >> 16); vb[18 * 8] = (u16)u3; vb[19 * 8] = (u16)(u3 >> 16); }
        }
    }
    if (w == 0) *(u32x2*)(ws + OFF_XG + ((size_t)(tb * 64 + lane) * 8 + cg) * 8) = u32x2{0u, 0u};
}

constexpr int QKV_LDS = 163840;
template <bool FAST>
__global__ __launch_bounds__(256, 1) void k_qkv(const u16* __restrict__ x, const u16* __restrict__ wq, const u16* __restrict__ wk,
                                                const u16* __restrict__ wv, const u16* __restrict__ wo, char* __restrict__ ws,
                                                const u16* __restrict__ w1, const u16* __restrict__ w2) {
    extern __shared__ __attribute__((aligned(16))) char lds_raw[];
    lds_ptr lds = (lds_ptr)lds_raw;
    char* L = lds_raw;
    const int tid = threadIdx.x, lane = tid & 63, w = __builtin_amdgcn_readfirstlane(tid >> 6);
    const int b = blockIdx.x, slot = b >> 3, tb = ((b & 7) >> 1) * 8 + (slot >> 2), cg = (b & 1) * 4 + (slot & 3);
    const int mt0 = cg >> 1, isv = cg & 1;
    const int n16 = lane & 15, g = lane >> 4;
    SST_DECL; SST(0);
    unsigned code_touch = 0u, wtouch[2] = {0u, 0u};
    if (!FAST && w == 2) {
        uint64_t a[5];
        KADDR(a[0], "_Z6k_attnILb1EEvPcPKtS2_S2_S2_"); KADDR(a[1], "_Z6k_attnILb0EEvPcPKtS2_S2_S2_");
        KADDR(a[2], "_Z5k_mlpILb1EEvPcPKtS2_S2_Pt"); KADDR(a[3], "_Z5k_mlpILb0EEvPcPKtS2_S2_Pt"); KADDR(a[4], "_Z5k_endILi0EEvv");
        uint64_t lo = a[0], hi = a[0];
#pragma unroll
        for (int i = 1; i < 5; ++i) { lo = a[i] < lo ? a[i] : lo; hi = a[i] > hi ? a[i] : hi; }
        const uint64_t pa = lo + (uint64_t)(slot * 64 + lane) * 128;
        if (pa < hi) code_touch = *(const __attribute__((address_space(1))) unsigned*)pa;
        SBAR();
    }
    f32x4 acc[4][5], acck[2];
#pragma unroll
    for (int i = 0; i < 4; ++i)
#pragma unroll
        for (int j = 0; j < 5; ++j) acc[i][j] = f32x4{0.f, 0.f, 0.f, 0.f};
    acck[0] = f32x4{0.f, 0.f, 0.f, 0.f}; acck[1] = acck[0];
    f32x4 csq[5], cs0, cs1, stq[4], stk[4];
    float sm[4] = {0.f, 0.f, 0.f, 0.f}, sq[4] = {0.f, 0.f, 0.f, 0.f};
    float cs[5] = {0.f, 0.f, 0.f, 0.f, 0.f}, csk[2] = {0.f, 0.f};
    if (FAST) {
        const bf16x8* xb = (const bf16x8*)x + (size_t)(tb * 4 * 20 + 5 * w) * 64 + lane;
        const bf16x8* wb = (const bf16x8*)(ws + OFF_WQF) + (size_t)(5 * cg * 20 + 5 * w) * 64 + lane;
        const bf16x8* kb = (const bf16x8*)(ws + OFF_WQF) + (size_t)((40 + 2 * isv) * 20 + 5 * w) * 64 + lane;
        bf16x8 xf[5][4], xk[5], wf[5][5], wkf[5][2];
#define QKV_LD(ks) do { _Pragma("unroll") for (int i = 0; i < 4; ++i) xf[ks][i] = xb[(((i + w) & 3) * 20 + (ks)) * 64]; \
        _Pragma("unroll") for (int nt = 0; nt < 5; ++nt) wf[ks][nt] = wb[(nt * 20 + (ks)) * 64]; \
        xk[ks] = xb[(mt0 * 20 + (ks)) * 64]; wkf[ks][0] = kb[(ks) * 64]; wkf[ks][1] = kb[(20 + (ks)) * 64]; SBAR(); } while (0)
        QKV_LD(0); QKV_LD(1); QKV_LD(2); QKV_LD(3);
#pragma unroll
        for (int ks = 0; ks < 5; ++ks) {
#pragma unroll
            for (int nt = 0; nt < 5; ++nt)
#pragma unroll
                for (int i = 0; i < 4; ++i) acc[i][nt] = MFMA16_32(wf[ks][nt], xf[ks][i], acc[i][nt]);
            acck[0] = MFMA16_32(wkf[ks][0], xk[ks], acck[0]);
            acck[1] = MFMA16_32(wkf[ks][1], xk[ks], acck[1]);
            SBAR();
            if (ks == 0) {
                QKV_LD(4);
                const float* csp = (const float*)(ws + OFF_CS);
#pragma unroll
                for (int nt = 0; nt < 5; ++nt) csq[nt] = *(const f32x4*)(csp + 80 * cg + 16 * nt + 4 * g);
                cs0 = *(const f32x4*)(csp + 640 + 32 * isv + 4 * g); cs1 = *(const f32x4*)(csp + 640 + 32 * isv + 16);
                const f32x4* sp = (const f32x4*)(ws + OFF_XS) + (size_t)(tb * 64 + n16) * 4;
#pragma unroll
                for (int i = 0; i < 4; ++i) { stq[i] = sp[16 * w * 4 + i]; stk[i] = sp[16 * mt0 * 4 + i]; }
                SBAR();
            }
        }
    } else {
        lds_ptr ra = lds + w * 40960, rb = ra + 20480;
        const char* xs = (const char*)(x + (size_t)(tb * 64) * 640 + 160 * w);
#pragma unroll
        for (int j = 0; j < 5; ++j) {
            const int c = 64 * j + lane;
            const unsigned xo = (unsigned)(c / 20) * 1280u + (unsigned)(c % 20) * 16u;
#pragma unroll
            for (int i = 0; i < 4; ++i) glds16(xs + (size_t)i * 20480 + xo, ra + i * 5120 + j * 1024);
        }
        const char* src = (const char*)(wq + (size_t)(160 * w) * 640 + 80 * cg);
        const unsigned loff = (unsigned)(lane / 10) * 1280u + (unsigned)(lane % 10) * 16u;
        const char* src2 = (const char*)((isv ? wv : wk) + (size_t)(160 * w) * 20);
#pragma unroll
        for (int i = 0; i < 16; ++i) glds16(src + (size_t)i * 7680 + loff, rb + i * 960);
#pragma unroll
        for (int i = 0; i < 3; ++i) glds16(src2 + i * 1024 + lane * 16, rb + 15488 + i * 1024);
        if (lane < 48) glds16(src2 + 3 * 1024 + lane * 16, rb + 15488 + 3 * 1024);
        SST(1);
        VMCNT(20);
        bf16x8 xf[4][5], xk[5];
        {
            const char* xl = L + w * 40960 + n16 * 320 + g * 16;
#pragma unroll
            for (int i = 0; i < 4; ++i)
#pragma unroll
                for (int ks = 0; ks < 5; ++ks) xf[i][ks] = *(const bf16x8*)(xl + ((i + w) & 3) * 5120 + ks * 64);
#pragma unroll
            for (int ks = 0; ks < 5; ++ks) xk[ks] = *(const bf16x8*)(xl + mt0 * 5120 + ks * 64);
        }
        asm volatile("s_waitcnt lgkmcnt(0)" ::: "memory");
        SBAR();
#pragma unroll
        for (int i = 0; i < 10; ++i) glds16(src + (size_t)(16 + i) * 7680 + loff, ra + i * 960);
        if (lane < 40) glds16(src + (size_t)26 * 7680 + loff, ra + 10 * 960);
#pragma unroll
        for (int i = 0; i < 2; ++i) glds16(src2 + 3840 + i * 1024 + lane * 16, ra + 10240 + i * 1024);
        if (lane < 32) glds16(src2 + 3840 + 2 * 1024 + lane * 16, ra + 10240 + 2 * 1024);
        SST(2);
        const unsigned ONES = 0x3f803f80u;
#pragma unroll
        for (int i = 0; i < 4; ++i)
#pragma unroll
            for (int ks = 0; ks < 5; ++ks) {
                u32x4 u = __builtin_bit_cast(u32x4, xf[i][ks]);
#pragma unroll
                for (int e = 0; e < 4; ++e) { sm[i] = dot2(u[e], ONES, sm[i]); sq[i] = dot2(u[e], u[e], sq[i]); }
            }
        SBAR();
        VMCNT(14);
        SST(3);
        const int q4 = (lane & 15) >> 2, p4 = lane & 3;
        lds_cptr wb0 = (lds_cptr)rb + (8 * g + q4) * 160 + p4 * 8, wb1 = (lds_cptr)ra + (8 * g + q4) * 160 + p4 * 8 - 3 * 5120;
        lds_cptr kb0 = (lds_cptr)rb + 15488 + (8 * g + q4) * 40 + p4 * 8, kb1 = (lds_cptr)ra + 10240 + (8 * g + q4) * 40 + p4 * 8 - 3 * 1280;
        bf16x8 wf[2][7];
#define QKV_TR(ks, s_) do { lds_cptr wb = (ks) < 3 ? wb0 : wb1, kb = (ks) < 3 ? kb0 : kb1; \
        _Pragma("unroll") for (int nt = 0; nt < 5; ++nt) wf[s_][nt] = cat8(tr_read(wb + (ks) * 5120 + nt * 32), tr_read(wb + (ks) * 5120 + 640 + nt * 32)); \
        _Pragma("unroll") for (int nt = 0; nt < 2; ++nt) wf[s_][5 + nt] = cat8(tr_read(kb + (ks) * 1280 + nt * 32), tr_read(kb + (ks) * 1280 + 160 + nt * 32)); SBAR(); } while (0)
        QKV_TR(0, 0);
#pragma unroll
        for (int ks = 0; ks < 5; ++ks) {
            if (ks == 2) {
                VMCNT(0); SBAR();
                {
                    const int u0 = (b * 4 + w) * 2;
#pragma unroll
                    for (int i = 0; i < 2; ++i) {
                        const int u = u0 + i;
                        const char* base = u < 864 ? (const char*)w1 + (size_t)u * 8192 : (u < 1728 ? (const char*)w2 + (size_t)(u - 864) * 8192
                                                   : (const char*)wo + (size_t)(u < 1828 ? u - 1728 : 0) * 8192);
                        wtouch[i] = *(const unsigned*)(base + lane * 128);
                    }
                    SBAR();
                }
            }
            if (ks < 4) QKV_TR(ks + 1, (ks + 1) & 1);
#pragma unroll
            for (int nt = 0; nt < 5; ++nt) {
                u32x4 u = __builtin_bit_cast(u32x4, wf[ks & 1][nt]);
#pragma unroll
                for (int e = 0; e < 4; ++e) cs[nt] = dot2(u[e], ONES, cs[nt]);
#pragma unroll
                for (int i = 0; i < 4; ++i) acc[i][nt] = MFMA16_32(wf[ks & 1][nt], xf[i][ks], acc[i][nt]);
            }
#pragma unroll
            for (int nt = 0; nt < 2; ++nt) {
                u32x4 u = __builtin_bit_cast(u32x4, wf[ks & 1][5 + nt]);
#pragma unroll
                for (int e = 0; e < 4; ++e) csk[nt] = dot2(u[e], ONES, csk[nt]);
                acck[nt] = MFMA16_32(wf[ks & 1][5 + nt], xk[ks], acck[nt]);
            }
            SBAR();
        }
#pragma unroll
        for (int i = 0; i < 4; ++i) { sm[i] = add32(add16(sm[i])); sq[i] = add32(add16(sq[i])); }
#pragma unroll
        for (int i = 0; i < 5; ++i) cs[i] = add32(add16(cs[i]));
#pragma unroll
        for (int i = 0; i < 2; ++i) csk[i] = add32(add16(csk[i]));
    }
    SST(4);
    if (!FAST) RAW_BARRIER();
    qkv_finish<FAST>(ws, L, tb, cg, w, lane, acc, acck, csq, cs0, cs1, stq, stk, sm, sq, cs, csk);
    if (!FAST && w == 1) {
        if (b < 80) {
            const int ks = b >> 2, ct = b & 3; int cn = 16 * (ct & 1) + n16; if (cn >= 20) cn = 0;
            *(u32x4*)(ws + OFF_WQF + (size_t)((40 + ct) * 20 + ks) * 1024 + lane * 16) =
                gather8(((ct & 2) ? wv : wk) + (size_t)(32 * ks + 8 * g) * 20 + cn, 20);
        } else if (b == 80 && lane < 4) {
            const unsigned v = (lane & 2) ? 0x3f803f80u : 0u;
            *(u32x4*)(ws + OFF_CONST + lane * 16) = u32x4{v, v, v, v};
        }
    }
    PIN(code_touch); PIN(wtouch[0]); PIN(wtouch[1]);
    SST(7); SST_FLUSHK(ws, FAST ? 5 : 0);
}

constexpr int PBW = 2688, ATT_LDS = 32 * PBW;
template <bool PREP>
__global__ __launch_bounds__(512, 2) void k_attn(char* __restrict__ ws, const u16* __restrict__ w1, const u16* __restrict__ w2,
                                                 const u16* __restrict__ wo, const u16* __restrict__ wq) {
    extern __shared__ __attribute__((aligned(16))) char lds_raw[];
    char* L = lds_raw;
    const int tid = threadIdx.x, lane = tid & 63, w = __builtin_amdgcn_readfirstlane(tid >> 6);
    const int b = blockIdx.x, slot = b >> 3, tb = ((b & 7) >> 1) * 8 + (slot >> 2), cg = (b & 1) * 4 + (slot & 3);
    const int r32 = lane & 31, h = lane >> 5, n16 = lane & 15, g = lane >> 4;
    const int cgp = w & 1, kq = w >> 1;
    SST_DECL; SST(0);
    float kn0 = *(const float*)(ws + OFF_KN + lane * 4), kn1 = *(const float*)(ws + OFF_KN + 256 + lane * 4);
    float qmax = *(const float*)(ws + OFF_QN + ((tb * 8 + cg) * 4 + (lane & 3)) * 4);
    bf16x8 q1[4]; bf16x4 q2[4];
    {
        const char* qg = ws + OFF_Q + (size_t)(tb * 8 + cg) * 12288 + cgp * 6144 + r32 * 48;
#pragma unroll
        for (int j = 0; j < 4; ++j) { q1[j] = *(const bf16x8*)(qg + j * 1536 + 16 * h); q2[j] = *(const bf16x4*)(qg + j * 1536 + 32 + 8 * h); }
    }
    SBAR();
    const int kr = (r32 & 19) | ((r32 & 4) << 1) | ((r32 & 8) >> 1);
    const unsigned ko1 = (unsigned)OFF_K1 + (unsigned)(512 * kq + kr) * 32u + 16u * h;
    const unsigned ko2 = h ? (unsigned)OFF_CONST + 32u : (unsigned)OFF_K2 + (unsigned)(512 * kq + kr) * 8u;
    const unsigned ks2 = h ? 0u : 256u;
    const unsigned vo = r32 < 20 ? (unsigned)OFF_V + (unsigned)(32 * kq) * 640u + (unsigned)(h * 20 + r32) * 16u
                                 : (unsigned)OFF_CONST + (r32 == 20 ? 32u : 0u);
    const unsigned vs = r32 < 20 ? 640u : 0u;
    bf16x8 k1[3], v0[3], v1[3]; bf16x4 k2[3];
#define K_LOAD(t) do { k1[(t) % 3] = *(const bf16x8*)(ws + ko1 + (t) * 1024); k2[(t) % 3] = *(const bf16x4*)(ws + (ko2 + (t) * ks2)); } while (0)
#define V_LOAD(t) do { v0[(t) % 3] = *(const bf16x8*)(ws + (vo + (2 * (t)) * vs)); v1[(t) % 3] = *(const bf16x8*)(ws + (vo + (2 * (t) + 1) * vs)); } while (0)
    K_LOAD(0); V_LOAD(0); SBAR(); K_LOAD(1); V_LOAD(1); SBAR();
    PIN(kn0); PIN(kn1);
    const float kmax2 = wavemax(fmaxf(kn0, kn1));
    qmax = rowmax16(qmax);
    const float kb = __builtin_amdgcn_sqrtf(kmax2);
    const bool safe = __builtin_amdgcn_readfirstlane((int)(qmax * kb <= SAFE_BOUND)) != 0;
    const f32x16 zero16 = {0.f, 0.f, 0.f, 0.f, 0.f, 0.f, 0.f, 0.f, 0.f, 0.f, 0.f, 0.f, 0.f, 0.f, 0.f, 0.f};
    if (safe) {
        if (h) {
#pragma unroll
            for (int j = 0; j < 4; ++j) q2[j][0] = (short)pk2(-bf2f((u16)q2[j][0]) * kb, 0.f);
        }
    } else {
        float m[4];
#pragma unroll
        for (int j = 0; j < 4; ++j) { m[j] = -INFINITY; if (h) q2[j] = bf16x4{0, 0, 0, 0}; }
        for (int t = 0; t < 16; ++t) {
            const bf16x8 ka = *(const bf16x8*)(ws + ko1 + t * 1024);
            const bf16x4 kc = *(const bf16x4*)(ws + (ko2 + t * ks2));
#pragma unroll
            for (int j = 0; j < 4; ++j) {
                f32x16 s = MFMA32_16(ka, q1[j], zero16);
                SBAR(); asm volatile("s_nop 15" ::: "memory"); SBAR();
                s = MFMA32_8(kc, q2[j], s);
#pragma unroll
                for (int e = 0; e < 16; ++e) m[j] = fmaxf(m[j], s[e]);
                SBAR();
            }
        }
#pragma unroll
        for (int j = 0; j < 4; ++j) {
            const float mm = fmaxf(m[j], xor32(m[j]));
            if (h == 0) *(float*)(L + ((w * 4 + j) * 32 + r32) * 4) = mm;
        }
        RAW_BARRIER();
#pragma unroll
        for (int j = 0; j < 4; ++j) {
            float mm = -INFINITY;
#pragma unroll
            for (int qq = 0; qq < 4; ++qq) mm = fmaxf(mm, *(const float*)(L + (((2 * qq + cgp) * 4 + j) * 32 + r32) * 4));
            const unsigned hi = __float_as_uint(mm) & 0xffff0000u;
            const float r1 = mm - __uint_as_float(hi);
            const unsigned mid = __float_as_uint(r1) & 0xffff0000u;
            const float r2 = r1 - __uint_as_float(mid);
            const unsigned lo = __float_as_uint(r2) & 0xffff0000u;
            if (h) q2[j] = __builtin_bit_cast(bf16x4, u32x2{hi ^ 0x80000000u, ((mid ^ 0x80000000u) >> 16) | (lo ^ 0x80000000u)});
        }
        RAW_BARRIER();
    }
    SST(1);
    f32x16 o[4], sa, sb;
#pragma unroll
    for (int j = 0; j < 4; ++j) o[j] = zero16;
    u32x4 p1prev = {0u, 0u, 0u, 0u};
    f32x4 ea, eb;
    sa = MFMA32_16(k1[0], q1[0], zero16);
    SBAR(); asm volatile("s_nop 15" ::: "memory"); SBAR();
    sa = MFMA32_8(k2[0], q2[0], sa);
    SBAR(); asm volatile("s_nop 15" ::: "memory"); SBAR();
    u32x4 prep[4]; char* prep_dst = nullptr; int prep_st = 0;
#pragma unroll
    for (int t = 0; t < 16; ++t) {
#pragma unroll
        for (int j = 0; j < 4; ++j) {
            const int it = 4 * t + j;
            f32x16& cur = (it & 1) ? sb : sa; f32x16& nxt = (it & 1) ? sa : sb;
            const int tn = j < 3 ? t : t + 1, jn = (j + 1) & 3;
            const int tp = j > 0 ? t : t - 1, jp = (j + 3) & 3;
            u32x4 p0, p1;
            if (j == 0 && t + 2 < 16) K_LOAD(t + 2);
            if (j == 1 && t + 2 < 16) V_LOAD(t + 2);
            if (PREP && j == 2 && (t == 1 || t == 8)) {
                int id = (t >> 3) * 2048 + b * 8 + w; if (id >= NPREP) id -= 2048;
                const u16* src; int ld;
                if (id < 1728) { const int ks = id / 27, c4 = id - ks * 27; src = w1 + (size_t)(32 * ks) * F_ + 64 * c4; ld = F_;
                                 prep_dst = ws + OFF_W1F + (size_t)(4 * c4 * 64 + ks) * 1024; prep_st = 64 * 1024; }
                else if (id < 3456) { const int jj = id - 1728, ks = jj >> 5, c4 = jj & 31; src = w2 + (size_t)(32 * ks) * S_ + 64 * c4; ld = S_;
                                      prep_dst = ws + OFF_W2F + (size_t)(4 * c4 * 54 + ks) * 1024; prep_st = 54 * 1024; }
                else { int jj = id - 3456; const bool isq = jj >= 200; if (isq) jj -= 200; const int ks = jj / 10, c4 = jj - ks * 10;
                       src = (isq ? wq : wo) + (size_t)(32 * ks) * 640 + 64 * c4; ld = 640;
                       prep_dst = ws + (isq ? OFF_WQF : OFF_WOF) + (size_t)(4 * c4 * 20 + ks) * 1024; prep_st = 20 * 1024; }
                src += (size_t)(lane >> 3) * ld + (lane & 7) * 8;
#pragma unroll
                for (int i = 0; i < 4; ++i) prep[i] = *(const u32x4*)(src + (size_t)(8 * i) * ld);
            }
            if (PREP && j == 2 && (t == 6 || t == 13)) {
#pragma unroll
                for (int i = 0; i < 4; ++i) *(u32x4*)(L + w * 4096 + i * 1024 + lane * 16) = prep[i];
            }
            if (PREP && j == 3 && (t == 6 || t == 13)) {
                lds_cptr tp_ = (lds_cptr)(lds_ptr)L + w * 4096 + (8 * g + (n16 >> 2)) * 128 + (lane & 3) * 8;
#pragma unroll
                for (int i = 0; i < 4; ++i) prep[i] = __builtin_bit_cast(u32x4, cat8(tr_read(tp_ + i * 32), tr_read(tp_ + 512 + i * 32)));
            }
            if (PREP && j == 0 && (t == 7 || t == 14)) {
#pragma unroll
                for (int i = 0; i < 4; ++i) PREP_STORE((u32x4*)(prep_dst + (size_t)i * prep_st + lane * 16), prep[i]);
            }
            SBAR();
            if (it < 63) nxt = MFMA32_16(k1[tn % 3], q1[jn], zero16);
            SBAR();
            p0[0] = pk2(EX2(cur[0]), EX2(cur[1])); p0[1] = pk2(EX2(cur[2]), EX2(cur[3]));
            PIN(p0); SBAR();
            if (it > 0) o[jp] = MFMA32_16(v1[(tp + 3) % 3], __builtin_bit_cast(bf16x8, p1prev), o[jp]);
            SBAR();
            p0[2] = pk2(EX2(cur[4]), EX2(cur[5])); p0[3] = pk2(EX2(cur[6]), EX2(cur[7]));
            PIN(p0); SBAR();
            if (it < 63) nxt = MFMA32_8(k2[tn % 3], q2[jn], nxt);
            SBAR();
            p1[0] = pk2(EX2(cur[8]), EX2(cur[9])); p1[1] = pk2(EX2(cur[10]), EX2(cur[11]));
            PIN(p1); SBAR();
            o[j] = MFMA32_16(v0[t % 3], __builtin_bit_cast(bf16x8, p0), o[j]);
            SBAR();
            p1[2] = pk2(EX2(cur[12]), EX2(cur[13])); p1[3] = pk2(EX2(cur[14]), EX2(cur[15]));
            PIN(p1); SBAR();
            p1prev = p1;
        }
        if (t == 7) SST(2);
    }
    o[3] = MFMA32_16(v1[15 % 3], __builtin_bit_cast(bf16x8, p1prev), o[3]);
    SST(3);
    if (PREP) RAW_BARRIER();
#pragma unroll
    for (int j = 0; j < 4; ++j) {
        char* pb = L + (kq * 8 + cgp * 4 + j) * PBW;
        *(f32x4*)(pb + lane * 16) = f32x4{o[j][0], o[j][1], o[j][2], o[j][3]};
        *(f32x4*)(pb + 1024 + lane * 16) = f32x4{o[j][4], o[j][5], o[j][6], o[j][7]};
        if (h == 0) *(f32x4*)(pb + 2048 + r32 * 16) = f32x4{o[j][8], o[j][9], o[j][10], o[j][11]};
        else *(float*)(pb + 2560 + r32 * 4) = o[j][8];
    }
    RAW_BARRIER();
    SST(4);
    {
        const char* pb = L + w * PBW;
        f32x4 a0 = {0.f, 0.f, 0.f, 0.f}, a1 = a0, a2 = a0; float l = 0.f;
#pragma unroll
        for (int qq = 0; qq < 4; ++qq) {
            a0 += *(const f32x4*)(pb + qq * 8 * PBW + lane * 16);
            a1 += *(const f32x4*)(pb + qq * 8 * PBW + 1024 + lane * 16);
            a2 += *(const f32x4*)(pb + qq * 8 * PBW + 2048 + r32 * 16);
            l += *(const float*)(pb + qq * 8 * PBW + 2560 + r32 * 4);
        }
        const float inv = __builtin_amdgcn_rcpf(l);
        const int tl = 32 * (w & 1) + r32, hd0 = 80 * cg + 20 * (w >> 1);
        char* ob = ws + OFF_O + (size_t)(tb * 4 + (tl >> 4)) * 20480 + (tl & 15) * 16;
        auto st = [&](int hd, const f32x4& a) {
            const u32x2 v_ = u32x2{pk2(a[0] * inv, a[1] * inv), pk2(a[2] * inv, a[3] * inv)};
            ST8(ob + (hd >> 5) * 1024 + ((hd & 31) >> 3) * 256 + (hd & 7) * 2, v_);
        };
        st(hd0 + 4 * h, a0); st(hd0 + 8 + 4 * h, a1);
        if (h == 0) st(hd0 + 16, a2);
    }
    if (b == 0 && tid < 48) *(unsigned*)(ws + OFF_CNT + tid * 256) = 0u;
    SST(5); SST_FLUSHK(ws, PREP ? 1 : 6);
}

template <bool XF>
__global__ __launch_bounds__(256, 1) void k_wo(char* __restrict__ ws, const u16* __restrict__ xin) {
    __shared__ __attribute__((aligned(16))) char L[61440];
    const int tid = threadIdx.x, lane = tid & 63, w = __builtin_amdgcn_readfirstlane(tid >> 6);
    const int b = blockIdx.x, slot = b >> 3, tb = ((b & 7) >> 1) * 8 + (slot >> 2), cg = (b & 1) * 4 + (slot & 3);
    const int n16 = lane & 15, g = lane >> 4;
    SST_DECL; SST(0);
    f32x4 acc[4][5];
#pragma unroll
    for (int i = 0; i < 4; ++i)
#pragma unroll
        for (int j = 0; j < 5; ++j) acc[i][j] = f32x4{0.f, 0.f, 0.f, 0.f};
    const int tok = tb * 64 + 16 * w + n16;
    u32x2 xi[5];
    {
        const bf16x8* xb = (const bf16x8*)(ws + OFF_O) + (size_t)(tb * 4 * 20 + 5 * w) * 64 + lane;
        const bf16x8* wb = (const bf16x8*)(ws + OFF_WOF) + (size_t)(5 * cg * 20 + 5 * w) * 64 + lane;
        bf16x8 xf[5][4], wf[5][5];
#define WO_LD(ks) do { _Pragma("unroll") for (int i = 0; i < 4; ++i) xf[ks][i] = xb[(((i + w) & 3) * 20 + (ks)) * 64]; \
        _Pragma("unroll") for (int nt = 0; nt < 5; ++nt) wf[ks][nt] = wb[(nt * 20 + (ks)) * 64]; SBAR(); } while (0)
        WO_LD(0); WO_LD(1); WO_LD(2); WO_LD(3);
#pragma unroll
        for (int ks = 0; ks < 5; ++ks) {
#pragma unroll
            for (int nt = 0; nt < 5; ++nt)
#pragma unroll
                for (int i = 0; i < 4; ++i) acc[i][nt] = MFMA16_32(wf[ks][nt], xf[ks][i], acc[i][nt]);
            SBAR();
            if (ks == 0) {
                WO_LD(4);
#pragma unroll
                for (int nt = 0; nt < 5; ++nt) {
                    const int d = 80 * cg + 16 * nt + 4 * g;
                    if (XF) xi[nt] = *(const u32x2*)(xin + ((size_t)((tok >> 4) * 20 + (d >> 5)) * 64 + ((d & 31) >> 3) * 16 + n16) * 8 + (d & 7));
                    else xi[nt] = *(const u32x2*)(xin + (size_t)tok * 640 + d);
                }
                SBAR();
            }
        }
    }
    SST(1);
#pragma unroll
    for (int j = 1; j < 4; ++j) {
        const int o = (w + j) & 3;
#pragma unroll
        for (int nt = 0; nt < 5; ++nt) *(f32x4*)(L + (((o * 3 + (j - 1)) * 5 + nt) * 64 + lane) * 16) = acc[j][nt];
    }
    RAW_BARRIER();
    SST(2);
    float xv[20]; u32x2 x1o[5];
    {
        f32x4 rr[3][5];
#pragma unroll
        for (int j = 0; j < 3; ++j)
#pragma unroll
            for (int nt = 0; nt < 5; ++nt) rr[j][nt] = *(const f32x4*)(L + (((w * 3 + j) * 5 + nt) * 64 + lane) * 16);
        SBAR();
        float s = 0.f;
#pragma unroll
        for (int nt = 0; nt < 5; ++nt) {
            const f32x4 a = (acc[0][nt] + rr[0][nt]) + (rr[1][nt] + rr[2][nt]);
            const unsigned u0 = pk2(bf_lo(xi[nt][0]) + a[0], bf_hi(xi[nt][0]) + a[1]);
            const unsigned u1 = pk2(bf_lo(xi[nt][1]) + a[2], bf_hi(xi[nt][1]) + a[3]);
            x1o[nt] = u32x2{u0, u1};
            xv[4 * nt] = bf_lo(u0); xv[4 * nt + 1] = bf_hi(u0); xv[4 * nt + 2] = bf_lo(u1); xv[4 * nt + 3] = bf_hi(u1);
            s += (xv[4 * nt] + xv[4 * nt + 1]) + (xv[4 * nt + 2] + xv[4 * nt + 3]);
        }
        const float mw = add32(add16(s)) * (1.f / 80.f);
        float s2 = 0.f;
#pragma unroll
        for (int i = 0; i < 20; ++i) { const float dd = xv[i] - mw; s2 += dd * dd; }
        s2 = add32(add16(s2));
        gu64* gp = (gu64*)(ws + OFF_XG) + (size_t)tok * 8 + cg;
        if (g == 0) __hip_atomic_store(gp, ((unsigned long long)(__float_as_uint(s2) | 0x80000000u) << 32) | __float_as_uint(mw),
                                       __ATOMIC_RELAXED, __HIP_MEMORY_SCOPE_AGENT);
    }
    SST(3);
    float mean, var;
    {
        gu64* gp = (gu64*)(ws + OFF_XG) + (size_t)tok * 8 + 2 * g;
        unsigned long long v0, v1;
        for (int spin = 0; spin < (1 << 20); ++spin) {
            v0 = __hip_atomic_load(gp, __ATOMIC_RELAXED, __HIP_MEMORY_SCOPE_AGENT);
            v1 = __hip_atomic_load(gp + 1, __ATOMIC_RELAXED, __HIP_MEMORY_SCOPE_AGENT);
            if (__all((long long)(v0 & v1) < 0)) break;
            __builtin_amdgcn_s_sleep(4);
        }
        const float m0 = __uint_as_float((unsigned)v0), m1 = __uint_as_float((unsigned)v1);
        mean = add32(add16(m0 + m1)) * 0.125f;
        const float d0 = m0 - mean, d1 = m1 - mean;
        var = add32(add16(__uint_as_float((unsigned)(v0 >> 32) & 0x7fffffffu) + __uint_as_float((unsigned)(v1 >> 32) & 0x7fffffffu) +
                          80.f * (d0 * d0 + d1 * d1))) * (1.f / 640.f);
    }
    SST(4);
    {
        const float r1 = __builtin_amdgcn_rsqf(var + LN_EPS);
        const float r2 = r1 * __builtin_amdgcn_rsqf(var * r1 * r1 + LN_EPS);
        char* hb = L + w * 15360;
#pragma unroll
        for (int nt = 0; nt < 5; ++nt)
            *(u32x2*)(hb + n16 * 176 + (16 * nt + 4 * g) * 2) =
                u32x2{pk2((xv[4 * nt] - mean) * r2, (xv[4 * nt + 1] - mean) * r2), pk2((xv[4 * nt + 2] - mean) * r2, (xv[4 * nt + 3] - mean) * r2)};
        asm volatile("s_waitcnt lgkmcnt(0)" ::: "memory");
        const int q4 = (lane & 15) >> 2, p4 = lane & 3;
        lds_cptr hr = (lds_cptr)(lds_ptr)L + w * 15360 + (8 * (g & 1) + q4) * 176 + p4 * 8 + (g >> 1) * 32;
        const int s0 = tb * 64 + 16 * w + 8 * (g & 1);
        u16* hf = (u16*)(ws + OFF_H) + ((size_t)(5 * cg + (g >> 1)) * 64 + (s0 >> 5)) * 512 + (((s0 & 31) >> 3) * 16 + n16) * 8;
        const bf16x8 f0 = cat8(tr_read(hr), tr_read(hr + 704));
        const bf16x8 f1 = cat8(tr_read(hr + 64), tr_read(hr + 704 + 64));
        const bf16x8 f2 = cat8(tr_read(hr + (g < 2 ? 128 : 0)), tr_read(hr + 704 + (g < 2 ? 128 : 0)));
        *(bf16x8*)(hf) = f0;
        *(bf16x8*)(hf + 2 * 64 * 512) = f1;
        if (g < 2) *(bf16x8*)(hf + 4 * 64 * 512) = f2;
        u16* x1r = (u16*)(ws + OFF_X1) + (size_t)tok * 640 + 80 * cg + 4 * g;
#pragma unroll
        for (int nt = 0; nt < 5; ++nt) *(u32x2*)(x1r + 16 * nt) = x1o[nt];
    }
    SST(5); SST_FLUSHK(ws, XF ? 7 : 2);
}

template <int MODE, int NS, int PF>
DI void gemm_body(const bf16x8* __restrict__ po, const bf16x8* __restrict__ pf, const int (&ooff)[4], int fstride, f32x4 (&acc)[4][5]) {
    bf16x8 ro[PF][4], rf[PF][5];
#pragma unroll
    for (int s = 0; s < PF; ++s) {
#pragma unroll
        for (int j = 0; j < 4; ++j) ro[s][j] = po[ooff[j] + s * 64];
#pragma unroll
        for (int i = 0; i < 5; ++i) rf[s][i] = pf[i * fstride + s * 64];
        SBAR();
    }
#pragma unroll
    for (int s = 0; s < NS; ++s) {
        const int sl = s % PF;
        SBAR();
#pragma unroll
        for (int j = 0; j < 4; ++j)
#pragma unroll
            for (int i = 0; i < 5; ++i) {
                if (MODE == 1) acc[j][i] = MFMA16_32(ro[sl][j], rf[sl][i], acc[j][i]);
                else acc[j][i] = MFMA16_32(rf[sl][i], ro[sl][j], acc[j][i]);
            }
        SBAR();
        if (s + PF < NS) {
#pragma unroll
            for (int j = 0; j < 4; ++j) ro[sl][j] = po[ooff[j] + (s + PF) * 64];
#pragma unroll
            for (int i = 0; i < 5; ++i) rf[sl][i] = pf[i * fstride + (s + PF) * 64];
        }
    }
}

template <int MODE>
__global__ __launch_bounds__(256, 1) void k_gemm(char* __restrict__ ws, const u16* __restrict__ bias, u16* __restrict__ out) {
    __shared__ __attribute__((aligned(16))) f32x4 red[4 * 3 * 5 * 64];
    const int tid = threadIdx.x, lane = tid & 63, w = __builtin_amdgcn_readfirstlane(tid >> 6);
    const int b = blockIdx.x, xcd = b & 7, slot = b >> 3;
    const int n16 = lane & 15, g = lane >> 4;
    constexpr int KS = MODE == 1 ? 64 : 54;
    int ot, dt;
    if (MODE == 1) {
        ot = 7 * (xcd >> 1) + (slot >> 2); dt = 4 * (xcd & 1) + (slot & 3);
        if (slot >= 28 || ot >= 27) return;
    } else {
        ot = 8 * (xcd >> 1) + (slot >> 2); dt = 4 * (xcd & 1) + (slot & 3);
    }
    SST_DECL; SST(0);
    u32x2 bb = {0u, 0u}, xi[5]; float bs = 0.f;
    if (MODE == 1) bb = *(const u32x2*)(bias + 64 * ot + 16 * w + 4 * g);
    else {
        const int s = 64 * ot + 16 * w + n16;
        bs = bf2f(bias[s]);
        const u16* x1 = (const u16*)(ws + OFF_X1) + (size_t)s * 640 + 80 * dt + 4 * g;
#pragma unroll
        for (int i = 0; i < 5; ++i) xi[i] = *(const u32x2*)(x1 + 16 * i);
    }
    const int k0 = MODE == 1 ? 16 * w : (w < 2 ? 14 * w : 28 + 13 * (w - 2));
    const bf16x8* po = (const bf16x8*)(ws + (MODE == 1 ? OFF_W1F : OFF_W2F)) + ((size_t)(4 * ot) * KS + k0) * 64 + lane;
    const bf16x8* pf = (const bf16x8*)(ws + (MODE == 1 ? OFF_H : OFF_U)) + ((size_t)(5 * dt) * KS + k0) * 64 + lane;
    int ooff[4];
#pragma unroll
    for (int j = 0; j < 4; ++j) ooff[j] = ((j + w) & 3) * KS * 64;
    f32x4 acc[4][5];
#pragma unroll
    for (int j = 0; j < 4; ++j)
#pragma unroll
        for (int i = 0; i < 5; ++i) acc[j][i] = f32x4{0.f, 0.f, 0.f, 0.f};
    if (MODE == 1) gemm_body<1, 16, 6>(po, pf, ooff, KS * 64, acc);
    else if (w < 2) gemm_body<2, 14, 6>(po, pf, ooff, KS * 64, acc);
    else gemm_body<2, 13, 6>(po, pf, ooff, KS * 64, acc);
    SST(1);
#pragma unroll
    for (int j = 1; j < 4; ++j) {
        const int o = (w + j) & 3;
#pragma unroll
        for (int i = 0; i < 5; ++i) red[((o * 3 + (j - 1)) * 5 + i) * 64 + lane] = acc[j][i];
    }
    RAW_BARRIER();
#pragma unroll
    for (int j = 0; j < 3; ++j)
#pragma unroll
        for (int i = 0; i < 5; ++i) acc[1 + j][i] = red[((w * 3 + j) * 5 + i) * 64 + lane];
    SBAR();
#pragma unroll
    for (int i = 0; i < 5; ++i) acc[0][i] = (acc[0][i] + acc[1][i]) + (acc[2][i] + acc[3][i]);
    SST(2);
    if (MODE == 1) {
        const int f = 64 * ot + 16 * w + 4 * g;
        const float bv[4] = {bf_lo(bb[0]), bf_hi(bb[0]), bf_lo(bb[1]), bf_hi(bb[1])};
        u16* uf = (u16*)(ws + OFF_U);
#pragma unroll
        for (int i = 0; i < 5; ++i) {
            float r[4];
#pragma unroll
            for (int e = 0; e < 4; ++e) {
                const float u = acc[0][i][e] + bv[e];
                const float z = u * (1.f + 0.044715f * u * u) * (-2.f * 0.7978845608028654f * 1.4426950408889634f);
                r[e] = u * __builtin_amdgcn_rcpf(1.f + EX2(z));
            }
            *(u32x2*)(uf + ((size_t)((5 * dt + i) * 54 + (f >> 5)) * 64 + ((f & 31) >> 3) * 16 + n16) * 8 + (f & 7)) =
                u32x2{pk2(r[0], r[1]), pk2(r[2], r[3])};
        }
    } else {
        const int s = 64 * ot + 16 * w + n16;
        float xv[20]; float sum = 0.f;
#pragma unroll
        for (int i = 0; i < 5; ++i) {
            const int d = 80 * dt + 16 * i + 4 * g;
            u16* o = MODE == 3 ? out + (size_t)s * 640 + d
                               : out + ((size_t)((4 * ot + w) * 20 + (d >> 5)) * 64 + ((d & 31) >> 3) * 16 + n16) * 8 + (d & 7);
            const unsigned u0 = pk2(bf_lo(xi[i][0]) + acc[0][i][0] + bs, bf_hi(xi[i][0]) + acc[0][i][1] + bs);
            const unsigned u1 = pk2(bf_lo(xi[i][1]) + acc[0][i][2] + bs, bf_hi(xi[i][1]) + acc[0][i][3] + bs);
            *(u32x2*)o = u32x2{u0, u1};
            xv[4 * i] = bf_lo(u0); xv[4 * i + 1] = bf_hi(u0); xv[4 * i + 2] = bf_lo(u1); xv[4 * i + 3] = bf_hi(u1);
            sum += (xv[4 * i] + xv[4 * i + 1]) + (xv[4 * i + 2] + xv[4 * i + 3]);
        }
        if (MODE == 2) {
            const float mw = add32(add16(sum)) * (1.f / 80.f);
            float s2 = 0.f;
#pragma unroll
            for (int i = 0; i < 20; ++i) { const float dd = xv[i] - mw; s2 += dd * dd; }
            s2 = add32(add16(s2));
            if (g == 0) *(f32x2*)(ws + OFF_XS + ((size_t)s * 8 + dt) * 8) = f32x2{mw, s2};
        }
    }
    SST(3); SST_FLUSHK(ws, MODE == 1 ? (out ? 8 : 3) : (MODE == 2 ? 4 : 9));
}

typedef __attribute__((address_space(1))) unsigned gu32;
constexpr int AUXL = 16;
#define LD_SC1(off) __builtin_bit_cast(bf16x8, __builtin_amdgcn_raw_buffer_load_b128(rs, (int)(off), 0, 16))
DI void edge_publish(gu32* cnt) {
    RAW_BARRIER();
    if (threadIdx.x == 0) __hip_atomic_fetch_add(cnt, 1u, __ATOMIC_RELAXED, __HIP_MEMORY_SCOPE_AGENT);
}
DI void edge_wait(gu32* cnt, unsigned want) {
    if (threadIdx.x == 0) {
        for (int spin = 0; spin < (1 << 20); ++spin) {
            if (__hip_atomic_load(cnt, __ATOMIC_RELAXED, __HIP_MEMORY_SCOPE_AGENT) >= want) break;
            __builtin_amdgcn_s_sleep(1);
        }
        __builtin_amdgcn_fence(__ATOMIC_ACQUIRE, "wavefront");
    }
    RAW_BARRIER();
}
#define W1_A 6
#define W2_FIRST 7
constexpr int MLP_LDS = 73728;
template <bool R1>
__global__ __launch_bounds__(256, 1) void k_mlp(char* __restrict__ ws, const u16* __restrict__ xin, const u16* __restrict__ b1,
                                                const u16* __restrict__ b2, u16* __restrict__ out) {
    extern __shared__ __attribute__((aligned(16))) char lds_raw[];
    char* L = lds_raw;
    const int tid = threadIdx.x, lane = tid & 63, w = __builtin_amdgcn_readfirstlane(tid >> 6);
    const int b = blockIdx.x, slot = b >> 3, tb = ((b & 7) >> 1) * 8 + (slot >> 2), cg = (b & 1) * 4 + (slot & 3);
    const int ot1 = ((b & 7) >> 1) * 7 + (slot >> 2);
    const bool act1 = slot < 28 && ot1 < 27;
    const int n16 = lane & 15, g = lane >> 4;
    const __amdgpu_buffer_rsrc_t rs = __builtin_amdgcn_make_buffer_rsrc((void*)ws, 0, 0x8000000, 0x00020000);
    gu32* cnt = (gu32*)(ws + OFF_CNT);
    SST_DECL; SST(0);
    f32x4 acc[4][5];
#define ACC_ZERO() do { _Pragma("unroll") for (int i_ = 0; i_ < 4; ++i_) _Pragma("unroll") for (int j_ = 0; j_ < 5; ++j_) acc[i_][j_] = f32x4{0.f, 0.f, 0.f, 0.f}; } while (0)
#define ACC_REDUCE() do { \
    _Pragma("unroll") for (int j_ = 1; j_ < 4; ++j_) { const int o_ = (w + j_) & 3; \
        _Pragma("unroll") for (int i_ = 0; i_ < 5; ++i_) *(f32x4*)(L + (((o_ * 3 + (j_ - 1)) * 5 + i_) * 64 + lane) * 16) = acc[j_][i_]; } \
    RAW_BARRIER(); \
    _Pragma("unroll") for (int j_ = 0; j_ < 3; ++j_) _Pragma("unroll") for (int i_ = 0; i_ < 5; ++i_) \
        acc[1 + j_][i_] = *(const f32x4*)(L + (((w * 3 + j_) * 5 + i_) * 64 + lane) * 16); \
    SBAR(); \
    _Pragma("unroll") for (int i_ = 0; i_ < 5; ++i_) acc[0][i_] = (acc[0][i_] + acc[1][i_]) + (acc[2][i_] + acc[3][i_]); } while (0)
    const int tok = tb * 64 + 16 * w + n16;
    bf16x8 wr[16][4];
    int ro1[4], ro2[4];
#pragma unroll
    for (int j = 0; j < 4; ++j) { ro1[j] = ((j + w) & 3) * 64 * 64; ro2[j] = ((j + w) & 3) * 54 * 64; }
    const bf16x8* pw1 = (const bf16x8*)(ws + OFF_W1F) + ((size_t)(4 * (act1 ? ot1 : 0)) * 64 + 16 * w) * 64 + lane;
    const int k2 = w < 2 ? 14 * w : 28 + 13 * (w - 2);
    const bf16x8* pw2 = (const bf16x8*)(ws + OFF_W2F) + ((size_t)(4 * tb) * 54 + k2) * 64 + lane;
#define W1_LD(lo, hi) do { _Pragma("unroll") for (int s_ = (lo); s_ < (hi); ++s_) _Pragma("unroll") for (int j_ = 0; j_ < 4; ++j_) wr[s_][j_] = pw1[ro1[j_] + s_ * 64]; SBAR(); } while (0)
#define W2_LD(lo, hi) do { _Pragma("unroll") for (int s_ = (lo); s_ < (hi); ++s_) _Pragma("unroll") for (int j_ = 0; j_ < 4; ++j_) wr[s_][j_] = pw2[ro2[j_] + s_ * 64]; SBAR(); } while (0)

    u32x2 x1o[5], bb; float bs;
    {
        ACC_ZERO();
        u32x2 xi[5];
        const bf16x8* xb = (const bf16x8*)(ws + OFF_O) + (size_t)(tb * 4 * 20 + 5 * w) * 64 + lane;
        const bf16x8* wb = (const bf16x8*)(ws + OFF_WOF) + (size_t)(5 * cg * 20 + 5 * w) * 64 + lane;
        bf16x8 xf[5][4], wf[5][5];
        WO_LD(0); WO_LD(1); WO_LD(2); WO_LD(3);
#pragma unroll
        for (int ks = 0; ks < 5; ++ks) {
#pragma unroll
            for (int nt = 0; nt < 5; ++nt)
#pragma unroll
                for (int i = 0; i < 4; ++i) acc[i][nt] = MFMA16_32(wf[ks][nt], xf[ks][i], acc[i][nt]);
            SBAR();
            if (ks == 0) {
                WO_LD(4);
#pragma unroll
                for (int nt = 0; nt < 5; ++nt) {
                    const int d = 80 * cg + 16 * nt + 4 * g;
                    if (!R1) xi[nt] = *(const u32x2*)(xin + ((size_t)((tok >> 4) * 20 + (d >> 5)) * 64 + ((d & 31) >> 3) * 16 + n16) * 8 + (d & 7));
                    else xi[nt] = *(const u32x2*)(xin + (size_t)tok * 640 + d);
                }
                bb = *(const u32x2*)(b1 + 64 * (act1 ? ot1 : 0) + 16 * w + 4 * g);
                bs = bf2f(b2[tok]);
                SBAR();
            }
            if (ks == 1) W1_LD(0, 2);
            if (ks == 2) W1_LD(2, 4);
            if (ks == 3) W1_LD(4, 6);
            if (ks == 4) W1_LD(6, W1_A);
        }
        SST(1);
        ACC_REDUCE();
        float xv[20], s = 0.f;
#pragma unroll
        for (int nt = 0; nt < 5; ++nt) {
            const unsigned u0 = pk2(bf_lo(xi[nt][0]) + acc[0][nt][0], bf_hi(xi[nt][0]) + acc[0][nt][1]);
            const unsigned u1 = pk2(bf_lo(xi[nt][1]) + acc[0][nt][2], bf_hi(xi[nt][1]) + acc[0][nt][3]);
            x1o[nt] = u32x2{u0, u1};
            xv[4 * nt] = bf_lo(u0); xv[4 * nt + 1] = bf_hi(u0); xv[4 * nt + 2] = bf_lo(u1); xv[4 * nt + 3] = bf_hi(u1);
            s += (xv[4 * nt] + xv[4 * nt + 1]) + (xv[4 * nt + 2] + xv[4 * nt + 3]);
        }
        const float mw = add32(add16(s)) * (1.f / 80.f);
        float s2 = 0.f;
#pragma unroll
        for (int i = 0; i < 20; ++i) { const float dd = xv[i] - mw; s2 += dd * dd; }
        s2 = add32(add16(s2));
        gu64* gp = (gu64*)(ws + OFF_XG) + (size_t)tok * 8;
        if (g == 0) __hip_atomic_store(gp + cg, ((unsigned long long)(__float_as_uint(s2) | 0x80000000u) << 32) | __float_as_uint(mw),
                                       __ATOMIC_RELAXED, __HIP_MEMORY_SCOPE_AGENT);
        SBAR();
        SST(2);
        unsigned long long v0, v1;
        for (int spin = 0; spin < (1 << 20); ++spin) {
            v0 = __hip_atomic_load(gp + 2 * g, __ATOMIC_RELAXED, __HIP_MEMORY_SCOPE_AGENT);
            v1 = __hip_atomic_load(gp + 2 * g + 1, __ATOMIC_RELAXED, __HIP_MEMORY_SCOPE_AGENT);
            if (__all((long long)(v0 & v1) < 0)) break;
            __builtin_amdgcn_s_sleep(4);
        }
        SST(3);
        const float m0 = __uint_as_float((unsigned)v0), m1 = __uint_as_float((unsigned)v1);
        const float mean = add32(add16(m0 + m1)) * 0.125f;
        const float d0 = m0 - mean, d1 = m1 - mean;
        const float var = add32(add16(__uint_as_float((unsigned)(v0 >> 32) & 0x7fffffffu) + __uint_as_float((unsigned)(v1 >> 32) & 0x7fffffffu) +
                                      80.f * (d0 * d0 + d1 * d1))) * (1.f / 640.f);
        const float r1 = __builtin_amdgcn_rsqf(var + LN_EPS);
        const float r2 = r1 * __builtin_amdgcn_rsqf(var * r1 * r1 + LN_EPS);
        char* hb = L + w * 15360;
#pragma unroll
        for (int nt = 0; nt < 5; ++nt)
            *(u32x2*)(hb + n16 * 176 + (16 * nt + 4 * g) * 2) =
                u32x2{pk2((xv[4 * nt] - mean) * r2, (xv[4 * nt + 1] - mean) * r2), pk2((xv[4 * nt + 2] - mean) * r2, (xv[4 * nt + 3] - mean) * r2)};
        asm volatile("s_waitcnt lgkmcnt(0)" ::: "memory");
        const int q4 = (lane & 15) >> 2, p4 = lane & 3;
        lds_cptr hr = (lds_cptr)(lds_ptr)L + w * 15360 + (8 * (g & 1) + q4) * 176 + p4 * 8 + (g >> 1) * 32;
        const int s0 = tb * 64 + 16 * w + 8 * (g & 1);
        const int hoff = (int)OFF_H + ((5 * cg + (g >> 1)) * 64 + (s0 >> 5)) * 1024 + (((s0 & 31) >> 3) * 16 + n16) * 16;
        const bf16x8 f0 = cat8(tr_read(hr), tr_read(hr + 704));
        const bf16x8 f1 = cat8(tr_read(hr + 64), tr_read(hr + 704 + 64));
        const bf16x8 f2 = cat8(tr_read(hr + (g < 2 ? 128 : 0)), tr_read(hr + 704 + (g < 2 ? 128 : 0)));
        __builtin_amdgcn_raw_buffer_store_b128(__builtin_bit_cast(u32x4, f0), rs, hoff, 0, 16);
        __builtin_amdgcn_raw_buffer_store_b128(__builtin_bit_cast(u32x4, f1), rs, hoff + 2 * 65536, 0, 16);
        if (g < 2) __builtin_amdgcn_raw_buffer_store_b128(__builtin_bit_cast(u32x4, f2), rs, hoff + 4 * 65536, 0, 16);
        SBAR();
        W1_LD(W1_A, W1_A + 4);
        VMCNT(16);
        SST(4);
        edge_publish(cnt + cg * 64);
    }
    if (act1) {
        W1_LD(W1_A + 4, 16);
        edge_wait(cnt + cg * 64, 32);
        SST(5);
        ACC_ZERO();
        const int ho = (int)OFF_H + ((5 * cg) * 64 + 16 * w) * 1024 + lane * 16;
        constexpr int PF = 6;
        bf16x8 rf[PF][5];
#pragma unroll
        for (int s = 0; s < PF; ++s) {
#pragma unroll
            for (int i = 0; i < 5; ++i) rf[s][i] = LD_SC1(ho + i * 65536 + s * 1024);
            SBAR();
        }
#pragma unroll
        for (int s = 0; s < 16; ++s) {
#pragma unroll
            for (int j = 0; j < 4; ++j)
#pragma unroll
                for (int i = 0; i < 5; ++i) acc[j][i] = MFMA16_32(wr[s][j], rf[s % PF][i], acc[j][i]);
            SBAR();
            if (s + PF < 16) {
#pragma unroll
                for (int i = 0; i < 5; ++i) rf[s % PF][i] = LD_SC1(ho + i * 65536 + (s + PF) * 1024);
                SBAR();
            }
            if (s >= W2_FIRST && s - W2_FIRST < 9) W2_LD(s - W2_FIRST, s - W2_FIRST + 1);
        }
        SST(6);
        ACC_REDUCE();
        const int f = 64 * ot1 + 16 * w + 4 * g;
        const float bv[4] = {bf_lo(bb[0]), bf_hi(bb[0]), bf_lo(bb[1]), bf_hi(bb[1])};
        const int uo = (int)OFF_U + ((5 * cg) * 54 + (f >> 5)) * 1024 + (((f & 31) >> 3) * 16 + n16) * 16 + (f & 7) * 2;
#pragma unroll
        for (int i = 0; i < 5; ++i) {
            float r[4];
#pragma unroll
            for (int e = 0; e < 4; ++e) {
                const float u = acc[0][i][e] + bv[e];
                const float z = u * (1.f + 0.044715f * u * u) * (-2.f * 0.7978845608028654f * 1.4426950408889634f);
                r[e] = u * __builtin_amdgcn_rcpf(1.f + EX2(z));
            }
            __builtin_amdgcn_raw_buffer_store_b64(u32x2{pk2(r[0], r[1]), pk2(r[2], r[3])}, rs, uo + i * 54 * 1024, 0, 16);
        }
        SBAR();
        W2_LD(9, 13);
        VMCNT(16);
        SST(7);
        edge_publish(cnt + (8 + cg) * 64);
    } else {
        W2_LD(0, 13);
    }
    {
        if (w < 2) W2_LD(13, 14);
        edge_wait(cnt + (8 + cg) * 64, 27);
        SST(8);
        ACC_ZERO();
        const int uo = (int)OFF_U + ((5 * cg) * 54 + k2) * 1024 + lane * 16;
        constexpr int PF = 6;
        bf16x8 rf[PF][5];
#pragma unroll
        for (int s = 0; s < PF; ++s) {
#pragma unroll
            for (int i = 0; i < 5; ++i) rf[s][i] = LD_SC1(uo + i * 54 * 1024 + s * 1024);
            SBAR();
        }
#pragma unroll
        for (int s = 0; s < 14; ++s) {
            if (s < 13 || w < 2) {
#pragma unroll
                for (int j = 0; j < 4; ++j)
#pragma unroll
                    for (int i = 0; i < 5; ++i) acc[j][i] = MFMA16_32(rf[s % PF][i], wr[s][j], acc[j][i]);
            }
            SBAR();
            if (s + PF < 13 || (s + PF == 13 && w < 2)) {
#pragma unroll
                for (int i = 0; i < 5; ++i) rf[s % PF][i] = LD_SC1(uo + i * 54 * 1024 + (s + PF) * 1024);
                SBAR();
            }
        }
        SST(9);
        ACC_REDUCE();
        float xv[20], sum = 0.f;
#pragma unroll
        for (int i = 0; i < 5; ++i) {
            const int d = 80 * cg + 16 * i + 4 * g;
            const unsigned u0 = pk2(bf_lo(x1o[i][0]) + acc[0][i][0] + bs, bf_hi(x1o[i][0]) + acc[0][i][1] + bs);
            const unsigned u1 = pk2(bf_lo(x1o[i][1]) + acc[0][i][2] + bs, bf_hi(x1o[i][1]) + acc[0][i][3] + bs);
            if (R1) __builtin_amdgcn_raw_buffer_store_b64(u32x2{u0, u1}, rs,
                        (int)OFF_X2 + ((4 * tb + w) * 20 + (d >> 5)) * 1024 + (((d & 31) >> 3) * 16 + n16) * 16 + (d & 7) * 2, 0, 16);
            else *(u32x2*)(out + (size_t)tok * 640 + d) = u32x2{u0, u1};
            xv[4 * i] = bf_lo(u0); xv[4 * i + 1] = bf_hi(u0); xv[4 * i + 2] = bf_lo(u1); xv[4 * i + 3] = bf_hi(u1);
            sum += (xv[4 * i] + xv[4 * i + 1]) + (xv[4 * i + 2] + xv[4 * i + 3]);
        }
        if (R1) {
            const float mw = add32(add16(sum)) * (1.f / 80.f);
            float s2 = 0.f;
#pragma unroll
            for (int i = 0; i < 20; ++i) { const float dd = xv[i] - mw; s2 += dd * dd; }
            s2 = add32(add16(s2));
            if (g == 0) __builtin_amdgcn_raw_buffer_store_b64(u32x2{__float_as_uint(mw), __float_as_uint(s2)}, rs, (int)OFF_XS + ((tb * 8 + cg) * 64 + 16 * w + n16) * 8, 0, 16);
        }
    }
    if (R1) {
        const int mt0 = cg >> 1, isv = cg & 1;
        const bf16x8* wb = (const bf16x8*)(ws + OFF_WQF) + (size_t)(5 * cg * 20 + 5 * w) * 64 + lane;
        const bf16x8* kb = (const bf16x8*)(ws + OFF_WQF) + (size_t)((40 + 2 * isv) * 20 + 5 * w) * 64 + lane;
        bf16x8 wf[5][5], wkf[5][2], xf[5][4], xk[5];
        SBAR();
#pragma unroll
        for (int i = 0; i < 16; ++i) wf[i / 5][i % 5] = wb[((i % 5) * 20 + i / 5) * 64];
        SBAR();
        VMCNT(16);
        SST(10);
        edge_publish(cnt + (16 + tb) * 64);
#pragma unroll
        for (int i = 16; i < 25; ++i) wf[i / 5][i % 5] = wb[((i % 5) * 20 + i / 5) * 64];
#pragma unroll
        for (int ks = 0; ks < 5; ++ks) { wkf[ks][0] = kb[ks * 64]; wkf[ks][1] = kb[(20 + ks) * 64]; }
        f32x4 csq[5], cs0, cs1, stq[4], stk[4], acck[2];
        {
            const float* csp = (const float*)(ws + OFF_CS);
#pragma unroll
            for (int nt = 0; nt < 5; ++nt) csq[nt] = *(const f32x4*)(csp + 80 * cg + 16 * nt + 4 * g);
            cs0 = *(const f32x4*)(csp + 640 + 32 * isv + 4 * g); cs1 = *(const f32x4*)(csp + 640 + 32 * isv + 16);
        }
        SBAR();
        edge_wait(cnt + (16 + tb) * 64, 8);
        SST(11);
        const int xo = (int)OFF_X2 + (tb * 4 * 20 + 5 * w) * 1024 + lane * 16;
#pragma unroll
        for (int ks = 0; ks < 5; ++ks) {
#pragma unroll
            for (int i = 0; i < 4; ++i) xf[ks][i] = LD_SC1(xo + (((i + w) & 3) * 20 + ks) * 1024);
            xk[ks] = LD_SC1(xo + (mt0 * 20 + ks) * 1024);
            SBAR();
        }
#pragma unroll
        for (int i = 0; i < 4; ++i) {
            const u32x2 a0 = __builtin_amdgcn_raw_buffer_load_b64(rs, (int)OFF_XS + ((tb * 8 + 2 * i) * 64 + 16 * w + n16) * 8, 0, AUXL);
            const u32x2 a1 = __builtin_amdgcn_raw_buffer_load_b64(rs, (int)OFF_XS + ((tb * 8 + 2 * i + 1) * 64 + 16 * w + n16) * 8, 0, AUXL);
            const u32x2 c0 = __builtin_amdgcn_raw_buffer_load_b64(rs, (int)OFF_XS + ((tb * 8 + 2 * i) * 64 + 16 * mt0 + n16) * 8, 0, AUXL);
            const u32x2 c1 = __builtin_amdgcn_raw_buffer_load_b64(rs, (int)OFF_XS + ((tb * 8 + 2 * i + 1) * 64 + 16 * mt0 + n16) * 8, 0, AUXL);
            stq[i] = __builtin_bit_cast(f32x4, u32x4{a0[0], a0[1], a1[0], a1[1]});
            stk[i] = __builtin_bit_cast(f32x4, u32x4{c0[0], c0[1], c1[0], c1[1]});
        }
        SBAR();
        ACC_ZERO();
        acck[0] = f32x4{0.f, 0.f, 0.f, 0.f}; acck[1] = acck[0];
#pragma unroll
        for (int ks = 0; ks < 5; ++ks) {
#pragma unroll
            for (int nt = 0; nt < 5; ++nt)
#pragma unroll
                for (int i = 0; i < 4; ++i) acc[i][nt] = MFMA16_32(wf[ks][nt], xf[ks][i], acc[i][nt]);
            acck[0] = MFMA16_32(wkf[ks][0], xk[ks], acck[0]);
            acck[1] = MFMA16_32(wkf[ks][1], xk[ks], acck[1]);
            SBAR();
        }
        SST(12);
        const float z4[4] = {0.f, 0.f, 0.f, 0.f}, z5[5] = {0.f, 0.f, 0.f, 0.f, 0.f}, z2[2] = {0.f, 0.f};
        qkv_finish<true>(ws, L, tb, cg, w, lane, acc, acck, csq, cs0, cs1, stq, stk, z4, z4, z5, z2);
    }
    SST_FLUSHK(ws, R1 ? 2 : 7);
}

template <int N> __global__ void k_end() {}

extern "C" void kernel_launch(void* const* d_in, const int* in_sizes, int n_in, void* d_out, int out_size, void* d_ws,
                              size_t ws_size, hipStream_t stream) {
    const u16* x = (const u16*)d_in[0];
    const u16* wq = (const u16*)d_in[1];
    const u16* wk = (const u16*)d_in[2];
    const u16* wv = (const u16*)d_in[3];
    const u16* wo = (const u16*)d_in[4];
    const u16* w1 = (const u16*)d_in[6];
    const u16* b1 = (const u16*)d_in[7];
    const u16* w2 = (const u16*)d_in[8];
    const u16* b2 = (const u16*)d_in[9];
    char* ws = (char*)d_ws;
    u16* x2 = (u16*)(ws + OFF_X2);
    (void)hipFuncSetAttribute((const void*)k_qkv<false>, hipFuncAttributeMaxDynamicSharedMemorySize, QKV_LDS);
    (void)hipFuncSetAttribute((const void*)k_attn<true>, hipFuncAttributeMaxDynamicSharedMemorySize, ATT_LDS);
    (void)hipFuncSetAttribute((const void*)k_attn<false>, hipFuncAttributeMaxDynamicSharedMemorySize, ATT_LDS);
    (void)hipFuncSetAttribute((const void*)k_mlp<true>, hipFuncAttributeMaxDynamicSharedMemorySize, MLP_LDS);
    (void)hipFuncSetAttribute((const void*)k_mlp<false>, hipFuncAttributeMaxDynamicSharedMemorySize, MLP_LDS);
    (void)hipFuncSetAttribute((const void*)k_end<0>, hipFuncAttributeMaxDynamicSharedMemorySize, 0);
    k_qkv<false><<<256, 256, QKV_LDS, stream>>>(x, wq, wk, wv, wo, ws, w1, w2);
    k_attn<true><<<256, 512, ATT_LDS, stream>>>(ws, w1, w2, wo, wq);
    k_mlp<true><<<256, 256, MLP_LDS, stream>>>(ws, x, b1, b2, nullptr);
    k_attn<false><<<256, 512, ATT_LDS, stream>>>(ws, w1, w2, wo, wq);
    k_mlp<false><<<256, 256, MLP_LDS, stream>>>(ws, x2, b1, b2, (u16*)d_out);
}
```

```cpp
#define NO_L2WARM
#include <hip/hip_runtime.h>
#include <hip/hip_bf16.h>
#include <stdint.h>

typedef unsigned short u16;
typedef short bf16x8 __attribute__((ext_vector_type(8)));
typedef short bf16x4 __attribute__((ext_vector_type(4)));
typedef float f32x16 __attribute__((ext_vector_type(16)));
typedef float f32x4 __attribute__((ext_vector_type(4)));
typedef float f32x2 __attribute__((ext_vector_type(2)));
typedef unsigned u32x4 __attribute__((ext_vector_type(4)));
typedef unsigned u32x2 __attribute__((ext_vector_type(2)));
typedef __bf16 bf2_t __attribute__((ext_vector_type(2)));
typedef __attribute__((address_space(3))) char* lds_ptr;
typedef __attribute__((address_space(3))) const char* lds_cptr;
typedef __attribute__((address_space(1))) unsigned long long gu64;

#define DI __device__ __forceinline__
#define SBAR() __builtin_amdgcn_sched_barrier(0)
#define PIN(x) asm volatile("" : "+v"(x))
#define MFMA32_16(a, b, c) __builtin_amdgcn_mfma_f32_32x32x16_bf16(a, b, c, 0, 0, 0)
#define MFMA32_8(a, b, c) __builtin_amdgcn_mfma_f32_32x32x8bf16_1k(a, b, c, 0, 0, 0)
#define MFMA16_32(a, b, c) __builtin_amdgcn_mfma_f32_16x16x32_bf16(a, b, c, 0, 0, 0)
#define EX2(x) __builtin_amdgcn_exp2f(x)
#define RAW_BARRIER() asm volatile("s_waitcnt lgkmcnt(0)\n\ts_barrier" ::: "memory")
#define VMCNT(n) asm volatile("s_waitcnt vmcnt(%0)" :: "i"(n) : "memory")

#define SST_DECL do {} while (0)
#define SST(i) do {} while (0)
#define SST_FLUSHK(ws, kid) do {} while (0)
#define PREP_STORE(p, v) asm volatile("global_store_dwordx4 %0, %1, off sc1\n\ts_nop 1" :: "v"(p), "v"(v) : "memory")
#define ST8(p, v) (*(u32x2*)(p) = (v))
#define KADDR(dst, sym) asm volatile("s_getpc_b64 s[40:41]\n\ts_add_u32 s40, s40, " sym "@rel32@lo+4\n\ts_addc_u32 s41, s41, " sym "@rel32@hi+12\n\ts_mov_b64 %0, s[40:41]" : "=s"(dst) :: "s40", "s41", "scc")
constexpr int S_ = 2048, D_ = 640, F_ = 1728;
constexpr float LN_EPS = 1e-5f;
constexpr float QSCALE = 0.22360679774997896f * 1.4426950408889634f;
constexpr float SAFE_BOUND = 60.0f;

DI unsigned pk2(float a, float b) { f32x2 v = {a, b}; bf2_t r = __builtin_convertvector(v, bf2_t); return __builtin_bit_cast(unsigned, r); }
DI float bf_lo(unsigned u) { return __uint_as_float(u << 16); }
DI float bf_hi(unsigned u) { return __uint_as_float(u & 0xffff0000u); }
DI float bf2f(u16 v) { return __uint_as_float((unsigned)v << 16); }
DI float dot2(unsigned a, unsigned b, float c) { return __builtin_amdgcn_fdot2_f32_bf16(__builtin_bit_cast(bf2_t, a), __builtin_bit_cast(bf2_t, b), c, false); }
DI bf16x4 tr_read(lds_cptr p) { return __builtin_amdgcn_ds_read_tr16_b64_v4i16((__attribute__((address_space(3))) bf16x4*)p); }
DI bf16x8 cat8(bf16x4 a, bf16x4 b) { return __builtin_shufflevector(a, b, 0, 1, 2, 3, 4, 5, 6, 7); }
DI void glds16(const void* g, lds_ptr l) {
    unsigned keep;
    asm volatile("s_mov_b32 %0, m0\n\ts_mov_b32 m0, %2\n\ts_nop 0\n\tglobal_load_lds_dwordx4 %1, off\n\ts_mov_b32 m0, %0"
                 : "=&s"(keep) : "v"(g), "s"((unsigned)(uintptr_t)l) : "memory");
}
DI float xor32(float v) { return __shfl_xor(v, 32); }
DI float add32(float v) {
    auto r = __builtin_amdgcn_permlane32_swap(__float_as_uint(v), __float_as_uint(v), false, false);
    return __uint_as_float(r[0]) + __uint_as_float(r[1]);
}
DI float add16(float v) {
    auto r = __builtin_amdgcn_permlane16_swap(__float_as_uint(v), __float_as_uint(v), false, false);
    return __uint_as_float(r[0]) + __uint_as_float(r[1]);
}
DI float max16x(float v) { auto r = __builtin_amdgcn_permlane16_swap(__float_as_uint(v), __float_as_uint(v), false, false); return fmaxf(__uint_as_float(r[0]), __uint_as_float(r[1])); }
DI float max32x(float v) { auto r = __builtin_amdgcn_permlane32_swap(__float_as_uint(v), __float_as_uint(v), false, false); return fmaxf(__uint_as_float(r[0]), __uint_as_float(r[1])); }
template <int CTRL> DI float dppf(float v) { return __int_as_float(__builtin_amdgcn_update_dpp(0, __float_as_int(v), CTRL, 0xf, 0xf, false)); }
DI float rowmax16(float v) {
    v = fmaxf(v, dppf<0x128>(v)); v = fmaxf(v, dppf<0x124>(v)); v = fmaxf(v, dppf<0x122>(v)); v = fmaxf(v, dppf<0x121>(v)); return v;
}
DI float wavemax(float v) { return max32x(max16x(rowmax16(v))); }
DI u32x4 gather8(const u16* s, int ld) {
    u32x4 r;
#pragma unroll
    for (int e = 0; e < 4; ++e) r[e] = (unsigned)s[(2 * e) * ld] | ((unsigned)s[(2 * e + 1) * ld] << 16);
    return r;
}

constexpr size_t OFF_Q = 0;
constexpr size_t OFF_K1 = OFF_Q + (size_t)2048 * 32 * 48;
constexpr size_t OFF_K2 = OFF_K1 + 2048 * 32;
constexpr size_t OFF_V = OFF_K2 + 2048 * 8;
constexpr size_t OFF_KN = OFF_V + 128 * 640;
constexpr size_t OFF_QN = OFF_KN + 512;
constexpr size_t OFF_CS = OFF_QN + 4096;
constexpr size_t OFF_CONST = OFF_CS + 4096;
constexpr size_t OFF_CNT = OFF_CONST + 256;
constexpr size_t OFF_XG = OFF_CNT + 48 * 256;
constexpr size_t OFF_XS = OFF_XG + 262144;
constexpr size_t OFF_X1 = OFF_XS + 131072;
constexpr size_t OFF_H = OFF_X1 + (size_t)S_ * D_ * 2;
constexpr size_t OFF_U = OFF_H + (size_t)S_ * D_ * 2;
constexpr size_t OFF_X2 = OFF_U + (size_t)D_ * F_ * 2;
constexpr size_t OFF_O = OFF_X2 + (size_t)S_ * D_ * 2;
constexpr size_t OFF_W1F = OFF_O + (size_t)S_ * D_ * 2;
constexpr size_t OFF_W2F = OFF_W1F + (size_t)S_ * F_ * 2;
constexpr size_t OFF_WQF = OFF_W2F + (size_t)S_ * F_ * 2;
constexpr size_t OFF_WOF = OFF_WQF + (size_t)44 * 20 * 1024;
constexpr int NPREP = 1728 + 1728 + 200 + 200;

DI void chan8(const f32x4 (&st)[4], float& mean, float& var) {
    float m = 0.f, m2 = 0.f;
#pragma unroll
    for (int i = 0; i < 4; ++i) { m += st[i][0] + st[i][2]; m2 += st[i][1] + st[i][3]; }
    m *= 0.125f;
#pragma unroll
    for (int i = 0; i < 4; ++i) { const float d0 = st[i][0] - m, d1 = st[i][2] - m; m2 += 80.f * (d0 * d0 + d1 * d1); }
    mean = m; var = m2 * (1.f / 640.f);
}

template <bool FAST>
DI void qkv_finish(char* __restrict__ ws, char* L, int tb, int cg, int w, int lane, f32x4 (&acc)[4][5], f32x4 (&acck)[2],
                   const f32x4 (&csq)[5], const f32x4& cs0, const f32x4& cs1, const f32x4 (&stq)[4], const f32x4 (&stk)[4],
                   const float (&sm)[4], const float (&sq)[4], const float (&cs)[5], const float (&csk)[2]) {
    const int n16 = lane & 15, g = lane >> 4, mt0 = cg >> 1, isv = cg & 1;
    constexpr int R_OFF = 0, PK_OFF = 61440, ST_OFF = 69632, CS_OFF = 71680;
#pragma unroll
    for (int j = 1; j < 4; ++j) {
        const int o = (w + j) & 3;
#pragma unroll
        for (int nt = 0; nt < 5; ++nt) *(f32x4*)(L + R_OFF + (((o * 3 + (j - 1)) * 5 + nt) * 64 + lane) * 16) = acc[j][nt];
    }
    *(f32x4*)(L + PK_OFF + ((w * 2 + 0) * 64 + lane) * 16) = acck[0];
    *(f32x4*)(L + PK_OFF + ((w * 2 + 1) * 64 + lane) * 16) = acck[1];
    if (!FAST && g == 0) {
#pragma unroll
        for (int i = 0; i < 4; ++i) *(f32x2*)(L + ST_OFF + (w * 64 + ((i + w) & 3) * 16 + n16) * 8) = f32x2{sm[i], sq[i]};
#pragma unroll
        for (int nt = 0; nt < 5; ++nt) *(float*)(L + CS_OFF + w * 448 + (nt * 16 + n16) * 4) = cs[nt];
#pragma unroll
        for (int nt = 0; nt < 2; ++nt) *(float*)(L + CS_OFF + w * 448 + (80 + nt * 16 + n16) * 4) = csk[nt];
    }
    char* qrow = ws + OFF_Q + (size_t)(tb * 8 + cg) * 12288 + (16 * w + n16) * 48;
    int qoff[5];
#pragma unroll
    for (int nt = 0; nt < 5; ++nt) { const int G = 4 * nt + g, hl = G / 5; qoff[nt] = hl * 3072 + (G - 5 * hl) * 8; PIN(qoff[nt]); }
    RAW_BARRIER();
    auto stats = [&](int tl, const f32x4 (&st)[4], float& mean, float& rstd) {
        float var;
        if (FAST) chan8(st, mean, var);
        else {
            float s = 0.f, s2 = 0.f;
#pragma unroll
            for (int ww = 0; ww < 4; ++ww) { f32x2 v = *(const f32x2*)(L + ST_OFF + (ww * 64 + tl) * 8); s += v[0]; s2 += v[1]; }
            mean = s * (1.f / 640.f);
            var = fmaxf(s2 * (1.f / 640.f) - mean * mean, 0.f);
        }
        rstd = __builtin_amdgcn_rsqf(var + LN_EPS);
    };
    {
        f32x4 rr[3][5], cc[4][5];
#pragma unroll
        for (int j = 0; j < 3; ++j)
#pragma unroll
            for (int nt = 0; nt < 5; ++nt) rr[j][nt] = *(const f32x4*)(L + R_OFF + (((w * 3 + j) * 5 + nt) * 64 + lane) * 16);
        if (!FAST) {
#pragma unroll
            for (int ww = 0; ww < 4; ++ww)
#pragma unroll
                for (int nt = 0; nt < 5; ++nt) cc[ww][nt] = *(const f32x4*)(L + CS_OFF + ww * 448 + (nt * 16 + 4 * g) * 4);
        }
        float mean, rstd; stats(16 * w + n16, stq, mean, rstd);
        SBAR();
        const float sc = rstd * QSCALE;
        float n2[5];
#pragma unroll
        for (int nt = 0; nt < 5; ++nt) {
            const f32x4 a = (acc[0][nt] + rr[0][nt]) + (rr[1][nt] + rr[2][nt]);
            f32x4 s;
            if (FAST) s = csq[nt];
            else {
                s = (cc[0][nt] + cc[1][nt]) + (cc[2][nt] + cc[3][nt]);
                if (tb == 0 && w == 0 && n16 == 0) *(f32x4*)(ws + OFF_CS + (80 * cg + 16 * nt + 4 * g) * 4) = s;
            }
            const unsigned u0 = pk2((a[0] - mean * s[0]) * sc, (a[1] - mean * s[1]) * sc);
            const unsigned u1 = pk2((a[2] - mean * s[2]) * sc, (a[3] - mean * s[3]) * sc);
            { const u32x2 v_ = u32x2{u0, u1}; ST8(qrow + qoff[nt], v_); }
            n2[nt] = dot2(u1, u1, dot2(u0, u0, 0.f));
        }
        float hn[4];
        hn[0] = n2[0] + (g == 0 ? n2[1] : 0.f);
        hn[1] = (g > 0 ? n2[1] : 0.f) + (g < 2 ? n2[2] : 0.f);
        hn[2] = (g > 1 ? n2[2] : 0.f) + (g < 3 ? n2[3] : 0.f);
        hn[3] = (g == 3 ? n2[3] : 0.f) + n2[4];
#pragma unroll
        for (int hh = 0; hh < 4; ++hh) hn[hh] = add32(add16(hn[hh]));
        const float n2a = (g & 1) ? hn[1] : hn[0], n2b = (g & 1) ? hn[3] : hn[2];
        const unsigned qb = pk2(__builtin_amdgcn_sqrtf((g & 2) ? n2b : n2a), 0.f);
        { const u32x2 v_ = u32x2{qb, 0u}; ST8(qrow + g * 3072 + 40, v_); }
        float m = bf_lo(qb);
        if (!(m == m)) m = INFINITY;
        m = wavemax(m);
        if (lane == 0) *(float*)(ws + OFF_QN + ((tb * 8 + cg) * 4 + w) * 4) = m;
    }
    if (w == 3) {
        const int t16 = tb * 4 + mt0;
        float mean, rstd; stats(mt0 * 16 + n16, stk, mean, rstd);
        f32x4 a0 = {0.f, 0.f, 0.f, 0.f}, a1 = a0, s0 = a0, s1 = a0;
#pragma unroll
        for (int ww = 0; ww < 4; ++ww) {
            a0 += *(const f32x4*)(L + PK_OFF + ((ww * 2 + 0) * 64 + lane) * 16);
            a1 += *(const f32x4*)(L + PK_OFF + ((ww * 2 + 1) * 64 + n16) * 16);
            if (!FAST) {
                s0 += *(const f32x4*)(L + CS_OFF + ww * 448 + (80 + 4 * g) * 4);
                s1 += *(const f32x4*)(L + CS_OFF + ww * 448 + 96 * 4);
            }
        }
        if (FAST) { s0 = cs0; s1 = cs1; }
        else if (tb == 0 && cg < 2 && n16 == 0) {
            *(f32x4*)(ws + OFF_CS + (640 + 32 * isv + 4 * g) * 4) = s0;
            if (g == 0) *(f32x4*)(ws + OFF_CS + (640 + 32 * isv + 16) * 4) = s1;
        }
        const unsigned u0 = pk2((a0[0] - mean * s0[0]) * rstd, (a0[1] - mean * s0[1]) * rstd);
        const unsigned u1 = pk2((a0[2] - mean * s0[2]) * rstd, (a0[3] - mean * s0[3]) * rstd);
        const unsigned u2 = pk2((a1[0] - mean * s1[0]) * rstd, (a1[1] - mean * s1[1]) * rstd);
        const unsigned u3 = pk2((a1[2] - mean * s1[2]) * rstd, (a1[3] - mean * s1[3]) * rstd);
        if (!isv) {
            const int key = t16 * 16 + n16;
            *(u32x2*)(ws + OFF_K1 + key * 32 + g * 8) = u32x2{u0, u1};
            if (g == 0) *(u32x2*)(ws + OFF_K2 + key * 8) = u32x2{u2, u3};
            float n2 = dot2(u1, u1, dot2(u0, u0, 0.f));
            n2 = add32(add16(n2));
            n2 = dot2(u3, u3, dot2(u2, u2, n2));
            if (!(n2 == n2)) n2 = INFINITY;
            n2 = rowmax16(n2);
            if (lane == 0) *(float*)(ws + OFF_KN + t16 * 4) = n2;
        } else {
            u16* vb = (u16*)(ws + OFF_V + t16 * 640 + (n16 >> 3) * 320) + (n16 & 7);
            vb[(4 * g + 0) * 8] = (u16)u0; vb[(4 * g + 1) * 8] = (u16)(u0 >> 16);
            vb[(4 * g + 2) * 8] = (u16)u1; vb[(4 * g + 3) * 8] = (u16)(u1 >> 16);
            if (g == 0) { vb[16 * 8] = (u16)u2; vb[17 * 8] = (u16)(u2 >> 16); vb[18 * 8] = (u16)u3; vb[19 * 8] = (u16)(u3 >> 16); }
        }
    }
    if (w == 0) *(u32x2*)(ws + OFF_XG + ((size_t)(tb * 64 + lane) * 8 + cg) * 8) = u32x2{0u, 0u};
}

constexpr int QKV_LDS = 163840;
template <bool FAST>
__global__ __launch_bounds__(256, 1) void k_qkv(const u16* __restrict__ x, const u16* __restrict__ wq, const u16* __restrict__ wk,
                                                const u16* __restrict__ wv, const u16* __restrict__ wo, char* __restrict__ ws,
                                                const u16* __restrict__ w1, const u16* __restrict__ w2) {
    extern __shared__ __attribute__((aligned(16))) char lds_raw[];
    lds_ptr lds = (lds_ptr)lds_raw;
    char* L = lds_raw;
    const int tid = threadIdx.x, lane = tid & 63, w = __builtin_amdgcn_readfirstlane(tid >> 6);
    const int b = blockIdx.x, slot = b >> 3, tb = ((b & 7) >> 1) * 8 + (slot >> 2), cg = (b & 1) * 4 + (slot & 3);
    const int mt0 = cg >> 1, isv = cg & 1;
    const int n16 = lane & 15, g = lane >> 4;
    SST_DECL; SST(0);
    unsigned code_touch = 0u, wtouch[2] = {0u, 0u};
    if (!FAST && w == 2) {
        uint64_t a[5];
        KADDR(a[0], "_Z6k_attnILb1EEvPcPKtS2_S2_S2_"); KADDR(a[1], "_Z6k_attnILb0EEvPcPKtS2_S2_S2_");
        KADDR(a[2], "_Z5k_mlpILb1EEvPcPKtS2_S2_Pt"); KADDR(a[3], "_Z5k_mlpILb0EEvPcPKtS2_S2_Pt"); KADDR(a[4], "_Z5k_endILi0EEvv");
        uint64_t lo = a[0], hi = a[0];
#pragma unroll
        for (int i = 1; i < 5; ++i) { lo = a[i] < lo ? a[i] : lo; hi = a[i] > hi ? a[i] : hi; }
        const uint64_t pa = lo + (uint64_t)(slot * 64 + lane) * 128;
        if (pa < hi) code_touch = *(const __attribute__((address_space(1))) unsigned*)pa;
        SBAR();
    }
    f32x4 acc[4][5], acck[2];
#pragma unroll
    for (int i = 0; i < 4; ++i)
#pragma unroll
        for (int j = 0; j < 5; ++j) acc[i][j] = f32x4{0.f, 0.f, 0.f, 0.f};
    acck[0] = f32x4{0.f, 0.f, 0.f, 0.f}; acck[1] = acck[0];
    f32x4 csq[5], cs0, cs1, stq[4], stk[4];
    float sm[4] = {0.f, 0.f, 0.f, 0.f}, sq[4] = {0.f, 0.f, 0.f, 0.f};
    float cs[5] = {0.f, 0.f, 0.f, 0.f, 0.f}, csk[2] = {0.f, 0.f};
    if (FAST) {
        const bf16x8* xb = (const bf16x8*)x + (size_t)(tb * 4 * 20 + 5 * w) * 64 + lane;
        const bf16x8* wb = (const bf16x8*)(ws + OFF_WQF) + (size_t)(5 * cg * 20 + 5 * w) * 64 + lane;
        const bf16x8* kb = (const bf16x8*)(ws + OFF_WQF) + (size_t)((40 + 2 * isv) * 20 + 5 * w) * 64 + lane;
        bf16x8 xf[5][4], xk[5], wf[5][5], wkf[5][2];
#define QKV_LD(ks) do { _Pragma("unroll") for (int i = 0; i < 4; ++i) xf[ks][i] = xb[(((i + w) & 3) * 20 + (ks)) * 64]; \
        _Pragma("unroll") for (int nt = 0; nt < 5; ++nt) wf[ks][nt] = wb[(nt * 20 + (ks)) * 64]; \
        xk[ks] = xb[(mt0 * 20 + (ks)) * 64]; wkf[ks][0] = kb[(ks) * 64]; wkf[ks][1] = kb[(20 + (ks)) * 64]; SBAR(); } while (0)
        QKV_LD(0); QKV_LD(1); QKV_LD(2); QKV_LD(3);
#pragma unroll
        for (int ks = 0; ks < 5; ++ks) {
#pragma unroll
            for (int nt = 0; nt < 5; ++nt)
#pragma unroll
                for (int i = 0; i < 4; ++i) acc[i][nt] = MFMA16_32(wf[ks][nt], xf[ks][i], acc[i][nt]);
            acck[0] = MFMA16_32(wkf[ks][0], xk[ks], acck[0]);
            acck[1] = MFMA16_32(wkf[ks][1], xk[ks], acck[1]);
            SBAR();
            if (ks == 0) {
                QKV_LD(4);
                const float* csp = (const float*)(ws + OFF_CS);
#pragma unroll
                for (int nt = 0; nt < 5; ++nt) csq[nt] = *(const f32x4*)(csp + 80 * cg + 16 * nt + 4 * g);
                cs0 = *(const f32x4*)(csp + 640 + 32 * isv + 4 * g); cs1 = *(const f32x4*)(csp + 640 + 32 * isv + 16);
                const f32x4* sp = (const f32x4*)(ws + OFF_XS) + (size_t)(tb * 64 + n16) * 4;
#pragma unroll
                for (int i = 0; i < 4; ++i) { stq[i] = sp[16 * w * 4 + i]; stk[i] = sp[16 * mt0 * 4 + i]; }
                SBAR();
            }
        }
    } else {
        lds_ptr ra = lds + w * 40960, rb = ra + 20480;
        const char* xs = (const char*)(x + (size_t)(tb * 64) * 640 + 160 * w);
#pragma unroll
        for (int j = 0; j < 5; ++j) {
            const int c = 64 * j + lane;
            const unsigned xo = (unsigned)(c / 20) * 1280u + (unsigned)(c % 20) * 16u;
#pragma unroll
            for (int i = 0; i < 4; ++i) glds16(xs + (size_t)i * 20480 + xo, ra + i * 5120 + j * 1024);
        }
        const char* src = (const char*)(wq + (size_t)(160 * w) * 640 + 80 * cg);
        const unsigned loff = (unsigned)(lane / 10) * 1280u + (unsigned)(lane % 10) * 16u;
        const char* src2 = (const char*)((isv ? wv : wk) + (size_t)(160 * w) * 20);
#pragma unroll
        for (int i = 0; i < 16; ++i) glds16(src + (size_t)i * 7680 + loff, rb + i * 960);
#pragma unroll
        for (int i = 0; i < 3; ++i) glds16(src2 + i * 1024 + lane * 16, rb + 15488 + i * 1024);
        if (lane < 48) glds16(src2 + 3 * 1024 + lane * 16, rb + 15488 + 3 * 1024);
        SST(1);
        VMCNT(20);
        bf16x8 xf[4][5], xk[5];
        {
            const char* xl = L + w * 40960 + n16 * 320 + g * 16;
#pragma unroll
            for (int i = 0; i < 4; ++i)
#pragma unroll
                for (int ks = 0; ks < 5; ++ks) xf[i][ks] = *(const bf16x8*)(xl + ((i + w) & 3) * 5120 + ks * 64);
#pragma unroll
            for (int ks = 0; ks < 5; ++ks) xk[ks] = *(const bf16x8*)(xl + mt0 * 5120 + ks * 64);
        }
        asm volatile("s_waitcnt lgkmcnt(0)" ::: "memory");
        SBAR();
#pragma unroll
        for (int i = 0; i < 10; ++i) glds16(src + (size_t)(16 + i) * 7680 + loff, ra + i * 960);
        if (lane < 40) glds16(src + (size_t)26 * 7680 + loff, ra + 10 * 960);
#pragma unroll
        for (int i = 0; i < 2; ++i) glds16(src2 + 3840 + i * 1024 + lane * 16, ra + 10240 + i * 1024);
        if (lane < 32) glds16(src2 + 3840 + 2 * 1024 + lane * 16, ra + 10240 + 2 * 1024);
        SST(2);
        const unsigned ONES = 0x3f803f80u;
#pragma unroll
        for (int i = 0; i < 4; ++i)
#pragma unroll
            for (int ks = 0; ks < 5; ++ks) {
                u32x4 u = __builtin_bit_cast(u32x4, xf[i][ks]);
#pragma unroll
                for (int e = 0; e < 4; ++e) { sm[i] = dot2(u[e], ONES, sm[i]); sq[i] = dot2(u[e], u[e], sq[i]); }
            }
        SBAR();
        VMCNT(14);
        SST(3);
        const int q4 = (lane & 15) >> 2, p4 = lane & 3;
        lds_cptr wb0 = (lds_cptr)rb + (8 * g + q4) * 160 + p4 * 8, wb1 = (lds_cptr)ra + (8 * g + q4) * 160 + p4 * 8 - 3 * 5120;
        lds_cptr kb0 = (lds_cptr)rb + 15488 + (8 * g + q4) * 40 + p4 * 8, kb1 = (lds_cptr)ra + 10240 + (8 * g + q4) * 40 + p4 * 8 - 3 * 1280;
        bf16x8 wf[2][7];
#define QKV_TR(ks, s_) do { lds_cptr wb = (ks) < 3 ? wb0 : wb1, kb = (ks) < 3 ? kb0 : kb1; \
        _Pragma("unroll") for (int nt = 0; nt < 5; ++nt) wf[s_][nt] = cat8(tr_read(wb + (ks) * 5120 + nt * 32), tr_read(wb + (ks) * 5120 + 640 + nt * 32)); \
        _Pragma("unroll") for (int nt = 0; nt < 2; ++nt) wf[s_][5 + nt] = cat8(tr_read(kb + (ks) * 1280 + nt * 32), tr_read(kb + (ks) * 1280 + 160 + nt * 32)); SBAR(); } while (0)
        QKV_TR(0, 0);
#pragma unroll
        for (int ks = 0; ks < 5; ++ks) {
            if (ks == 2) {
                VMCNT(0); SBAR();
            }
            if (ks < 4) QKV_TR(ks + 1, (ks + 1) & 1);
#pragma unroll
            for (int nt = 0; nt < 5; ++nt) {
                u32x4 u = __builtin_bit_cast(u32x4, wf[ks & 1][nt]);
#pragma unroll
                for (int e = 0; e < 4; ++e) cs[nt] = dot2(u[e], ONES, cs[nt]);
#pragma unroll
                for (int i = 0; i < 4; ++i) acc[i][nt] = MFMA16_32(wf[ks & 1][nt], xf[i][ks], acc[i][nt]);
            }
#pragma unroll
            for (int nt = 0; nt < 2; ++nt) {
                u32x4 u = __builtin_bit_cast(u32x4, wf[ks & 1][5 + nt]);
#pragma unroll
                for (int e = 0; e < 4; ++e) csk[nt] = dot2(u[e], ONES, csk[nt]);
                acck[nt] = MFMA16_32(wf[ks & 1][5 + nt], xk[ks], acck[nt]);
            }
            SBAR();
        }
#pragma unroll
        for (int i = 0; i < 4; ++i) { sm[i] = add32(add16(sm[i])); sq[i] = add32(add16(sq[i])); }
#pragma unroll
        for (int i = 0; i < 5; ++i) cs[i] = add32(add16(cs[i]));
#pragma unroll
        for (int i = 0; i < 2; ++i) csk[i] = add32(add16(csk[i]));
    }
    SST(4);
    if (!FAST) RAW_BARRIER();
    qkv_finish<FAST>(ws, L, tb, cg, w, lane, acc, acck, csq, cs0, cs1, stq, stk, sm, sq, cs, csk);
    if (!FAST && w == 1) {
        if (b < 80) {
            const int ks = b >> 2, ct = b & 3; int cn = 16 * (ct & 1) + n16; if (cn >= 20) cn = 0;
            *(u32x4*)(ws + OFF_WQF + (size_t)((40 + ct) * 20 + ks) * 1024 + lane * 16) =
                gather8(((ct & 2) ? wv : wk) + (size_t)(32 * ks + 8 * g) * 20 + cn, 20);
        } else if (b == 80 && lane < 4) {
            const unsigned v = (lane & 2) ? 0x3f803f80u : 0u;
            *(u32x4*)(ws + OFF_CONST + lane * 16) = u32x4{v, v, v, v};
        }
    }
    PIN(code_touch); PIN(wtouch[0]); PIN(wtouch[1]);
    SST(7); SST_FLUSHK(ws, FAST ? 5 : 0);
}

constexpr int PJ0 = 1, PJ1 = 8, PJD = 5;
constexpr int PBW = 2688, ATT_LDS = 32 * PBW;
template <bool PREP>
__global__ __launch_bounds__(512, 2) void k_attn(char* __restrict__ ws, const u16* __restrict__ w1, const u16* __restrict__ w2,
                                                 const u16* __restrict__ wo, const u16* __restrict__ wq) {
    extern __shared__ __attribute__((aligned(16))) char lds_raw[];
    char* L = lds_raw;
    const int tid = threadIdx.x, lane = tid & 63, w = __builtin_amdgcn_readfirstlane(tid >> 6);
    const int b = blockIdx.x, slot = b >> 3, tb = ((b & 7) >> 1) * 8 + (slot >> 2), cg = (b & 1) * 4 + (slot & 3);
    const int r32 = lane & 31, h = lane >> 5, n16 = lane & 15, g = lane >> 4;
    const int cgp = w & 1, kq = w >> 1;
    SST_DECL; SST(0);
    float kn0 = *(const float*)(ws + OFF_KN + lane * 4), kn1 = *(const float*)(ws + OFF_KN + 256 + lane * 4);
    float qmax = *(const float*)(ws + OFF_QN + ((tb * 8 + cg) * 4 + (lane & 3)) * 4);
    bf16x8 q1[4]; bf16x4 q2[4];
    {
        const char* qg = ws + OFF_Q + (size_t)(tb * 8 + cg) * 12288 + cgp * 6144 + r32 * 48;
#pragma unroll
        for (int j = 0; j < 4; ++j) { q1[j] = *(const bf16x8*)(qg + j * 1536 + 16 * h); q2[j] = *(const bf16x4*)(qg + j * 1536 + 32 + 8 * h); }
    }
    SBAR();
    const int kr = (r32 & 19) | ((r32 & 4) << 1) | ((r32 & 8) >> 1);
    const unsigned ko1 = (unsigned)OFF_K1 + (unsigned)(512 * kq + kr) * 32u + 16u * h;
    const unsigned ko2 = h ? (unsigned)OFF_CONST + 32u : (unsigned)OFF_K2 + (unsigned)(512 * kq + kr) * 8u;
    const unsigned ks2 = h ? 0u : 256u;
    const unsigned vo = r32 < 20 ? (unsigned)OFF_V + (unsigned)(32 * kq) * 640u + (unsigned)(h * 20 + r32) * 16u
                                 : (unsigned)OFF_CONST + (r32 == 20 ? 32u : 0u);
    const unsigned vs = r32 < 20 ? 640u : 0u;
    bf16x8 k1[3], v0[3], v1[3]; bf16x4 k2[3];
#define K_LOAD(t) do { k1[(t) % 3] = *(const bf16x8*)(ws + ko1 + (t) * 1024); k2[(t) % 3] = *(const bf16x4*)(ws + (ko2 + (t) * ks2)); } while (0)
#define V_LOAD(t) do { v0[(t) % 3] = *(const bf16x8*)(ws + (vo + (2 * (t)) * vs)); v1[(t) % 3] = *(const bf16x8*)(ws + (vo + (2 * (t) + 1) * vs)); } while (0)
    K_LOAD(0); V_LOAD(0); SBAR(); K_LOAD(1); V_LOAD(1); SBAR();
    PIN(kn0); PIN(kn1);
    const float kmax2 = wavemax(fmaxf(kn0, kn1));
    qmax = rowmax16(qmax);
    const float kb = __builtin_amdgcn_sqrtf(kmax2);
    const bool safe = __builtin_amdgcn_readfirstlane((int)(qmax * kb <= SAFE_BOUND)) != 0;
    const f32x16 zero16 = {0.f, 0.f, 0.f, 0.f, 0.f, 0.f, 0.f, 0.f, 0.f, 0.f, 0.f, 0.f, 0.f, 0.f, 0.f, 0.f};
    if (safe) {
        if (h) {
#pragma unroll
            for (int j = 0; j < 4; ++j) q2[j][0] = (short)pk2(-bf2f((u16)q2[j][0]) * kb, 0.f);
        }
    } else {
        float m[4];
#pragma unroll
        for (int j = 0; j < 4; ++j) { m[j] = -INFINITY; if (h) q2[j] = bf16x4{0, 0, 0, 0}; }
        for (int t = 0; t < 16; ++t) {
            const bf16x8 ka = *(const bf16x8*)(ws + ko1 + t * 1024);
            const bf16x4 kc = *(const bf16x4*)(ws + (ko2 + t * ks2));
#pragma unroll
            for (int j = 0; j < 4; ++j) {
                f32x16 s = MFMA32_16(ka, q1[j], zero16);
                SBAR(); asm volatile("s_nop 15" ::: "memory"); SBAR();
                s = MFMA32_8(kc, q2[j], s);
#pragma unroll
                for (int e = 0; e < 16; ++e) m[j] = fmaxf(m[j], s[e]);
                SBAR();
            }
        }
#pragma unroll
        for (int j = 0; j < 4; ++j) {
            const float mm = fmaxf(m[j], xor32(m[j]));
            if (h == 0) *(float*)(L + ((w * 4 + j) * 32 + r32) * 4) = mm;
        }
        RAW_BARRIER();
#pragma unroll
        for (int j = 0; j < 4; ++j) {
            float mm = -INFINITY;
#pragma unroll
            for (int qq = 0; qq < 4; ++qq) mm = fmaxf(mm, *(const float*)(L + (((2 * qq + cgp) * 4 + j) * 32 + r32) * 4));
            const unsigned hi = __float_as_uint(mm) & 0xffff0000u;
            const float r1 = mm - __uint_as_float(hi);
            const unsigned mid = __float_as_uint(r1) & 0xffff0000u;
            const float r2 = r1 - __uint_as_float(mid);
            const unsigned lo = __float_as_uint(r2) & 0xffff0000u;
            if (h) q2[j] = __builtin_bit_cast(bf16x4, u32x2{hi ^ 0x80000000u, ((mid ^ 0x80000000u) >> 16) | (lo ^ 0x80000000u)});
        }
        RAW_BARRIER();
    }
    SST(1);
    f32x16 o[4], sa, sb;
#pragma unroll
    for (int j = 0; j < 4; ++j) o[j] = zero16;
    u32x4 p1prev = {0u, 0u, 0u, 0u};
    f32x4 ea, eb;
    sa = MFMA32_16(k1[0], q1[0], zero16);
    SBAR(); asm volatile("s_nop 15" ::: "memory"); SBAR();
    sa = MFMA32_8(k2[0], q2[0], sa);
    SBAR(); asm volatile("s_nop 15" ::: "memory"); SBAR();
    u32x4 prep[4]; char* prep_dst = nullptr; int prep_st = 0;
    unsigned l2t[3] = {0u, 0u, 0u};
#pragma unroll
    for (int t = 0; t < 16; ++t) {
#pragma unroll
        for (int j = 0; j < 4; ++j) {
            const int it = 4 * t + j;
            f32x16& cur = (it & 1) ? sb : sa; f32x16& nxt = (it & 1) ? sa : sb;
            const int tn = j < 3 ? t : t + 1, jn = (j + 1) & 3;
            const int tp = j > 0 ? t : t - 1, jp = (j + 3) & 3;
            u32x4 p0, p1;
            if (j == 0 && t + 2 < 16) K_LOAD(t + 2);
            if (j == 1 && t + 2 < 16) V_LOAD(t + 2);
            if (PREP && j == 2 && (t == PJ0 || t == PJ1)) {
                int id = (t == PJ1 ? 2048 : 0) + b * 8 + w; if (id >= NPREP) id -= 2048;
                const u16* src; int ld;
                if (id < 1728) { const int ks = id / 27, c4 = id - ks * 27; src = w1 + (size_t)(32 * ks) * F_ + 64 * c4; ld = F_;
                                 prep_dst = ws + OFF_W1F + (size_t)(4 * c4 * 64 + ks) * 1024; prep_st = 64 * 1024; }
                else if (id < 3456) { const int jj = id - 1728, ks = jj >> 5, c4 = jj & 31; src = w2 + (size_t)(32 * ks) * S_ + 64 * c4; ld = S_;
                                      prep_dst = ws + OFF_W2F + (size_t)(4 * c4 * 54 + ks) * 1024; prep_st = 54 * 1024; }
                else { int jj = id - 3456; const bool isq = jj >= 200; if (isq) jj -= 200; const int ks = jj / 10, c4 = jj - ks * 10;
                       src = (isq ? wq : wo) + (size_t)(32 * ks) * 640 + 64 * c4; ld = 640;
                       prep_dst = ws + (isq ? OFF_WQF : OFF_WOF) + (size_t)(4 * c4 * 20 + ks) * 1024; prep_st = 20 * 1024; }
                src += (size_t)(lane >> 3) * ld + (lane & 7) * 8;
#pragma unroll
                for (int i = 0; i < 4; ++i) prep[i] = *(const u32x4*)(src + (size_t)(8 * i) * ld);
            }
            if (PREP && j == 2 && (t == PJ0 + PJD || t == PJ1 + PJD)) {
#pragma unroll
                for (int i = 0; i < 4; ++i) *(u32x4*)(L + w * 4096 + i * 1024 + lane * 16) = prep[i];
            }
            if (PREP && j == 3 && (t == PJ0 + PJD || t == PJ1 + PJD)) {
                lds_cptr tp_ = (lds_cptr)(lds_ptr)L + w * 4096 + (8 * g + (n16 >> 2)) * 128 + (lane & 3) * 8;
#pragma unroll
                for (int i = 0; i < 4; ++i) prep[i] = __builtin_bit_cast(u32x4, cat8(tr_read(tp_ + i * 32), tr_read(tp_ + 512 + i * 32)));
            }
            if (PREP && j == 0 && (t == PJ0 + PJD + 1 || t == PJ1 + PJD + 1)) {
#pragma unroll
                for (int i = 0; i < 4; ++i) PREP_STORE((u32x4*)(prep_dst + (size_t)i * prep_st + lane * 16), prep[i]);
            }
            SBAR();
            if (it < 63) nxt = MFMA32_16(k1[tn % 3], q1[jn], zero16);
            SBAR();
            p0[0] = pk2(EX2(cur[0]), EX2(cur[1])); p0[1] = pk2(EX2(cur[2]), EX2(cur[3]));
            PIN(p0); SBAR();
            if (it > 0) o[jp] = MFMA32_16(v1[(tp + 3) % 3], __builtin_bit_cast(bf16x8, p1prev), o[jp]);
            SBAR();
            p0[2] = pk2(EX2(cur[4]), EX2(cur[5])); p0[3] = pk2(EX2(cur[6]), EX2(cur[7]));
            PIN(p0); SBAR();
            if (it < 63) nxt = MFMA32_8(k2[tn % 3], q2[jn], nxt);
            SBAR();
            p1[0] = pk2(EX2(cur[8]), EX2(cur[9])); p1[1] = pk2(EX2(cur[10]), EX2(cur[11]));
            PIN(p1); SBAR();
            o[j] = MFMA32_16(v0[t % 3], __builtin_bit_cast(bf16x8, p0), o[j]);
            SBAR();
            p1[2] = pk2(EX2(cur[12]), EX2(cur[13])); p1[3] = pk2(EX2(cur[14]), EX2(cur[15]));
            PIN(p1); SBAR();
            p1prev = p1;
        }
        if (t == 7) SST(2);
    }
    o[3] = MFMA32_16(v1[15 % 3], __builtin_bit_cast(bf16x8, p1prev), o[3]);
    SST(3);
    if (PREP) RAW_BARRIER();
#pragma unroll
    for (int j = 0; j < 4; ++j) {
        char* pb = L + (kq * 8 + cgp * 4 + j) * PBW;
        *(f32x4*)(pb + lane * 16) = f32x4{o[j][0], o[j][1], o[j][2], o[j][3]};
        *(f32x4*)(pb + 1024 + lane * 16) = f32x4{o[j][4], o[j][5], o[j][6], o[j][7]};
        if (h == 0) *(f32x4*)(pb + 2048 + r32 * 16) = f32x4{o[j][8], o[j][9], o[j][10], o[j][11]};
        else *(float*)(pb + 2560 + r32 * 4) = o[j][8];
    }
    RAW_BARRIER();
    SST(4);
    {
        const char* pb = L + w * PBW;
        f32x4 a0 = {0.f, 0.f, 0.f, 0.f}, a1 = a0, a2 = a0; float l = 0.f;
#pragma unroll
        for (int qq = 0; qq < 4; ++qq) {
            a0 += *(const f32x4*)(pb + qq * 8 * PBW + lane * 16);
            a1 += *(const f32x4*)(pb + qq * 8 * PBW + 1024 + lane * 16);
            a2 += *(const f32x4*)(pb + qq * 8 * PBW + 2048 + r32 * 16);
            l += *(const float*)(pb + qq * 8 * PBW + 2560 + r32 * 4);
        }
        const float inv = __builtin_amdgcn_rcpf(l);
        const int tl = 32 * (w & 1) + r32, hd0 = 80 * cg + 20 * (w >> 1);
        char* ob = ws + OFF_O + (size_t)(tb * 4 + (tl >> 4)) * 20480 + (tl & 15) * 16;
        auto st = [&](int hd, const f32x4& a) {
            const u32x2 v_ = u32x2{pk2(a[0] * inv, a[1] * inv), pk2(a[2] * inv, a[3] * inv)};
            ST8(ob + (hd >> 5) * 1024 + ((hd & 31) >> 3) * 256 + (hd & 7) * 2, v_);
        };
        st(hd0 + 4 * h, a0); st(hd0 + 8 + 4 * h, a1);
        if (h == 0) st(hd0 + 16, a2);
    }
    if (b == 0 && tid < 48) *(unsigned*)(ws + OFF_CNT + tid * 256) = 0u;
    PIN(l2t[0]); PIN(l2t[1]); PIN(l2t[2]);
    SST(5); SST_FLUSHK(ws, PREP ? 1 : 6);
}

template <bool XF>
__global__ __launch_bounds__(256, 1) void k_wo(char* __restrict__ ws, const u16* __restrict__ xin) {
    __shared__ __attribute__((aligned(16))) char L[61440];
    const int tid = threadIdx.x, lane = tid & 63, w = __builtin_amdgcn_readfirstlane(tid >> 6);
    const int b = blockIdx.x, slot = b >> 3, tb = ((b & 7) >> 1) * 8 + (slot >> 2), cg = (b & 1) * 4 + (slot & 3);
    const int n16 = lane & 15, g = lane >> 4;
    SST_DECL; SST(0);
    f32x4 acc[4][5];
#pragma unroll
    for (int i = 0; i < 4; ++i)
#pragma unroll
        for (int j = 0; j < 5; ++j) acc[i][j] = f32x4{0.f, 0.f, 0.f, 0.f};
    const int tok = tb * 64 + 16 * w + n16;
    u32x2 xi[5];
    {
        const bf16x8* xb = (const bf16x8*)(ws + OFF_O) + (size_t)(tb * 4 * 20 + 5 * w) * 64 + lane;
        const bf16x8* wb = (const bf16x8*)(ws + OFF_WOF) + (size_t)(5 * cg * 20 + 5 * w) * 64 + lane;
        bf16x8 xf[5][4], wf[5][5];
#define WO_LD(ks) do { _Pragma("unroll") for (int i = 0; i < 4; ++i) xf[ks][i] = xb[(((i + w) & 3) * 20 + (ks)) * 64]; \
        _Pragma("unroll") for (int nt = 0; nt < 5; ++nt) wf[ks][nt] = wb[(nt * 20 + (ks)) * 64]; SBAR(); } while (0)
        WO_LD(0); WO_LD(1); WO_LD(2); WO_LD(3);
#pragma unroll
        for (int ks = 0; ks < 5; ++ks) {
#pragma unroll
            for (int nt = 0; nt < 5; ++nt)
#pragma unroll
                for (int i = 0; i < 4; ++i) acc[i][nt] = MFMA16_32(wf[ks][nt], xf[ks][i], acc[i][nt]);
            SBAR();
            if (ks == 0) {
                WO_LD(4);
#pragma unroll
                for (int nt = 0; nt < 5; ++nt) {
                    const int d = 80 * cg + 16 * nt + 4 * g;
                    if (XF) xi[nt] = *(const u32x2*)(xin + ((size_t)((tok >> 4) * 20 + (d >> 5)) * 64 + ((d & 31) >> 3) * 16 + n16) * 8 + (d & 7));
                    else xi[nt] = *(const u32x2*)(xin + (size_t)tok * 640 + d);
                }
                SBAR();
            }
        }
    }
    SST(1);
#pragma unroll
    for (int j = 1; j < 4; ++j) {
        const int o = (w + j) & 3;
#pragma unroll
        for (int nt = 0; nt < 5; ++nt) *(f32x4*)(L + (((o * 3 + (j - 1)) * 5 + nt) * 64 + lane) * 16) = acc[j][nt];
    }
    RAW_BARRIER();
    SST(2);
    float xv[20]; u32x2 x1o[5];
    {
        f32x4 rr[3][5];
#pragma unroll
        for (int j = 0; j < 3; ++j)
#pragma unroll
            for (int nt = 0; nt < 5; ++nt) rr[j][nt] = *(const f32x4*)(L + (((w * 3 + j) * 5 + nt) * 64 + lane) * 16);
        SBAR();
        float s = 0.f;
#pragma unroll
        for (int nt = 0; nt < 5; ++nt) {
            const f32x4 a = (acc[0][nt] + rr[0][nt]) + (rr[1][nt] + rr[2][nt]);
            const unsigned u0 = pk2(bf_lo(xi[nt][0]) + a[0], bf_hi(xi[nt][0]) + a[1]);
            const unsigned u1 = pk2(bf_lo(xi[nt][1]) + a[2], bf_hi(xi[nt][1]) + a[3]);
            x1o[nt] = u32x2{u0, u1};
            xv[4 * nt] = bf_lo(u0); xv[4 * nt + 1] = bf_hi(u0); xv[4 * nt + 2] = bf_lo(u1); xv[4 * nt + 3] = bf_hi(u1);
            s += (xv[4 * nt] + xv[4 * nt + 1]) + (xv[4 * nt + 2] + xv[4 * nt + 3]);
        }
        const float mw = add32(add16(s)) * (1.f / 80.f);
        float s2 = 0.f;
#pragma unroll
        for (int i = 0; i < 20; ++i) { const float dd = xv[i] - mw; s2 += dd * dd; }
        s2 = add32(add16(s2));
        gu64* gp = (gu64*)(ws + OFF_XG) + (size_t)tok * 8 + cg;
        if (g == 0) __hip_atomic_store(gp, ((unsigned long long)(__float_as_uint(s2) | 0x80000000u) << 32) | __float_as_uint(mw),
                                       __ATOMIC_RELAXED, __HIP_MEMORY_SCOPE_AGENT);
    }
    SST(3);
    float mean, var;
    {
        gu64* gp = (gu64*)(ws + OFF_XG) + (size_t)tok * 8 + 2 * g;
        unsigned long long v0, v1;
        for (int spin = 0; spin < (1 << 20); ++spin) {
            v0 = __hip_atomic_load(gp, __ATOMIC_RELAXED, __HIP_MEMORY_SCOPE_AGENT);
            v1 = __hip_atomic_load(gp + 1, __ATOMIC_RELAXED, __HIP_MEMORY_SCOPE_AGENT);
            if (__all((long long)(v0 & v1) < 0)) break;
            __builtin_amdgcn_s_sleep(4);
        }
        const float m0 = __uint_as_float((unsigned)v0), m1 = __uint_as_float((unsigned)v1);
        mean = add32(add16(m0 + m1)) * 0.125f;
        const float d0 = m0 - mean, d1 = m1 - mean;
        var = add32(add16(__uint_as_float((unsigned)(v0 >> 32) & 0x7fffffffu) + __uint_as_float((unsigned)(v1 >> 32) & 0x7fffffffu) +
                          80.f * (d0 * d0 + d1 * d1))) * (1.f / 640.f);
    }
    SST(4);
    {
        const float r1 = __builtin_amdgcn_rsqf(var + LN_EPS);
        const float r2 = r1 * __builtin_amdgcn_rsqf(var * r1 * r1 + LN_EPS);
        char* hb = L + w * 15360;
#pragma unroll
        for (int nt = 0; nt < 5; ++nt)
            *(u32x2*)(hb + n16 * 176 + (16 * nt + 4 * g) * 2) =
                u32x2{pk2((xv[4 * nt] - mean) * r2, (xv[4 * nt + 1] - mean) * r2), pk2((xv[4 * nt + 2] - mean) * r2, (xv[4 * nt + 3] - mean) * r2)};
        asm volatile("s_waitcnt lgkmcnt(0)" ::: "memory");
        const int q4 = (lane & 15) >> 2, p4 = lane & 3;
        lds_cptr hr = (lds_cptr)(lds_ptr)L + w * 15360 + (8 * (g & 1) + q4) * 176 + p4 * 8 + (g >> 1) * 32;
        const int s0 = tb * 64 + 16 * w + 8 * (g & 1);
        u16* hf = (u16*)(ws + OFF_H) + ((size_t)(5 * cg + (g >> 1)) * 64 + (s0 >> 5)) * 512 + (((s0 & 31) >> 3) * 16 + n16) * 8;
        const bf16x8 f0 = cat8(tr_read(hr), tr_read(hr + 704));
        const bf16x8 f1 = cat8(tr_read(hr + 64), tr_read(hr + 704 + 64));
        const bf16x8 f2 = cat8(tr_read(hr + (g < 2 ? 128 : 0)), tr_read(hr + 704 + (g < 2 ? 128 : 0)));
        *(bf16x8*)(hf) = f0;
        *(bf16x8*)(hf + 2 * 64 * 512) = f1;
        if (g < 2) *(bf16x8*)(hf + 4 * 64 * 512) = f2;
        u16* x1r = (u16*)(ws + OFF_X1) + (size_t)tok * 640 + 80 * cg + 4 * g;
#pragma unroll
        for (int nt = 0; nt < 5; ++nt) *(u32x2*)(x1r + 16 * nt) = x1o[nt];
    }
    SST(5); SST_FLUSHK(ws, XF ? 7 : 2);
}

template <int MODE, int NS, int PF>
DI void gemm_body(const bf16x8* __restrict__ po, const bf16x8* __restrict__ pf, const int (&ooff)[4], int fstride, f32x4 (&acc)[4][5]) {
    bf16x8 ro[PF][4], rf[PF][5];
#pragma unroll
    for (int s = 0; s < PF; ++s) {
#pragma unroll
        for (int j = 0; j < 4; ++j) ro[s][j] = po[ooff[j] + s * 64];
#pragma unroll
        for (int i = 0; i < 5; ++i) rf[s][i] = pf[i * fstride + s * 64];
        SBAR();
    }
#pragma unroll
    for (int s = 0; s < NS; ++s) {
        const int sl = s % PF;
        SBAR();
#pragma unroll
        for (int j = 0; j < 4; ++j)
#pragma unroll
            for (int i = 0; i < 5; ++i) {
                if (MODE == 1) acc[j][i] = MFMA16_32(ro[sl][j], rf[sl][i], acc[j][i]);
                else acc[j][i] = MFMA16_32(rf[sl][i], ro[sl][j], acc[j][i]);
            }
        SBAR();
        if (s + PF < NS) {
#pragma unroll
            for (int j = 0; j < 4; ++j) ro[sl][j] = po[ooff[j] + (s + PF) * 64];
#pragma unroll
            for (int i = 0; i < 5; ++i) rf[sl][i] = pf[i * fstride + (s + PF) * 64];
        }
    }
}

template <int MODE>
__global__ __launch_bounds__(256, 1) void k_gemm(char* __restrict__ ws, const u16* __restrict__ bias, u16* __restrict__ out) {
    __shared__ __attribute__((aligned(16))) f32x4 red[4 * 3 * 5 * 64];
    const int tid = threadIdx.x, lane = tid & 63, w = __builtin_amdgcn_readfirstlane(tid >> 6);
    const int b = blockIdx.x, xcd = b & 7, slot = b >> 3;
    const int n16 = lane & 15, g = lane >> 4;
    constexpr int KS = MODE == 1 ? 64 : 54;
    int ot, dt;
    if (MODE == 1) {
        ot = 7 * (xcd >> 1) + (slot >> 2); dt = 4 * (xcd & 1) + (slot & 3);
        if (slot >= 28 || ot >= 27) return;
    } else {
        ot = 8 * (xcd >> 1) + (slot >> 2); dt = 4 * (xcd & 1) + (slot & 3);
    }
    SST_DECL; SST(0);
    u32x2 bb = {0u, 0u}, xi[5]; float bs = 0.f;
    if (MODE == 1) bb = *(const u32x2*)(bias + 64 * ot + 16 * w + 4 * g);
    else {
        const int s = 64 * ot + 16 * w + n16;
        bs = bf2f(bias[s]);
        const u16* x1 = (const u16*)(ws + OFF_X1) + (size_t)s * 640 + 80 * dt + 4 * g;
#pragma unroll
        for (int i = 0; i < 5; ++i) xi[i] = *(const u32x2*)(x1 + 16 * i);
    }
    const int k0 = MODE == 1 ? 16 * w : (w < 2 ? 14 * w : 28 + 13 * (w - 2));
    const bf16x8* po = (const bf16x8*)(ws + (MODE == 1 ? OFF_W1F : OFF_W2F)) + ((size_t)(4 * ot) * KS + k0) * 64 + lane;
    const bf16x8* pf = (const bf16x8*)(ws + (MODE == 1 ? OFF_H : OFF_U)) + ((size_t)(5 * dt) * KS + k0) * 64 + lane;
    int ooff[4];
#pragma unroll
    for (int j = 0; j < 4; ++j) ooff[j] = ((j + w) & 3) * KS * 64;
    f32x4 acc[4][5];
#pragma unroll
    for (int j = 0; j < 4; ++j)
#pragma unroll
        for (int i = 0; i < 5; ++i) acc[j][i] = f32x4{0.f, 0.f, 0.f, 0.f};
    if (MODE == 1) gemm_body<1, 16, 6>(po, pf, ooff, KS * 64, acc);
    else if (w < 2) gemm_body<2, 14, 6>(po, pf, ooff, KS * 64, acc);
    else gemm_body<2, 13, 6>(po, pf, ooff, KS * 64, acc);
    SST(1);
#pragma unroll
    for (int j = 1; j < 4; ++j) {
        const int o = (w + j) & 3;
#pragma unroll
        for (int i = 0; i < 5; ++i) red[((o * 3 + (j - 1)) * 5 + i) * 64 + lane] = acc[j][i];
    }
    RAW_BARRIER();
#pragma unroll
    for (int j = 0; j < 3; ++j)
#pragma unroll
        for (int i = 0; i < 5; ++i) acc[1 + j][i] = red[((w * 3 + j) * 5 + i) * 64 + lane];
    SBAR();
#pragma unroll
    for (int i = 0; i < 5; ++i) acc[0][i] = (acc[0][i] + acc[1][i]) + (acc[2][i] + acc[3][i]);
    SST(2);
    if (MODE == 1) {
        const int f = 64 * ot + 16 * w + 4 * g;
        const float bv[4] = {bf_lo(bb[0]), bf_hi(bb[0]), bf_lo(bb[1]), bf_hi(bb[1])};
        u16* uf = (u16*)(ws + OFF_U);
#pragma unroll
        for (int i = 0; i < 5; ++i) {
            float r[4];
#pragma unroll
            for (int e = 0; e < 4; ++e) {
                const float u = acc[0][i][e] + bv[e];
                const float z = u * (1.f + 0.044715f * u * u) * (-2.f * 0.7978845608028654f * 1.4426950408889634f);
                r[e] = u * __builtin_amdgcn_rcpf(1.f + EX2(z));
            }
            *(u32x2*)(uf + ((size_t)((5 * dt + i) * 54 + (f >> 5)) * 64 + ((f & 31) >> 3) * 16 + n16) * 8 + (f & 7)) =
                u32x2{pk2(r[0], r[1]), pk2(r[2], r[3])};
        }
    } else {
        const int s = 64 * ot + 16 * w + n16;
        float xv[20]; float sum = 0.f;
#pragma unroll
        for (int i = 0; i < 5; ++i) {
            const int d = 80 * dt + 16 * i + 4 * g;
            u16* o = MODE == 3 ? out + (size_t)s * 640 + d
                               : out + ((size_t)((4 * ot + w) * 20 + (d >> 5)) * 64 + ((d & 31) >> 3) * 16 + n16) * 8 + (d & 7);
            const unsigned u0 = pk2(bf_lo(xi[i][0]) + acc[0][i][0] + bs, bf_hi(xi[i][0]) + acc[0][i][1] + bs);
            const unsigned u1 = pk2(bf_lo(xi[i][1]) + acc[0][i][2] + bs, bf_hi(xi[i][1]) + acc[0][i][3] + bs);
            *(u32x2*)o = u32x2{u0, u1};
            xv[4 * i] = bf_lo(u0); xv[4 * i + 1] = bf_hi(u0); xv[4 * i + 2] = bf_lo(u1); xv[4 * i + 3] = bf_hi(u1);
            sum += (xv[4 * i] + xv[4 * i + 1]) + (xv[4 * i + 2] + xv[4 * i + 3]);
        }
        if (MODE == 2) {
            const float mw = add32(add16(sum)) * (1.f / 80.f);
            float s2 = 0.f;
#pragma unroll
            for (int i = 0; i < 20; ++i) { const float dd = xv[i] - mw; s2 += dd * dd; }
            s2 = add32(add16(s2));
            if (g == 0) *(f32x2*)(ws + OFF_XS + ((size_t)s * 8 + dt) * 8) = f32x2{mw, s2};
        }
    }
    SST(3); SST_FLUSHK(ws, MODE == 1 ? (out ? 8 : 3) : (MODE == 2 ? 4 : 9));
}

typedef __attribute__((address_space(1))) unsigned gu32;
constexpr int AUXL = 16;
#define LD_SC1(off) __builtin_bit_cast(bf16x8, __builtin_amdgcn_raw_buffer_load_b128(rs, (int)(off), 0, 16))
DI void edge_publish(gu32* cnt) {
    RAW_BARRIER();
    if (threadIdx.x == 0) __hip_atomic_fetch_add(cnt, 1u, __ATOMIC_RELAXED, __HIP_MEMORY_SCOPE_AGENT);
}
DI void edge_wait(gu32* cnt, unsigned want) {
    if (threadIdx.x == 0) {
        for (int spin = 0; spin < (1 << 20); ++spin) {
            if (__hip_atomic_load(cnt, __ATOMIC_RELAXED, __HIP_MEMORY_SCOPE_AGENT) >= want) break;
            __builtin_amdgcn_s_sleep(1);
        }
        __builtin_amdgcn_fence(__ATOMIC_ACQUIRE, "wavefront");
    }
    RAW_BARRIER();
}
#define W1_A 6
#define W2_FIRST 7
constexpr int MLP_LDS = 73728;
template <bool R1>
__global__ __launch_bounds__(256, 1) void k_mlp(char* __restrict__ ws, const u16* __restrict__ xin, const u16* __restrict__ b1,
                                                const u16* __restrict__ b2, u16* __restrict__ out) {
    extern __shared__ __attribute__((aligned(16))) char lds_raw[];
    char* L = lds_raw;
    const int tid = threadIdx.x, lane = tid & 63, w = __builtin_amdgcn_readfirstlane(tid >> 6);
    const int b = blockIdx.x, slot = b >> 3, tb = ((b & 7) >> 1) * 8 + (slot >> 2), cg = (b & 1) * 4 + (slot & 3);
    const int ot1 = ((b & 7) >> 1) * 7 + (slot >> 2);
    const bool act1 = slot < 28 && ot1 < 27;
    const int n16 = lane & 15, g = lane >> 4;
    const __amdgpu_buffer_rsrc_t rs = __builtin_amdgcn_make_buffer_rsrc((void*)ws, 0, 0x8000000, 0x00020000);
    gu32* cnt = (gu32*)(ws + OFF_CNT);
    SST_DECL; SST(0);
    f32x4 acc[4][5];
#define ACC_ZERO() do { _Pragma("unroll") for (int i_ = 0; i_ < 4; ++i_) _Pragma("unroll") for (int j_ = 0; j_ < 5; ++j_) acc[i_][j_] = f32x4{0.f, 0.f, 0.f, 0.f}; } while (0)
#define ACC_REDUCE() do { \
    _Pragma("unroll") for (int j_ = 1; j_ < 4; ++j_) { const int o_ = (w + j_) & 3; \
        _Pragma("unroll") for (int i_ = 0; i_ < 5; ++i_) *(f32x4*)(L + (((o_ * 3 + (j_ - 1)) * 5 + i_) * 64 + lane) * 16) = acc[j_][i_]; } \
    RAW_BARRIER(); \
    _Pragma("unroll") for (int j_ = 0; j_ < 3; ++j_) _Pragma("unroll") for (int i_ = 0; i_ < 5; ++i_) \
        acc[1 + j_][i_] = *(const f32x4*)(L + (((w * 3 + j_) * 5 + i_) * 64 + lane) * 16); \
    SBAR(); \
    _Pragma("unroll") for (int i_ = 0; i_ < 5; ++i_) acc[0][i_] = (acc[0][i_] + acc[1][i_]) + (acc[2][i_] + acc[3][i_]); } while (0)
    const int tok = tb * 64 + 16 * w + n16;
    bf16x8 wr[16][4];
    int ro1[4], ro2[4];
#pragma unroll
    for (int j = 0; j < 4; ++j) { ro1[j] = ((j + w) & 3) * 64 * 64; ro2[j] = ((j + w) & 3) * 54 * 64; }
    const bf16x8* pw1 = (const bf16x8*)(ws + OFF_W1F) + ((size_t)(4 * (act1 ? ot1 : 0)) * 64 + 16 * w) * 64 + lane;
    const int k2 = w < 2 ? 14 * w : 28 + 13 * (w - 2);
    const bf16x8* pw2 = (const bf16x8*)(ws + OFF_W2F) + ((size_t)(4 * tb) * 54 + k2) * 64 + lane;
#define W1_LD(lo, hi) do { _Pragma("unroll") for (int s_ = (lo); s_ < (hi); ++s_) _Pragma("unroll") for (int j_ = 0; j_ < 4; ++j_) wr[s_][j_] = pw1[ro1[j_] + s_ * 64]; SBAR(); } while (0)
#define W2_LD(lo, hi) do { _Pragma("unroll") for (int s_ = (lo); s_ < (hi); ++s_) _Pragma("unroll") for (int j_ = 0; j_ < 4; ++j_) wr[s_][j_] = pw2[ro2[j_] + s_ * 64]; SBAR(); } while (0)

    u32x2 x1o[5], bb; float bs;
    {
        ACC_ZERO();
        u32x2 xi[5];
        const bf16x8* xb = (const bf16x8*)(ws + OFF_O) + (size_t)(tb * 4 * 20 + 5 * w) * 64 + lane;
        const bf16x8* wb = (const bf16x8*)(ws + OFF_WOF) + (size_t)(5 * cg * 20 + 5 * w) * 64 + lane;
        bf16x8 xf[5][4], wf[5][5];
        WO_LD(0); WO_LD(1); WO_LD(2); WO_LD(3);
#pragma unroll
        for (int ks = 0; ks < 5; ++ks) {
#pragma unroll
            for (int nt = 0; nt < 5; ++nt)
#pragma unroll
                for (int i = 0; i < 4; ++i) acc[i][nt] = MFMA16_32(wf[ks][nt], xf[ks][i], acc[i][nt]);
            SBAR();
            if (ks == 0) {
                WO_LD(4);
#pragma unroll
                for (int nt = 0; nt < 5; ++nt) {
                    const int d = 80 * cg + 16 * nt + 4 * g;
                    if (!R1) xi[nt] = *(const u32x2*)(xin + ((size_t)((tok >> 4) * 20 + (d >> 5)) * 64 + ((d & 31) >> 3) * 16 + n16) * 8 + (d & 7));
                    else xi[nt] = *(const u32x2*)(xin + (size_t)tok * 640 + d);
                }
                bb = *(const u32x2*)(b1 + 64 * (act1 ? ot1 : 0) + 16 * w + 4 * g);
                bs = bf2f(b2[tok]);
                SBAR();
            }
            if (ks == 1) W1_LD(0, 2);
            if (ks == 2) W1_LD(2, 4);
            if (ks == 3) W1_LD(4, 6);
            if (ks == 4) W1_LD(6, W1_A);
        }
        SST(1);
        ACC_REDUCE();
        float xv[20], s = 0.f;
#pragma unroll
        for (int nt = 0; nt < 5; ++nt) {
            const unsigned u0 = pk2(bf_lo(xi[nt][0]) + acc[0][nt][0], bf_hi(xi[nt][0]) + acc[0][nt][1]);
            const unsigned u1 = pk2(bf_lo(xi[nt][1]) + acc[0][nt][2], bf_hi(xi[nt][1]) + acc[0][nt][3]);
            x1o[nt] = u32x2{u0, u1};
            xv[4 * nt] = bf_lo(u0); xv[4 * nt + 1] = bf_hi(u0); xv[4 * nt + 2] = bf_lo(u1); xv[4 * nt + 3] = bf_hi(u1);
            s += (xv[4 * nt] + xv[4 * nt + 1]) + (xv[4 * nt + 2] + xv[4 * nt + 3]);
        }
        const float mw = add32(add16(s)) * (1.f / 80.f);
        float s2 = 0.f;
#pragma unroll
        for (int i = 0; i < 20; ++i) { const float dd = xv[i] - mw; s2 += dd * dd; }
        s2 = add32(add16(s2));
        gu64* gp = (gu64*)(ws + OFF_XG) + (size_t)tok * 8;
        if (g == 0) __hip_atomic_store(gp + cg, ((unsigned long long)(__float_as_uint(s2) | 0x80000000u) << 32) | __float_as_uint(mw),
                                       __ATOMIC_RELAXED, __HIP_MEMORY_SCOPE_AGENT);
        SBAR();
        SST(2);
        unsigned long long v0, v1;
        for (int spin = 0; spin < (1 << 20); ++spin) {
            v0 = __hip_atomic_load(gp + 2 * g, __ATOMIC_RELAXED, __HIP_MEMORY_SCOPE_AGENT);
            v1 = __hip_atomic_load(gp + 2 * g + 1, __ATOMIC_RELAXED, __HIP_MEMORY_SCOPE_AGENT);
            if (__all((long long)(v0 & v1) < 0)) break;
            __builtin_amdgcn_s_sleep(4);
        }
        SST(3);
        const float m0 = __uint_as_float((unsigned)v0), m1 = __uint_as_float((unsigned)v1);
        const float mean = add32(add16(m0 + m1)) * 0.125f;
        const float d0 = m0 - mean, d1 = m1 - mean;
        const float var = add32(add16(__uint_as_float((unsigned)(v0 >> 32) & 0x7fffffffu) + __uint_as_float((unsigned)(v1 >> 32) & 0x7fffffffu) +
                                      80.f * (d0 * d0 + d1 * d1))) * (1.f / 640.f);
        const float r1 = __builtin_amdgcn_rsqf(var + LN_EPS);
        const float r2 = r1 * __builtin_amdgcn_rsqf(var * r1 * r1 + LN_EPS);
        char* hb = L + w * 15360;
#pragma unroll
        for (int nt = 0; nt < 5; ++nt)
            *(u32x2*)(hb + n16 * 176 + (16 * nt + 4 * g) * 2) =
                u32x2{pk2((xv[4 * nt] - mean) * r2, (xv[4 * nt + 1] - mean) * r2), pk2((xv[4 * nt + 2] - mean) * r2, (xv[4 * nt + 3] - mean) * r2)};
        asm volatile("s_waitcnt lgkmcnt(0)" ::: "memory");
        const int q4 = (lane & 15) >> 2, p4 = lane & 3;
        lds_cptr hr = (lds_cptr)(lds_ptr)L + w * 15360 + (8 * (g & 1) + q4) * 176 + p4 * 8 + (g >> 1) * 32;
        const int s0 = tb * 64 + 16 * w + 8 * (g & 1);
        const int hoff = (int)OFF_H + ((5 * cg + (g >> 1)) * 64 + (s0 >> 5)) * 1024 + (((s0 & 31) >> 3) * 16 + n16) * 16;
        const bf16x8 f0 = cat8(tr_read(hr), tr_read(hr + 704));
        const bf16x8 f1 = cat8(tr_read(hr + 64), tr_read(hr + 704 + 64));
        const bf16x8 f2 = cat8(tr_read(hr + (g < 2 ? 128 : 0)), tr_read(hr + 704 + (g < 2 ? 128 : 0)));
        __builtin_amdgcn_raw_buffer_store_b128(__builtin_bit_cast(u32x4, f0), rs, hoff, 0, 16);
        __builtin_amdgcn_raw_buffer_store_b128(__builtin_bit_cast(u32x4, f1), rs, hoff + 2 * 65536, 0, 16);
        if (g < 2) __builtin_amdgcn_raw_buffer_store_b128(__builtin_bit_cast(u32x4, f2), rs, hoff + 4 * 65536, 0, 16);
        SBAR();
        W1_LD(W1_A, W1_A + 4);
        VMCNT(16);
        SST(4);
        edge_publish(cnt + cg * 64);
    }
    if (act1) {
        W1_LD(W1_A + 4, 16);
        edge_wait(cnt + cg * 64, 32);
        SST(5);
        ACC_ZERO();
        const int ho = (int)OFF_H + ((5 * cg) * 64 + 16 * w) * 1024 + lane * 16;
        constexpr int PF = 6;
        bf16x8 rf[PF][5];
#pragma unroll
        for (int s = 0; s < PF; ++s) {
#pragma unroll
            for (int i = 0; i < 5; ++i) rf[s][i] = LD_SC1(ho + i * 65536 + s * 1024);
            SBAR();
        }
#pragma unroll
        for (int s = 0; s < 16; ++s) {
#pragma unroll
            for (int j = 0; j < 4; ++j)
#pragma unroll
                for (int i = 0; i < 5; ++i) acc[j][i] = MFMA16_32(wr[s][j], rf[s % PF][i], acc[j][i]);
            SBAR();
            if (s + PF < 16) {
#pragma unroll
                for (int i = 0; i < 5; ++i) rf[s % PF][i] = LD_SC1(ho + i * 65536 + (s + PF) * 1024);
                SBAR();
            }
            if (s >= W2_FIRST && s - W2_FIRST < 9) W2_LD(s - W2_FIRST, s - W2_FIRST + 1);
        }
        SST(6);
        ACC_REDUCE();
        const int f = 64 * ot1 + 16 * w + 4 * g;
        const float bv[4] = {bf_lo(bb[0]), bf_hi(bb[0]), bf_lo(bb[1]), bf_hi(bb[1])};
        const int uo = (int)OFF_U + ((5 * cg) * 54 + (f >> 5)) * 1024 + (((f & 31) >> 3) * 16 + n16) * 16 + (f & 7) * 2;
#pragma unroll
        for (int i = 0; i < 5; ++i) {
            float r[4];
#pragma unroll
            for (int e = 0; e < 4; ++e) {
                const float u = acc[0][i][e] + bv[e];
                const float z = u * (1.f + 0.044715f * u * u) * (-2.f * 0.7978845608028654f * 1.4426950408889634f);
                r[e] = u * __builtin_amdgcn_rcpf(1.f + EX2(z));
            }
            __builtin_amdgcn_raw_buffer_store_b64(u32x2{pk2(r[0], r[1]), pk2(r[2], r[3])}, rs, uo + i * 54 * 1024, 0, 16);
        }
        SBAR();
        W2_LD(9, 13);
        VMCNT(16);
        SST(7);
        edge_publish(cnt + (8 + cg) * 64);
    } else {
        W2_LD(0, 13);
    }
    {
        if (w < 2) W2_LD(13, 14);
        edge_wait(cnt + (8 + cg) * 64, 27);
        SST(8);
        ACC_ZERO();
        const int uo = (int)OFF_U + ((5 * cg) * 54 + k2) * 1024 + lane * 16;
        constexpr int PF = 6;
        bf16x8 rf[PF][5];
#pragma unroll
        for (int s = 0; s < PF; ++s) {
#pragma unroll
            for (int i = 0; i < 5; ++i) rf[s][i] = LD_SC1(uo + i * 54 * 1024 + s * 1024);
            SBAR();
        }
#pragma unroll
        for (int s = 0; s < 14; ++s) {
            if (s < 13 || w < 2) {
#pragma unroll
                for (int j = 0; j < 4; ++j)
#pragma unroll
                    for (int i = 0; i < 5; ++i) acc[j][i] = MFMA16_32(rf[s % PF][i], wr[s][j], acc[j][i]);
            }
            SBAR();
            if (s + PF < 13 || (s + PF == 13 && w < 2)) {
#pragma unroll
                for (int i = 0; i < 5; ++i) rf[s % PF][i] = LD_SC1(uo + i * 54 * 1024 + (s + PF) * 1024);
                SBAR();
            }
        }
        SST(9);
        ACC_REDUCE();
        float xv[20], sum = 0.f;
#pragma unroll
        for (int i = 0; i < 5; ++i) {
            const int d = 80 * cg + 16 * i + 4 * g;
            const unsigned u0 = pk2(bf_lo(x1o[i][0]) + acc[0][i][0] + bs, bf_hi(x1o[i][0]) + acc[0][i][1] + bs);
            const unsigned u1 = pk2(bf_lo(x1o[i][1]) + acc[0][i][2] + bs, bf_hi(x1o[i][1]) + acc[0][i][3] + bs);
            if (R1) __builtin_amdgcn_raw_buffer_store_b64(u32x2{u0, u1}, rs,
                        (int)OFF_X2 + ((4 * tb + w) * 20 + (d >> 5)) * 1024 + (((d & 31) >> 3) * 16 + n16) * 16 + (d & 7) * 2, 0, 16);
            else *(u32x2*)(out + (size_t)tok * 640 + d) = u32x2{u0, u1};
            xv[4 * i] = bf_lo(u0); xv[4 * i + 1] = bf_hi(u0); xv[4 * i + 2] = bf_lo(u1); xv[4 * i + 3] = bf_hi(u1);
            sum += (xv[4 * i] + xv[4 * i + 1]) + (xv[4 * i + 2] + xv[4 * i + 3]);
        }
        if (R1) {
            const float mw = add32(add16(sum)) * (1.f / 80.f);
            float s2 = 0.f;
#pragma unroll
            for (int i = 0; i < 20; ++i) { const float dd = xv[i] - mw; s2 += dd * dd; }
            s2 = add32(add16(s2));
            if (g == 0) __builtin_amdgcn_raw_buffer_store_b64(u32x2{__float_as_uint(mw), __float_as_uint(s2)}, rs, (int)OFF_XS + ((tb * 8 + cg) * 64 + 16 * w + n16) * 8, 0, 16);
        }
    }
    if (R1) {
        const int mt0 = cg >> 1, isv = cg & 1;
        const bf16x8* wb = (const bf16x8*)(ws + OFF_WQF) + (size_t)(5 * cg * 20 + 5 * w) * 64 + lane;
        const bf16x8* kb = (const bf16x8*)(ws + OFF_WQF) + (size_t)((40 + 2 * isv) * 20 + 5 * w) * 64 + lane;
        bf16x8 wf[5][5], wkf[5][2], xf[5][4], xk[5];
        SBAR();
#pragma unroll
        for (int i = 0; i < 16; ++i) wf[i / 5][i % 5] = wb[((i % 5) * 20 + i / 5) * 64];
        SBAR();
        VMCNT(16);
        SST(10);
        edge_publish(cnt + (16 + tb) * 64);
#pragma unroll
        for (int i = 16; i < 25; ++i) wf[i / 5][i % 5] = wb[((i % 5) * 20 + i / 5) * 64];
#pragma unroll
        for (int ks = 0; ks < 5; ++ks) { wkf[ks][0] = kb[ks * 64]; wkf[ks][1] = kb[(20 + ks) * 64]; }
        f32x4 csq[5], cs0, cs1, stq[4], stk[4], acck[2];
        {
            const float* csp = (const float*)(ws + OFF_CS);
#pragma unroll
            for (int nt = 0; nt < 5; ++nt) csq[nt] = *(const f32x4*)(csp + 80 * cg + 16 * nt + 4 * g);
            cs0 = *(const f32x4*)(csp + 640 + 32 * isv + 4 * g); cs1 = *(const f32x4*)(csp + 640 + 32 * isv + 16);
        }
        SBAR();
        edge_wait(cnt + (16 + tb) * 64, 8);
        SST(11);
        const int xo = (int)OFF_X2 + (tb * 4 * 20 + 5 * w) * 1024 + lane * 16;
#pragma unroll
        for (int ks = 0; ks < 5; ++ks) {
#pragma unroll
            for (int i = 0; i < 4; ++i) xf[ks][i] = LD_SC1(xo + (((i + w) & 3) * 20 + ks) * 1024);
            xk[ks] = LD_SC1(xo + (mt0 * 20 + ks) * 1024);
            SBAR();
        }
#pragma unroll
        for (int i = 0; i < 4; ++i) {
            const u32x2 a0 = __builtin_amdgcn_raw_buffer_load_b64(rs, (int)OFF_XS + ((tb * 8 + 2 * i) * 64 + 16 * w + n16) * 8, 0, AUXL);
            const u32x2 a1 = __builtin_amdgcn_raw_buffer_load_b64(rs, (int)OFF_XS + ((tb * 8 + 2 * i + 1) * 64 + 16 * w + n16) * 8, 0, AUXL);
            const u32x2 c0 = __builtin_amdgcn_raw_buffer_load_b64(rs, (int)OFF_XS + ((tb * 8 + 2 * i) * 64 + 16 * mt0 + n16) * 8, 0, AUXL);
            const u32x2 c1 = __builtin_amdgcn_raw_buffer_load_b64(rs, (int)OFF_XS + ((tb * 8 + 2 * i + 1) * 64 + 16 * mt0 + n16) * 8, 0, AUXL);
            stq[i] = __builtin_bit_cast(f32x4, u32x4{a0[0], a0[1], a1[0], a1[1]});
            stk[i] = __builtin_bit_cast(f32x4, u32x4{c0[0], c0[1], c1[0], c1[1]});
        }
        SBAR();
        ACC_ZERO();
        acck[0] = f32x4{0.f, 0.f, 0.f, 0.f}; acck[1] = acck[0];
#pragma unroll
        for (int ks = 0; ks < 5; ++ks) {
#pragma unroll
            for (int nt = 0; nt < 5; ++nt)
#pragma unroll
                for (int i = 0; i < 4; ++i) acc[i][nt] = MFMA16_32(wf[ks][nt], xf[ks][i], acc[i][nt]);
            acck[0] = MFMA16_32(wkf[ks][0], xk[ks], acck[0]);
            acck[1] = MFMA16_32(wkf[ks][1], xk[ks], acck[1]);
            SBAR();
        }
        SST(12);
        const float z4[4] = {0.f, 0.f, 0.f, 0.f}, z5[5] = {0.f, 0.f, 0.f, 0.f, 0.f}, z2[2] = {0.f, 0.f};
        qkv_finish<true>(ws, L, tb, cg, w, lane, acc, acck, csq, cs0, cs1, stq, stk, z4, z4, z5, z2);
    }
    SST_FLUSHK(ws, R1 ? 2 : 7);
}

template <int N> __global__ void k_end() {}

extern "C" void kernel_launch(void* const* d_in, const int* in_sizes, int n_in, void* d_out, int out_size, void* d_ws,
                              size_t ws_size, hipStream_t stream) {
    const u16* x = (const u16*)d_in[0];
    const u16* wq = (const u16*)d_in[1];
    const u16* wk = (const u16*)d_in[2];
    const u16* wv = (const u16*)d_in[3];
    const u16* wo = (const u16*)d_in[4];
    const u16* w1 = (const u16*)d_in[6];
    const u16* b1 = (const u16*)d_in[7];
    const u16* w2 = (const u16*)d_in[8];
    const u16* b2 = (const u16*)d_in[9];
    char* ws = (char*)d_ws;
    u16* x2 = (u16*)(ws + OFF_X2);
    (void)hipFuncSetAttribute((const void*)k_qkv<false>, hipFuncAttributeMaxDynamicSharedMemorySize, QKV_LDS);
    (void)hipFuncSetAttribute((const void*)k_attn<true>, hipFuncAttributeMaxDynamicSharedMemorySize, ATT_LDS);
    (void)hipFuncSetAttribute((const void*)k_attn<false>, hipFuncAttributeMaxDynamicSharedMemorySize, ATT_LDS);
    (void)hipFuncSetAttribute((const void*)k_mlp<true>, hipFuncAttributeMaxDynamicSharedMemorySize, MLP_LDS);
    (void)hipFuncSetAttribute((const void*)k_mlp<false>, hipFuncAttributeMaxDynamicSharedMemorySize, MLP_LDS);
    (void)hipFuncSetAttribute((const void*)k_end<0>, hipFuncAttributeMaxDynamicSharedMemorySize, 0);
    k_qkv<false><<<256, 256, QKV_LDS, stream>>>(x, wq, wk, wv, wo, ws, w1, w2);
    k_attn<true><<<256, 512, ATT_LDS, stream>>>(ws, w1, w2, wo, wq);
    k_mlp<true><<<256, 256, MLP_LDS, stream>>>(ws, x, b1, b2, nullptr);
    k_attn<false><<<256, 512, ATT_LDS, stream>>>(ws, w1, w2, wo, wq);
    k_mlp<false><<<256, 256, MLP_LDS, stream>>>(ws, x2, b1, b2, (u16*)d_out);
}
```

```cpp
#define WS_END
#include <hip/hip_runtime.h>
#include <hip/hip_bf16.h>
#include <stdint.h>

typedef unsigned short u16;
typedef short bf16x8 __attribute__((ext_vector_type(8)));
typedef short bf16x4 __attribute__((ext_vector_type(4)));
typedef float f32x16 __attribute__((ext_vector_type(16)));
typedef float f32x4 __attribute__((ext_vector_type(4)));
typedef float f32x2 __attribute__((ext_vector_type(2)));
typedef unsigned u32x4 __attribute__((ext_vector_type(4)));
typedef unsigned u32x2 __attribute__((ext_vector_type(2)));
typedef __bf16 bf2_t __attribute__((ext_vector_type(2)));
typedef __attribute__((address_space(3))) char* lds_ptr;
typedef __attribute__((address_space(3))) const char* lds_cptr;
typedef __attribute__((address_space(1))) unsigned long long gu64;

#define DI __device__ __forceinline__
#define SBAR() __builtin_amdgcn_sched_barrier(0)
#define PIN(x) asm volatile("" : "+v"(x))
#define MFMA32_16(a, b, c) __builtin_amdgcn_mfma_f32_32x32x16_bf16(a, b, c, 0, 0, 0)
#define MFMA32_8(a, b, c) __builtin_amdgcn_mfma_f32_32x32x8bf16_1k(a, b, c, 0, 0, 0)
#define MFMA16_32(a, b, c) __builtin_amdgcn_mfma_f32_16x16x32_bf16(a, b, c, 0, 0, 0)
#define EX2(x) __builtin_amdgcn_exp2f(x)
#define RAW_BARRIER() asm volatile("s_waitcnt lgkmcnt(0)\n\ts_barrier" ::: "memory")
#define VMCNT(n) asm volatile("s_waitcnt vmcnt(%0)" :: "i"(n) : "memory")

#define SST_DECL do {} while (0)
#define SST(i) do {} while (0)
#define SST_FLUSHK(ws, kid) do {} while (0)
#define PREP_STORE(p, v) asm volatile("global_store_dwordx4 %0, %1, off sc1\n\ts_nop 1" :: "v"(p), "v"(v) : "memory")
#define ST8(p, v) (*(u32x2*)(p) = (v))
#define KADDR(dst, sym) asm volatile("s_getpc_b64 s[40:41]\n\ts_add_u32 s40, s40, " sym "@rel32@lo+4\n\ts_addc_u32 s41, s41, " sym "@rel32@hi+12\n\ts_mov_b64 %0, s[40:41]" : "=s"(dst) :: "s40", "s41", "scc")
constexpr int S_ = 2048, D_ = 640, F_ = 1728;
constexpr float LN_EPS = 1e-5f;
constexpr float QSCALE = 0.22360679774997896f * 1.4426950408889634f;
constexpr float SAFE_BOUND = 60.0f;

DI unsigned pk2(float a, float b) { f32x2 v = {a, b}; bf2_t r = __builtin_convertvector(v, bf2_t); return __builtin_bit_cast(unsigned, r); }
DI float bf_lo(unsigned u) { return __uint_as_float(u << 16); }
DI float bf_hi(unsigned u) { return __uint_as_float(u & 0xffff0000u); }
DI float bf2f(u16 v) { return __uint_as_float((unsigned)v << 16); }
DI float dot2(unsigned a, unsigned b, float c) { return __builtin_amdgcn_fdot2_f32_bf16(__builtin_bit_cast(bf2_t, a), __builtin_bit_cast(bf2_t, b), c, false); }
DI bf16x4 tr_read(lds_cptr p) { return __builtin_amdgcn_ds_read_tr16_b64_v4i16((__attribute__((address_space(3))) bf16x4*)p); }
DI bf16x8 cat8(bf16x4 a, bf16x4 b) { return __builtin_shufflevector(a, b, 0, 1, 2, 3, 4, 5, 6, 7); }
DI void glds16(const void* g, lds_ptr l) {
    unsigned keep;
    asm volatile("s_mov_b32 %0, m0\n\ts_mov_b32 m0, %2\n\ts_nop 0\n\tglobal_load_lds_dwordx4 %1, off\n\ts_mov_b32 m0, %0"
                 : "=&s"(keep) : "v"(g), "s"((unsigned)(uintptr_t)l) : "memory");
}
DI float xor32(float v) { return __shfl_xor(v, 32); }
DI float add32(float v) {
    auto r = __builtin_amdgcn_permlane32_swap(__float_as_uint(v), __float_as_uint(v), false, false);
    return __uint_as_float(r[0]) + __uint_as_float(r[1]);
}
DI float add16(float v) {
    auto r = __builtin_amdgcn_permlane16_swap(__float_as_uint(v), __float_as_uint(v), false, false);
    return __uint_as_float(r[0]) + __uint_as_float(r[1]);
}
DI float max16x(float v) { auto r = __builtin_amdgcn_permlane16_swap(__float_as_uint(v), __float_as_uint(v), false, false); return fmaxf(__uint_as_float(r[0]), __uint_as_float(r[1])); }
DI float max32x(float v) { auto r = __builtin_amdgcn_permlane32_swap(__float_as_uint(v), __float_as_uint(v), false, false); return fmaxf(__uint_as_float(r[0]), __uint_as_float(r[1])); }
template <int CTRL> DI float dppf(float v) { return __int_as_float(__builtin_amdgcn_update_dpp(0, __float_as_int(v), CTRL, 0xf, 0xf, false)); }
DI float rowmax16(float v) {
    v = fmaxf(v, dppf<0x128>(v)); v = fmaxf(v, dppf<0x124>(v)); v = fmaxf(v, dppf<0x122>(v)); v = fmaxf(v, dppf<0x121>(v)); return v;
}
DI float wavemax(float v) { return max32x(max16x(rowmax16(v))); }
DI u32x4 gather8(const u16* s, int ld) {
    u32x4 r;
#pragma unroll
    for (int e = 0; e < 4; ++e) r[e] = (unsigned)s[(2 * e) * ld] | ((unsigned)s[(2 * e + 1) * ld] << 16);
    return r;
}

constexpr size_t OFF_Q = 0;
constexpr size_t OFF_K1 = OFF_Q + (size_t)2048 * 32 * 48;
constexpr size_t OFF_K2 = OFF_K1 + 2048 * 32;
constexpr size_t OFF_V = OFF_K2 + 2048 * 8;
constexpr size_t OFF_KN = OFF_V + 128 * 640;
constexpr size_t OFF_QN = OFF_KN + 512;
constexpr size_t OFF_CS = OFF_QN + 4096;
constexpr size_t OFF_CONST = OFF_CS + 4096;
constexpr size_t OFF_CNT = OFF_CONST + 256;
constexpr size_t OFF_XG = OFF_CNT + 48 * 256;
constexpr size_t OFF_XS = OFF_XG + 262144;
constexpr size_t OFF_X1 = OFF_XS + 131072;
constexpr size_t OFF_H = OFF_X1 + (size_t)S_ * D_ * 2;
constexpr size_t OFF_U = OFF_H + (size_t)S_ * D_ * 2;
constexpr size_t OFF_X2 = OFF_U + (size_t)D_ * F_ * 2;
constexpr size_t OFF_O = OFF_X2 + (size_t)S_ * D_ * 2;
constexpr size_t OFF_W1F = OFF_O + (size_t)S_ * D_ * 2;
constexpr size_t OFF_W2F = OFF_W1F + (size_t)S_ * F_ * 2;
constexpr size_t OFF_WQF = OFF_W2F + (size_t)S_ * F_ * 2;
constexpr size_t OFF_WOF = OFF_WQF + (size_t)44 * 20 * 1024;
constexpr size_t WS_NEED = OFF_WOF + (size_t)800 * 1024;
constexpr int NPREP = 1728 + 1728 + 200 + 200;

DI void chan8(const f32x4 (&st)[4], float& mean, float& var) {
    float m = 0.f, m2 = 0.f;
#pragma unroll
    for (int i = 0; i < 4; ++i) { m += st[i][0] + st[i][2]; m2 += st[i][1] + st[i][3]; }
    m *= 0.125f;
#pragma unroll
    for (int i = 0; i < 4; ++i) { const float d0 = st[i][0] - m, d1 = st[i][2] - m; m2 += 80.f * (d0 * d0 + d1 * d1); }
    mean = m; var = m2 * (1.f / 640.f);
}

template <bool FAST>
DI void qkv_finish(char* __restrict__ ws, char* L, int tb, int cg, int w, int lane, f32x4 (&acc)[4][5], f32x4 (&acck)[2],
                   const f32x4 (&csq)[5], const f32x4& cs0, const f32x4& cs1, const f32x4 (&stq)[4], const f32x4 (&stk)[4],
                   const float (&sm)[4], const float (&sq)[4], const float (&cs)[5], const float (&csk)[2]) {
    const int n16 = lane & 15, g = lane >> 4, mt0 = cg >> 1, isv = cg & 1;
    constexpr int R_OFF = 0, PK_OFF = 61440, ST_OFF = 69632, CS_OFF = 71680;
#pragma unroll
    for (int j = 1; j < 4; ++j) {
        const int o = (w + j) & 3;
#pragma unroll
        for (int nt = 0; nt < 5; ++nt) *(f32x4*)(L + R_OFF + (((o * 3 + (j - 1)) * 5 + nt) * 64 + lane) * 16) = acc[j][nt];
    }
    *(f32x4*)(L + PK_OFF + ((w * 2 + 0) * 64 + lane) * 16) = acck[0];
    *(f32x4*)(L + PK_OFF + ((w * 2 + 1) * 64 + lane) * 16) = acck[1];
    if (!FAST && g == 0) {
#pragma unroll
        for (int i = 0; i < 4; ++i) *(f32x2*)(L + ST_OFF + (w * 64 + ((i + w) & 3) * 16 + n16) * 8) = f32x2{sm[i], sq[i]};
#pragma unroll
        for (int nt = 0; nt < 5; ++nt) *(float*)(L + CS_OFF + w * 448 + (nt * 16 + n16) * 4) = cs[nt];
#pragma unroll
        for (int nt = 0; nt < 2; ++nt) *(float*)(L + CS_OFF + w * 448 + (80 + nt * 16 + n16) * 4) = csk[nt];
    }
    char* qrow = ws + OFF_Q + (size_t)(tb * 8 + cg) * 12288 + (16 * w + n16) * 48;
    int qoff[5];
#pragma unroll
    for (int nt = 0; nt < 5; ++nt) { const int G = 4 * nt + g, hl = G / 5; qoff[nt] = hl * 3072 + (G - 5 * hl) * 8; PIN(qoff[nt]); }
    RAW_BARRIER();
    auto stats = [&](int tl, const f32x4 (&st)[4], float& mean, float& rstd) {
        float var;
        if (FAST) chan8(st, mean, var);
        else {
            float s = 0.f, s2 = 0.f;
#pragma unroll
            for (int ww = 0; ww < 4; ++ww) { f32x2 v = *(const f32x2*)(L + ST_OFF + (ww * 64 + tl) * 8); s += v[0]; s2 += v[1]; }
            mean = s * (1.f / 640.f);
            var = fmaxf(s2 * (1.f / 640.f) - mean * mean, 0.f);
        }
        rstd = __builtin_amdgcn_rsqf(var + LN_EPS);
    };
    {
        f32x4 rr[3][5], cc[4][5];
#pragma unroll
        for (int j = 0; j < 3; ++j)
#pragma unroll
            for (int nt = 0; nt < 5; ++nt) rr[j][nt] = *(const f32x4*)(L + R_OFF + (((w * 3 + j) * 5 + nt) * 64 + lane) * 16);
        if (!FAST) {
#pragma unroll
            for (int ww = 0; ww < 4; ++ww)
#pragma unroll
                for (int nt = 0; nt < 5; ++nt) cc[ww][nt] = *(const f32x4*)(L + CS_OFF + ww * 448 + (nt * 16 + 4 * g) * 4);
        }
        float mean, rstd; stats(16 * w + n16, stq, mean, rstd);
        SBAR();
        const float sc = rstd * QSCALE;
        float n2[5];
#pragma unroll
        for (int nt = 0; nt < 5; ++nt) {
            const f32x4 a = (acc[0][nt] + rr[0][nt]) + (rr[1][nt] + rr[2][nt]);
            f32x4 s;
            if (FAST) s = csq[nt];
            else {
                s = (cc[0][nt] + cc[1][nt]) + (cc[2][nt] + cc[3][nt]);
                if (tb == 0 && w == 0 && n16 == 0) *(f32x4*)(ws + OFF_CS + (80 * cg + 16 * nt + 4 * g) * 4) = s;
            }
            const unsigned u0 = pk2((a[0] - mean * s[0]) * sc, (a[1] - mean * s[1]) * sc);
            const unsigned u1 = pk2((a[2] - mean * s[2]) * sc, (a[3] - mean * s[3]) * sc);
            { const u32x2 v_ = u32x2{u0, u1}; ST8(qrow + qoff[nt], v_); }
            n2[nt] = dot2(u1, u1, dot2(u0, u0, 0.f));
        }
        float hn[4];
        hn[0] = n2[0] + (g == 0 ? n2[1] : 0.f);
        hn[1] = (g > 0 ? n2[1] : 0.f) + (g < 2 ? n2[2] : 0.f);
        hn[2] = (g > 1 ? n2[2] : 0.f) + (g < 3 ? n2[3] : 0.f);
        hn[3] = (g == 3 ? n2[3] : 0.f) + n2[4];
#pragma unroll
        for (int hh = 0; hh < 4; ++hh) hn[hh] = add32(add16(hn[hh]));
        const float n2a = (g & 1) ? hn[1] : hn[0], n2b = (g & 1) ? hn[3] : hn[2];
        const unsigned qb = pk2(__builtin_amdgcn_sqrtf((g & 2) ? n2b : n2a), 0.f);
        { const u32x2 v_ = u32x2{qb, 0u}; ST8(qrow + g * 3072 + 40, v_); }
        float m = bf_lo(qb);
        if (!(m == m)) m = INFINITY;
        m = wavemax(m);
        if (lane == 0) *(float*)(ws + OFF_QN + ((tb * 8 + cg) * 4 + w) * 4) = m;
    }
    if (w == 3) {
        const int t16 = tb * 4 + mt0;
        float mean, rstd; stats(mt0 * 16 + n16, stk, mean, rstd);
        f32x4 a0 = {0.f, 0.f, 0.f, 0.f}, a1 = a0, s0 = a0, s1 = a0;
#pragma unroll
        for (int ww = 0; ww < 4; ++ww) {
            a0 += *(const f32x4*)(L + PK_OFF + ((ww * 2 + 0) * 64 + lane) * 16);
            a1 += *(const f32x4*)(L + PK_OFF + ((ww * 2 + 1) * 64 + n16) * 16);
            if (!FAST) {
                s0 += *(const f32x4*)(L + CS_OFF + ww * 448 + (80 + 4 * g) * 4);
                s1 += *(const f32x4*)(L + CS_OFF + ww * 448 + 96 * 4);
            }
        }
        if (FAST) { s0 = cs0; s1 = cs1; }
        else if (tb == 0 && cg < 2 && n16 == 0) {
            *(f32x4*)(ws + OFF_CS + (640 + 32 * isv + 4 * g) * 4) = s0;
            if (g == 0) *(f32x4*)(ws + OFF_CS + (640 + 32 * isv + 16) * 4) = s1;
        }
        const unsigned u0 = pk2((a0[0] - mean * s0[0]) * rstd, (a0[1] - mean * s0[1]) * rstd);
        const unsigned u1 = pk2((a0[2] - mean * s0[2]) * rstd, (a0[3] - mean * s0[3]) * rstd);
        const unsigned u2 = pk2((a1[0] - mean * s1[0]) * rstd, (a1[1] - mean * s1[1]) * rstd);
        const unsigned u3 = pk2((a1[2] - mean * s1[2]) * rstd, (a1[3] - mean * s1[3]) * rstd);
        if (!isv) {
            const int key = t16 * 16 + n16;
            *(u32x2*)(ws + OFF_K1 + key * 32 + g * 8) = u32x2{u0, u1};
            if (g == 0) *(u32x2*)(ws + OFF_K2 + key * 8) = u32x2{u2, u3};
            float n2 = dot2(u1, u1, dot2(u0, u0, 0.f));
            n2 = add32(add16(n2));
            n2 = dot2(u3, u3, dot2(u2, u2, n2));
            if (!(n2 == n2)) n2 = INFINITY;
            n2 = rowmax16(n2);
            if (lane == 0) *(float*)(ws + OFF_KN + t16 * 4) = n2;
        } else {
            u16* vb = (u16*)(ws + OFF_V + t16 * 640 + (n16 >> 3) * 320) + (n16 & 7);
            vb[(4 * g + 0) * 8] = (u16)u0; vb[(4 * g + 1) * 8] = (u16)(u0 >> 16);
            vb[(4 * g + 2) * 8] = (u16)u1; vb[(4 * g + 3) * 8] = (u16)(u1 >> 16);
            if (g == 0) { vb[16 * 8] = (u16)u2; vb[17 * 8] = (u16)(u2 >> 16); vb[18 * 8] = (u16)u3; vb[19 * 8] = (u16)(u3 >> 16); }
        }
    }
    if (w == 0) *(u32x2*)(ws + OFF_XG + ((size_t)(tb * 64 + lane) * 8 + cg) * 8) = u32x2{0u, 0u};
}

constexpr int QKV_LDS = 163840;
template <bool FAST>
__global__ __launch_bounds__(256, 1) void k_qkv(const u16* __restrict__ x, const u16* __restrict__ wq, const u16* __restrict__ wk,
                                                const u16* __restrict__ wv, const u16* __restrict__ wo, char* __restrict__ ws,
                                                const u16* __restrict__ w1, const u16* __restrict__ w2) {
    extern __shared__ __attribute__((aligned(16))) char lds_raw[];
    lds_ptr lds = (lds_ptr)lds_raw;
    char* L = lds_raw;
    const int tid = threadIdx.x, lane = tid & 63, w = __builtin_amdgcn_readfirstlane(tid >> 6);
    const int b = blockIdx.x, slot = b >> 3, tb = ((b & 7) >> 1) * 8 + (slot >> 2), cg = (b & 1) * 4 + (slot & 3);
    const int mt0 = cg >> 1, isv = cg & 1;
    const int n16 = lane & 15, g = lane >> 4;
    SST_DECL; SST(0);
    unsigned code_touch = 0u, wtouch[2] = {0u, 0u};
    if (!FAST && w == 2) {
        uint64_t a[5];
        KADDR(a[0], "_Z6k_attnILb1EEvPcPKtS2_S2_S2_"); KADDR(a[1], "_Z6k_attnILb0EEvPcPKtS2_S2_S2_");
        KADDR(a[2], "_Z5k_mlpILb1EEvPcPKtS2_S2_Pt"); KADDR(a[3], "_Z5k_mlpILb0EEvPcPKtS2_S2_Pt"); KADDR(a[4], "_Z5k_endILi0EEvv");
        uint64_t lo = a[0], hi = a[0];
#pragma unroll
        for (int i = 1; i < 5; ++i) { lo = a[i] < lo ? a[i] : lo; hi = a[i] > hi ? a[i] : hi; }
        const uint64_t pa = lo + (uint64_t)(slot * 64 + lane) * 128;
        if (pa < hi) code_touch = *(const __attribute__((address_space(1))) unsigned*)pa;
        SBAR();
    }
    f32x4 acc[4][5], acck[2];
#pragma unroll
    for (int i = 0; i < 4; ++i)
#pragma unroll
        for (int j = 0; j < 5; ++j) acc[i][j] = f32x4{0.f, 0.f, 0.f, 0.f};
    acck[0] = f32x4{0.f, 0.f, 0.f, 0.f}; acck[1] = acck[0];
    f32x4 csq[5], cs0, cs1, stq[4], stk[4];
    float sm[4] = {0.f, 0.f, 0.f, 0.f}, sq[4] = {0.f, 0.f, 0.f, 0.f};
    float cs[5] = {0.f, 0.f, 0.f, 0.f, 0.f}, csk[2] = {0.f, 0.f};
    if (FAST) {
        const bf16x8* xb = (const bf16x8*)x + (size_t)(tb * 4 * 20 + 5 * w) * 64 + lane;
        const bf16x8* wb = (const bf16x8*)(ws + OFF_WQF) + (size_t)(5 * cg * 20 + 5 * w) * 64 + lane;
        const bf16x8* kb = (const bf16x8*)(ws + OFF_WQF) + (size_t)((40 + 2 * isv) * 20 + 5 * w) * 64 + lane;
        bf16x8 xf[5][4], xk[5], wf[5][5], wkf[5][2];
#define QKV_LD(ks) do { _Pragma("unroll") for (int i = 0; i < 4; ++i) xf[ks][i] = xb[(((i + w) & 3) * 20 + (ks)) * 64]; \
        _Pragma("unroll") for (int nt = 0; nt < 5; ++nt) wf[ks][nt] = wb[(nt * 20 + (ks)) * 64]; \
        xk[ks] = xb[(mt0 * 20 + (ks)) * 64]; wkf[ks][0] = kb[(ks) * 64]; wkf[ks][1] = kb[(20 + (ks)) * 64]; SBAR(); } while (0)
        QKV_LD(0); QKV_LD(1); QKV_LD(2); QKV_LD(3);
#pragma unroll
        for (int ks = 0; ks < 5; ++ks) {
#pragma unroll
            for (int nt = 0; nt < 5; ++nt)
#pragma unroll
                for (int i = 0; i < 4; ++i) acc[i][nt] = MFMA16_32(wf[ks][nt], xf[ks][i], acc[i][nt]);
            acck[0] = MFMA16_32(wkf[ks][0], xk[ks], acck[0]);
            acck[1] = MFMA16_32(wkf[ks][1], xk[ks], acck[1]);
            SBAR();
            if (ks == 0) {
                QKV_LD(4);
                const float* csp = (const float*)(ws + OFF_CS);
#pragma unroll
                for (int nt = 0; nt < 5; ++nt) csq[nt] = *(const f32x4*)(csp + 80 * cg + 16 * nt + 4 * g);
                cs0 = *(const f32x4*)(csp + 640 + 32 * isv + 4 * g); cs1 = *(const f32x4*)(csp + 640 + 32 * isv + 16);
                const f32x4* sp = (const f32x4*)(ws + OFF_XS) + (size_t)(tb * 64 + n16) * 4;
#pragma unroll
                for (int i = 0; i < 4; ++i) { stq[i] = sp[16 * w * 4 + i]; stk[i] = sp[16 * mt0 * 4 + i]; }
                SBAR();
            }
        }
    } else {
        lds_ptr ra = lds + w * 40960, rb = ra + 20480;
        const char* xs = (const char*)(x + (size_t)(tb * 64) * 640 + 160 * w);
#pragma unroll
        for (int j = 0; j < 5; ++j) {
            const int c = 64 * j + lane;
            const unsigned xo = (unsigned)(c / 20) * 1280u + (unsigned)(c % 20) * 16u;
#pragma unroll
            for (int i = 0; i < 4; ++i) glds16(xs + (size_t)i * 20480 + xo, ra + i * 5120 + j * 1024);
        }
        const char* src = (const char*)(wq + (size_t)(160 * w) * 640 + 80 * cg);
        const unsigned loff = (unsigned)(lane / 10) * 1280u + (unsigned)(lane % 10) * 16u;
        const char* src2 = (const char*)((isv ? wv : wk) + (size_t)(160 * w) * 20);
#pragma unroll
        for (int i = 0; i < 16; ++i) glds16(src + (size_t)i * 7680 + loff, rb + i * 960);
#pragma unroll
        for (int i = 0; i < 3; ++i) glds16(src2 + i * 1024 + lane * 16, rb + 15488 + i * 1024);
        if (lane < 48) glds16(src2 + 3 * 1024 + lane * 16, rb + 15488 + 3 * 1024);
        SST(1);
        VMCNT(20);
        bf16x8 xf[4][5], xk[5];
        {
            const char* xl = L + w * 40960 + n16 * 320 + g * 16;
#pragma unroll
            for (int i = 0; i < 4; ++i)
#pragma unroll
                for (int ks = 0; ks < 5; ++ks) xf[i][ks] = *(const bf16x8*)(xl + ((i + w) & 3) * 5120 + ks * 64);
#pragma unroll
            for (int ks = 0; ks < 5; ++ks) xk[ks] = *(const bf16x8*)(xl + mt0 * 5120 + ks * 64);
        }
        asm volatile("s_waitcnt lgkmcnt(0)" ::: "memory");
        SBAR();
#pragma unroll
        for (int i = 0; i < 10; ++i) glds16(src + (size_t)(16 + i) * 7680 + loff, ra + i * 960);
        if (lane < 40) glds16(src + (size_t)26 * 7680 + loff, ra + 10 * 960);
#pragma unroll
        for (int i = 0; i < 2; ++i) glds16(src2 + 3840 + i * 1024 + lane * 16, ra + 10240 + i * 1024);
        if (lane < 32) glds16(src2 + 3840 + 2 * 1024 + lane * 16, ra + 10240 + 2 * 1024);
        SST(2);
        const unsigned ONES = 0x3f803f80u;
#pragma unroll
        for (int i = 0; i < 4; ++i)
#pragma unroll
            for (int ks = 0; ks < 5; ++ks) {
                u32x4 u = __builtin_bit_cast(u32x4, xf[i][ks]);
#pragma unroll
                for (int e = 0; e < 4; ++e) { sm[i] = dot2(u[e], ONES, sm[i]); sq[i] = dot2(u[e], u[e], sq[i]); }
            }
        SBAR();
        VMCNT(14);
        SST(3);
        const int q4 = (lane & 15) >> 2, p4 = lane & 3;
        lds_cptr wb0 = (lds_cptr)rb + (8 * g + q4) * 160 + p4 * 8, wb1 = (lds_cptr)ra + (8 * g + q4) * 160 + p4 * 8 - 3 * 5120;
        lds_cptr kb0 = (lds_cptr)rb + 15488 + (8 * g + q4) * 40 + p4 * 8, kb1 = (lds_cptr)ra + 10240 + (8 * g + q4) * 40 + p4 * 8 - 3 * 1280;
        bf16x8 wf[2][7];
#define QKV_TR(ks, s_) do { lds_cptr wb = (ks) < 3 ? wb0 : wb1, kb = (ks) < 3 ? kb0 : kb1; \
        _Pragma("unroll") for (int nt = 0; nt < 5; ++nt) wf[s_][nt] = cat8(tr_read(wb + (ks) * 5120 + nt * 32), tr_read(wb + (ks) * 5120 + 640 + nt * 32)); \
        _Pragma("unroll") for (int nt = 0; nt < 2; ++nt) wf[s_][5 + nt] = cat8(tr_read(kb + (ks) * 1280 + nt * 32), tr_read(kb + (ks) * 1280 + 160 + nt * 32)); SBAR(); } while (0)
        QKV_TR(0, 0);
#pragma unroll
        for (int ks = 0; ks < 5; ++ks) {
            if (ks == 2) {
                VMCNT(0); SBAR();
            }
            if (ks < 4) QKV_TR(ks + 1, (ks + 1) & 1);
#pragma unroll
            for (int nt = 0; nt < 5; ++nt) {
                u32x4 u = __builtin_bit_cast(u32x4, wf[ks & 1][nt]);
#pragma unroll
                for (int e = 0; e < 4; ++e) cs[nt] = dot2(u[e], ONES, cs[nt]);
#pragma unroll
                for (int i = 0; i < 4; ++i) acc[i][nt] = MFMA16_32(wf[ks & 1][nt], xf[i][ks], acc[i][nt]);
            }
#pragma unroll
            for (int nt = 0; nt < 2; ++nt) {
                u32x4 u = __builtin_bit_cast(u32x4, wf[ks & 1][5 + nt]);
#pragma unroll
                for (int e = 0; e < 4; ++e) csk[nt] = dot2(u[e], ONES, csk[nt]);
                acck[nt] = MFMA16_32(wf[ks & 1][5 + nt], xk[ks], acck[nt]);
            }
            SBAR();
        }
#pragma unroll
        for (int i = 0; i < 4; ++i) { sm[i] = add32(add16(sm[i])); sq[i] = add32(add16(sq[i])); }
#pragma unroll
        for (int i = 0; i < 5; ++i) cs[i] = add32(add16(cs[i]));
#pragma unroll
        for (int i = 0; i < 2; ++i) csk[i] = add32(add16(csk[i]));
    }
    SST(4);
    if (!FAST) RAW_BARRIER();
    qkv_finish<FAST>(ws, L, tb, cg, w, lane, acc, acck, csq, cs0, cs1, stq, stk, sm, sq, cs, csk);
    if (!FAST && w == 1) {
        if (b < 80) {
            const int ks = b >> 2, ct = b & 3; int cn = 16 * (ct & 1) + n16; if (cn >= 20) cn = 0;
            *(u32x4*)(ws + OFF_WQF + (size_t)((40 + ct) * 20 + ks) * 1024 + lane * 16) =
                gather8(((ct & 2) ? wv : wk) + (size_t)(32 * ks + 8 * g) * 20 + cn, 20);
        } else if (b == 80 && lane < 4) {
            const unsigned v = (lane & 2) ? 0x3f803f80u : 0u;
            *(u32x4*)(ws + OFF_CONST + lane * 16) = u32x4{v, v, v, v};
        }
    }
    PIN(code_touch); PIN(wtouch[0]); PIN(wtouch[1]);
    SST(7); SST_FLUSHK(ws, FAST ? 5 : 0);
}

constexpr int PJ0 = 1, PJ1 = 8, PJD = 5;
constexpr int PBW = 2688, ATT_LDS = 32 * PBW;
template <bool PREP>
__global__ __launch_bounds__(512, 2) void k_attn(char* __restrict__ ws, const u16* __restrict__ w1, const u16* __restrict__ w2,
                                                 const u16* __restrict__ wo, const u16* __restrict__ wq) {
    extern __shared__ __attribute__((aligned(16))) char lds_raw[];
    char* L = lds_raw;
    const int tid = threadIdx.x, lane = tid & 63, w = __builtin_amdgcn_readfirstlane(tid >> 6);
    const int b = blockIdx.x, slot = b >> 3, tb = ((b & 7) >> 1) * 8 + (slot >> 2), cg = (b & 1) * 4 + (slot & 3);
    const int r32 = lane & 31, h = lane >> 5, n16 = lane & 15, g = lane >> 4;
    const int cgp = w & 1, kq = w >> 1;
    SST_DECL; SST(0);
    float kn0 = *(const float*)(ws + OFF_KN + lane * 4), kn1 = *(const float*)(ws + OFF_KN + 256 + lane * 4);
    float qmax = *(const float*)(ws + OFF_QN + ((tb * 8 + cg) * 4 + (lane & 3)) * 4);
    bf16x8 q1[4]; bf16x4 q2[4];
    {
        const char* qg = ws + OFF_Q + (size_t)(tb * 8 + cg) * 12288 + cgp * 6144 + r32 * 48;
#pragma unroll
        for (int j = 0; j < 4; ++j) { q1[j] = *(const bf16x8*)(qg + j * 1536 + 16 * h); q2[j] = *(const bf16x4*)(qg + j * 1536 + 32 + 8 * h); }
    }
    SBAR();
    const int kr = (r32 & 19) | ((r32 & 4) << 1) | ((r32 & 8) >> 1);
    const unsigned ko1 = (unsigned)OFF_K1 + (unsigned)(512 * kq + kr) * 32u + 16u * h;
    const unsigned ko2 = h ? (unsigned)OFF_CONST + 32u : (unsigned)OFF_K2 + (unsigned)(512 * kq + kr) * 8u;
    const unsigned ks2 = h ? 0u : 256u;
    const unsigned vo = r32 < 20 ? (unsigned)OFF_V + (unsigned)(32 * kq) * 640u + (unsigned)(h * 20 + r32) * 16u
                                 : (unsigned)OFF_CONST + (r32 == 20 ? 32u : 0u);
    const unsigned vs = r32 < 20 ? 640u : 0u;
    bf16x8 k1[3], v0[3], v1[3]; bf16x4 k2[3];
#define K_LOAD(t) do { k1[(t) % 3] = *(const bf16x8*)(ws + ko1 + (t) * 1024); k2[(t) % 3] = *(const bf16x4*)(ws + (ko2 + (t) * ks2)); } while (0)
#define V_LOAD(t) do { v0[(t) % 3] = *(const bf16x8*)(ws + (vo + (2 * (t)) * vs)); v1[(t) % 3] = *(const bf16x8*)(ws + (vo + (2 * (t) + 1) * vs)); } while (0)
    K_LOAD(0); V_LOAD(0); SBAR(); K_LOAD(1); V_LOAD(1); SBAR();
    PIN(kn0); PIN(kn1);
    const float kmax2 = wavemax(fmaxf(kn0, kn1));
    qmax = rowmax16(qmax);
    const float kb = __builtin_amdgcn_sqrtf(kmax2);
    const bool safe = __builtin_amdgcn_readfirstlane((int)(qmax * kb <= SAFE_BOUND)) != 0;
    const f32x16 zero16 = {0.f, 0.f, 0.f, 0.f, 0.f, 0.f, 0.f, 0.f, 0.f, 0.f, 0.f, 0.f, 0.f, 0.f, 0.f, 0.f};
    if (safe) {
        if (h) {
#pragma unroll
            for (int j = 0; j < 4; ++j) q2[j][0] = (short)pk2(-bf2f((u16)q2[j][0]) * kb, 0.f);
        }
    } else {
        float m[4];
#pragma unroll
        for (int j = 0; j < 4; ++j) { m[j] = -INFINITY; if (h) q2[j] = bf16x4{0, 0, 0, 0}; }
        for (int t = 0; t < 16; ++t) {
            const bf16x8 ka = *(const bf16x8*)(ws + ko1 + t * 1024);
            const bf16x4 kc = *(const bf16x4*)(ws + (ko2 + t * ks2));
#pragma unroll
            for (int j = 0; j < 4; ++j) {
                f32x16 s = MFMA32_16(ka, q1[j], zero16);
                SBAR(); asm volatile("s_nop 15" ::: "memory"); SBAR();
                s = MFMA32_8(kc, q2[j], s);
#pragma unroll
                for (int e = 0; e < 16; ++e) m[j] = fmaxf(m[j], s[e]);
                SBAR();
            }
        }
#pragma unroll
        for (int j = 0; j < 4; ++j) {
            const float mm = fmaxf(m[j], xor32(m[j]));
            if (h == 0) *(float*)(L + ((w * 4 + j) * 32 + r32) * 4) = mm;
        }
        RAW_BARRIER();
#pragma unroll
        for (int j = 0; j < 4; ++j) {
            float mm = -INFINITY;
#pragma unroll
            for (int qq = 0; qq < 4; ++qq) mm = fmaxf(mm, *(const float*)(L + (((2 * qq + cgp) * 4 + j) * 32 + r32) * 4));
            const unsigned hi = __float_as_uint(mm) & 0xffff0000u;
            const float r1 = mm - __uint_as_float(hi);
            const unsigned mid = __float_as_uint(r1) & 0xffff0000u;
            const float r2 = r1 - __uint_as_float(mid);
            const unsigned lo = __float_as_uint(r2) & 0xffff0000u;
            if (h) q2[j] = __builtin_bit_cast(bf16x4, u32x2{hi ^ 0x80000000u, ((mid ^ 0x80000000u) >> 16) | (lo ^ 0x80000000u)});
        }
        RAW_BARRIER();
    }
    SST(1);
    f32x16 o[4], sa, sb;
#pragma unroll
    for (int j = 0; j < 4; ++j) o[j] = zero16;
    u32x4 p1prev = {0u, 0u, 0u, 0u};
    f32x4 ea, eb;
    sa = MFMA32_16(k1[0], q1[0], zero16);
    SBAR(); asm volatile("s_nop 15" ::: "memory"); SBAR();
    sa = MFMA32_8(k2[0], q2[0], sa);
    SBAR(); asm volatile("s_nop 15" ::: "memory"); SBAR();
    u32x4 prep[4]; char* prep_dst = nullptr; int prep_st = 0;
    unsigned l2t[3] = {0u, 0u, 0u};
#pragma unroll
    for (int t = 0; t < 16; ++t) {
#pragma unroll
        for (int j = 0; j < 4; ++j) {
            const int it = 4 * t + j;
            f32x16& cur = (it & 1) ? sb : sa; f32x16& nxt = (it & 1) ? sa : sb;
            const int tn = j < 3 ? t : t + 1, jn = (j + 1) & 3;
            const int tp = j > 0 ? t : t - 1, jp = (j + 3) & 3;
            u32x4 p0, p1;
            if (j == 0 && t + 2 < 16) K_LOAD(t + 2);
            if (j == 1 && t + 2 < 16) V_LOAD(t + 2);
            if (PREP && j == 2 && (t == PJ0 || t == PJ1)) {
                int id = (t == PJ1 ? 2048 : 0) + b * 8 + w; if (id >= NPREP) id -= 2048;
                const u16* src; int ld;
                if (id < 1728) { const int ks = id / 27, c4 = id - ks * 27; src = w1 + (size_t)(32 * ks) * F_ + 64 * c4; ld = F_;
                                 prep_dst = ws + OFF_W1F + (size_t)(4 * c4 * 64 + ks) * 1024; prep_st = 64 * 1024; }
                else if (id < 3456) { const int jj = id - 1728, ks = jj >> 5, c4 = jj & 31; src = w2 + (size_t)(32 * ks) * S_ + 64 * c4; ld = S_;
                                      prep_dst = ws + OFF_W2F + (size_t)(4 * c4 * 54 + ks) * 1024; prep_st = 54 * 1024; }
                else { int jj = id - 3456; const bool isq = jj >= 200; if (isq) jj -= 200; const int ks = jj / 10, c4 = jj - ks * 10;
                       src = (isq ? wq : wo) + (size_t)(32 * ks) * 640 + 64 * c4; ld = 640;
                       prep_dst = ws + (isq ? OFF_WQF : OFF_WOF) + (size_t)(4 * c4 * 20 + ks) * 1024; prep_st = 20 * 1024; }
                src += (size_t)(lane >> 3) * ld + (lane & 7) * 8;
#pragma unroll
                for (int i = 0; i < 4; ++i) prep[i] = *(const u32x4*)(src + (size_t)(8 * i) * ld);
            }
            if (PREP && j == 2 && (t == PJ0 + PJD || t == PJ1 + PJD)) {
#pragma unroll
                for (int i = 0; i < 4; ++i) *(u32x4*)(L + w * 4096 + i * 1024 + lane * 16) = prep[i];
            }
            if (PREP && j == 3 && (t == PJ0 + PJD || t == PJ1 + PJD)) {
                lds_cptr tp_ = (lds_cptr)(lds_ptr)L + w * 4096 + (8 * g + (n16 >> 2)) * 128 + (lane & 3) * 8;
#pragma unroll
                for (int i = 0; i < 4; ++i) prep[i] = __builtin_bit_cast(u32x4, cat8(tr_read(tp_ + i * 32), tr_read(tp_ + 512 + i * 32)));
            }
            if (PREP && j == 0 && (t == PJ0 + PJD + 1 || t == PJ1 + PJD + 1)) {
#pragma unroll
                for (int i = 0; i < 4; ++i) PREP_STORE((u32x4*)(prep_dst + (size_t)i * prep_st + lane * 16), prep[i]);
            }
            SBAR();
            if (it < 63) nxt = MFMA32_16(k1[tn % 3], q1[jn], zero16);
            SBAR();
            p0[0] = pk2(EX2(cur[0]), EX2(cur[1])); p0[1] = pk2(EX2(cur[2]), EX2(cur[3]));
            PIN(p0); SBAR();
            if (it > 0) o[jp] = MFMA32_16(v1[(tp + 3) % 3], __builtin_bit_cast(bf16x8, p1prev), o[jp]);
            SBAR();
            p0[2] = pk2(EX2(cur[4]), EX2(cur[5])); p0[3] = pk2(EX2(cur[6]), EX2(cur[7]));
            PIN(p0); SBAR();
            if (it < 63) nxt = MFMA32_8(k2[tn % 3], q2[jn], nxt);
            SBAR();
            p1[0] = pk2(EX2(cur[8]), EX2(cur[9])); p1[1] = pk2(EX2(cur[10]), EX2(cur[11]));
            PIN(p1); SBAR();
            o[j] = MFMA32_16(v0[t % 3], __builtin_bit_cast(bf16x8, p0), o[j]);
            SBAR();
            p1[2] = pk2(EX2(cur[12]), EX2(cur[13])); p1[3] = pk2(EX2(cur[14]), EX2(cur[15]));
            PIN(p1); SBAR();
            p1prev = p1;
        }
        if (t == 7) SST(2);
    }
    o[3] = MFMA32_16(v1[15 % 3], __builtin_bit_cast(bf16x8, p1prev), o[3]);
    SST(3);
    if (PREP) RAW_BARRIER();
#pragma unroll
    for (int j = 0; j < 4; ++j) {
        char* pb = L + (kq * 8 + cgp * 4 + j) * PBW;
        *(f32x4*)(pb + lane * 16) = f32x4{o[j][0], o[j][1], o[j][2], o[j][3]};
        *(f32x4*)(pb + 1024 + lane * 16) = f32x4{o[j][4], o[j][5], o[j][6], o[j][7]};
        if (h == 0) *(f32x4*)(pb + 2048 + r32 * 16) = f32x4{o[j][8], o[j][9], o[j][10], o[j][11]};
        else *(float*)(pb + 2560 + r32 * 4) = o[j][8];
    }
    RAW_BARRIER();
    SST(4);
    {
        const char* pb = L + w * PBW;
        f32x4 a0 = {0.f, 0.f, 0.f, 0.f}, a1 = a0, a2 = a0; float l = 0.f;
#pragma unroll
        for (int qq = 0; qq < 4; ++qq) {
            a0 += *(const f32x4*)(pb + qq * 8 * PBW + lane * 16);
            a1 += *(const f32x4*)(pb + qq * 8 * PBW + 1024 + lane * 16);
            a2 += *(const f32x4*)(pb + qq * 8 * PBW + 2048 + r32 * 16);
            l += *(const float*)(pb + qq * 8 * PBW + 2560 + r32 * 4);
        }
        const float inv = __builtin_amdgcn_rcpf(l);
        const int tl = 32 * (w & 1) + r32, hd0 = 80 * cg + 20 * (w >> 1);
        char* ob = ws + OFF_O + (size_t)(tb * 4 + (tl >> 4)) * 20480 + (tl & 15) * 16;
        auto st = [&](int hd, const f32x4& a) {
            const u32x2 v_ = u32x2{pk2(a[0] * inv, a[1] * inv), pk2(a[2] * inv, a[3] * inv)};
            ST8(ob + (hd >> 5) * 1024 + ((hd & 31) >> 3) * 256 + (hd & 7) * 2, v_);
        };
        st(hd0 + 4 * h, a0); st(hd0 + 8 + 4 * h, a1);
        if (h == 0) st(hd0 + 16, a2);
    }
    if (b == 0 && tid < 48) *(unsigned*)(ws + OFF_CNT + tid * 256) = 0u;
    PIN(l2t[0]); PIN(l2t[1]); PIN(l2t[2]);
    SST(5); SST_FLUSHK(ws, PREP ? 1 : 6);
}

template <bool XF>
__global__ __launch_bounds__(256, 1) void k_wo(char* __restrict__ ws, const u16* __restrict__ xin) {
    __shared__ __attribute__((aligned(16))) char L[61440];
    const int tid = threadIdx.x, lane = tid & 63, w = __builtin_amdgcn_readfirstlane(tid >> 6);
    const int b = blockIdx.x, slot = b >> 3, tb = ((b & 7) >> 1) * 8 + (slot >> 2), cg = (b & 1) * 4 + (slot & 3);
    const int n16 = lane & 15, g = lane >> 4;
    SST_DECL; SST(0);
    f32x4 acc[4][5];
#pragma unroll
    for (int i = 0; i < 4; ++i)
#pragma unroll
        for (int j = 0; j < 5; ++j) acc[i][j] = f32x4{0.f, 0.f, 0.f, 0.f};
    const int tok = tb * 64 + 16 * w + n16;
    u32x2 xi[5];
    {
        const bf16x8* xb = (const bf16x8*)(ws + OFF_O) + (size_t)(tb * 4 * 20 + 5 * w) * 64 + lane;
        const bf16x8* wb = (const bf16x8*)(ws + OFF_WOF) + (size_t)(5 * cg * 20 + 5 * w) * 64 + lane;
        bf16x8 xf[5][4], wf[5][5];
#define WO_LD(ks) do { _Pragma("unroll") for (int i = 0; i < 4; ++i) xf[ks][i] = xb[(((i + w) & 3) * 20 + (ks)) * 64]; \
        _Pragma("unroll") for (int nt = 0; nt < 5; ++nt) wf[ks][nt] = wb[(nt * 20 + (ks)) * 64]; SBAR(); } while (0)
        WO_LD(0); WO_LD(1); WO_LD(2); WO_LD(3);
#pragma unroll
        for (int ks = 0; ks < 5; ++ks) {
#pragma unroll
            for (int nt = 0; nt < 5; ++nt)
#pragma unroll
                for (int i = 0; i < 4; ++i) acc[i][nt] = MFMA16_32(wf[ks][nt], xf[ks][i], acc[i][nt]);
            SBAR();
            if (ks == 0) {
                WO_LD(4);
#pragma unroll
                for (int nt = 0; nt < 5; ++nt) {
                    const int d = 80 * cg + 16 * nt + 4 * g;
                    if (XF) xi[nt] = *(const u32x2*)(xin + ((size_t)((tok >> 4) * 20 + (d >> 5)) * 64 + ((d & 31) >> 3) * 16 + n16) * 8 + (d & 7));
                    else xi[nt] = *(const u32x2*)(xin + (size_t)tok * 640 + d);
                }
                SBAR();
            }
        }
    }
    SST(1);
#pragma unroll
    for (int j = 1; j < 4; ++j) {
        const int o = (w + j) & 3;
#pragma unroll
        for (int nt = 0; nt < 5; ++nt) *(f32x4*)(L + (((o * 3 + (j - 1)) * 5 + nt) * 64 + lane) * 16) = acc[j][nt];
    }
    RAW_BARRIER();
    SST(2);
    float xv[20]; u32x2 x1o[5];
    {
        f32x4 rr[3][5];
#pragma unroll
        for (int j = 0; j < 3; ++j)
#pragma unroll
            for (int nt = 0; nt < 5; ++nt) rr[j][nt] = *(const f32x4*)(L + (((w * 3 + j) * 5 + nt) * 64 + lane) * 16);
        SBAR();
        float s = 0.f;
#pragma unroll
        for (int nt = 0; nt < 5; ++nt) {
            const f32x4 a = (acc[0][nt] + rr[0][nt]) + (rr[1][nt] + rr[2][nt]);
            const unsigned u0 = pk2(bf_lo(xi[nt][0]) + a[0], bf_hi(xi[nt][0]) + a[1]);
            const unsigned u1 = pk2(bf_lo(xi[nt][1]) + a[2], bf_hi(xi[nt][1]) + a[3]);
            x1o[nt] = u32x2{u0, u1};
            xv[4 * nt] = bf_lo(u0); xv[4 * nt + 1] = bf_hi(u0); xv[4 * nt + 2] = bf_lo(u1); xv[4 * nt + 3] = bf_hi(u1);
            s += (xv[4 * nt] + xv[4 * nt + 1]) + (xv[4 * nt + 2] + xv[4 * nt + 3]);
        }
        const float mw = add32(add16(s)) * (1.f / 80.f);
        float s2 = 0.f;
#pragma unroll
        for (int i = 0; i < 20; ++i) { const float dd = xv[i] - mw; s2 += dd * dd; }
        s2 = add32(add16(s2));
        gu64* gp = (gu64*)(ws + OFF_XG) + (size_t)tok * 8 + cg;
        if (g == 0) __hip_atomic_store(gp, ((unsigned long long)(__float_as_uint(s2) | 0x80000000u) << 32) | __float_as_uint(mw),
                                       __ATOMIC_RELAXED, __HIP_MEMORY_SCOPE_AGENT);
    }
    SST(3);
    float mean, var;
    {
        gu64* gp = (gu64*)(ws + OFF_XG) + (size_t)tok * 8 + 2 * g;
        unsigned long long v0, v1;
        for (int spin = 0; spin < (1 << 20); ++spin) {
            v0 = __hip_atomic_load(gp, __ATOMIC_RELAXED, __HIP_MEMORY_SCOPE_AGENT);
            v1 = __hip_atomic_load(gp + 1, __ATOMIC_RELAXED, __HIP_MEMORY_SCOPE_AGENT);
            if (__all((long long)(v0 & v1) < 0)) break;
            __builtin_amdgcn_s_sleep(4);
        }
        const float m0 = __uint_as_float((unsigned)v0), m1 = __uint_as_float((unsigned)v1);
        mean = add32(add16(m0 + m1)) * 0.125f;
        const float d0 = m0 - mean, d1 = m1 - mean;
        var = add32(add16(__uint_as_float((unsigned)(v0 >> 32) & 0x7fffffffu) + __uint_as_float((unsigned)(v1 >> 32) & 0x7fffffffu) +
                          80.f * (d0 * d0 + d1 * d1))) * (1.f / 640.f);
    }
    SST(4);
    {
        const float r1 = __builtin_amdgcn_rsqf(var + LN_EPS);
        const float r2 = r1 * __builtin_amdgcn_rsqf(var * r1 * r1 + LN_EPS);
        char* hb = L + w * 15360;
#pragma unroll
        for (int nt = 0; nt < 5; ++nt)
            *(u32x2*)(hb + n16 * 176 + (16 * nt + 4 * g) * 2) =
                u32x2{pk2((xv[4 * nt] - mean) * r2, (xv[4 * nt + 1] - mean) * r2), pk2((xv[4 * nt + 2] - mean) * r2, (xv[4 * nt + 3] - mean) * r2)};
        asm volatile("s_waitcnt lgkmcnt(0)" ::: "memory");
        const int q4 = (lane & 15) >> 2, p4 = lane & 3;
        lds_cptr hr = (lds_cptr)(lds_ptr)L + w * 15360 + (8 * (g & 1) + q4) * 176 + p4 * 8 + (g >> 1) * 32;
        const int s0 = tb * 64 + 16 * w + 8 * (g & 1);
        u16* hf = (u16*)(ws + OFF_H) + ((size_t)(5 * cg + (g >> 1)) * 64 + (s0 >> 5)) * 512 + (((s0 & 31) >> 3) * 16 + n16) * 8;
        const bf16x8 f0 = cat8(tr_read(hr), tr_read(hr + 704));
        const bf16x8 f1 = cat8(tr_read(hr + 64), tr_read(hr + 704 + 64));
        const bf16x8 f2 = cat8(tr_read(hr + (g < 2 ? 128 : 0)), tr_read(hr + 704 + (g < 2 ? 128 : 0)));
        *(bf16x8*)(hf) = f0;
        *(bf16x8*)(hf + 2 * 64 * 512) = f1;
        if (g < 2) *(bf16x8*)(hf + 4 * 64 * 512) = f2;
        u16* x1r = (u16*)(ws + OFF_X1) + (size_t)tok * 640 + 80 * cg + 4 * g;
#pragma unroll
        for (int nt = 0; nt < 5; ++nt) *(u32x2*)(x1r + 16 * nt) = x1o[nt];
    }
    SST(5); SST_FLUSHK(ws, XF ? 7 : 2);
}

template <int MODE, int NS, int PF>
DI void gemm_body(const bf16x8* __restrict__ po, const bf16x8* __restrict__ pf, const int (&ooff)[4], int fstride, f32x4 (&acc)[4][5]) {
    bf16x8 ro[PF][4], rf[PF][5];
#pragma unroll
    for (int s = 0; s < PF; ++s) {
#pragma unroll
        for (int j = 0; j < 4; ++j) ro[s][j] = po[ooff[j] + s * 64];
#pragma unroll
        for (int i = 0; i < 5; ++i) rf[s][i] = pf[i * fstride + s * 64];
        SBAR();
    }
#pragma unroll
    for (int s = 0; s < NS; ++s) {
        const int sl = s % PF;
        SBAR();
#pragma unroll
        for (int j = 0; j < 4; ++j)
#pragma unroll
            for (int i = 0; i < 5; ++i) {
                if (MODE == 1) acc[j][i] = MFMA16_32(ro[sl][j], rf[sl][i], acc[j][i]);
                else acc[j][i] = MFMA16_32(rf[sl][i], ro[sl][j], acc[j][i]);
            }
        SBAR();
        if (s + PF < NS) {
#pragma unroll
            for (int j = 0; j < 4; ++j) ro[sl][j] = po[ooff[j] + (s + PF) * 64];
#pragma unroll
            for (int i = 0; i < 5; ++i) rf[sl][i] = pf[i * fstride + (s + PF) * 64];
        }
    }
}

template <int MODE>
__global__ __launch_bounds__(256, 1) void k_gemm(char* __restrict__ ws, const u16* __restrict__ bias, u16* __restrict__ out) {
    __shared__ __attribute__((aligned(16))) f32x4 red[4 * 3 * 5 * 64];
    const int tid = threadIdx.x, lane = tid & 63, w = __builtin_amdgcn_readfirstlane(tid >> 6);
    const int b = blockIdx.x, xcd = b & 7, slot = b >> 3;
    const int n16 = lane & 15, g = lane >> 4;
    constexpr int KS = MODE == 1 ? 64 : 54;
    int ot, dt;
    if (MODE == 1) {
        ot = 7 * (xcd >> 1) + (slot >> 2); dt = 4 * (xcd & 1) + (slot & 3);
        if (slot >= 28 || ot >= 27) return;
    } else {
        ot = 8 * (xcd >> 1) + (slot >> 2); dt = 4 * (xcd & 1) + (slot & 3);
    }
    SST_DECL; SST(0);
    u32x2 bb = {0u, 0u}, xi[5]; float bs = 0.f;
    if (MODE == 1) bb = *(const u32x2*)(bias + 64 * ot + 16 * w + 4 * g);
    else {
        const int s = 64 * ot + 16 * w + n16;
        bs = bf2f(bias[s]);
        const u16* x1 = (const u16*)(ws + OFF_X1) + (size_t)s * 640 + 80 * dt + 4 * g;
#pragma unroll
        for (int i = 0; i < 5; ++i) xi[i] = *(const u32x2*)(x1 + 16 * i);
    }
    const int k0 = MODE == 1 ? 16 * w : (w < 2 ? 14 * w : 28 + 13 * (w - 2));
    const bf16x8* po = (const bf16x8*)(ws + (MODE == 1 ? OFF_W1F : OFF_W2F)) + ((size_t)(4 * ot) * KS + k0) * 64 + lane;
    const bf16x8* pf = (const bf16x8*)(ws + (MODE == 1 ? OFF_H : OFF_U)) + ((size_t)(5 * dt) * KS + k0) * 64 + lane;
    int ooff[4];
#pragma unroll
    for (int j = 0; j < 4; ++j) ooff[j] = ((j + w) & 3) * KS * 64;
    f32x4 acc[4][5];
#pragma unroll
    for (int j = 0; j < 4; ++j)
#pragma unroll
        for (int i = 0; i < 5; ++i) acc[j][i] = f32x4{0.f, 0.f, 0.f, 0.f};
    if (MODE == 1) gemm_body<1, 16, 6>(po, pf, ooff, KS * 64, acc);
    else if (w < 2) gemm_body<2, 14, 6>(po, pf, ooff, KS * 64, acc);
    else gemm_body<2, 13, 6>(po, pf, ooff, KS * 64, acc);
    SST(1);
#pragma unroll
    for (int j = 1; j < 4; ++j) {
        const int o = (w + j) & 3;
#pragma unroll
        for (int i = 0; i < 5; ++i) red[((o * 3 + (j - 1)) * 5 + i) * 64 + lane] = acc[j][i];
    }
    RAW_BARRIER();
#pragma unroll
    for (int j = 0; j < 3; ++j)
#pragma unroll
        for (int i = 0; i < 5; ++i) acc[1 + j][i] = red[((w * 3 + j) * 5 + i) * 64 + lane];
    SBAR();
#pragma unroll
    for (int i = 0; i < 5; ++i) acc[0][i] = (acc[0][i] + acc[1][i]) + (acc[2][i] + acc[3][i]);
    SST(2);
    if (MODE == 1) {
        const int f = 64 * ot + 16 * w + 4 * g;
        const float bv[4] = {bf_lo(bb[0]), bf_hi(bb[0]), bf_lo(bb[1]), bf_hi(bb[1])};
        u16* uf = (u16*)(ws + OFF_U);
#pragma unroll
        for (int i = 0; i < 5; ++i) {
            float r[4];
#pragma unroll
            for (int e = 0; e < 4; ++e) {
                const float u = acc[0][i][e] + bv[e];
                const float z = u * (1.f + 0.044715f * u * u) * (-2.f * 0.7978845608028654f * 1.4426950408889634f);
                r[e] = u * __builtin_amdgcn_rcpf(1.f + EX2(z));
            }
            *(u32x2*)(uf + ((size_t)((5 * dt + i) * 54 + (f >> 5)) * 64 + ((f & 31) >> 3) * 16 + n16) * 8 + (f & 7)) =
                u32x2{pk2(r[0], r[1]), pk2(r[2], r[3])};
        }
    } else {
        const int s = 64 * ot + 16 * w + n16;
        float xv[20]; float sum = 0.f;
#pragma unroll
        for (int i = 0; i < 5; ++i) {
            const int d = 80 * dt + 16 * i + 4 * g;
            u16* o = MODE == 3 ? out + (size_t)s * 640 + d
                               : out + ((size_t)((4 * ot + w) * 20 + (d >> 5)) * 64 + ((d & 31) >> 3) * 16 + n16) * 8 + (d & 7);
            const unsigned u0 = pk2(bf_lo(xi[i][0]) + acc[0][i][0] + bs, bf_hi(xi[i][0]) + acc[0][i][1] + bs);
            const unsigned u1 = pk2(bf_lo(xi[i][1]) + acc[0][i][2] + bs, bf_hi(xi[i][1]) + acc[0][i][3] + bs);
            *(u32x2*)o = u32x2{u0, u1};
            xv[4 * i] = bf_lo(u0); xv[4 * i + 1] = bf_hi(u0); xv[4 * i + 2] = bf_lo(u1); xv[4 * i + 3] = bf_hi(u1);
            sum += (xv[4 * i] + xv[4 * i + 1]) + (xv[4 * i + 2] + xv[4 * i + 3]);
        }
        if (MODE == 2) {
            const float mw = add32(add16(sum)) * (1.f / 80.f);
            float s2 = 0.f;
#pragma unroll
            for (int i = 0; i < 20; ++i) { const float dd = xv[i] - mw; s2 += dd * dd; }
            s2 = add32(add16(s2));
            if (g == 0) *(f32x2*)(ws + OFF_XS + ((size_t)s * 8 + dt) * 8) = f32x2{mw, s2};
        }
    }
    SST(3); SST_FLUSHK(ws, MODE == 1 ? (out ? 8 : 3) : (MODE == 2 ? 4 : 9));
}

typedef __attribute__((address_space(1))) unsigned gu32;
constexpr int AUXL = 16;
#define LD_SC1(off) __builtin_bit_cast(bf16x8, __builtin_amdgcn_raw_buffer_load_b128(rs, (int)(off), 0, 16))
DI void edge_publish(gu32* cnt) {
    RAW_BARRIER();
    if (threadIdx.x == 0) __hip_atomic_fetch_add(cnt, 1u, __ATOMIC_RELAXED, __HIP_MEMORY_SCOPE_AGENT);
}
DI void edge_wait(gu32* cnt, unsigned want) {
    if (threadIdx.x == 0) {
        for (int spin = 0; spin < (1 << 20); ++spin) {
            if (__hip_atomic_load(cnt, __ATOMIC_RELAXED, __HIP_MEMORY_SCOPE_AGENT) >= want) break;
            __builtin_amdgcn_s_sleep(1);
        }
        __builtin_amdgcn_fence(__ATOMIC_ACQUIRE, "wavefront");
    }
    RAW_BARRIER();
}
#define W1_A 6
#define W2_FIRST 7
constexpr int MLP_LDS = 73728;
template <bool R1>
__global__ __launch_bounds__(256, 1) void k_mlp(char* __restrict__ ws, const u16* __restrict__ xin, const u16* __restrict__ b1,
                                                const u16* __restrict__ b2, u16* __restrict__ out) {
    extern __shared__ __attribute__((aligned(16))) char lds_raw[];
    char* L = lds_raw;
    const int tid = threadIdx.x, lane = tid & 63, w = __builtin_amdgcn_readfirstlane(tid >> 6);
    const int b = blockIdx.x, slot = b >> 3, tb = ((b & 7) >> 1) * 8 + (slot >> 2), cg = (b & 1) * 4 + (slot & 3);
    const int ot1 = ((b & 7) >> 1) * 7 + (slot >> 2);
    const bool act1 = slot < 28 && ot1 < 27;
    const int n16 = lane & 15, g = lane >> 4;
    const __amdgpu_buffer_rsrc_t rs = __builtin_amdgcn_make_buffer_rsrc((void*)ws, 0, (int)WS_NEED, 0x00020000);
    gu32* cnt = (gu32*)(ws + OFF_CNT);
    SST_DECL; SST(0);
    f32x4 acc[4][5];
#define ACC_ZERO() do { _Pragma("unroll") for (int i_ = 0; i_ < 4; ++i_) _Pragma("unroll") for (int j_ = 0; j_ < 5; ++j_) acc[i_][j_] = f32x4{0.f, 0.f, 0.f, 0.f}; } while (0)
#define ACC_REDUCE() do { \
    _Pragma("unroll") for (int j_ = 1; j_ < 4; ++j_) { const int o_ = (w + j_) & 3; \
        _Pragma("unroll") for (int i_ = 0; i_ < 5; ++i_) *(f32x4*)(L + (((o_ * 3 + (j_ - 1)) * 5 + i_) * 64 + lane) * 16) = acc[j_][i_]; } \
    RAW_BARRIER(); \
    _Pragma("unroll") for (int j_ = 0; j_ < 3; ++j_) _Pragma("unroll") for (int i_ = 0; i_ < 5; ++i_) \
        acc[1 + j_][i_] = *(const f32x4*)(L + (((w * 3 + j_) * 5 + i_) * 64 + lane) * 16); \
    SBAR(); \
    _Pragma("unroll") for (int i_ = 0; i_ < 5; ++i_) acc[0][i_] = (acc[0][i_] + acc[1][i_]) + (acc[2][i_] + acc[3][i_]); } while (0)
    const int tok = tb * 64 + 16 * w + n16;
    bf16x8 wr[16][4];
    int ro1[4], ro2[4];
#pragma unroll
    for (int j = 0; j < 4; ++j) { ro1[j] = ((j + w) & 3) * 64 * 64; ro2[j] = ((j + w) & 3) * 54 * 64; }
    const bf16x8* pw1 = (const bf16x8*)(ws + OFF_W1F) + ((size_t)(4 * (act1 ? ot1 : 0)) * 64 + 16 * w) * 64 + lane;
    const int k2 = w < 2 ? 14 * w : 28 + 13 * (w - 2);
    const bf16x8* pw2 = (const bf16x8*)(ws + OFF_W2F) + ((size_t)(4 * tb) * 54 + k2) * 64 + lane;
#define W1_LD(lo, hi) do { _Pragma("unroll") for (int s_ = (lo); s_ < (hi); ++s_) _Pragma("unroll") for (int j_ = 0; j_ < 4; ++j_) wr[s_][j_] = pw1[ro1[j_] + s_ * 64]; SBAR(); } while (0)
#define W2_LD(lo, hi) do { _Pragma("unroll") for (int s_ = (lo); s_ < (hi); ++s_) _Pragma("unroll") for (int j_ = 0; j_ < 4; ++j_) wr[s_][j_] = pw2[ro2[j_] + s_ * 64]; SBAR(); } while (0)

    u32x2 x1o[5], bb; float bs;
    {
        ACC_ZERO();
        u32x2 xi[5];
        const bf16x8* xb = (const bf16x8*)(ws + OFF_O) + (size_t)(tb * 4 * 20 + 5 * w) * 64 + lane;
        const bf16x8* wb = (const bf16x8*)(ws + OFF_WOF) + (size_t)(5 * cg * 20 + 5 * w) * 64 + lane;
        bf16x8 xf[5][4], wf[5][5];
        WO_LD(0); WO_LD(1); WO_LD(2); WO_LD(3);
#pragma unroll
        for (int ks = 0; ks < 5; ++ks) {
#pragma unroll
            for (int nt = 0; nt < 5; ++nt)
#pragma unroll
                for (int i = 0; i < 4; ++i) acc[i][nt] = MFMA16_32(wf[ks][nt], xf[ks][i], acc[i][nt]);
            SBAR();
            if (ks == 0) {
                WO_LD(4);
#pragma unroll
                for (int nt = 0; nt < 5; ++nt) {
                    const int d = 80 * cg + 16 * nt + 4 * g;
                    if (!R1) xi[nt] = *(const u32x2*)(xin + ((size_t)((tok >> 4) * 20 + (d >> 5)) * 64 + ((d & 31) >> 3) * 16 + n16) * 8 + (d & 7));
                    else xi[nt] = *(const u32x2*)(xin + (size_t)tok * 640 + d);
                }
                bb = *(const u32x2*)(b1 + 64 * (act1 ? ot1 : 0) + 16 * w + 4 * g);
                bs = bf2f(b2[tok]);
                SBAR();
            }
            if (ks == 1) W1_LD(0, 2);
            if (ks == 2) W1_LD(2, 4);
            if (ks == 3) W1_LD(4, 6);
            if (ks == 4) W1_LD(6, W1_A);
        }
        SST(1);
        ACC_REDUCE();
        float xv[20], s = 0.f;
#pragma unroll
        for (int nt = 0; nt < 5; ++nt) {
            const unsigned u0 = pk2(bf_lo(xi[nt][0]) + acc[0][nt][0], bf_hi(xi[nt][0]) + acc[0][nt][1]);
            const unsigned u1 = pk2(bf_lo(xi[nt][1]) + acc[0][nt][2], bf_hi(xi[nt][1]) + acc[0][nt][3]);
            x1o[nt] = u32x2{u0, u1};
            xv[4 * nt] = bf_lo(u0); xv[4 * nt + 1] = bf_hi(u0); xv[4 * nt + 2] = bf_lo(u1); xv[4 * nt + 3] = bf_hi(u1);
            s += (xv[4 * nt] + xv[4 * nt + 1]) + (xv[4 * nt + 2] + xv[4 * nt + 3]);
        }
        const float mw = add32(add16(s)) * (1.f / 80.f);
        float s2 = 0.f;
#pragma unroll
        for (int i = 0; i < 20; ++i) { const float dd = xv[i] - mw; s2 += dd * dd; }
        s2 = add32(add16(s2));
        gu64* gp = (gu64*)(ws + OFF_XG) + (size_t)tok * 8;
        if (g == 0) __hip_atomic_store(gp + cg, ((unsigned long long)(__float_as_uint(s2) | 0x80000000u) << 32) | __float_as_uint(mw),
                                       __ATOMIC_RELAXED, __HIP_MEMORY_SCOPE_AGENT);
        SBAR();
        SST(2);
        unsigned long long v0, v1;
        for (int spin = 0; spin < (1 << 20); ++spin) {
            v0 = __hip_atomic_load(gp + 2 * g, __ATOMIC_RELAXED, __HIP_MEMORY_SCOPE_AGENT);
            v1 = __hip_atomic_load(gp + 2 * g + 1, __ATOMIC_RELAXED, __HIP_MEMORY_SCOPE_AGENT);
            if (__all((long long)(v0 & v1) < 0)) break;
            __builtin_amdgcn_s_sleep(4);
        }
        SST(3);
        const float m0 = __uint_as_float((unsigned)v0), m1 = __uint_as_float((unsigned)v1);
        const float mean = add32(add16(m0 + m1)) * 0.125f;
        const float d0 = m0 - mean, d1 = m1 - mean;
        const float var = add32(add16(__uint_as_float((unsigned)(v0 >> 32) & 0x7fffffffu) + __uint_as_float((unsigned)(v1 >> 32) & 0x7fffffffu) +
                                      80.f * (d0 * d0 + d1 * d1))) * (1.f / 640.f);
        const float r1 = __builtin_amdgcn_rsqf(var + LN_EPS);
        const float r2 = r1 * __builtin_amdgcn_rsqf(var * r1 * r1 + LN_EPS);
        char* hb = L + w * 15360;
#pragma unroll
        for (int nt = 0; nt < 5; ++nt)
            *(u32x2*)(hb + n16 * 176 + (16 * nt + 4 * g) * 2) =
                u32x2{pk2((xv[4 * nt] - mean) * r2, (xv[4 * nt + 1] - mean) * r2), pk2((xv[4 * nt + 2] - mean) * r2, (xv[4 * nt + 3] - mean) * r2)};
        asm volatile("s_waitcnt lgkmcnt(0)" ::: "memory");
        const int q4 = (lane & 15) >> 2, p4 = lane & 3;
        lds_cptr hr = (lds_cptr)(lds_ptr)L + w * 15360 + (8 * (g & 1) + q4) * 176 + p4 * 8 + (g >> 1) * 32;
        const int s0 = tb * 64 + 16 * w + 8 * (g & 1);
        const int hoff = (int)OFF_H + ((5 * cg + (g >> 1)) * 64 + (s0 >> 5)) * 1024 + (((s0 & 31) >> 3) * 16 + n16) * 16;
        const bf16x8 f0 = cat8(tr_read(hr), tr_read(hr + 704));
        const bf16x8 f1 = cat8(tr_read(hr + 64), tr_read(hr + 704 + 64));
        const bf16x8 f2 = cat8(tr_read(hr + (g < 2 ? 128 : 0)), tr_read(hr + 704 + (g < 2 ? 128 : 0)));
        __builtin_amdgcn_raw_buffer_store_b128(__builtin_bit_cast(u32x4, f0), rs, hoff, 0, 16);
        __builtin_amdgcn_raw_buffer_store_b128(__builtin_bit_cast(u32x4, f1), rs, hoff + 2 * 65536, 0, 16);
        if (g < 2) __builtin_amdgcn_raw_buffer_store_b128(__builtin_bit_cast(u32x4, f2), rs, hoff + 4 * 65536, 0, 16);
        SBAR();
        W1_LD(W1_A, W1_A + 4);
        VMCNT(16);
        SST(4);
        edge_publish(cnt + cg * 64);
    }
    if (act1) {
        W1_LD(W1_A + 4, 16);
        edge_wait(cnt + cg * 64, 32);
        SST(5);
        ACC_ZERO();
        const int ho = (int)OFF_H + ((5 * cg) * 64 + 16 * w) * 1024 + lane * 16;
        constexpr int PF = 6;
        bf16x8 rf[PF][5];
#pragma unroll
        for (int s = 0; s < PF; ++s) {
#pragma unroll
            for (int i = 0; i < 5; ++i) rf[s][i] = LD_SC1(ho + i * 65536 + s * 1024);
            SBAR();
        }
#pragma unroll
        for (int s = 0; s < 16; ++s) {
#pragma unroll
            for (int j = 0; j < 4; ++j)
#pragma unroll
                for (int i = 0; i < 5; ++i) acc[j][i] = MFMA16_32(wr[s][j], rf[s % PF][i], acc[j][i]);
            SBAR();
            if (s + PF < 16) {
#pragma unroll
                for (int i = 0; i < 5; ++i) rf[s % PF][i] = LD_SC1(ho + i * 65536 + (s + PF) * 1024);
                SBAR();
            }
            if (s >= W2_FIRST && s - W2_FIRST < 9) W2_LD(s - W2_FIRST, s - W2_FIRST + 1);
        }
        SST(6);
        ACC_REDUCE();
        const int f = 64 * ot1 + 16 * w + 4 * g;
        const float bv[4] = {bf_lo(bb[0]), bf_hi(bb[0]), bf_lo(bb[1]), bf_hi(bb[1])};
        const int uo = (int)OFF_U + ((5 * cg) * 54 + (f >> 5)) * 1024 + (((f & 31) >> 3) * 16 + n16) * 16 + (f & 7) * 2;
#pragma unroll
        for (int i = 0; i < 5; ++i) {
            float r[4];
#pragma unroll
            for (int e = 0; e < 4; ++e) {
                const float u = acc[0][i][e] + bv[e];
                const float z = u * (1.f + 0.044715f * u * u) * (-2.f * 0.7978845608028654f * 1.4426950408889634f);
                r[e] = u * __builtin_amdgcn_rcpf(1.f + EX2(z));
            }
            __builtin_amdgcn_raw_buffer_store_b64(u32x2{pk2(r[0], r[1]), pk2(r[2], r[3])}, rs, uo + i * 54 * 1024, 0, 16);
        }
        SBAR();
        W2_LD(9, 13);
        VMCNT(16);
        SST(7);
        edge_publish(cnt + (8 + cg) * 64);
    } else {
        W2_LD(0, 13);
    }
    {
        if (w < 2) W2_LD(13, 14);
        edge_wait(cnt + (8 + cg) * 64, 27);
        SST(8);
        ACC_ZERO();
        const int uo = (int)OFF_U + ((5 * cg) * 54 + k2) * 1024 + lane * 16;
        constexpr int PF = 6;
        bf16x8 rf[PF][5];
#pragma unroll
        for (int s = 0; s < PF; ++s) {
#pragma unroll
            for (int i = 0; i < 5; ++i) rf[s][i] = LD_SC1(uo + i * 54 * 1024 + s * 1024);
            SBAR();
        }
#pragma unroll
        for (int s = 0; s < 14; ++s) {
            if (s < 13 || w < 2) {
#pragma unroll
                for (int j = 0; j < 4; ++j)
#pragma unroll
                    for (int i = 0; i < 5; ++i) acc[j][i] = MFMA16_32(rf[s % PF][i], wr[s][j], acc[j][i]);
            }
            SBAR();
            if (s + PF < 13 || (s + PF == 13 && w < 2)) {
#pragma unroll
                for (int i = 0; i < 5; ++i) rf[s % PF][i] = LD_SC1(uo + i * 54 * 1024 + (s + PF) * 1024);
                SBAR();
            }
        }
        SST(9);
        ACC_REDUCE();
        float xv[20], sum = 0.f;
#pragma unroll
        for (int i = 0; i < 5; ++i) {
            const int d = 80 * cg + 16 * i + 4 * g;
            const unsigned u0 = pk2(bf_lo(x1o[i][0]) + acc[0][i][0] + bs, bf_hi(x1o[i][0]) + acc[0][i][1] + bs);
            const unsigned u1 = pk2(bf_lo(x1o[i][1]) + acc[0][i][2] + bs, bf_hi(x1o[i][1]) + acc[0][i][3] + bs);
            if (R1) __builtin_amdgcn_raw_buffer_store_b64(u32x2{u0, u1}, rs,
                        (int)OFF_X2 + ((4 * tb + w) * 20 + (d >> 5)) * 1024 + (((d & 31) >> 3) * 16 + n16) * 16 + (d & 7) * 2, 0, 16);
            else *(u32x2*)(out + (size_t)tok * 640 + d) = u32x2{u0, u1};
            xv[4 * i] = bf_lo(u0); xv[4 * i + 1] = bf_hi(u0); xv[4 * i + 2] = bf_lo(u1); xv[4 * i + 3] = bf_hi(u1);
            sum += (xv[4 * i] + xv[4 * i + 1]) + (xv[4 * i + 2] + xv[4 * i + 3]);
        }
        if (R1) {
            const float mw = add32(add16(sum)) * (1.f / 80.f);
            float s2 = 0.f;
#pragma unroll
            for (int i = 0; i < 20; ++i) { const float dd = xv[i] - mw; s2 += dd * dd; }
            s2 = add32(add16(s2));
            if (g == 0) __builtin_amdgcn_raw_buffer_store_b64(u32x2{__float_as_uint(mw), __float_as_uint(s2)}, rs, (int)OFF_XS + ((tb * 8 + cg) * 64 + 16 * w + n16) * 8, 0, 16);
        }
    }
    if (R1) {
        const int mt0 = cg >> 1, isv = cg & 1;
        const bf16x8* wb = (const bf16x8*)(ws + OFF_WQF) + (size_t)(5 * cg * 20 + 5 * w) * 64 + lane;
        const bf16x8* kb = (const bf16x8*)(ws + OFF_WQF) + (size_t)((40 + 2 * isv) * 20 + 5 * w) * 64 + lane;
        bf16x8 wf[5][5], wkf[5][2], xf[5][4], xk[5];
        SBAR();
#pragma unroll
        for (int i = 0; i < 16; ++i) wf[i / 5][i % 5] = wb[((i % 5) * 20 + i / 5) * 64];
        SBAR();
        VMCNT(16);
        SST(10);
        edge_publish(cnt + (16 + tb) * 64);
#pragma unroll
        for (int i = 16; i < 25; ++i) wf[i / 5][i % 5] = wb[((i % 5) * 20 + i / 5) * 64];
#pragma unroll
        for (int ks = 0; ks < 5; ++ks) { wkf[ks][0] = kb[ks * 64]; wkf[ks][1] = kb[(20 + ks) * 64]; }
        f32x4 csq[5], cs0, cs1, stq[4], stk[4], acck[2];
        {
            const float* csp = (const float*)(ws + OFF_CS);
#pragma unroll
            for (int nt = 0; nt < 5; ++nt) csq[nt] = *(const f32x4*)(csp + 80 * cg + 16 * nt + 4 * g);
            cs0 = *(const f32x4*)(csp + 640 + 32 * isv + 4 * g); cs1 = *(const f32x4*)(csp + 640 + 32 * isv + 16);
        }
        SBAR();
        edge_wait(cnt + (16 + tb) * 64, 8);
        SST(11);
        const int xo = (int)OFF_X2 + (tb * 4 * 20 + 5 * w) * 1024 + lane * 16;
#pragma unroll
        for (int ks = 0; ks < 5; ++ks) {
#pragma unroll
            for (int i = 0; i < 4; ++i) xf[ks][i] = LD_SC1(xo + (((i + w) & 3) * 20 + ks) * 1024);
            xk[ks] = LD_SC1(xo + (mt0 * 20 + ks) * 1024);
            SBAR();
        }
#pragma unroll
        for (int i = 0; i < 4; ++i) {
            const u32x2 a0 = __builtin_amdgcn_raw_buffer_load_b64(rs, (int)OFF_XS + ((tb * 8 + 2 * i) * 64 + 16 * w + n16) * 8, 0, AUXL);
            const u32x2 a1 = __builtin_amdgcn_raw_buffer_load_b64(rs, (int)OFF_XS + ((tb * 8 + 2 * i + 1) * 64 + 16 * w + n16) * 8, 0, AUXL);
            const u32x2 c0 = __builtin_amdgcn_raw_buffer_load_b64(rs, (int)OFF_XS + ((tb * 8 + 2 * i) * 64 + 16 * mt0 + n16) * 8, 0, AUXL);
            const u32x2 c1 = __builtin_amdgcn_raw_buffer_load_b64(rs, (int)OFF_XS + ((tb * 8 + 2 * i + 1) * 64 + 16 * mt0 + n16) * 8, 0, AUXL);
            stq[i] = __builtin_bit_cast(f32x4, u32x4{a0[0], a0[1], a1[0], a1[1]});
            stk[i] = __builtin_bit_cast(f32x4, u32x4{c0[0], c0[1], c1[0], c1[1]});
        }
        SBAR();
        ACC_ZERO();
        acck[0] = f32x4{0.f, 0.f, 0.f, 0.f}; acck[1] = acck[0];
#pragma unroll
        for (int ks = 0; ks < 5; ++ks) {
#pragma unroll
            for (int nt = 0; nt < 5; ++nt)
#pragma unroll
                for (int i = 0; i < 4; ++i) acc[i][nt] = MFMA16_32(wf[ks][nt], xf[ks][i], acc[i][nt]);
            acck[0] = MFMA16_32(wkf[ks][0], xk[ks], acck[0]);
            acck[1] = MFMA16_32(wkf[ks][1], xk[ks], acck[1]);
            SBAR();
        }
        SST(12);
        const float z4[4] = {0.f, 0.f, 0.f, 0.f}, z5[5] = {0.f, 0.f, 0.f, 0.f, 0.f}, z2[2] = {0.f, 0.f};
        qkv_finish<true>(ws, L, tb, cg, w, lane, acc, acck, csq, cs0, cs1, stq, stk, z4, z4, z5, z2);
    }
    SST_FLUSHK(ws, R1 ? 2 : 7);
}

template <int N> __global__ void k_end() {}

extern "C" void kernel_launch(void* const* d_in, const int* in_sizes, int n_in, void* d_out, int out_size, void* d_ws,
                              size_t ws_size, hipStream_t stream) {
    const u16* x = (const u16*)d_in[0];
    const u16* wq = (const u16*)d_in[1];
    const u16* wk = (const u16*)d_in[2];
    const u16* wv = (const u16*)d_in[3];
    const u16* wo = (const u16*)d_in[4];
    const u16* w1 = (const u16*)d_in[6];
    const u16* b1 = (const u16*)d_in[7];
    const u16* w2 = (const u16*)d_in[8];
    const u16* b2 = (const u16*)d_in[9];
    char* ws = (char*)d_ws + ((ws_size - WS_NEED) & ~(size_t)4095);
    u16* x2 = (u16*)(ws + OFF_X2);
    (void)hipFuncSetAttribute((const void*)k_qkv<false>, hipFuncAttributeMaxDynamicSharedMemorySize, QKV_LDS);
    (void)hipFuncSetAttribute((const void*)k_attn<true>, hipFuncAttributeMaxDynamicSharedMemorySize, ATT_LDS);
    (void)hipFuncSetAttribute((const void*)k_attn<false>, hipFuncAttributeMaxDynamicSharedMemorySize, ATT_LDS);
    (void)hipFuncSetAttribute((const void*)k_mlp<true>, hipFuncAttributeMaxDynamicSharedMemorySize, MLP_LDS);
    (void)hipFuncSetAttribute((const void*)k_mlp<false>, hipFuncAttributeMaxDynamicSharedMemorySize, MLP_LDS);
    (void)hipFuncSetAttribute((const void*)k_end<0>, hipFuncAttributeMaxDynamicSharedMemorySize, 0);
    k_qkv<false><<<256, 256, QKV_LDS, stream>>>(x, wq, wk, wv, wo, ws, w1, w2);
    k_attn<true><<<256, 512, ATT_LDS, stream>>>(ws, w1, w2, wo, wq);
    k_mlp<true><<<256, 256, MLP_LDS, stream>>>(ws, x, b1, b2, nullptr);
    k_attn<false><<<256, 512, ATT_LDS, stream>>>(ws, w1, w2, wo, wq);
    k_mlp<false><<<256, 256, MLP_LDS, stream>>>(ws, x2, b1, b2, (u16*)d_out);
}
```

```cpp
#include <hip/hip_runtime.h>
#include <hip/hip_bf16.h>
#include <stdint.h>

typedef unsigned short u16;
typedef short bf16x8 __attribute__((ext_vector_type(8)));
typedef short bf16x4 __attribute__((ext_vector_type(4)));
typedef float f32x16 __attribute__((ext_vector_type(16)));
typedef float f32x4 __attribute__((ext_vector_type(4)));
typedef float f32x2 __attribute__((ext_vector_type(2)));
typedef unsigned u32x4 __attribute__((ext_vector_type(4)));
typedef unsigned u32x2 __attribute__((ext_vector_type(2)));
typedef __bf16 bf2_t __attribute__((ext_vector_type(2)));
typedef __attribute__((address_space(3))) char* lds_ptr;
typedef __attribute__((address_space(3))) const char* lds_cptr;
typedef __attribute__((address_space(1))) unsigned long long gu64;

#define DI __device__ __forceinline__
#define SBAR() __builtin_amdgcn_sched_barrier(0)
#define PIN(x) asm volatile("" : "+v"(x))
#define MFMA32_16(a, b, c) __builtin_amdgcn_mfma_f32_32x32x16_bf16(a, b, c, 0, 0, 0)
#define MFMA32_8(a, b, c) __builtin_amdgcn_mfma_f32_32x32x8bf16_1k(a, b, c, 0, 0, 0)
#define MFMA16_32(a, b, c) __builtin_amdgcn_mfma_f32_16x16x32_bf16(a, b, c, 0, 0, 0)
#define EX2(x) __builtin_amdgcn_exp2f(x)
#define RAW_BARRIER() asm volatile("s_waitcnt lgkmcnt(0)\n\ts_barrier" ::: "memory")
#define VMCNT(n) asm volatile("s_waitcnt vmcnt(%0)" :: "i"(n) : "memory")

#define SST_DECL do {} while (0)
#define SST(i) do {} while (0)
#define SST_FLUSHK(ws, kid) do {} while (0)
#define PREP_STORE(p, v) asm volatile("global_store_dwordx4 %0, %1, off sc1\n\ts_nop 1" :: "v"(p), "v"(v) : "memory")
#define ST8(p, v) (*(u32x2*)(p) = (v))
constexpr int S_ = 2048, D_ = 640, F_ = 1728;
constexpr float LN_EPS = 1e-5f;
constexpr float QSCALE = 0.22360679774997896f * 1.4426950408889634f;
constexpr float SAFE_BOUND = 60.0f;

DI unsigned pk2(float a, float b) { f32x2 v = {a, b}; bf2_t r = __builtin_convertvector(v, bf2_t); return __builtin_bit_cast(unsigned, r); }
DI float bf_lo(unsigned u) { return __uint_as_float(u << 16); }
DI float bf_hi(unsigned u) { return __uint_as_float(u & 0xffff0000u); }
DI float bf2f(u16 v) { return __uint_as_float((unsigned)v << 16); }
DI float dot2(unsigned a, unsigned b, float c) { return __builtin_amdgcn_fdot2_f32_bf16(__builtin_bit_cast(bf2_t, a), __builtin_bit_cast(bf2_t, b), c, false); }
DI bf16x4 tr_read(lds_cptr p) { return __builtin_amdgcn_ds_read_tr16_b64_v4i16((__attribute__((address_space(3))) bf16x4*)p); }
DI bf16x8 cat8(bf16x4 a, bf16x4 b) { return __builtin_shufflevector(a, b, 0, 1, 2, 3, 4, 5, 6, 7); }
DI void glds16(const void* g, lds_ptr l) {
    unsigned keep;
    asm volatile("s_mov_b32 %0, m0\n\ts_mov_b32 m0, %2\n\ts_nop 0\n\tglobal_load_lds_dwordx4 %1, off\n\ts_mov_b32 m0, %0"
                 : "=&s"(keep) : "v"(g), "s"((unsigned)(uintptr_t)l) : "memory");
}
DI float xor32(float v) { return __shfl_xor(v, 32); }
DI float add32(float v) {
    auto r = __builtin_amdgcn_permlane32_swap(__float_as_uint(v), __float_as_uint(v), false, false);
    return __uint_as_float(r[0]) + __uint_as_float(r[1]);
}
DI float add16(float v) {
    auto r = __builtin_amdgcn_permlane16_swap(__float_as_uint(v), __float_as_uint(v), false, false);
    return __uint_as_float(r[0]) + __uint_as_float(r[1]);
}
DI float max16x(float v) { auto r = __builtin_amdgcn_permlane16_swap(__float_as_uint(v), __float_as_uint(v), false, false); return fmaxf(__uint_as_float(r[0]), __uint_as_float(r[1])); }
DI float max32x(float v) { auto r = __builtin_amdgcn_permlane32_swap(__float_as_uint(v), __float_as_uint(v), false, false); return fmaxf(__uint_as_float(r[0]), __uint_as_float(r[1])); }
template <int CTRL> DI float dppf(float v) { return __int_as_float(__builtin_amdgcn_update_dpp(0, __float_as_int(v), CTRL, 0xf, 0xf, false)); }
DI float rowmax16(float v) {
    v = fmaxf(v, dppf<0x128>(v)); v = fmaxf(v, dppf<0x124>(v)); v = fmaxf(v, dppf<0x122>(v)); v = fmaxf(v, dppf<0x121>(v)); return v;
}
DI float wavemax(float v) { return max32x(max16x(rowmax16(v))); }
DI u32x4 gather8(const u16* s, int ld) {
    u32x4 r;
#pragma unroll
    for (int e = 0; e < 4; ++e) r[e] = (unsigned)s[(2 * e) * ld] | ((unsigned)s[(2 * e + 1) * ld] << 16);
    return r;
}

constexpr size_t OFF_Q = 0;
constexpr size_t OFF_K1 = OFF_Q + (size_t)2048 * 32 * 48;
constexpr size_t OFF_K2 = OFF_K1 + 2048 * 32;
constexpr size_t OFF_V = OFF_K2 + 2048 * 8;
constexpr size_t OFF_KN = OFF_V + 128 * 640;
constexpr size_t OFF_QN = OFF_KN + 512;
constexpr size_t OFF_CS = OFF_QN + 4096;
constexpr size_t OFF_CONST = OFF_CS + 4096;
constexpr size_t OFF_CNT = OFF_CONST + 256;
constexpr size_t OFF_XG = OFF_CNT + 48 * 256;
constexpr size_t OFF_XS = OFF_XG + 262144;
constexpr size_t OFF_X1 = OFF_XS + 131072;
constexpr size_t OFF_H = OFF_X1 + (size_t)S_ * D_ * 2;
constexpr size_t OFF_U = OFF_H + (size_t)S_ * D_ * 2;
constexpr size_t OFF_X2 = OFF_U + (size_t)D_ * F_ * 2;
constexpr size_t OFF_O = OFF_X2 + (size_t)S_ * D_ * 2;
constexpr size_t OFF_W1F = OFF_O + (size_t)S_ * D_ * 2;
constexpr size_t OFF_W2F = OFF_W1F + (size_t)S_ * F_ * 2;
constexpr size_t OFF_WQF = OFF_W2F + (size_t)S_ * F_ * 2;
constexpr size_t OFF_WOF = OFF_WQF + (size_t)44 * 20 * 1024;
constexpr int NPREP = 1728 + 1728 + 200 + 200;

DI void chan8(const f32x4 (&st)[4], float& mean, float& var) {
    float m = 0.f, m2 = 0.f;
#pragma unroll
    for (int i = 0; i < 4; ++i) { m += st[i][0] + st[i][2]; m2 += st[i][1] + st[i][3]; }
    m *= 0.125f;
#pragma unroll
    for (int i = 0; i < 4; ++i) { const float d0 = st[i][0] - m, d1 = st[i][2] - m; m2 += 80.f * (d0 * d0 + d1 * d1); }
    mean = m; var = m2 * (1.f / 640.f);
}

template <bool FAST>
DI void qkv_finish(char* __restrict__ ws, char* L, int tb, int cg, int w, int lane, f32x4 (&acc)[4][5], f32x4 (&acck)[2],
                   const f32x4 (&csq)[5], const f32x4& cs0, const f32x4& cs1, const f32x4 (&stq)[4], const f32x4 (&stk)[4],
                   const float (&sm)[4], const float (&sq)[4], const float (&cs)[5], const float (&csk)[2]) {
    const int n16 = lane & 15, g = lane >> 4, mt0 = cg >> 1, isv = cg & 1;
    constexpr int R_OFF = 0, PK_OFF = 61440, ST_OFF = 69632, CS_OFF = 71680;
#pragma unroll
    for (int j = 1; j < 4; ++j) {
        const int o = (w + j) & 3;
#pragma unroll
        for (int nt = 0; nt < 5; ++nt) *(f32x4*)(L + R_OFF + (((o * 3 + (j - 1)) * 5 + nt) * 64 + lane) * 16) = acc[j][nt];
    }
    *(f32x4*)(L + PK_OFF + ((w * 2 + 0) * 64 + lane) * 16) = acck[0];
    *(f32x4*)(L + PK_OFF + ((w * 2 + 1) * 64 + lane) * 16) = acck[1];
    if (!FAST && g == 0) {
#pragma unroll
        for (int i = 0; i < 4; ++i) *(f32x2*)(L + ST_OFF + (w * 64 + ((i + w) & 3) * 16 + n16) * 8) = f32x2{sm[i], sq[i]};
#pragma unroll
        for (int nt = 0; nt < 5; ++nt) *(float*)(L + CS_OFF + w * 448 + (nt * 16 + n16) * 4) = cs[nt];
#pragma unroll
        for (int nt = 0; nt < 2; ++nt) *(float*)(L + CS_OFF + w * 448 + (80 + nt * 16 + n16) * 4) = csk[nt];
    }
    char* qrow = ws + OFF_Q + (size_t)(tb * 8 + cg) * 12288 + (16 * w + n16) * 48;
    int qoff[5];
#pragma unroll
    for (int nt = 0; nt < 5; ++nt) { const int G = 4 * nt + g, hl = G / 5; qoff[nt] = hl * 3072 + (G - 5 * hl) * 8; PIN(qoff[nt]); }
    RAW_BARRIER();
    auto stats = [&](int tl, const f32x4 (&st)[4], float& mean, float& rstd) {
        float var;
        if (FAST) chan8(st, mean, var);
        else {
            float s = 0.f, s2 = 0.f;
#pragma unroll
            for (int ww = 0; ww < 4; ++ww) { f32x2 v = *(const f32x2*)(L + ST_OFF + (ww * 64 + tl) * 8); s += v[0]; s2 += v[1]; }
            mean = s * (1.f / 640.f);
            var = fmaxf(s2 * (1.f / 640.f) - mean * mean, 0.f);
        }
        rstd = __builtin_amdgcn_rsqf(var + LN_EPS);
    };
    {
        f32x4 rr[3][5], cc[4][5];
#pragma unroll
        for (int j = 0; j < 3; ++j)
#pragma unroll
            for (int nt = 0; nt < 5; ++nt) rr[j][nt] = *(const f32x4*)(L + R_OFF + (((w * 3 + j) * 5 + nt) * 64 + lane) * 16);
        if (!FAST) {
#pragma unroll
            for (int ww = 0; ww < 4; ++ww)
#pragma unroll
                for (int nt = 0; nt < 5; ++nt) cc[ww][nt] = *(const f32x4*)(L + CS_OFF + ww * 448 + (nt * 16 + 4 * g) * 4);
        }
        float mean, rstd; stats(16 * w + n16, stq, mean, rstd);
        SBAR();
        const float sc = rstd * QSCALE;
        float n2[5];
#pragma unroll
        for (int nt = 0; nt < 5; ++nt) {
            const f32x4 a = (acc[0][nt] + rr[0][nt]) + (rr[1][nt] + rr[2][nt]);
            f32x4 s;
            if (FAST) s = csq[nt];
            else {
                s = (cc[0][nt] + cc[1][nt]) + (cc[2][nt] + cc[3][nt]);
                if (tb == 0 && w == 0 && n16 == 0) *(f32x4*)(ws + OFF_CS + (80 * cg + 16 * nt + 4 * g) * 4) = s;
            }
            const unsigned u0 = pk2((a[0] - mean * s[0]) * sc, (a[1] - mean * s[1]) * sc);
            const unsigned u1 = pk2((a[2] - mean * s[2]) * sc, (a[3] - mean * s[3]) * sc);
            { const u32x2 v_ = u32x2{u0, u1}; ST8(qrow + qoff[nt], v_); }
            n2[nt] = dot2(u1, u1, dot2(u0, u0, 0.f));
        }
        float hn[4];
        hn[0] = n2[0] + (g == 0 ? n2[1] : 0.f);
        hn[1] = (g > 0 ? n2[1] : 0.f) + (g < 2 ? n2[2] : 0.f);
        hn[2] = (g > 1 ? n2[2] : 0.f) + (g < 3 ? n2[3] : 0.f);
        hn[3] = (g == 3 ? n2[3] : 0.f) + n2[4];
#pragma unroll
        for (int hh = 0; hh < 4; ++hh) hn[hh] = add32(add16(hn[hh]));
        const float n2a = (g & 1) ? hn[1] : hn[0], n2b = (g & 1) ? hn[3] : hn[2];
        const unsigned qb = pk2(__builtin_amdgcn_sqrtf((g & 2) ? n2b : n2a), 0.f);
        { const u32x2 v_ = u32x2{qb, 0u}; ST8(qrow + g * 3072 + 40, v_); }
        float m = bf_lo(qb);
        if (!(m == m)) m = INFINITY;
        m = wavemax(m);
        if (lane == 0) *(float*)(ws + OFF_QN + ((tb * 8 + cg) * 4 + w) * 4) = m;
    }
    if (w == 3) {
        const int t16 = tb * 4 + mt0;
        float mean, rstd; stats(mt0 * 16 + n16, stk, mean, rstd);
        f32x4 a0 = {0.f, 0.f, 0.f, 0.f}, a1 = a0, s0 = a0, s1 = a0;
#pragma unroll
        for (int ww = 0; ww < 4; ++ww) {
            a0 += *(const f32x4*)(L + PK_OFF + ((ww * 2 + 0) * 64 + lane) * 16);
            a1 += *(const f32x4*)(L + PK_OFF + ((ww * 2 + 1) * 64 + n16) * 16);
            if (!FAST) {
                s0 += *(const f32x4*)(L + CS_OFF + ww * 448 + (80 + 4 * g) * 4);
                s1 += *(const f32x4*)(L + CS_OFF + ww * 448 + 96 * 4);
            }
        }
        if (FAST) { s0 = cs0; s1 = cs1; }
        else if (tb == 0 && cg < 2 && n16 == 0) {
            *(f32x4*)(ws + OFF_CS + (640 + 32 * isv + 4 * g) * 4) = s0;
            if (g == 0) *(f32x4*)(ws + OFF_CS + (640 + 32 * isv + 16) * 4) = s1;
        }
        const unsigned u0 = pk2((a0[0] - mean * s0[0]) * rstd, (a0[1] - mean * s0[1]) * rstd);
        const unsigned u1 = pk2((a0[2] - mean * s0[2]) * rstd, (a0[3] - mean * s0[3]) * rstd);
        const unsigned u2 = pk2((a1[0] - mean * s1[0]) * rstd, (a1[1] - mean * s1[1]) * rstd);
        const unsigned u3 = pk2((a1[2] - mean * s1[2]) * rstd, (a1[3] - mean * s1[3]) * rstd);
        if (!isv) {
            const int key = t16 * 16 + n16;
            *(u32x2*)(ws + OFF_K1 + key * 32 + g * 8) = u32x2{u0, u1};
            if (g == 0) *(u32x2*)(ws + OFF_K2 + key * 8) = u32x2{u2, u3};
            float n2 = dot2(u1, u1, dot2(u0, u0, 0.f));
            n2 = add32(add16(n2));
            n2 = dot2(u3, u3, dot2(u2, u2, n2));
            if (!(n2 == n2)) n2 = INFINITY;
            n2 = rowmax16(n2);
            if (lane == 0) *(float*)(ws + OFF_KN + t16 * 4) = n2;
        } else {
            u16* vb = (u16*)(ws + OFF_V + t16 * 640 + (n16 >> 3) * 320) + (n16 & 7);
            vb[(4 * g + 0) * 8] = (u16)u0; vb[(4 * g + 1) * 8] = (u16)(u0 >> 16);
            vb[(4 * g + 2) * 8] = (u16)u1; vb[(4 * g + 3) * 8] = (u16)(u1 >> 16);
            if (g == 0) { vb[16 * 8] = (u16)u2; vb[17 * 8] = (u16)(u2 >> 16); vb[18 * 8] = (u16)u3; vb[19 * 8] = (u16)(u3 >> 16); }
        }
    }
    if (w == 0) *(u32x2*)(ws + OFF_XG + ((size_t)(tb * 64 + lane) * 8 + cg) * 8) = u32x2{0u, 0u};
}

constexpr int QKV_LDS = 163840;
template <bool FAST>
__global__ __launch_bounds__(256, 1) void k_qkv(const u16* __restrict__ x, const u16* __restrict__ wq, const u16* __restrict__ wk,
                                                const u16* __restrict__ wv, const u16* __restrict__ wo, char* __restrict__ ws) {
    extern __shared__ __attribute__((aligned(16))) char lds_raw[];
    lds_ptr lds = (lds_ptr)lds_raw;
    char* L = lds_raw;
    const int tid = threadIdx.x, lane = tid & 63, w = __builtin_amdgcn_readfirstlane(tid >> 6);
    const int b = blockIdx.x, slot = b >> 3, tb = ((b & 7) >> 1) * 8 + (slot >> 2), cg = (b & 1) * 4 + (slot & 3);
    const int mt0 = cg >> 1, isv = cg & 1;
    const int n16 = lane & 15, g = lane >> 4;
    SST_DECL; SST(0);
    f32x4 acc[4][5], acck[2];
#pragma unroll
    for (int i = 0; i < 4; ++i)
#pragma unroll
        for (int j = 0; j < 5; ++j) acc[i][j] = f32x4{0.f, 0.f, 0.f, 0.f};
    acck[0] = f32x4{0.f, 0.f, 0.f, 0.f}; acck[1] = acck[0];
    f32x4 csq[5], cs0, cs1, stq[4], stk[4];
    float sm[4] = {0.f, 0.f, 0.f, 0.f}, sq[4] = {0.f, 0.f, 0.f, 0.f};
    float cs[5] = {0.f, 0.f, 0.f, 0.f, 0.f}, csk[2] = {0.f, 0.f};
    if (FAST) {
        const bf16x8* xb = (const bf16x8*)x + (size_t)(tb * 4 * 20 + 5 * w) * 64 + lane;
        const bf16x8* wb = (const bf16x8*)(ws + OFF_WQF) + (size_t)(5 * cg * 20 + 5 * w) * 64 + lane;
        const bf16x8* kb = (const bf16x8*)(ws + OFF_WQF) + (size_t)((40 + 2 * isv) * 20 + 5 * w) * 64 + lane;
        bf16x8 xf[5][4], xk[5], wf[5][5], wkf[5][2];
#define QKV_LD(ks) do { _Pragma("unroll") for (int i = 0; i < 4; ++i) xf[ks][i] = xb[(((i + w) & 3) * 20 + (ks)) * 64]; \
        _Pragma("unroll") for (int nt = 0; nt < 5; ++nt) wf[ks][nt] = wb[(nt * 20 + (ks)) * 64]; \
        xk[ks] = xb[(mt0 * 20 + (ks)) * 64]; wkf[ks][0] = kb[(ks) * 64]; wkf[ks][1] = kb[(20 + (ks)) * 64]; SBAR(); } while (0)
        QKV_LD(0); QKV_LD(1); QKV_LD(2); QKV_LD(3);
#pragma unroll
        for (int ks = 0; ks < 5; ++ks) {
#pragma unroll
            for (int nt = 0; nt < 5; ++nt)
#pragma unroll
                for (int i = 0; i < 4; ++i) acc[i][nt] = MFMA16_32(wf[ks][nt], xf[ks][i], acc[i][nt]);
            acck[0] = MFMA16_32(wkf[ks][0], xk[ks], acck[0]);
            acck[1] = MFMA16_32(wkf[ks][1], xk[ks], acck[1]);
            SBAR();
            if (ks == 0) {
                QKV_LD(4);
                const float* csp = (const float*)(ws + OFF_CS);
#pragma unroll
                for (int nt = 0; nt < 5; ++nt) csq[nt] = *(const f32x4*)(csp + 80 * cg + 16 * nt + 4 * g);
                cs0 = *(const f32x4*)(csp + 640 + 32 * isv + 4 * g); cs1 = *(const f32x4*)(csp + 640 + 32 * isv + 16);
                const f32x4* sp = (const f32x4*)(ws + OFF_XS) + (size_t)(tb * 64 + n16) * 4;
#pragma unroll
                for (int i = 0; i < 4; ++i) { stq[i] = sp[16 * w * 4 + i]; stk[i] = sp[16 * mt0 * 4 + i]; }
                SBAR();
            }
        }
    } else {
        lds_ptr ra = lds + w * 40960, rb = ra + 20480;
        const char* xs = (const char*)(x + (size_t)(tb * 64) * 640 + 160 * w);
#pragma unroll
        for (int j = 0; j < 5; ++j) {
            const int c = 64 * j + lane;
            const unsigned xo = (unsigned)(c / 20) * 1280u + (unsigned)(c % 20) * 16u;
#pragma unroll
            for (int i = 0; i < 4; ++i) glds16(xs + (size_t)i * 20480 + xo, ra + i * 5120 + j * 1024);
        }
        const char* src = (const char*)(wq + (size_t)(160 * w) * 640 + 80 * cg);
        const unsigned loff = (unsigned)(lane / 10) * 1280u + (unsigned)(lane % 10) * 16u;
        const char* src2 = (const char*)((isv ? wv : wk) + (size_t)(160 * w) * 20);
#pragma unroll
        for (int i = 0; i < 16; ++i) glds16(src + (size_t)i * 7680 + loff, rb + i * 960);
#pragma unroll
        for (int i = 0; i < 3; ++i) glds16(src2 + i * 1024 + lane * 16, rb + 15488 + i * 1024);
        if (lane < 48) glds16(src2 + 3 * 1024 + lane * 16, rb + 15488 + 3 * 1024);
        SST(1);
        VMCNT(20);
        bf16x8 xf[4][5], xk[5];
        {
            const char* xl = L + w * 40960 + n16 * 320 + g * 16;
#pragma unroll
            for (int i = 0; i < 4; ++i)
#pragma unroll
                for (int ks = 0; ks < 5; ++ks) xf[i][ks] = *(const bf16x8*)(xl + ((i + w) & 3) * 5120 + ks * 64);
#pragma unroll
            for (int ks = 0; ks < 5; ++ks) xk[ks] = *(const bf16x8*)(xl + mt0 * 5120 + ks * 64);
        }
        asm volatile("s_waitcnt lgkmcnt(0)" ::: "memory");
        SBAR();
#pragma unroll
        for (int i = 0; i < 10; ++i) glds16(src + (size_t)(16 + i) * 7680 + loff, ra + i * 960);
        if (lane < 40) glds16(src + (size_t)26 * 7680 + loff, ra + 10 * 960);
#pragma unroll
        for (int i = 0; i < 2; ++i) glds16(src2 + 3840 + i * 1024 + lane * 16, ra + 10240 + i * 1024);
        if (lane < 32) glds16(src2 + 3840 + 2 * 1024 + lane * 16, ra + 10240 + 2 * 1024);
        SST(2);
        const unsigned ONES = 0x3f803f80u;
#pragma unroll
        for (int i = 0; i < 4; ++i)
#pragma unroll
            for (int ks = 0; ks < 5; ++ks) {
                u32x4 u = __builtin_bit_cast(u32x4, xf[i][ks]);
#pragma unroll
                for (int e = 0; e < 4; ++e) { sm[i] = dot2(u[e], ONES, sm[i]); sq[i] = dot2(u[e], u[e], sq[i]); }
            }
        SBAR();
        VMCNT(14);
        SST(3);
        const int q4 = (lane & 15) >> 2, p4 = lane & 3;
        lds_cptr wb0 = (lds_cptr)rb + (8 * g + q4) * 160 + p4 * 8, wb1 = (lds_cptr)ra + (8 * g + q4) * 160 + p4 * 8 - 3 * 5120;
        lds_cptr kb0 = (lds_cptr)rb + 15488 + (8 * g + q4) * 40 + p4 * 8, kb1 = (lds_cptr)ra + 10240 + (8 * g + q4) * 40 + p4 * 8 - 3 * 1280;
        bf16x8 wf[2][7];
#define QKV_TR(ks, s_) do { lds_cptr wb = (ks) < 3 ? wb0 : wb1, kb = (ks) < 3 ? kb0 : kb1; \
        _Pragma("unroll") for (int nt = 0; nt < 5; ++nt) wf[s_][nt] = cat8(tr_read(wb + (ks) * 5120 + nt * 32), tr_read(wb + (ks) * 5120 + 640 + nt * 32)); \
        _Pragma("unroll") for (int nt = 0; nt < 2; ++nt) wf[s_][5 + nt] = cat8(tr_read(kb + (ks) * 1280 + nt * 32), tr_read(kb + (ks) * 1280 + 160 + nt * 32)); SBAR(); } while (0)
        QKV_TR(0, 0);
#pragma unroll
        for (int ks = 0; ks < 5; ++ks) {
            if (ks == 2) { VMCNT(0); SBAR(); }
            if (ks < 4) QKV_TR(ks + 1, (ks + 1) & 1);
#pragma unroll
            for (int nt = 0; nt < 5; ++nt) {
                u32x4 u = __builtin_bit_cast(u32x4, wf[ks & 1][nt]);
#pragma unroll
                for (int e = 0; e < 4; ++e) cs[nt] = dot2(u[e], ONES, cs[nt]);
#pragma unroll
                for (int i = 0; i < 4; ++i) acc[i][nt] = MFMA16_32(wf[ks & 1][nt], xf[i][ks], acc[i][nt]);
            }
#pragma unroll
            for (int nt = 0; nt < 2; ++nt) {
                u32x4 u = __builtin_bit_cast(u32x4, wf[ks & 1][5 + nt]);
#pragma unroll
                for (int e = 0; e < 4; ++e) csk[nt] = dot2(u[e], ONES, csk[nt]);
                acck[nt] = MFMA16_32(wf[ks & 1][5 + nt], xk[ks], acck[nt]);
            }
            SBAR();
        }
#pragma unroll
        for (int i = 0; i < 4; ++i) { sm[i] = add32(add16(sm[i])); sq[i] = add32(add16(sq[i])); }
#pragma unroll
        for (int i = 0; i < 5; ++i) cs[i] = add32(add16(cs[i]));
#pragma unroll
        for (int i = 0; i < 2; ++i) csk[i] = add32(add16(csk[i]));
    }
    SST(4);
    if (!FAST) RAW_BARRIER();
    qkv_finish<FAST>(ws, L, tb, cg, w, lane, acc, acck, csq, cs0, cs1, stq, stk, sm, sq, cs, csk);
    if (!FAST && w == 1) {
        if (b < 80) {
            const int ks = b >> 2, ct = b & 3; int cn = 16 * (ct & 1) + n16; if (cn >= 20) cn = 0;
            *(u32x4*)(ws + OFF_WQF + (size_t)((40 + ct) * 20 + ks) * 1024 + lane * 16) =
                gather8(((ct & 2) ? wv : wk) + (size_t)(32 * ks + 8 * g) * 20 + cn, 20);
        } else if (b == 80 && lane < 4) {
            const unsigned v = (lane & 2) ? 0x3f803f80u : 0u;
            *(u32x4*)(ws + OFF_CONST + lane * 16) = u32x4{v, v, v, v};
        }
    }
    SST(7); SST_FLUSHK(ws, FAST ? 5 : 0);
}

constexpr int PBW = 2688, ATT_LDS = 32 * PBW;
template <bool PREP>
__global__ __launch_bounds__(512, 2) void k_attn(char* __restrict__ ws, const u16* __restrict__ w1, const u16* __restrict__ w2,
                                                 const u16* __restrict__ wo, const u16* __restrict__ wq) {
    extern __shared__ __attribute__((aligned(16))) char lds_raw[];
    char* L = lds_raw;
    const int tid = threadIdx.x, lane = tid & 63, w = __builtin_amdgcn_readfirstlane(tid >> 6);
    const int b = blockIdx.x, slot = b >> 3, tb = ((b & 7) >> 1) * 8 + (slot >> 2), cg = (b & 1) * 4 + (slot & 3);
    const int r32 = lane & 31, h = lane >> 5, n16 = lane & 15, g = lane >> 4;
    const int cgp = w & 1, kq = w >> 1;
    SST_DECL; SST(0);
    float kn0 = *(const float*)(ws + OFF_KN + lane * 4), kn1 = *(const float*)(ws + OFF_KN + 256 + lane * 4);
    float qmax = *(const float*)(ws + OFF_QN + ((tb * 8 + cg) * 4 + (lane & 3)) * 4);
    bf16x8 q1[4]; bf16x4 q2[4];
    {
        const char* qg = ws + OFF_Q + (size_t)(tb * 8 + cg) * 12288 + cgp * 6144 + r32 * 48;
#pragma unroll
        for (int j = 0; j < 4; ++j) { q1[j] = *(const bf16x8*)(qg + j * 1536 + 16 * h); q2[j] = *(const bf16x4*)(qg + j * 1536 + 32 + 8 * h); }
    }
    SBAR();
    const int kr = (r32 & 19) | ((r32 & 4) << 1) | ((r32 & 8) >> 1);
    const unsigned ko1 = (unsigned)OFF_K1 + (unsigned)(512 * kq + kr) * 32u + 16u * h;
    const unsigned ko2 = h ? (unsigned)OFF_CONST + 32u : (unsigned)OFF_K2 + (unsigned)(512 * kq + kr) * 8u;
    const unsigned ks2 = h ? 0u : 256u;
    const unsigned vo = r32 < 20 ? (unsigned)OFF_V + (unsigned)(32 * kq) * 640u + (unsigned)(h * 20 + r32) * 16u
                                 : (unsigned)OFF_CONST + (r32 == 20 ? 32u : 0u);
    const unsigned vs = r32 < 20 ? 640u : 0u;
    bf16x8 k1[3], v0[3], v1[3]; bf16x4 k2[3];
#define K_LOAD(t) do { k1[(t) % 3] = *(const bf16x8*)(ws + ko1 + (t) * 1024); k2[(t) % 3] = *(const bf16x4*)(ws + (ko2 + (t) * ks2)); } while (0)
#define V_LOAD(t) do { v0[(t) % 3] = *(const bf16x8*)(ws + (vo + (2 * (t)) * vs)); v1[(t) % 3] = *(const bf16x8*)(ws + (vo + (2 * (t) + 1) * vs)); } while (0)
    K_LOAD(0); V_LOAD(0); SBAR(); K_LOAD(1); V_LOAD(1); SBAR();
    PIN(kn0); PIN(kn1);
    const float kmax2 = wavemax(fmaxf(kn0, kn1));
    qmax = rowmax16(qmax);
    const float kb = __builtin_amdgcn_sqrtf(kmax2);
    const bool safe = __builtin_amdgcn_readfirstlane((int)(qmax * kb <= SAFE_BOUND)) != 0;
    const f32x16 zero16 = {0.f, 0.f, 0.f, 0.f, 0.f, 0.f, 0.f, 0.f, 0.f, 0.f, 0.f, 0.f, 0.f, 0.f, 0.f, 0.f};
    if (safe) {
        if (h) {
#pragma unroll
            for (int j = 0; j < 4; ++j) q2[j][0] = (short)pk2(-bf2f((u16)q2[j][0]) * kb, 0.f);
        }
    } else {
        float m[4];
#pragma unroll
        for (int j = 0; j < 4; ++j) { m[j] = -INFINITY; if (h) q2[j] = bf16x4{0, 0, 0, 0}; }
        for (int t = 0; t < 16; ++t) {
            const bf16x8 ka = *(const bf16x8*)(ws + ko1 + t * 1024);
            const bf16x4 kc = *(const bf16x4*)(ws + (ko2 + t * ks2));
#pragma unroll
            for (int j = 0; j < 4; ++j) {
                f32x16 s = MFMA32_16(ka, q1[j], zero16);
                SBAR(); asm volatile("s_nop 15" ::: "memory"); SBAR();
                s = MFMA32_8(kc, q2[j], s);
#pragma unroll
                for (int e = 0; e < 16; ++e) m[j] = fmaxf(m[j], s[e]);
                SBAR();
            }
        }
#pragma unroll
        for (int j = 0; j < 4; ++j) {
            const float mm = fmaxf(m[j], xor32(m[j]));
            if (h == 0) *(float*)(L + ((w * 4 + j) * 32 + r32) * 4) = mm;
        }
        RAW_BARRIER();
#pragma unroll
        for (int j = 0; j < 4; ++j) {
            float mm = -INFINITY;
#pragma unroll
            for (int qq = 0; qq < 4; ++qq) mm = fmaxf(mm, *(const float*)(L + (((2 * qq + cgp) * 4 + j) * 32 + r32) * 4));
            const unsigned hi = __float_as_uint(mm) & 0xffff0000u;
            const float r1 = mm - __uint_as_float(hi);
            const unsigned mid = __float_as_uint(r1) & 0xffff0000u;
            const float r2 = r1 - __uint_as_float(mid);
            const unsigned lo = __float_as_uint(r2) & 0xffff0000u;
            if (h) q2[j] = __builtin_bit_cast(bf16x4, u32x2{hi ^ 0x80000000u, ((mid ^ 0x80000000u) >> 16) | (lo ^ 0x80000000u)});
        }
        RAW_BARRIER();
    }
    SST(1);
    f32x16 o[4], sa, sb;
#pragma unroll
    for (int j = 0; j < 4; ++j) o[j] = zero16;
    u32x4 p1prev;
    sa = MFMA32_16(k1[0], q1[0], zero16);
    SBAR(); asm volatile("s_nop 15" ::: "memory"); SBAR();
    sa = MFMA32_8(k2[0], q2[0], sa);
    SBAR(); asm volatile("s_nop 15" ::: "memory"); SBAR();
    u32x4 prep[4]; char* prep_dst = nullptr; int prep_st = 0;
#pragma unroll
    for (int t = 0; t < 16; ++t) {
#pragma unroll
        for (int j = 0; j < 4; ++j) {
            const int it = 4 * t + j;
            f32x16& cur = (it & 1) ? sb : sa; f32x16& nxt = (it & 1) ? sa : sb;
            const int tn = j < 3 ? t : t + 1, jn = (j + 1) & 3;
            const int tp = j > 0 ? t : t - 1, jp = (j + 3) & 3;
            u32x4 p0, p1;
            if (j == 0 && t + 2 < 16) K_LOAD(t + 2);
            if (j == 1 && t + 2 < 16) V_LOAD(t + 2);
            if (PREP && j == 2 && (t == 1 || t == 8)) {
                int id = (t >> 3) * 2048 + b * 8 + w; if (id >= NPREP) id -= 2048;
                const u16* src; int ld;
                if (id < 1728) { const int ks = id / 27, c4 = id - ks * 27; src = w1 + (size_t)(32 * ks) * F_ + 64 * c4; ld = F_;
                                 prep_dst = ws + OFF_W1F + (size_t)(4 * c4 * 64 + ks) * 1024; prep_st = 64 * 1024; }
                else if (id < 3456) { const int jj = id - 1728, ks = jj >> 5, c4 = jj & 31; src = w2 + (size_t)(32 * ks) * S_ + 64 * c4; ld = S_;
                                      prep_dst = ws + OFF_W2F + (size_t)(4 * c4 * 54 + ks) * 1024; prep_st = 54 * 1024; }
                else { int jj = id - 3456; const bool isq = jj >= 200; if (isq) jj -= 200; const int ks = jj / 10, c4 = jj - ks * 10;
                       src = (isq ? wq : wo) + (size_t)(32 * ks) * 640 + 64 * c4; ld = 640;
                       prep_dst = ws + (isq ? OFF_WQF : OFF_WOF) + (size_t)(4 * c4 * 20 + ks) * 1024; prep_st = 20 * 1024; }
                src += (size_t)(lane >> 3) * ld + (lane & 7) * 8;
#pragma unroll
                for (int i = 0; i < 4; ++i) prep[i] = *(const u32x4*)(src + (size_t)(8 * i) * ld);
            }
            if (PREP && j == 2 && (t == 6 || t == 13)) {
#pragma unroll
                for (int i = 0; i < 4; ++i) *(u32x4*)(L + w * 4096 + i * 1024 + lane * 16) = prep[i];
            }
            if (PREP && j == 3 && (t == 6 || t == 13)) {
                lds_cptr tp_ = (lds_cptr)(lds_ptr)L + w * 4096 + (8 * g + (n16 >> 2)) * 128 + (lane & 3) * 8;
#pragma unroll
                for (int i = 0; i < 4; ++i) prep[i] = __builtin_bit_cast(u32x4, cat8(tr_read(tp_ + i * 32), tr_read(tp_ + 512 + i * 32)));
            }
            if (PREP && j == 0 && (t == 7 || t == 14)) {
#pragma unroll
                for (int i = 0; i < 4; ++i) PREP_STORE((u32x4*)(prep_dst + (size_t)i * prep_st + lane * 16), prep[i]);
            }
            SBAR();
            if (it < 63) nxt = MFMA32_16(k1[tn % 3], q1[jn], zero16);
            SBAR();
            p0[0] = pk2(EX2(cur[0]), EX2(cur[1])); p0[1] = pk2(EX2(cur[2]), EX2(cur[3]));
            PIN(p0); SBAR();
            if (it > 0) o[jp] = MFMA32_16(v1[(tp + 3) % 3], __builtin_bit_cast(bf16x8, p1prev), o[jp]);
            SBAR();
            p0[2] = pk2(EX2(cur[4]), EX2(cur[5])); p0[3] = pk2(EX2(cur[6]), EX2(cur[7]));
            PIN(p0); SBAR();
            if (it < 63) nxt = MFMA32_8(k2[tn % 3], q2[jn], nxt);
            SBAR();
            p1[0] = pk2(EX2(cur[8]), EX2(cur[9])); p1[1] = pk2(EX2(cur[10]), EX2(cur[11]));
            PIN(p1); SBAR();
            o[j] = MFMA32_16(v0[t % 3], __builtin_bit_cast(bf16x8, p0), o[j]);
            SBAR();
            p1[2] = pk2(EX2(cur[12]), EX2(cur[13])); p1[3] = pk2(EX2(cur[14]), EX2(cur[15]));
            PIN(p1); SBAR();
            p1prev = p1;
        }
        if (t == 7) SST(2);
    }
    o[3] = MFMA32_16(v1[15 % 3], __builtin_bit_cast(bf16x8, p1prev), o[3]);
    SST(3);
    if (PREP) RAW_BARRIER();
#pragma unroll
    for (int j = 0; j < 4; ++j) {
        char* pb = L + (kq * 8 + cgp * 4 + j) * PBW;
        *(f32x4*)(pb + lane * 16) = f32x4{o[j][0], o[j][1], o[j][2], o[j][3]};
        *(f32x4*)(pb + 1024 + lane * 16) = f32x4{o[j][4], o[j][5], o[j][6], o[j][7]};
        if (h == 0) *(f32x4*)(pb + 2048 + r32 * 16) = f32x4{o[j][8], o[j][9], o[j][10], o[j][11]};
        else *(float*)(pb + 2560 + r32 * 4) = o[j][8];
    }
    RAW_BARRIER();
    SST(4);
    {
        const char* pb = L + w * PBW;
        f32x4 a0 = {0.f, 0.f, 0.f, 0.f}, a1 = a0, a2 = a0; float l = 0.f;
#pragma unroll
        for (int qq = 0; qq < 4; ++qq) {
            a0 += *(const f32x4*)(pb + qq * 8 * PBW + lane * 16);
            a1 += *(const f32x4*)(pb + qq * 8 * PBW + 1024 + lane * 16);
            a2 += *(const f32x4*)(pb + qq * 8 * PBW + 2048 + r32 * 16);
            l += *(const float*)(pb + qq * 8 * PBW + 2560 + r32 * 4);
        }
        const float inv = __builtin_amdgcn_rcpf(l);
        const int tl = 32 * (w & 1) + r32, hd0 = 80 * cg + 20 * (w >> 1);
        char* ob = ws + OFF_O + (size_t)(tb * 4 + (tl >> 4)) * 20480 + (tl & 15) * 16;
        auto st = [&](int hd, const f32x4& a) {
            const u32x2 v_ = u32x2{pk2(a[0] * inv, a[1] * inv), pk2(a[2] * inv, a[3] * inv)};
            ST8(ob + (hd >> 5) * 1024 + ((hd & 31) >> 3) * 256 + (hd & 7) * 2, v_);
        };
        st(hd0 + 4 * h, a0); st(hd0 + 8 + 4 * h, a1);
        if (h == 0) st(hd0 + 16, a2);
    }
    if (b == 0 && tid < 48) *(unsigned*)(ws + OFF_CNT + tid * 256) = 0u;
    SST(5); SST_FLUSHK(ws, PREP ? 1 : 6);
}

template <bool XF>
__global__ __launch_bounds__(256, 1) void k_wo(char* __restrict__ ws, const u16* __restrict__ xin) {
    __shared__ __attribute__((aligned(16))) char L[61440];
    const int tid = threadIdx.x, lane = tid & 63, w = __builtin_amdgcn_readfirstlane(tid >> 6);
    const int b = blockIdx.x, slot = b >> 3, tb = ((b & 7) >> 1) * 8 + (slot >> 2), cg = (b & 1) * 4 + (slot & 3);
    const int n16 = lane & 15, g = lane >> 4;
    SST_DECL; SST(0);
    f32x4 acc[4][5];
#pragma unroll
    for (int i = 0; i < 4; ++i)
#pragma unroll
        for (int j = 0; j < 5; ++j) acc[i][j] = f32x4{0.f, 0.f, 0.f, 0.f};
    const int tok = tb * 64 + 16 * w + n16;
    u32x2 xi[5];
    {
        const bf16x8* xb = (const bf16x8*)(ws + OFF_O) + (size_t)(tb * 4 * 20 + 5 * w) * 64 + lane;
        const bf16x8* wb = (const bf16x8*)(ws + OFF_WOF) + (size_t)(5 * cg * 20 + 5 * w) * 64 + lane;
        bf16x8 xf[5][4], wf[5][5];
#define WO_LD(ks) do { _Pragma("unroll") for (int i = 0; i < 4; ++i) xf[ks][i] = xb[(((i + w) & 3) * 20 + (ks)) * 64]; \
        _Pragma("unroll") for (int nt = 0; nt < 5; ++nt) wf[ks][nt] = wb[(nt * 20 + (ks)) * 64]; SBAR(); } while (0)
        WO_LD(0); WO_LD(1); WO_LD(2); WO_LD(3);
#pragma unroll
        for (int ks = 0; ks < 5; ++ks) {
#pragma unroll
            for (int nt = 0; nt < 5; ++nt)
#pragma unroll
                for (int i = 0; i < 4; ++i) acc[i][nt] = MFMA16_32(wf[ks][nt], xf[ks][i], acc[i][nt]);
            SBAR();
            if (ks == 0) {
                WO_LD(4);
#pragma unroll
                for (int nt = 0; nt < 5; ++nt) {
                    const int d = 80 * cg + 16 * nt + 4 * g;
                    if (XF) xi[nt] = *(const u32x2*)(xin + ((size_t)((tok >> 4) * 20 + (d >> 5)) * 64 + ((d & 31) >> 3) * 16 + n16) * 8 + (d & 7));
                    else xi[nt] = *(const u32x2*)(xin + (size_t)tok * 640 + d);
                }
                SBAR();
            }
        }
    }
    SST(1);
#pragma unroll
    for (int j = 1; j < 4; ++j) {
        const int o = (w + j) & 3;
#pragma unroll
        for (int nt = 0; nt < 5; ++nt) *(f32x4*)(L + (((o * 3 + (j - 1)) * 5 + nt) * 64 + lane) * 16) = acc[j][nt];
    }
    RAW_BARRIER();
    SST(2);
    float xv[20]; u32x2 x1o[5];
    {
        f32x4 rr[3][5];
#pragma unroll
        for (int j = 0; j < 3; ++j)
#pragma unroll
            for (int nt = 0; nt < 5; ++nt) rr[j][nt] = *(const f32x4*)(L + (((w * 3 + j) * 5 + nt) * 64 + lane) * 16);
        SBAR();
        float s = 0.f;
#pragma unroll
        for (int nt = 0; nt < 5; ++nt) {
            const f32x4 a = (acc[0][nt] + rr[0][nt]) + (rr[1][nt] + rr[2][nt]);
            const unsigned u0 = pk2(bf_lo(xi[nt][0]) + a[0], bf_hi(xi[nt][0]) + a[1]);
            const unsigned u1 = pk2(bf_lo(xi[nt][1]) + a[2], bf_hi(xi[nt][1]) + a[3]);
            x1o[nt] = u32x2{u0, u1};
            xv[4 * nt] = bf_lo(u0); xv[4 * nt + 1] = bf_hi(u0); xv[4 * nt + 2] = bf_lo(u1); xv[4 * nt + 3] = bf_hi(u1);
            s += (xv[4 * nt] + xv[4 * nt + 1]) + (xv[4 * nt + 2] + xv[4 * nt + 3]);
        }
        const float mw = add32(add16(s)) * (1.f / 80.f);
        float s2 = 0.f;
#pragma unroll
        for (int i = 0; i < 20; ++i) { const float dd = xv[i] - mw; s2 += dd * dd; }
        s2 = add32(add16(s2));
        gu64* gp = (gu64*)(ws + OFF_XG) + (size_t)tok * 8 + cg;
        if (g == 0) __hip_atomic_store(gp, ((unsigned long long)(__float_as_uint(s2) | 0x80000000u) << 32) | __float_as_uint(mw),
                                       __ATOMIC_RELAXED, __HIP_MEMORY_SCOPE_AGENT);
    }
    SST(3);
    float mean, var;
    {
        gu64* gp = (gu64*)(ws + OFF_XG) + (size_t)tok * 8 + 2 * g;
        unsigned long long v0, v1;
        for (int spin = 0; spin < (1 << 20); ++spin) {
            v0 = __hip_atomic_load(gp, __ATOMIC_RELAXED, __HIP_MEMORY_SCOPE_AGENT);
            v1 = __hip_atomic_load(gp + 1, __ATOMIC_RELAXED, __HIP_MEMORY_SCOPE_AGENT);
            if (__all((long long)(v0 & v1) < 0)) break;
            __builtin_amdgcn_s_sleep(4);
        }
        const float m0 = __uint_as_float((unsigned)v0), m1 = __uint_as_float((unsigned)v1);
        mean = add32(add16(m0 + m1)) * 0.125f;
        const float d0 = m0 - mean, d1 = m1 - mean;
        var = add32(add16(__uint_as_float((unsigned)(v0 >> 32) & 0x7fffffffu) + __uint_as_float((unsigned)(v1 >> 32) & 0x7fffffffu) +
                          80.f * (d0 * d0 + d1 * d1))) * (1.f / 640.f);
    }
    SST(4);
    {
        const float r1 = __builtin_amdgcn_rsqf(var + LN_EPS);
        const float r2 = r1 * __builtin_amdgcn_rsqf(var * r1 * r1 + LN_EPS);
        char* hb = L + w * 15360;
#pragma unroll
        for (int nt = 0; nt < 5; ++nt)
            *(u32x2*)(hb + n16 * 176 + (16 * nt + 4 * g) * 2) =
                u32x2{pk2((xv[4 * nt] - mean) * r2, (xv[4 * nt + 1] - mean) * r2), pk2((xv[4 * nt + 2] - mean) * r2, (xv[4 * nt + 3] - mean) * r2)};
        asm volatile("s_waitcnt lgkmcnt(0)" ::: "memory");
        const int q4 = (lane & 15) >> 2, p4 = lane & 3;
        lds_cptr hr = (lds_cptr)(lds_ptr)L + w * 15360 + (8 * (g & 1) + q4) * 176 + p4 * 8 + (g >> 1) * 32;
        const int s0 = tb * 64 + 16 * w + 8 * (g & 1);
        u16* hf = (u16*)(ws + OFF_H) + ((size_t)(5 * cg + (g >> 1)) * 64 + (s0 >> 5)) * 512 + (((s0 & 31) >> 3) * 16 + n16) * 8;
        const bf16x8 f0 = cat8(tr_read(hr), tr_read(hr + 704));
        const bf16x8 f1 = cat8(tr_read(hr + 64), tr_read(hr + 704 + 64));
        const bf16x8 f2 = cat8(tr_read(hr + (g < 2 ? 128 : 0)), tr_read(hr + 704 + (g < 2 ? 128 : 0)));
        *(bf16x8*)(hf) = f0;
        *(bf16x8*)(hf + 2 * 64 * 512) = f1;
        if (g < 2) *(bf16x8*)(hf + 4 * 64 * 512) = f2;
        u16* x1r = (u16*)(ws + OFF_X1) + (size_t)tok * 640 + 80 * cg + 4 * g;
#pragma unroll
        for (int nt = 0; nt < 5; ++nt) *(u32x2*)(x1r + 16 * nt) = x1o[nt];
    }
    SST(5); SST_FLUSHK(ws, XF ? 7 : 2);
}

template <int MODE, int NS, int PF>
DI void gemm_body(const bf16x8* __restrict__ po, const bf16x8* __restrict__ pf, const int (&ooff)[4], int fstride, f32x4 (&acc)[4][5]) {
    bf16x8 ro[PF][4], rf[PF][5];
#pragma unroll
    for (int s = 0; s < PF; ++s) {
#pragma unroll
        for (int j = 0; j < 4; ++j) ro[s][j] = po[ooff[j] + s * 64];
#pragma unroll
        for (int i = 0; i < 5; ++i) rf[s][i] = pf[i * fstride + s * 64];
        SBAR();
    }
#pragma unroll
    for (int s = 0; s < NS; ++s) {
        const int sl = s % PF;
        SBAR();
#pragma unroll
        for (int j = 0; j < 4; ++j)
#pragma unroll
            for (int i = 0; i < 5; ++i) {
                if (MODE == 1) acc[j][i] = MFMA16_32(ro[sl][j], rf[sl][i], acc[j][i]);
                else acc[j][i] = MFMA16_32(rf[sl][i], ro[sl][j], acc[j][i]);
            }
        SBAR();
        if (s + PF < NS) {
#pragma unroll
            for (int j = 0; j < 4; ++j) ro[sl][j] = po[ooff[j] + (s + PF) * 64];
#pragma unroll
            for (int i = 0; i < 5; ++i) rf[sl][i] = pf[i * fstride + (s + PF) * 64];
        }
    }
}

template <int MODE>
__global__ __launch_bounds__(256, 1) void k_gemm(char* __restrict__ ws, const u16* __restrict__ bias, u16* __restrict__ out) {
    __shared__ __attribute__((aligned(16))) f32x4 red[4 * 3 * 5 * 64];
    const int tid = threadIdx.x, lane = tid & 63, w = __builtin_amdgcn_readfirstlane(tid >> 6);
    const int b = blockIdx.x, xcd = b & 7, slot = b >> 3;
    const int n16 = lane & 15, g = lane >> 4;
    constexpr int KS = MODE == 1 ? 64 : 54;
    int ot, dt;
    if (MODE == 1) {
        ot = 7 * (xcd >> 1) + (slot >> 2); dt = 4 * (xcd & 1) + (slot & 3);
        if (slot >= 28 || ot >= 27) return;
    } else {
        ot = 8 * (xcd >> 1) + (slot >> 2); dt = 4 * (xcd & 1) + (slot & 3);
    }
    SST_DECL; SST(0);
    u32x2 bb = {0u, 0u}, xi[5]; float bs = 0.f;
    if (MODE == 1) bb = *(const u32x2*)(bias + 64 * ot + 16 * w + 4 * g);
    else {
        const int s = 64 * ot + 16 * w + n16;
        bs = bf2f(bias[s]);
        const u16* x1 = (const u16*)(ws + OFF_X1) + (size_t)s * 640 + 80 * dt + 4 * g;
#pragma unroll
        for (int i = 0; i < 5; ++i) xi[i] = *(const u32x2*)(x1 + 16 * i);
    }
    const int k0 = MODE == 1 ? 16 * w : (w < 2 ? 14 * w : 28 + 13 * (w - 2));
    const bf16x8* po = (const bf16x8*)(ws + (MODE == 1 ? OFF_W1F : OFF_W2F)) + ((size_t)(4 * ot) * KS + k0) * 64 + lane;
    const bf16x8* pf = (const bf16x8*)(ws + (MODE == 1 ? OFF_H : OFF_U)) + ((size_t)(5 * dt) * KS + k0) * 64 + lane;
    int ooff[4];
#pragma unroll
    for (int j = 0; j < 4; ++j) ooff[j] = ((j + w) & 3) * KS * 64;
    f32x4 acc[4][5];
#pragma unroll
    for (int j = 0; j < 4; ++j)
#pragma unroll
        for (int i = 0; i < 5; ++i) acc[j][i] = f32x4{0.f, 0.f, 0.f, 0.f};
    if (MODE == 1) gemm_body<1, 16, 6>(po, pf, ooff, KS * 64, acc);
    else if (w < 2) gemm_body<2, 14, 6>(po, pf, ooff, KS * 64, acc);
    else gemm_body<2, 13, 6>(po, pf, ooff, KS * 64, acc);
    SST(1);
#pragma unroll
    for (int j = 1; j < 4; ++j) {
        const int o = (w + j) & 3;
#pragma unroll
        for (int i = 0; i < 5; ++i) red[((o * 3 + (j - 1)) * 5 + i) * 64 + lane] = acc[j][i];
    }
    RAW_BARRIER();
#pragma unroll
    for (int j = 0; j < 3; ++j)
#pragma unroll
        for (int i = 0; i < 5; ++i) acc[1 + j][i] = red[((w * 3 + j) * 5 + i) * 64 + lane];
    SBAR();
#pragma unroll
    for (int i = 0; i < 5; ++i) acc[0][i] = (acc[0][i] + acc[1][i]) + (acc[2][i] + acc[3][i]);
    SST(2);
    if (MODE == 1) {
        const int f = 64 * ot + 16 * w + 4 * g;
        const float bv[4] = {bf_lo(bb[0]), bf_hi(bb[0]), bf_lo(bb[1]), bf_hi(bb[1])};
        u16* uf = (u16*)(ws + OFF_U);
#pragma unroll
        for (int i = 0; i < 5; ++i) {
            float r[4];
#pragma unroll
            for (int e = 0; e < 4; ++e) {
                const float u = acc[0][i][e] + bv[e];
                const float z = u * (1.f + 0.044715f * u * u) * (-2.f * 0.7978845608028654f * 1.4426950408889634f);
                r[e] = u * __builtin_amdgcn_rcpf(1.f + EX2(z));
            }
            *(u32x2*)(uf + ((size_t)((5 * dt + i) * 54 + (f >> 5)) * 64 + ((f & 31) >> 3) * 16 + n16) * 8 + (f & 7)) =
                u32x2{pk2(r[0], r[1]), pk2(r[2], r[3])};
        }
    } else {
        const int s = 64 * ot + 16 * w + n16;
        float xv[20]; float sum = 0.f;
#pragma unroll
        for (int i = 0; i < 5; ++i) {
            const int d = 80 * dt + 16 * i + 4 * g;
            u16* o = MODE == 3 ? out + (size_t)s * 640 + d
                               : out + ((size_t)((4 * ot + w) * 20 + (d >> 5)) * 64 + ((d & 31) >> 3) * 16 + n16) * 8 + (d & 7);
            const unsigned u0 = pk2(bf_lo(xi[i][0]) + acc[0][i][0] + bs, bf_hi(xi[i][0]) + acc[0][i][1] + bs);
            const unsigned u1 = pk2(bf_lo(xi[i][1]) + acc[0][i][2] + bs, bf_hi(xi[i][1]) + acc[0][i][3] + bs);
            *(u32x2*)o = u32x2{u0, u1};
            xv[4 * i] = bf_lo(u0); xv[4 * i + 1] = bf_hi(u0); xv[4 * i + 2] = bf_lo(u1); xv[4 * i + 3] = bf_hi(u1);
            sum += (xv[4 * i] + xv[4 * i + 1]) + (xv[4 * i + 2] + xv[4 * i + 3]);
        }
        if (MODE == 2) {
            const float mw = add32(add16(sum)) * (1.f / 80.f);
            float s2 = 0.f;
#pragma unroll
            for (int i = 0; i < 20; ++i) { const float dd = xv[i] - mw; s2 += dd * dd; }
            s2 = add32(add16(s2));
            if (g == 0) *(f32x2*)(ws + OFF_XS + ((size_t)s * 8 + dt) * 8) = f32x2{mw, s2};
        }
    }
    SST(3); SST_FLUSHK(ws, MODE == 1 ? (out ? 8 : 3) : (MODE == 2 ? 4 : 9));
}

typedef __attribute__((address_space(1))) unsigned gu32;
constexpr int AUXL = 16;
#define LD_SC1(off) __builtin_bit_cast(bf16x8, __builtin_amdgcn_raw_buffer_load_b128(rs, (int)(off), 0, 16))
DI void edge_publish(gu32* cnt) {
    RAW_BARRIER();
    if (threadIdx.x == 0) __hip_atomic_fetch_add(cnt, 1u, __ATOMIC_RELAXED, __HIP_MEMORY_SCOPE_AGENT);
}
DI void edge_wait(gu32* cnt, unsigned want) {
    if (threadIdx.x == 0) {
        for (int spin = 0; spin < (1 << 20); ++spin) {
            if (__hip_atomic_load(cnt, __ATOMIC_RELAXED, __HIP_MEMORY_SCOPE_AGENT) >= want) break;
            __builtin_amdgcn_s_sleep(1);
        }
        __builtin_amdgcn_fence(__ATOMIC_ACQUIRE, "wavefront");
    }
    RAW_BARRIER();
}
#define W1_A 6
#define W2_FIRST 7
constexpr int MLP_LDS = 73728;
template <bool R1>
__global__ __launch_bounds__(256, 1) void k_mlp(char* __restrict__ ws, const u16* __restrict__ xin, const u16* __restrict__ b1,
                                                const u16* __restrict__ b2, u16* __restrict__ out) {
    extern __shared__ __attribute__((aligned(16))) char lds_raw[];
    char* L = lds_raw;
    const int tid = threadIdx.x, lane = tid & 63, w = __builtin_amdgcn_readfirstlane(tid >> 6);
    const int b = blockIdx.x, slot = b >> 3, tb = ((b & 7) >> 1) * 8 + (slot >> 2), cg = (b & 1) * 4 + (slot & 3);
    const int ot1 = ((b & 7) >> 1) * 7 + (slot >> 2);
    const bool act1 = slot < 28 && ot1 < 27;
    const int n16 = lane & 15, g = lane >> 4;
    const __amdgpu_buffer_rsrc_t rs = __builtin_amdgcn_make_buffer_rsrc((void*)ws, 0, 0x8000000, 0x00020000);
    gu32* cnt = (gu32*)(ws + OFF_CNT);
    SST_DECL; SST(0);
    f32x4 acc[4][5];
#define ACC_ZERO() do { _Pragma("unroll") for (int i_ = 0; i_ < 4; ++i_) _Pragma("unroll") for (int j_ = 0; j_ < 5; ++j_) acc[i_][j_] = f32x4{0.f, 0.f, 0.f, 0.f}; } while (0)
#define ACC_REDUCE() do { \
    _Pragma("unroll") for (int j_ = 1; j_ < 4; ++j_) { const int o_ = (w + j_) & 3; \
        _Pragma("unroll") for (int i_ = 0; i_ < 5; ++i_) *(f32x4*)(L + (((o_ * 3 + (j_ - 1)) * 5 + i_) * 64 + lane) * 16) = acc[j_][i_]; } \
    RAW_BARRIER(); \
    _Pragma("unroll") for (int j_ = 0; j_ < 3; ++j_) _Pragma("unroll") for (int i_ = 0; i_ < 5; ++i_) \
        acc[1 + j_][i_] = *(const f32x4*)(L + (((w * 3 + j_) * 5 + i_) * 64 + lane) * 16); \
    SBAR(); \
    _Pragma("unroll") for (int i_ = 0; i_ < 5; ++i_) acc[0][i_] = (acc[0][i_] + acc[1][i_]) + (acc[2][i_] + acc[3][i_]); } while (0)
    const int tok = tb * 64 + 16 * w + n16;
    bf16x8 wr[16][4];
    int ro1[4], ro2[4];
#pragma unroll
    for (int j = 0; j < 4; ++j) { ro1[j] = ((j + w) & 3) * 64 * 64; ro2[j] = ((j + w) & 3) * 54 * 64; }
    const bf16x8* pw1 = (const bf16x8*)(ws + OFF_W1F) + ((size_t)(4 * (act1 ? ot1 : 0)) * 64 + 16 * w) * 64 + lane;
    const int k2 = w < 2 ? 14 * w : 28 + 13 * (w - 2);
    const bf16x8* pw2 = (const bf16x8*)(ws + OFF_W2F) + ((size_t)(4 * tb) * 54 + k2) * 64 + lane;
#define W1_LD(lo, hi) do { _Pragma("unroll") for (int s_ = (lo); s_ < (hi); ++s_) _Pragma("unroll") for (int j_ = 0; j_ < 4; ++j_) wr[s_][j_] = pw1[ro1[j_] + s_ * 64]; SBAR(); } while (0)
#define W2_LD(lo, hi) do { _Pragma("unroll") for (int s_ = (lo); s_ < (hi); ++s_) _Pragma("unroll") for (int j_ = 0; j_ < 4; ++j_) wr[s_][j_] = pw2[ro2[j_] + s_ * 64]; SBAR(); } while (0)

    u32x2 x1o[5], bb; float bs;
    {
        ACC_ZERO();
        u32x2 xi[5];
        const bf16x8* xb = (const bf16x8*)(ws + OFF_O) + (size_t)(tb * 4 * 20 + 5 * w) * 64 + lane;
        const bf16x8* wb = (const bf16x8*)(ws + OFF_WOF) + (size_t)(5 * cg * 20 + 5 * w) * 64 + lane;
        bf16x8 xf[5][4], wf[5][5];
        WO_LD(0); WO_LD(1); WO_LD(2); WO_LD(3);
#pragma unroll
        for (int ks = 0; ks < 5; ++ks) {
#pragma unroll
            for (int nt = 0; nt < 5; ++nt)
#pragma unroll
                for (int i = 0; i < 4; ++i) acc[i][nt] = MFMA16_32(wf[ks][nt], xf[ks][i], acc[i][nt]);
            SBAR();
            if (ks == 0) {
                WO_LD(4);
#pragma unroll
                for (int nt = 0; nt < 5; ++nt) {
                    const int d = 80 * cg + 16 * nt + 4 * g;
                    if (!R1) xi[nt] = *(const u32x2*)(xin + ((size_t)((tok >> 4) * 20 + (d >> 5)) * 64 + ((d & 31) >> 3) * 16 + n16) * 8 + (d & 7));
                    else xi[nt] = *(const u32x2*)(xin + (size_t)tok * 640 + d);
                }
                bb = *(const u32x2*)(b1 + 64 * (act1 ? ot1 : 0) + 16 * w + 4 * g);
                bs = bf2f(b2[tok]);
                SBAR();
            }
            if (ks == 1) W1_LD(0, 2);
            if (ks == 2) W1_LD(2, 4);
            if (ks == 3) W1_LD(4, 6);
            if (ks == 4) W1_LD(6, W1_A);
        }
        SST(1);
        ACC_REDUCE();
        float xv[20], s = 0.f;
#pragma unroll
        for (int nt = 0; nt < 5; ++nt) {
            const unsigned u0 = pk2(bf_lo(xi[nt][0]) + acc[0][nt][0], bf_hi(xi[nt][0]) + acc[0][nt][1]);
            const unsigned u1 = pk2(bf_lo(xi[nt][1]) + acc[0][nt][2], bf_hi(xi[nt][1]) + acc[0][nt][3]);
            x1o[nt] = u32x2{u0, u1};
            xv[4 * nt] = bf_lo(u0); xv[4 * nt + 1] = bf_hi(u0); xv[4 * nt + 2] = bf_lo(u1); xv[4 * nt + 3] = bf_hi(u1);
            s += (xv[4 * nt] + xv[4 * nt + 1]) + (xv[4 * nt + 2] + xv[4 * nt + 3]);
        }
        const float mw = add32(add16(s)) * (1.f / 80.f);
        float s2 = 0.f;
#pragma unroll
        for (int i = 0; i < 20; ++i) { const float dd = xv[i] - mw; s2 += dd * dd; }
        s2 = add32(add16(s2));
        gu64* gp = (gu64*)(ws + OFF_XG) + (size_t)tok * 8;
        if (g == 0) __hip_atomic_store(gp + cg, ((unsigned long long)(__float_as_uint(s2) | 0x80000000u) << 32) | __float_as_uint(mw),
                                       __ATOMIC_RELAXED, __HIP_MEMORY_SCOPE_AGENT);
        SBAR();
        SST(2);
        unsigned long long v0, v1;
        for (int spin = 0; spin < (1 << 20); ++spin) {
            v0 = __hip_atomic_load(gp + 2 * g, __ATOMIC_RELAXED, __HIP_MEMORY_SCOPE_AGENT);
            v1 = __hip_atomic_load(gp + 2 * g + 1, __ATOMIC_RELAXED, __HIP_MEMORY_SCOPE_AGENT);
            if (__all((long long)(v0 & v1) < 0)) break;
            __builtin_amdgcn_s_sleep(4);
        }
        SST(3);
        const float m0 = __uint_as_float((unsigned)v0), m1 = __uint_as_float((unsigned)v1);
        const float mean = add32(add16(m0 + m1)) * 0.125f;
        const float d0 = m0 - mean, d1 = m1 - mean;
        const float var = add32(add16(__uint_as_float((unsigned)(v0 >> 32) & 0x7fffffffu) + __uint_as_float((unsigned)(v1 >> 32) & 0x7fffffffu) +
                                      80.f * (d0 * d0 + d1 * d1))) * (1.f / 640.f);
        const float r1 = __builtin_amdgcn_rsqf(var + LN_EPS);
        const float r2 = r1 * __builtin_amdgcn_rsqf(var * r1 * r1 + LN_EPS);
        char* hb = L + w * 15360;
#pragma unroll
        for (int nt = 0; nt < 5; ++nt)
            *(u32x2*)(hb + n16 * 176 + (16 * nt + 4 * g) * 2) =
                u32x2{pk2((xv[4 * nt] - mean) * r2, (xv[4 * nt + 1] - mean) * r2), pk2((xv[4 * nt + 2] - mean) * r2, (xv[4 * nt + 3] - mean) * r2)};
        asm volatile("s_waitcnt lgkmcnt(0)" ::: "memory");
        const int q4 = (lane & 15) >> 2, p4 = lane & 3;
        lds_cptr hr = (lds_cptr)(lds_ptr)L + w * 15360 + (8 * (g & 1) + q4) * 176 + p4 * 8 + (g >> 1) * 32;
        const int s0 = tb * 64 + 16 * w + 8 * (g & 1);
        const int hoff = (int)OFF_H + ((5 * cg + (g >> 1)) * 64 + (s0 >> 5)) * 1024 + (((s0 & 31) >> 3) * 16 + n16) * 16;
        const bf16x8 f0 = cat8(tr_read(hr), tr_read(hr + 704));
        const bf16x8 f1 = cat8(tr_read(hr + 64), tr_read(hr + 704 + 64));
        const bf16x8 f2 = cat8(tr_read(hr + (g < 2 ? 128 : 0)), tr_read(hr + 704 + (g < 2 ? 128 : 0)));
        __builtin_amdgcn_raw_buffer_store_b128(__builtin_bit_cast(u32x4, f0), rs, hoff, 0, 16);
        __builtin_amdgcn_raw_buffer_store_b128(__builtin_bit_cast(u32x4, f1), rs, hoff + 2 * 65536, 0, 16);
        if (g < 2) __builtin_amdgcn_raw_buffer_store_b128(__builtin_bit_cast(u32x4, f2), rs, hoff + 4 * 65536, 0, 16);
        SBAR();
        W1_LD(W1_A, W1_A + 4);
        VMCNT(16);
        SST(4);
        edge_publish(cnt + cg * 64);
    }
    if (act1) {
        W1_LD(W1_A + 4, 16);
        edge_wait(cnt + cg * 64, 32);
        SST(5);
        ACC_ZERO();
        const int ho = (int)OFF_H + ((5 * cg) * 64 + 16 * w) * 1024 + lane * 16;
        constexpr int PF = 6;
        bf16x8 rf[PF][5];
#pragma unroll
        for (int s = 0; s < PF; ++s) {
#pragma unroll
            for (int i = 0; i < 5; ++i) rf[s][i] = LD_SC1(ho + i * 65536 + s * 1024);
            SBAR();
        }
#pragma unroll
        for (int s = 0; s < 16; ++s) {
#pragma unroll
            for (int j = 0; j < 4; ++j)
#pragma unroll
                for (int i = 0; i < 5; ++i) acc[j][i] = MFMA16_32(wr[s][j], rf[s % PF][i], acc[j][i]);
            SBAR();
            if (s + PF < 16) {
#pragma unroll
                for (int i = 0; i < 5; ++i) rf[s % PF][i] = LD_SC1(ho + i * 65536 + (s + PF) * 1024);
                SBAR();
            }
            if (s >= W2_FIRST && s - W2_FIRST < 9) W2_LD(s - W2_FIRST, s - W2_FIRST + 1);
        }
        SST(6);
        ACC_REDUCE();
        const int f = 64 * ot1 + 16 * w + 4 * g;
        const float bv[4] = {bf_lo(bb[0]), bf_hi(bb[0]), bf_lo(bb[1]), bf_hi(bb[1])};
        const int uo = (int)OFF_U + ((5 * cg) * 54 + (f >> 5)) * 1024 + (((f & 31) >> 3) * 16 + n16) * 16 + (f & 7) * 2;
#pragma unroll
        for (int i = 0; i < 5; ++i) {
            float r[4];
#pragma unroll
            for (int e = 0; e < 4; ++e) {
                const float u = acc[0][i][e] + bv[e];
                const float z = u * (1.f + 0.044715f * u * u) * (-2.f * 0.7978845608028654f * 1.4426950408889634f);
                r[e] = u * __builtin_amdgcn_rcpf(1.f + EX2(z));
            }
            __builtin_amdgcn_raw_buffer_store_b64(u32x2{pk2(r[0], r[1]), pk2(r[2], r[3])}, rs, uo + i * 54 * 1024, 0, 16);
        }
        SBAR();
        W2_LD(9, 13);
        VMCNT(16);
        SST(7);
        edge_publish(cnt + (8 + cg) * 64);
    } else {
        W2_LD(0, 13);
    }
    {
        if (w < 2) W2_LD(13, 14);
        edge_wait(cnt + (8 + cg) * 64, 27);
        SST(8);
        ACC_ZERO();
        const int uo = (int)OFF_U + ((5 * cg) * 54 + k2) * 1024 + lane * 16;
        constexpr int PF = 6;
        bf16x8 rf[PF][5];
#pragma unroll
        for (int s = 0; s < PF; ++s) {
#pragma unroll
            for (int i = 0; i < 5; ++i) rf[s][i] = LD_SC1(uo + i * 54 * 1024 + s * 1024);
            SBAR();
        }
#pragma unroll
        for (int s = 0; s < 14; ++s) {
            if (s < 13 || w < 2) {
#pragma unroll
                for (int j = 0; j < 4; ++j)
#pragma unroll
                    for (int i = 0; i < 5; ++i) acc[j][i] = MFMA16_32(rf[s % PF][i], wr[s][j], acc[j][i]);
            }
            SBAR();
            if (s + PF < 13 || (s + PF == 13 && w < 2)) {
#pragma unroll
                for (int i = 0; i < 5; ++i) rf[s % PF][i] = LD_SC1(uo + i * 54 * 1024 + (s + PF) * 1024);
                SBAR();
            }
        }
        SST(9);
        ACC_REDUCE();
        float xv[20], sum = 0.f;
#pragma unroll
        for (int i = 0; i < 5; ++i) {
            const int d = 80 * cg + 16 * i + 4 * g;
            const unsigned u0 = pk2(bf_lo(x1o[i][0]) + acc[0][i][0] + bs, bf_hi(x1o[i][0]) + acc[0][i][1] + bs);
            const unsigned u1 = pk2(bf_lo(x1o[i][1]) + acc[0][i][2] + bs, bf_hi(x1o[i][1]) + acc[0][i][3] + bs);
            if (R1) __builtin_amdgcn_raw_buffer_store_b64(u32x2{u0, u1}, rs,
                        (int)OFF_X2 + ((4 * tb + w) * 20 + (d >> 5)) * 1024 + (((d & 31) >> 3) * 16 + n16) * 16 + (d & 7) * 2, 0, 16);
            else *(u32x2*)(out + (size_t)tok * 640 + d) = u32x2{u0, u1};
            xv[4 * i] = bf_lo(u0); xv[4 * i + 1] = bf_hi(u0); xv[4 * i + 2] = bf_lo(u1); xv[4 * i + 3] = bf_hi(u1);
            sum += (xv[4 * i] + xv[4 * i + 1]) + (xv[4 * i + 2] + xv[4 * i + 3]);
        }
        if (R1) {
            const float mw = add32(add16(sum)) * (1.f / 80.f);
            float s2 = 0.f;
#pragma unroll
            for (int i = 0; i < 20; ++i) { const float dd = xv[i] - mw; s2 += dd * dd; }
            s2 = add32(add16(s2));
            if (g == 0) __builtin_amdgcn_raw_buffer_store_b64(u32x2{__float_as_uint(mw), __float_as_uint(s2)}, rs, (int)OFF_XS + ((tb * 8 + cg) * 64 + 16 * w + n16) * 8, 0, 16);
        }
    }
    if (R1) {
        const int mt0 = cg >> 1, isv = cg & 1;
        const bf16x8* wb = (const bf16x8*)(ws + OFF_WQF) + (size_t)(5 * cg * 20 + 5 * w) * 64 + lane;
        const bf16x8* kb = (const bf16x8*)(ws + OFF_WQF) + (size_t)((40 + 2 * isv) * 20 + 5 * w) * 64 + lane;
        bf16x8 wf[5][5], wkf[5][2], xf[5][4], xk[5];
        SBAR();
#pragma unroll
        for (int i = 0; i < 16; ++i) wf[i / 5][i % 5] = wb[((i % 5) * 20 + i / 5) * 64];
        SBAR();
        VMCNT(16);
        SST(10);
        edge_publish(cnt + (16 + tb) * 64);
#pragma unroll
        for (int i = 16; i < 25; ++i) wf[i / 5][i % 5] = wb[((i % 5) * 20 + i / 5) * 64];
#pragma unroll
        for (int ks = 0; ks < 5; ++ks) { wkf[ks][0] = kb[ks * 64]; wkf[ks][1] = kb[(20 + ks) * 64]; }
        f32x4 csq[5], cs0, cs1, stq[4], stk[4], acck[2];
        {
            const float* csp = (const float*)(ws + OFF_CS);
#pragma unroll
            for (int nt = 0; nt < 5; ++nt) csq[nt] = *(const f32x4*)(csp + 80 * cg + 16 * nt + 4 * g);
            cs0 = *(const f32x4*)(csp + 640 + 32 * isv + 4 * g); cs1 = *(const f32x4*)(csp + 640 + 32 * isv + 16);
        }
        SBAR();
        edge_wait(cnt + (16 + tb) * 64, 8);
        SST(11);
        const int xo = (int)OFF_X2 + (tb * 4 * 20 + 5 * w) * 1024 + lane * 16;
#pragma unroll
        for (int ks = 0; ks < 5; ++ks) {
#pragma unroll
            for (int i = 0; i < 4; ++i) xf[ks][i] = LD_SC1(xo + (((i + w) & 3) * 20 + ks) * 1024);
            xk[ks] = LD_SC1(xo + (mt0 * 20 + ks) * 1024);
            SBAR();
        }
#pragma unroll
        for (int i = 0; i < 4; ++i) {
            const u32x2 a0 = __builtin_amdgcn_raw_buffer_load_b64(rs, (int)OFF_XS + ((tb * 8 + 2 * i) * 64 + 16 * w + n16) * 8, 0, AUXL);
            const u32x2 a1 = __builtin_amdgcn_raw_buffer_load_b64(rs, (int)OFF_XS + ((tb * 8 + 2 * i + 1) * 64 + 16 * w + n16) * 8, 0, AUXL);
            const u32x2 c0 = __builtin_amdgcn_raw_buffer_load_b64(rs, (int)OFF_XS + ((tb * 8 + 2 * i) * 64 + 16 * mt0 + n16) * 8, 0, AUXL);
            const u32x2 c1 = __builtin_amdgcn_raw_buffer_load_b64(rs, (int)OFF_XS + ((tb * 8 + 2 * i + 1) * 64 + 16 * mt0 + n16) * 8, 0, AUXL);
            stq[i] = __builtin_bit_cast(f32x4, u32x4{a0[0], a0[1], a1[0], a1[1]});
            stk[i] = __builtin_bit_cast(f32x4, u32x4{c0[0], c0[1], c1[0], c1[1]});
        }
        SBAR();
        ACC_ZERO();
        acck[0] = f32x4{0.f, 0.f, 0.f, 0.f}; acck[1] = acck[0];
#pragma unroll
        for (int ks = 0; ks < 5; ++ks) {
#pragma unroll
            for (int nt = 0; nt < 5; ++nt)
#pragma unroll
                for (int i = 0; i < 4; ++i) acc[i][nt] = MFMA16_32(wf[ks][nt], xf[ks][i], acc[i][nt]);
            acck[0] = MFMA16_32(wkf[ks][0], xk[ks], acck[0]);
            acck[1] = MFMA16_32(wkf[ks][1], xk[ks], acck[1]);
            SBAR();
        }
        SST(12);
        const float z4[4] = {0.f, 0.f, 0.f, 0.f}, z5[5] = {0.f, 0.f, 0.f, 0.f, 0.f}, z2[2] = {0.f, 0.f};
        qkv_finish<true>(ws, L, tb, cg, w, lane, acc, acck, csq, cs0, cs1, stq, stk, z4, z4, z5, z2);
    }
    SST_FLUSHK(ws, R1 ? 2 : 7);
}

extern "C" void kernel_launch(void* const* d_in, const int* in_sizes, int n_in, void* d_out, int out_size, void* d_ws,
                              size_t ws_size, hipStream_t stream) {
    const u16* x = (const u16*)d_in[0];
    const u16* wq = (const u16*)d_in[1];
    const u16* wk = (const u16*)d_in[2];
    const u16* wv = (const u16*)d_in[3];
    const u16* wo = (const u16*)d_in[4];
    const u16* w1 = (const u16*)d_in[6];
    const u16* b1 = (const u16*)d_in[7];
    const u16* w2 = (const u16*)d_in[8];
    const u16* b2 = (const u16*)d_in[9];
    char* ws = (char*)d_ws;
    u16* x2 = (u16*)(ws + OFF_X2);
    (void)hipFuncSetAttribute((const void*)k_qkv<false>, hipFuncAttributeMaxDynamicSharedMemorySize, QKV_LDS);
    (void)hipFuncSetAttribute((const void*)k_qkv<true>, hipFuncAttributeMaxDynamicSharedMemorySize, QKV_LDS);
    (void)hipFuncSetAttribute((const void*)k_attn<true>, hipFuncAttributeMaxDynamicSharedMemorySize, ATT_LDS);
    (void)hipFuncSetAttribute((const void*)k_attn<false>, hipFuncAttributeMaxDynamicSharedMemorySize, ATT_LDS);
    (void)hipFuncSetAttribute((const void*)k_mlp<true>, hipFuncAttributeMaxDynamicSharedMemorySize, MLP_LDS);
    (void)hipFuncSetAttribute((const void*)k_mlp<false>, hipFuncAttributeMaxDynamicSharedMemorySize, MLP_LDS);
    k_qkv<false><<<256, 256, QKV_LDS, stream>>>(x, wq, wk, wv, wo, ws);
    k_attn<true><<<256, 512, ATT_LDS, stream>>>(ws, w1, w2, wo, wq);
    k_mlp<true><<<256, 256, MLP_LDS, stream>>>(ws, x, b1, b2, nullptr);
    k_attn<false><<<256, 512, ATT_LDS, stream>>>(ws, w1, w2, wo, wq);
    k_mlp<false><<<256, 256, MLP_LDS, stream>>>(ws, x2, b1, b2, (u16*)d_out);
}
```

```cpp
#define NO_TLB_WARM
#include <hip/hip_runtime.h>
#include <hip/hip_bf16.h>
#include <stdint.h>

typedef unsigned short u16;
typedef short bf16x8 __attribute__((ext_vector_type(8)));
typedef short bf16x4 __attribute__((ext_vector_type(4)));
typedef float f32x16 __attribute__((ext_vector_type(16)));
typedef float f32x4 __attribute__((ext_vector_type(4)));
typedef float f32x2 __attribute__((ext_vector_type(2)));
typedef unsigned u32x4 __attribute__((ext_vector_type(4)));
typedef unsigned u32x2 __attribute__((ext_vector_type(2)));
typedef __bf16 bf2_t __attribute__((ext_vector_type(2)));
typedef __attribute__((address_space(3))) char* lds_ptr;
typedef __attribute__((address_space(3))) const char* lds_cptr;
typedef __attribute__((address_space(1))) unsigned long long gu64;

#define DI __device__ __forceinline__
#define SBAR() __builtin_amdgcn_sched_barrier(0)
#define PIN(x) asm volatile("" : "+v"(x))
#define MFMA32_16(a, b, c) __builtin_amdgcn_mfma_f32_32x32x16_bf16(a, b, c, 0, 0, 0)
#define MFMA32_8(a, b, c) __builtin_amdgcn_mfma_f32_32x32x8bf16_1k(a, b, c, 0, 0, 0)
#define MFMA16_32(a, b, c) __builtin_amdgcn_mfma_f32_16x16x32_bf16(a, b, c, 0, 0, 0)
#define EX2(x) __builtin_amdgcn_exp2f(x)
#define RAW_BARRIER() asm volatile("s_waitcnt lgkmcnt(0)\n\ts_barrier" ::: "memory")
#define VMCNT(n) asm volatile("s_waitcnt vmcnt(%0)" :: "i"(n) : "memory")

#define SST_DECL do {} while (0)
#define SST(i) do {} while (0)
#define SST_FLUSHK(ws, kid) do {} while (0)
#define PREP_STORE(p, v) asm volatile("global_store_dwordx4 %0, %1, off sc1\n\ts_nop 1" :: "v"(p), "v"(v) : "memory")
#define ST8(p, v) (*(u32x2*)(p) = (v))
#define KADDR(dst, sym) asm volatile("s_getpc_b64 s[40:41]\n\ts_add_u32 s40, s40, " sym "@rel32@lo+4\n\ts_addc_u32 s41, s41, " sym "@rel32@hi+12\n\ts_mov_b64 %0, s[40:41]" : "=s"(dst) :: "s40", "s41", "scc")
constexpr int S_ = 2048, D_ = 640, F_ = 1728;
constexpr float LN_EPS = 1e-5f;
constexpr float QSCALE = 0.22360679774997896f * 1.4426950408889634f;
constexpr float SAFE_BOUND = 60.0f;

DI unsigned pk2(float a, float b) { f32x2 v = {a, b}; bf2_t r = __builtin_convertvector(v, bf2_t); return __builtin_bit_cast(unsigned, r); }
DI float bf_lo(unsigned u) { return __uint_as_float(u << 16); }
DI float bf_hi(unsigned u) { return __uint_as_float(u & 0xffff0000u); }
DI float bf2f(u16 v) { return __uint_as_float((unsigned)v << 16); }
DI float dot2(unsigned a, unsigned b, float c) { return __builtin_amdgcn_fdot2_f32_bf16(__builtin_bit_cast(bf2_t, a), __builtin_bit_cast(bf2_t, b), c, false); }
DI bf16x4 tr_read(lds_cptr p) { return __builtin_amdgcn_ds_read_tr16_b64_v4i16((__attribute__((address_space(3))) bf16x4*)p); }
DI bf16x8 cat8(bf16x4 a, bf16x4 b) { return __builtin_shufflevector(a, b, 0, 1, 2, 3, 4, 5, 6, 7); }
DI void glds16(const void* g, lds_ptr l) {
    unsigned keep;
    asm volatile("s_mov_b32 %0, m0\n\ts_mov_b32 m0, %2\n\ts_nop 0\n\tglobal_load_lds_dwordx4 %1, off\n\ts_mov_b32 m0, %0"
                 : "=&s"(keep) : "v"(g), "s"((unsigned)(uintptr_t)l) : "memory");
}
DI float xor32(float v) { return __shfl_xor(v, 32); }
DI float add32(float v) {
    auto r = __builtin_amdgcn_permlane32_swap(__float_as_uint(v), __float_as_uint(v), false, false);
    return __uint_as_float(r[0]) + __uint_as_float(r[1]);
}
DI float add16(float v) {
    auto r = __builtin_amdgcn_permlane16_swap(__float_as_uint(v), __float_as_uint(v), false, false);
    return __uint_as_float(r[0]) + __uint_as_float(r[1]);
}
DI float max16x(float v) { auto r = __builtin_amdgcn_permlane16_swap(__float_as_uint(v), __float_as_uint(v), false, false); return fmaxf(__uint_as_float(r[0]), __uint_as_float(r[1])); }
DI float max32x(float v) { auto r = __builtin_amdgcn_permlane32_swap(__float_as_uint(v), __float_as_uint(v), false, false); return fmaxf(__uint_as_float(r[0]), __uint_as_float(r[1])); }
template <int CTRL> DI float dppf(float v) { return __int_as_float(__builtin_amdgcn_update_dpp(0, __float_as_int(v), CTRL, 0xf, 0xf, false)); }
DI float rowmax16(float v) {
    v = fmaxf(v, dppf<0x128>(v)); v = fmaxf(v, dppf<0x124>(v)); v = fmaxf(v, dppf<0x122>(v)); v = fmaxf(v, dppf<0x121>(v)); return v;
}
DI float wavemax(float v) { return max32x(max16x(rowmax16(v))); }
DI u32x4 gather8(const u16* s, int ld) {
    u32x4 r;
#pragma unroll
    for (int e = 0; e < 4; ++e) r[e] = (unsigned)s[(2 * e) * ld] | ((unsigned)s[(2 * e + 1) * ld] << 16);
    return r;
}

constexpr size_t OFF_Q = 0;
constexpr size_t OFF_K1 = OFF_Q + (size_t)2048 * 32 * 48;
constexpr size_t OFF_K2 = OFF_K1 + 2048 * 32;
constexpr size_t OFF_V = OFF_K2 + 2048 * 8;
constexpr size_t OFF_KN = OFF_V + 128 * 640;
constexpr size_t OFF_QN = OFF_KN + 512;
constexpr size_t OFF_CS = OFF_QN + 4096;
constexpr size_t OFF_CONST = OFF_CS + 4096;
constexpr size_t OFF_CNT = OFF_CONST + 256;
constexpr size_t OFF_XG = OFF_CNT + 48 * 256;
constexpr size_t OFF_XS = OFF_XG + 262144;
constexpr size_t OFF_X1 = OFF_XS + 131072;
constexpr size_t OFF_H = OFF_X1 + (size_t)S_ * D_ * 2;
constexpr size_t OFF_U = OFF_H + (size_t)S_ * D_ * 2;
constexpr size_t OFF_X2 = OFF_U + (size_t)D_ * F_ * 2;
constexpr size_t OFF_O = OFF_X2 + (size_t)S_ * D_ * 2;
constexpr size_t OFF_W1F = OFF_O + (size_t)S_ * D_ * 2;
constexpr size_t OFF_W2F = OFF_W1F + (size_t)S_ * F_ * 2;
constexpr size_t OFF_WQF = OFF_W2F + (size_t)S_ * F_ * 2;
constexpr size_t OFF_WOF = OFF_WQF + (size_t)44 * 20 * 1024;
constexpr size_t WS_NEED = OFF_WOF + (size_t)800 * 1024;
#define TLB_STRIDE 32768
constexpr int NPREP = 1728 + 1728 + 200 + 200;

DI float rbf(float v) { return bf_lo(pk2(v, 0.f)); }
DI float ln_sd(float var) { return rbf(__builtin_amdgcn_sqrtf(rbf(rbf(var) + LN_EPS))); }
DI void ln2_scalars(float mu, float var, float& shift, float& scale) {
    const float m1 = rbf(mu), sd1 = ln_sd(var), i1 = 1.0f / sd1;
    const float m2 = rbf((mu - m1) * i1), sd2 = ln_sd(var * i1 * i1);
    shift = m1 + m2 * sd1; scale = i1 / sd2;
}
DI void chan8(const f32x4 (&st)[4], float& mean, float& var) {
    float m = 0.f, m2 = 0.f;
#pragma unroll
    for (int i = 0; i < 4; ++i) { m += st[i][0] + st[i][2]; m2 += st[i][1] + st[i][3]; }
    m *= 0.125f;
#pragma unroll
    for (int i = 0; i < 4; ++i) { const float d0 = st[i][0] - m, d1 = st[i][2] - m; m2 += 80.f * (d0 * d0 + d1 * d1); }
    mean = m; var = m2 * (1.f / 640.f);
}

template <bool FAST>
DI void qkv_finish(char* __restrict__ ws, char* L, int tb, int cg, int w, int lane, f32x4 (&acc)[4][5], f32x4 (&acck)[2],
                   const f32x4 (&csq)[5], const f32x4& cs0, const f32x4& cs1, const f32x4 (&stq)[4], const f32x4 (&stk)[4],
                   const float (&sm)[4], const float (&sq)[4], const float (&cs)[5], const float (&csk)[2]) {
    const int n16 = lane & 15, g = lane >> 4, mt0 = cg >> 1, isv = cg & 1;
    constexpr int R_OFF = 0, PK_OFF = 61440, ST_OFF = 69632, CS_OFF = 71680;
#pragma unroll
    for (int j = 1; j < 4; ++j) {
        const int o = (w + j) & 3;
#pragma unroll
        for (int nt = 0; nt < 5; ++nt) *(f32x4*)(L + R_OFF + (((o * 3 + (j - 1)) * 5 + nt) * 64 + lane) * 16) = acc[j][nt];
    }
    *(f32x4*)(L + PK_OFF + ((w * 2 + 0) * 64 + lane) * 16) = acck[0];
    *(f32x4*)(L + PK_OFF + ((w * 2 + 1) * 64 + lane) * 16) = acck[1];
    if (!FAST && g == 0) {
#pragma unroll
        for (int i = 0; i < 4; ++i) *(f32x2*)(L + ST_OFF + (w * 64 + ((i + w) & 3) * 16 + n16) * 8) = f32x2{sm[i], sq[i]};
#pragma unroll
        for (int nt = 0; nt < 5; ++nt) *(float*)(L + CS_OFF + w * 448 + (nt * 16 + n16) * 4) = cs[nt];
#pragma unroll
        for (int nt = 0; nt < 2; ++nt) *(float*)(L + CS_OFF + w * 448 + (80 + nt * 16 + n16) * 4) = csk[nt];
    }
    char* qrow = ws + OFF_Q + (size_t)(tb * 8 + cg) * 12288 + (16 * w + n16) * 48;
    int qoff[5];
#pragma unroll
    for (int nt = 0; nt < 5; ++nt) { const int G = 4 * nt + g, hl = G / 5; qoff[nt] = hl * 3072 + (G - 5 * hl) * 8; PIN(qoff[nt]); }
    RAW_BARRIER();
    auto stats = [&](int tl, const f32x4 (&st)[4], float& mean, float& rstd) {
        float var;
        if (FAST) chan8(st, mean, var);
        else {
            float s = 0.f, s2 = 0.f;
#pragma unroll
            for (int ww = 0; ww < 4; ++ww) { f32x2 v = *(const f32x2*)(L + ST_OFF + (ww * 64 + tl) * 8); s += v[0]; s2 += v[1]; }
            mean = s * (1.f / 640.f);
            var = fmaxf(s2 * (1.f / 640.f) - mean * mean, 0.f);
        }
        mean = rbf(mean);
        rstd = 1.0f / ln_sd(var);
    };
    {
        f32x4 rr[3][5], cc[4][5];
#pragma unroll
        for (int j = 0; j < 3; ++j)
#pragma unroll
            for (int nt = 0; nt < 5; ++nt) rr[j][nt] = *(const f32x4*)(L + R_OFF + (((w * 3 + j) * 5 + nt) * 64 + lane) * 16);
        if (!FAST) {
#pragma unroll
            for (int ww = 0; ww < 4; ++ww)
#pragma unroll
                for (int nt = 0; nt < 5; ++nt) cc[ww][nt] = *(const f32x4*)(L + CS_OFF + ww * 448 + (nt * 16 + 4 * g) * 4);
        }
        float mean, rstd; stats(16 * w + n16, stq, mean, rstd);
        SBAR();
        const float sc = rstd * QSCALE;
        float n2[5];
#pragma unroll
        for (int nt = 0; nt < 5; ++nt) {
            const f32x4 a = (acc[0][nt] + rr[0][nt]) + (rr[1][nt] + rr[2][nt]);
            f32x4 s;
            if (FAST) s = csq[nt];
            else {
                s = (cc[0][nt] + cc[1][nt]) + (cc[2][nt] + cc[3][nt]);
                if (tb == 0 && w == 0 && n16 == 0) *(f32x4*)(ws + OFF_CS + (80 * cg + 16 * nt + 4 * g) * 4) = s;
            }
            const unsigned u0 = pk2((a[0] - mean * s[0]) * sc, (a[1] - mean * s[1]) * sc);
            const unsigned u1 = pk2((a[2] - mean * s[2]) * sc, (a[3] - mean * s[3]) * sc);
            { const u32x2 v_ = u32x2{u0, u1}; ST8(qrow + qoff[nt], v_); }
            n2[nt] = dot2(u1, u1, dot2(u0, u0, 0.f));
        }
        float hn[4];
        hn[0] = n2[0] + (g == 0 ? n2[1] : 0.f);
        hn[1] = (g > 0 ? n2[1] : 0.f) + (g < 2 ? n2[2] : 0.f);
        hn[2] = (g > 1 ? n2[2] : 0.f) + (g < 3 ? n2[3] : 0.f);
        hn[3] = (g == 3 ? n2[3] : 0.f) + n2[4];
#pragma unroll
        for (int hh = 0; hh < 4; ++hh) hn[hh] = add32(add16(hn[hh]));
        const float n2a = (g & 1) ? hn[1] : hn[0], n2b = (g & 1) ? hn[3] : hn[2];
        const unsigned qb = pk2(__builtin_amdgcn_sqrtf((g & 2) ? n2b : n2a), 0.f);
        { const u32x2 v_ = u32x2{qb, 0u}; ST8(qrow + g * 3072 + 40, v_); }
        float m = bf_lo(qb);
        if (!(m == m)) m = INFINITY;
        m = wavemax(m);
        if (lane == 0) *(float*)(ws + OFF_QN + ((tb * 8 + cg) * 4 + w) * 4) = m;
    }
    if (w == 3) {
        const int t16 = tb * 4 + mt0;
        float mean, rstd; stats(mt0 * 16 + n16, stk, mean, rstd);
        f32x4 a0 = {0.f, 0.f, 0.f, 0.f}, a1 = a0, s0 = a0, s1 = a0;
#pragma unroll
        for (int ww = 0; ww < 4; ++ww) {
            a0 += *(const f32x4*)(L + PK_OFF + ((ww * 2 + 0) * 64 + lane) * 16);
            a1 += *(const f32x4*)(L + PK_OFF + ((ww * 2 + 1) * 64 + n16) * 16);
            if (!FAST) {
                s0 += *(const f32x4*)(L + CS_OFF + ww * 448 + (80 + 4 * g) * 4);
                s1 += *(const f32x4*)(L + CS_OFF + ww * 448 + 96 * 4);
            }
        }
        if (FAST) { s0 = cs0; s1 = cs1; }
        else if (tb == 0 && cg < 2 && n16 == 0) {
            *(f32x4*)(ws + OFF_CS + (640 + 32 * isv + 4 * g) * 4) = s0;
            if (g == 0) *(f32x4*)(ws + OFF_CS + (640 + 32 * isv + 16) * 4) = s1;
        }
        const unsigned u0 = pk2((a0[0] - mean * s0[0]) * rstd, (a0[1] - mean * s0[1]) * rstd);
        const unsigned u1 = pk2((a0[2] - mean * s0[2]) * rstd, (a0[3] - mean * s0[3]) * rstd);
        const unsigned u2 = pk2((a1[0] - mean * s1[0]) * rstd, (a1[1] - mean * s1[1]) * rstd);
        const unsigned u3 = pk2((a1[2] - mean * s1[2]) * rstd, (a1[3] - mean * s1[3]) * rstd);
        if (!isv) {
            const int key = t16 * 16 + n16;
            *(u32x2*)(ws + OFF_K1 + key * 32 + g * 8) = u32x2{u0, u1};
            if (g == 0) *(u32x2*)(ws + OFF_K2 + key * 8) = u32x2{u2, u3};
            float n2 = dot2(u1, u1, dot2(u0, u0, 0.f));
            n2 = add32(add16(n2));
            n2 = dot2(u3, u3, dot2(u2, u2, n2));
            if (!(n2 == n2)) n2 = INFINITY;
            n2 = rowmax16(n2);
            if (lane == 0) *(float*)(ws + OFF_KN + t16 * 4) = n2;
        } else {
            u16* vb = (u16*)(ws + OFF_V + t16 * 640 + (n16 >> 3) * 320) + (n16 & 7);
            vb[(4 * g + 0) * 8] = (u16)u0; vb[(4 * g + 1) * 8] = (u16)(u0 >> 16);
            vb[(4 * g + 2) * 8] = (u16)u1; vb[(4 * g + 3) * 8] = (u16)(u1 >> 16);
            if (g == 0) { vb[16 * 8] = (u16)u2; vb[17 * 8] = (u16)(u2 >> 16); vb[18 * 8] = (u16)u3; vb[19 * 8] = (u16)(u3 >> 16); }
        }
    }
    if (w == 0) *(u32x2*)(ws + OFF_XG + ((size_t)(tb * 64 + lane) * 8 + cg) * 8) = u32x2{0u, 0u};
}

constexpr int QKV_LDS = 163840;
template <bool FAST>
__global__ __launch_bounds__(256, 1) void k_qkv(const u16* __restrict__ x, const u16* __restrict__ wq, const u16* __restrict__ wk,
                                                const u16* __restrict__ wv, const u16* __restrict__ wo, char* __restrict__ ws,
                                                const u16* __restrict__ w1, const u16* __restrict__ w2) {
    extern __shared__ __attribute__((aligned(16))) char lds_raw[];
    lds_ptr lds = (lds_ptr)lds_raw;
    char* L = lds_raw;
    const int tid = threadIdx.x, lane = tid & 63, w = __builtin_amdgcn_readfirstlane(tid >> 6);
    const int b = blockIdx.x, slot = b >> 3, tb = ((b & 7) >> 1) * 8 + (slot >> 2), cg = (b & 1) * 4 + (slot & 3);
    const int mt0 = cg >> 1, isv = cg & 1;
    const int n16 = lane & 15, g = lane >> 4;
    SST_DECL; SST(0);
    unsigned code_touch = 0u, wtouch[2] = {0u, 0u};
    if (!FAST && w == 2) {
        uint64_t a[5];
        KADDR(a[0], "_Z6k_attnILb1EEvPcPKtS2_S2_S2_"); KADDR(a[1], "_Z6k_attnILb0EEvPcPKtS2_S2_S2_");
        KADDR(a[2], "_Z5k_mlpILb1EEvPcPKtS2_S2_Pt"); KADDR(a[3], "_Z5k_mlpILb0EEvPcPKtS2_S2_Pt"); KADDR(a[4], "_Z5k_endILi0EEvv");
        uint64_t lo = a[0], hi = a[0];
#pragma unroll
        for (int i = 1; i < 5; ++i) { lo = a[i] < lo ? a[i] : lo; hi = a[i] > hi ? a[i] : hi; }
        const uint64_t pa = lo + (uint64_t)(slot * 64 + lane) * 128;
        if (pa < hi) code_touch = *(const __attribute__((address_space(1))) unsigned*)pa;
        SBAR();
    }
    f32x4 acc[4][5], acck[2];
#pragma unroll
    for (int i = 0; i < 4; ++i)
#pragma unroll
        for (int j = 0; j < 5; ++j) acc[i][j] = f32x4{0.f, 0.f, 0.f, 0.f};
    acck[0] = f32x4{0.f, 0.f, 0.f, 0.f}; acck[1] = acck[0];
    f32x4 csq[5], cs0, cs1, stq[4], stk[4];
    float sm[4] = {0.f, 0.f, 0.f, 0.f}, sq[4] = {0.f, 0.f, 0.f, 0.f};
    float cs[5] = {0.f, 0.f, 0.f, 0.f, 0.f}, csk[2] = {0.f, 0.f};
    if (FAST) {
        const bf16x8* xb = (const bf16x8*)x + (size_t)(tb * 4 * 20 + 5 * w) * 64 + lane;
        const bf16x8* wb = (const bf16x8*)(ws + OFF_WQF) + (size_t)(5 * cg * 20 + 5 * w) * 64 + lane;
        const bf16x8* kb = (const bf16x8*)(ws + OFF_WQF) + (size_t)((40 + 2 * isv) * 20 + 5 * w) * 64 + lane;
        bf16x8 xf[5][4], xk[5], wf[5][5], wkf[5][2];
#define QKV_LD(ks) do { _Pragma("unroll") for (int i = 0; i < 4; ++i) xf[ks][i] = xb[(((i + w) & 3) * 20 + (ks)) * 64]; \
        _Pragma("unroll") for (int nt = 0; nt < 5; ++nt) wf[ks][nt] = wb[(nt * 20 + (ks)) * 64]; \
        xk[ks] = xb[(mt0 * 20 + (ks)) * 64]; wkf[ks][0] = kb[(ks) * 64]; wkf[ks][1] = kb[(20 + (ks)) * 64]; SBAR(); } while (0)
        QKV_LD(0); QKV_LD(1); QKV_LD(2); QKV_LD(3);
#pragma unroll
        for (int ks = 0; ks < 5; ++ks) {
#pragma unroll
            for (int nt = 0; nt < 5; ++nt)
#pragma unroll
                for (int i = 0; i < 4; ++i) acc[i][nt] = MFMA16_32(wf[ks][nt], xf[ks][i], acc[i][nt]);
            acck[0] = MFMA16_32(wkf[ks][0], xk[ks], acck[0]);
            acck[1] = MFMA16_32(wkf[ks][1], xk[ks], acck[1]);
            SBAR();
            if (ks == 0) {
                QKV_LD(4);
                const float* csp = (const float*)(ws + OFF_CS);
#pragma unroll
                for (int nt = 0; nt < 5; ++nt) csq[nt] = *(const f32x4*)(csp + 80 * cg + 16 * nt + 4 * g);
                cs0 = *(const f32x4*)(csp + 640 + 32 * isv + 4 * g); cs1 = *(const f32x4*)(csp + 640 + 32 * isv + 16);
                const f32x4* sp = (const f32x4*)(ws + OFF_XS) + (size_t)(tb * 64 + n16) * 4;
#pragma unroll
                for (int i = 0; i < 4; ++i) { stq[i] = sp[16 * w * 4 + i]; stk[i] = sp[16 * mt0 * 4 + i]; }
                SBAR();
            }
        }
    } else {
        lds_ptr ra = lds + w * 40960, rb = ra + 20480;
        const char* xs = (const char*)(x + (size_t)(tb * 64) * 640 + 160 * w);
#pragma unroll
        for (int j = 0; j < 5; ++j) {
            const int c = 64 * j + lane;
            const unsigned xo = (unsigned)(c / 20) * 1280u + (unsigned)(c % 20) * 16u;
#pragma unroll
            for (int i = 0; i < 4; ++i) glds16(xs + (size_t)i * 20480 + xo, ra + i * 5120 + j * 1024);
        }
        const char* src = (const char*)(wq + (size_t)(160 * w) * 640 + 80 * cg);
        const unsigned loff = (unsigned)(lane / 10) * 1280u + (unsigned)(lane % 10) * 16u;
        const char* src2 = (const char*)((isv ? wv : wk) + (size_t)(160 * w) * 20);
#pragma unroll
        for (int i = 0; i < 16; ++i) glds16(src + (size_t)i * 7680 + loff, rb + i * 960);
#pragma unroll
        for (int i = 0; i < 3; ++i) glds16(src2 + i * 1024 + lane * 16, rb + 15488 + i * 1024);
        if (lane < 48) glds16(src2 + 3 * 1024 + lane * 16, rb + 15488 + 3 * 1024);
        SST(1);
        VMCNT(20);
        bf16x8 xf[4][5], xk[5];
        {
            const char* xl = L + w * 40960 + n16 * 320 + g * 16;
#pragma unroll
            for (int i = 0; i < 4; ++i)
#pragma unroll
                for (int ks = 0; ks < 5; ++ks) xf[i][ks] = *(const bf16x8*)(xl + ((i + w) & 3) * 5120 + ks * 64);
#pragma unroll
            for (int ks = 0; ks < 5; ++ks) xk[ks] = *(const bf16x8*)(xl + mt0 * 5120 + ks * 64);
        }
        asm volatile("s_waitcnt lgkmcnt(0)" ::: "memory");
        SBAR();
#pragma unroll
        for (int i = 0; i < 10; ++i) glds16(src + (size_t)(16 + i) * 7680 + loff, ra + i * 960);
        if (lane < 40) glds16(src + (size_t)26 * 7680 + loff, ra + 10 * 960);
#pragma unroll
        for (int i = 0; i < 2; ++i) glds16(src2 + 3840 + i * 1024 + lane * 16, ra + 10240 + i * 1024);
        if (lane < 32) glds16(src2 + 3840 + 2 * 1024 + lane * 16, ra + 10240 + 2 * 1024);
        SST(2);
        const unsigned ONES = 0x3f803f80u;
#pragma unroll
        for (int i = 0; i < 4; ++i)
#pragma unroll
            for (int ks = 0; ks < 5; ++ks) {
                u32x4 u = __builtin_bit_cast(u32x4, xf[i][ks]);
#pragma unroll
                for (int e = 0; e < 4; ++e) { sm[i] = dot2(u[e], ONES, sm[i]); sq[i] = dot2(u[e], u[e], sq[i]); }
            }
        SBAR();
        VMCNT(14);
        SST(3);
        const int q4 = (lane & 15) >> 2, p4 = lane & 3;
        lds_cptr wb0 = (lds_cptr)rb + (8 * g + q4) * 160 + p4 * 8, wb1 = (lds_cptr)ra + (8 * g + q4) * 160 + p4 * 8 - 3 * 5120;
        lds_cptr kb0 = (lds_cptr)rb + 15488 + (8 * g + q4) * 40 + p4 * 8, kb1 = (lds_cptr)ra + 10240 + (8 * g + q4) * 40 + p4 * 8 - 3 * 1280;
        bf16x8 wf[2][7];
#define QKV_TR(ks, s_) do { lds_cptr wb = (ks) < 3 ? wb0 : wb1, kb = (ks) < 3 ? kb0 : kb1; \
        _Pragma("unroll") for (int nt = 0; nt < 5; ++nt) wf[s_][nt] = cat8(tr_read(wb + (ks) * 5120 + nt * 32), tr_read(wb + (ks) * 5120 + 640 + nt * 32)); \
        _Pragma("unroll") for (int nt = 0; nt < 2; ++nt) wf[s_][5 + nt] = cat8(tr_read(kb + (ks) * 1280 + nt * 32), tr_read(kb + (ks) * 1280 + 160 + nt * 32)); SBAR(); } while (0)
        QKV_TR(0, 0);
#pragma unroll
        for (int ks = 0; ks < 5; ++ks) {
            if (ks == 2) {
                VMCNT(0); SBAR();
            }
            if (ks < 4) QKV_TR(ks + 1, (ks + 1) & 1);
#pragma unroll
            for (int nt = 0; nt < 5; ++nt) {
                u32x4 u = __builtin_bit_cast(u32x4, wf[ks & 1][nt]);
#pragma unroll
                for (int e = 0; e < 4; ++e) cs[nt] = dot2(u[e], ONES, cs[nt]);
#pragma unroll
                for (int i = 0; i < 4; ++i) acc[i][nt] = MFMA16_32(wf[ks & 1][nt], xf[i][ks], acc[i][nt]);
            }
#pragma unroll
            for (int nt = 0; nt < 2; ++nt) {
                u32x4 u = __builtin_bit_cast(u32x4, wf[ks & 1][5 + nt]);
#pragma unroll
                for (int e = 0; e < 4; ++e) csk[nt] = dot2(u[e], ONES, csk[nt]);
                acck[nt] = MFMA16_32(wf[ks & 1][5 + nt], xk[ks], acck[nt]);
            }
            SBAR();
        }
#pragma unroll
        for (int i = 0; i < 4; ++i) { sm[i] = add32(add16(sm[i])); sq[i] = add32(add16(sq[i])); }
#pragma unroll
        for (int i = 0; i < 5; ++i) cs[i] = add32(add16(cs[i]));
#pragma unroll
        for (int i = 0; i < 2; ++i) csk[i] = add32(add16(csk[i]));
    }
    SST(4);
    if (!FAST) RAW_BARRIER();
    qkv_finish<FAST>(ws, L, tb, cg, w, lane, acc, acck, csq, cs0, cs1, stq, stk, sm, sq, cs, csk);
    if (!FAST && w == 1) {
        if (b < 80) {
            const int ks = b >> 2, ct = b & 3; int cn = 16 * (ct & 1) + n16; if (cn >= 20) cn = 0;
            *(u32x4*)(ws + OFF_WQF + (size_t)((40 + ct) * 20 + ks) * 1024 + lane * 16) =
                gather8(((ct & 2) ? wv : wk) + (size_t)(32 * ks + 8 * g) * 20 + cn, 20);
        } else if (b == 80 && lane < 4) {
            const unsigned v = (lane & 2) ? 0x3f803f80u : 0u;
            *(u32x4*)(ws + OFF_CONST + lane * 16) = u32x4{v, v, v, v};
        }
    }
    PIN(code_touch); PIN(wtouch[0]); PIN(wtouch[1]);
    SST(7); SST_FLUSHK(ws, FAST ? 5 : 0);
}

constexpr int PJ0 = 1, PJ1 = 8, PJD = 5;
constexpr int PBW = 2688, ATT_LDS = 32 * PBW;
template <bool PREP>
__global__ __launch_bounds__(512, 2) void k_attn(char* __restrict__ ws, const u16* __restrict__ w1, const u16* __restrict__ w2,
                                                 const u16* __restrict__ wo, const u16* __restrict__ wq) {
    extern __shared__ __attribute__((aligned(16))) char lds_raw[];
    char* L = lds_raw;
    const int tid = threadIdx.x, lane = tid & 63, w = __builtin_amdgcn_readfirstlane(tid >> 6);
    const int b = blockIdx.x, slot = b >> 3, tb = ((b & 7) >> 1) * 8 + (slot >> 2), cg = (b & 1) * 4 + (slot & 3);
    const int r32 = lane & 31, h = lane >> 5, n16 = lane & 15, g = lane >> 4;
    const int cgp = w & 1, kq = w >> 1;
    SST_DECL; SST(0);
    float kn0 = *(const float*)(ws + OFF_KN + lane * 4), kn1 = *(const float*)(ws + OFF_KN + 256 + lane * 4);
    float qmax = *(const float*)(ws + OFF_QN + ((tb * 8 + cg) * 4 + (lane & 3)) * 4);
    bf16x8 q1[4]; bf16x4 q2[4];
    {
        const char* qg = ws + OFF_Q + (size_t)(tb * 8 + cg) * 12288 + cgp * 6144 + r32 * 48;
#pragma unroll
        for (int j = 0; j < 4; ++j) { q1[j] = *(const bf16x8*)(qg + j * 1536 + 16 * h); q2[j] = *(const bf16x4*)(qg + j * 1536 + 32 + 8 * h); }
    }
    SBAR();
    const int kr = (r32 & 19) | ((r32 & 4) << 1) | ((r32 & 8) >> 1);
    const unsigned ko1 = (unsigned)OFF_K1 + (unsigned)(512 * kq + kr) * 32u + 16u * h;
    const unsigned ko2 = h ? (unsigned)OFF_CONST + 32u : (unsigned)OFF_K2 + (unsigned)(512 * kq + kr) * 8u;
    const unsigned ks2 = h ? 0u : 256u;
    const unsigned vo = r32 < 20 ? (unsigned)OFF_V + (unsigned)(32 * kq) * 640u + (unsigned)(h * 20 + r32) * 16u
                                 : (unsigned)OFF_CONST + (r32 == 20 ? 32u : 0u);
    const unsigned vs = r32 < 20 ? 640u : 0u;
    bf16x8 k1[3], v0[3], v1[3]; bf16x4 k2[3];
#define K_LOAD(t) do { k1[(t) % 3] = *(const bf16x8*)(ws + ko1 + (t) * 1024); k2[(t) % 3] = *(const bf16x4*)(ws + (ko2 + (t) * ks2)); } while (0)
#define V_LOAD(t) do { v0[(t) % 3] = *(const bf16x8*)(ws + (vo + (2 * (t)) * vs)); v1[(t) % 3] = *(const bf16x8*)(ws + (vo + (2 * (t) + 1) * vs)); } while (0)
    K_LOAD(0); V_LOAD(0); SBAR(); K_LOAD(1); V_LOAD(1); SBAR();
    PIN(kn0); PIN(kn1);
    const float kmax2 = wavemax(fmaxf(kn0, kn1));
    qmax = rowmax16(qmax);
    const float kb = __builtin_amdgcn_sqrtf(kmax2);
    const bool safe = __builtin_amdgcn_readfirstlane((int)(qmax * kb <= SAFE_BOUND)) != 0;
    const f32x16 zero16 = {0.f, 0.f, 0.f, 0.f, 0.f, 0.f, 0.f, 0.f, 0.f, 0.f, 0.f, 0.f, 0.f, 0.f, 0.f, 0.f};
    if (safe) {
        if (h) {
#pragma unroll
            for (int j = 0; j < 4; ++j) q2[j][0] = (short)pk2(-bf2f((u16)q2[j][0]) * kb, 0.f);
        }
    } else {
        float m[4];
#pragma unroll
        for (int j = 0; j < 4; ++j) { m[j] = -INFINITY; if (h) q2[j] = bf16x4{0, 0, 0, 0}; }
        for (int t = 0; t < 16; ++t) {
            const bf16x8 ka = *(const bf16x8*)(ws + ko1 + t * 1024);
            const bf16x4 kc = *(const bf16x4*)(ws + (ko2 + t * ks2));
#pragma unroll
            for (int j = 0; j < 4; ++j) {
                f32x16 s = MFMA32_16(ka, q1[j], zero16);
                SBAR(); asm volatile("s_nop 15" ::: "memory"); SBAR();
                s = MFMA32_8(kc, q2[j], s);
#pragma unroll
                for (int e = 0; e < 16; ++e) m[j] = fmaxf(m[j], s[e]);
                SBAR();
            }
        }
#pragma unroll
        for (int j = 0; j < 4; ++j) {
            const float mm = fmaxf(m[j], xor32(m[j]));
            if (h == 0) *(float*)(L + ((w * 4 + j) * 32 + r32) * 4) = mm;
        }
        RAW_BARRIER();
#pragma unroll
        for (int j = 0; j < 4; ++j) {
            float mm = -INFINITY;
#pragma unroll
            for (int qq = 0; qq < 4; ++qq) mm = fmaxf(mm, *(const float*)(L + (((2 * qq + cgp) * 4 + j) * 32 + r32) * 4));
            const unsigned hi = __float_as_uint(mm) & 0xffff0000u;
            const float r1 = mm - __uint_as_float(hi);
            const unsigned mid = __float_as_uint(r1) & 0xffff0000u;
            const float r2 = r1 - __uint_as_float(mid);
            const unsigned lo = __float_as_uint(r2) & 0xffff0000u;
            if (h) q2[j] = __builtin_bit_cast(bf16x4, u32x2{hi ^ 0x80000000u, ((mid ^ 0x80000000u) >> 16) | (lo ^ 0x80000000u)});
        }
        RAW_BARRIER();
    }
    SST(1);
    f32x16 o[4], sa, sb;
#pragma unroll
    for (int j = 0; j < 4; ++j) o[j] = zero16;
    u32x4 p1prev = {0u, 0u, 0u, 0u};
    f32x4 ea, eb;
    sa = MFMA32_16(k1[0], q1[0], zero16);
    SBAR(); asm volatile("s_nop 15" ::: "memory"); SBAR();
    sa = MFMA32_8(k2[0], q2[0], sa);
    SBAR(); asm volatile("s_nop 15" ::: "memory"); SBAR();
    u32x4 prep[4]; char* prep_dst = nullptr; int prep_st = 0;
    unsigned l2t[3] = {0u, 0u, 0u};
#pragma unroll
    for (int t = 0; t < 16; ++t) {
#pragma unroll
        for (int j = 0; j < 4; ++j) {
            const int it = 4 * t + j;
            f32x16& cur = (it & 1) ? sb : sa; f32x16& nxt = (it & 1) ? sa : sb;
            const int tn = j < 3 ? t : t + 1, jn = (j + 1) & 3;
            const int tp = j > 0 ? t : t - 1, jp = (j + 3) & 3;
            u32x4 p0, p1;
            if (j == 0 && t + 2 < 16) K_LOAD(t + 2);
            if (j == 1 && t + 2 < 16) V_LOAD(t + 2);
            if (PREP && j == 2 && (t == PJ0 || t == PJ1)) {
                int id = (t == PJ1 ? 2048 : 0) + b * 8 + w; if (id >= NPREP) id -= 2048;
                const u16* src; int ld;
                if (id < 1728) { const int ks = id / 27, c4 = id - ks * 27; src = w1 + (size_t)(32 * ks) * F_ + 64 * c4; ld = F_;
                                 prep_dst = ws + OFF_W1F + (size_t)(4 * c4 * 64 + ks) * 1024; prep_st = 64 * 1024; }
                else if (id < 3456) { const int jj = id - 1728, ks = jj >> 5, c4 = jj & 31; src = w2 + (size_t)(32 * ks) * S_ + 64 * c4; ld = S_;
                                      prep_dst = ws + OFF_W2F + (size_t)(4 * c4 * 54 + ks) * 1024; prep_st = 54 * 1024; }
                else { int jj = id - 3456; const bool isq = jj >= 200; if (isq) jj -= 200; const int ks = jj / 10, c4 = jj - ks * 10;
                       src = (isq ? wq : wo) + (size_t)(32 * ks) * 640 + 64 * c4; ld = 640;
                       prep_dst = ws + (isq ? OFF_WQF : OFF_WOF) + (size_t)(4 * c4 * 20 + ks) * 1024; prep_st = 20 * 1024; }
                src += (size_t)(lane >> 3) * ld + (lane & 7) * 8;
#pragma unroll
                for (int i = 0; i < 4; ++i) prep[i] = *(const u32x4*)(src + (size_t)(8 * i) * ld);
            }
            if (PREP && j == 2 && (t == PJ0 + PJD || t == PJ1 + PJD)) {
#pragma unroll
                for (int i = 0; i < 4; ++i) *(u32x4*)(L + w * 4096 + i * 1024 + lane * 16) = prep[i];
            }
            if (PREP && j == 3 && (t == PJ0 + PJD || t == PJ1 + PJD)) {
                lds_cptr tp_ = (lds_cptr)(lds_ptr)L + w * 4096 + (8 * g + (n16 >> 2)) * 128 + (lane & 3) * 8;
#pragma unroll
                for (int i = 0; i < 4; ++i) prep[i] = __builtin_bit_cast(u32x4, cat8(tr_read(tp_ + i * 32), tr_read(tp_ + 512 + i * 32)));
            }
            if (PREP && j == 0 && (t == PJ0 + PJD + 1 || t == PJ1 + PJD + 1)) {
#pragma unroll
                for (int i = 0; i < 4; ++i) PREP_STORE((u32x4*)(prep_dst + (size_t)i * prep_st + lane * 16), prep[i]);
            }
            SBAR();
            if (it < 63) nxt = MFMA32_16(k1[tn % 3], q1[jn], zero16);
            SBAR();
            p0[0] = pk2(EX2(cur[0]), EX2(cur[1])); p0[1] = pk2(EX2(cur[2]), EX2(cur[3]));
            PIN(p0); SBAR();
            if (it > 0) o[jp] = MFMA32_16(v1[(tp + 3) % 3], __builtin_bit_cast(bf16x8, p1prev), o[jp]);
            SBAR();
            p0[2] = pk2(EX2(cur[4]), EX2(cur[5])); p0[3] = pk2(EX2(cur[6]), EX2(cur[7]));
            PIN(p0); SBAR();
            if (it < 63) nxt = MFMA32_8(k2[tn % 3], q2[jn], nxt);
            SBAR();
            p1[0] = pk2(EX2(cur[8]), EX2(cur[9])); p1[1] = pk2(EX2(cur[10]), EX2(cur[11]));
            PIN(p1); SBAR();
            o[j] = MFMA32_16(v0[t % 3], __builtin_bit_cast(bf16x8, p0), o[j]);
            SBAR();
            p1[2] = pk2(EX2(cur[12]), EX2(cur[13])); p1[3] = pk2(EX2(cur[14]), EX2(cur[15]));
            PIN(p1); SBAR();
            p1prev = p1;
        }
        if (t == 7) SST(2);
    }
    o[3] = MFMA32_16(v1[15 % 3], __builtin_bit_cast(bf16x8, p1prev), o[3]);
    SST(3);
    if (PREP) RAW_BARRIER();
#pragma unroll
    for (int j = 0; j < 4; ++j) {
        char* pb = L + (kq * 8 + cgp * 4 + j) * PBW;
        *(f32x4*)(pb + lane * 16) = f32x4{o[j][0], o[j][1], o[j][2], o[j][3]};
        *(f32x4*)(pb + 1024 + lane * 16) = f32x4{o[j][4], o[j][5], o[j][6], o[j][7]};
        if (h == 0) *(f32x4*)(pb + 2048 + r32 * 16) = f32x4{o[j][8], o[j][9], o[j][10], o[j][11]};
        else *(float*)(pb + 2560 + r32 * 4) = o[j][8];
    }
    RAW_BARRIER();
    SST(4);
    {
        const char* pb = L + w * PBW;
        f32x4 a0 = {0.f, 0.f, 0.f, 0.f}, a1 = a0, a2 = a0; float l = 0.f;
#pragma unroll
        for (int qq = 0; qq < 4; ++qq) {
            a0 += *(const f32x4*)(pb + qq * 8 * PBW + lane * 16);
            a1 += *(const f32x4*)(pb + qq * 8 * PBW + 1024 + lane * 16);
            a2 += *(const f32x4*)(pb + qq * 8 * PBW + 2048 + r32 * 16);
            l += *(const float*)(pb + qq * 8 * PBW + 2560 + r32 * 4);
        }
        const float inv = __builtin_amdgcn_rcpf(l);
        const int tl = 32 * (w & 1) + r32, hd0 = 80 * cg + 20 * (w >> 1);
        char* ob = ws + OFF_O + (size_t)(tb * 4 + (tl >> 4)) * 20480 + (tl & 15) * 16;
        auto st = [&](int hd, const f32x4& a) {
            const u32x2 v_ = u32x2{pk2(a[0] * inv, a[1] * inv), pk2(a[2] * inv, a[3] * inv)};
            ST8(ob + (hd >> 5) * 1024 + ((hd & 31) >> 3) * 256 + (hd & 7) * 2, v_);
        };
        st(hd0 + 4 * h, a0); st(hd0 + 8 + 4 * h, a1);
        if (h == 0) st(hd0 + 16, a2);
    }
    if (b == 0 && tid < 48) *(unsigned*)(ws + OFF_CNT + tid * 256) = 0u;
    PIN(l2t[0]); PIN(l2t[1]); PIN(l2t[2]);
    SST(5); SST_FLUSHK(ws, PREP ? 1 : 6);
}

template <bool XF>
__global__ __launch_bounds__(256, 1) void k_wo(char* __restrict__ ws, const u16* __restrict__ xin) {
    __shared__ __attribute__((aligned(16))) char L[61440];
    const int tid = threadIdx.x, lane = tid & 63, w = __builtin_amdgcn_readfirstlane(tid >> 6);
    const int b = blockIdx.x, slot = b >> 3, tb = ((b & 7) >> 1) * 8 + (slot >> 2), cg = (b & 1) * 4 + (slot & 3);
    const int n16 = lane & 15, g = lane >> 4;
    SST_DECL; SST(0);
    f32x4 acc[4][5];
#pragma unroll
    for (int i = 0; i < 4; ++i)
#pragma unroll
        for (int j = 0; j < 5; ++j) acc[i][j] = f32x4{0.f, 0.f, 0.f, 0.f};
    const int tok = tb * 64 + 16 * w + n16;
    u32x2 xi[5];
    {
        const bf16x8* xb = (const bf16x8*)(ws + OFF_O) + (size_t)(tb * 4 * 20 + 5 * w) * 64 + lane;
        const bf16x8* wb = (const bf16x8*)(ws + OFF_WOF) + (size_t)(5 * cg * 20 + 5 * w) * 64 + lane;
        bf16x8 xf[5][4], wf[5][5];
#define WO_LD(ks) do { _Pragma("unroll") for (int i = 0; i < 4; ++i) xf[ks][i] = xb[(((i + w) & 3) * 20 + (ks)) * 64]; \
        _Pragma("unroll") for (int nt = 0; nt < 5; ++nt) wf[ks][nt] = wb[(nt * 20 + (ks)) * 64]; SBAR(); } while (0)
        WO_LD(0); WO_LD(1); WO_LD(2); WO_LD(3);
#pragma unroll
        for (int ks = 0; ks < 5; ++ks) {
#pragma unroll
            for (int nt = 0; nt < 5; ++nt)
#pragma unroll
                for (int i = 0; i < 4; ++i) acc[i][nt] = MFMA16_32(wf[ks][nt], xf[ks][i], acc[i][nt]);
            SBAR();
            if (ks == 0) {
                WO_LD(4);
#pragma unroll
                for (int nt = 0; nt < 5; ++nt) {
                    const int d = 80 * cg + 16 * nt + 4 * g;
                    if (XF) xi[nt] = *(const u32x2*)(xin + ((size_t)((tok >> 4) * 20 + (d >> 5)) * 64 + ((d & 31) >> 3) * 16 + n16) * 8 + (d & 7));
                    else xi[nt] = *(const u32x2*)(xin + (size_t)tok * 640 + d);
                }
                SBAR();
            }
        }
    }
    SST(1);
#pragma unroll
    for (int j = 1; j < 4; ++j) {
        const int o = (w + j) & 3;
#pragma unroll
        for (int nt = 0; nt < 5; ++nt) *(f32x4*)(L + (((o * 3 + (j - 1)) * 5 + nt) * 64 + lane) * 16) = acc[j][nt];
    }
    RAW_BARRIER();
    SST(2);
    float xv[20]; u32x2 x1o[5];
    {
        f32x4 rr[3][5];
#pragma unroll
        for (int j = 0; j < 3; ++j)
#pragma unroll
            for (int nt = 0; nt < 5; ++nt) rr[j][nt] = *(const f32x4*)(L + (((w * 3 + j) * 5 + nt) * 64 + lane) * 16);
        SBAR();
        float s = 0.f;
#pragma unroll
        for (int nt = 0; nt < 5; ++nt) {
            const f32x4 a = (acc[0][nt] + rr[0][nt]) + (rr[1][nt] + rr[2][nt]);
            const unsigned u0 = pk2(bf_lo(xi[nt][0]) + a[0], bf_hi(xi[nt][0]) + a[1]);
            const unsigned u1 = pk2(bf_lo(xi[nt][1]) + a[2], bf_hi(xi[nt][1]) + a[3]);
            x1o[nt] = u32x2{u0, u1};
            xv[4 * nt] = bf_lo(u0); xv[4 * nt + 1] = bf_hi(u0); xv[4 * nt + 2] = bf_lo(u1); xv[4 * nt + 3] = bf_hi(u1);
            s += (xv[4 * nt] + xv[4 * nt + 1]) + (xv[4 * nt + 2] + xv[4 * nt + 3]);
        }
        const float mw = add32(add16(s)) * (1.f / 80.f);
        float s2 = 0.f;
#pragma unroll
        for (int i = 0; i < 20; ++i) { const float dd = xv[i] - mw; s2 += dd * dd; }
        s2 = add32(add16(s2));
        gu64* gp = (gu64*)(ws + OFF_XG) + (size_t)tok * 8 + cg;
        if (g == 0) __hip_atomic_store(gp, ((unsigned long long)(__float_as_uint(s2) | 0x80000000u) << 32) | __float_as_uint(mw),
                                       __ATOMIC_RELAXED, __HIP_MEMORY_SCOPE_AGENT);
    }
    SST(3);
    float mean, var;
    {
        gu64* gp = (gu64*)(ws + OFF_XG) + (size_t)tok * 8 + 2 * g;
        unsigned long long v0, v1;
        for (int spin = 0; spin < (1 << 20); ++spin) {
            v0 = __hip_atomic_load(gp, __ATOMIC_RELAXED, __HIP_MEMORY_SCOPE_AGENT);
            v1 = __hip_atomic_load(gp + 1, __ATOMIC_RELAXED, __HIP_MEMORY_SCOPE_AGENT);
            if (__all((long long)(v0 & v1) < 0)) break;
            __builtin_amdgcn_s_sleep(4);
        }
        const float m0 = __uint_as_float((unsigned)v0), m1 = __uint_as_float((unsigned)v1);
        mean = add32(add16(m0 + m1)) * 0.125f;
        const float d0 = m0 - mean, d1 = m1 - mean;
        var = add32(add16(__uint_as_float((unsigned)(v0 >> 32) & 0x7fffffffu) + __uint_as_float((unsigned)(v1 >> 32) & 0x7fffffffu) +
                          80.f * (d0 * d0 + d1 * d1))) * (1.f / 640.f);
    }
    SST(4);
    {
        float shf, r2; ln2_scalars(mean, var, shf, r2);
        char* hb = L + w * 15360;
#pragma unroll
        for (int nt = 0; nt < 5; ++nt)
            *(u32x2*)(hb + n16 * 176 + (16 * nt + 4 * g) * 2) =
                u32x2{pk2((xv[4 * nt] - shf) * r2, (xv[4 * nt + 1] - shf) * r2), pk2((xv[4 * nt + 2] - shf) * r2, (xv[4 * nt + 3] - shf) * r2)};
        asm volatile("s_waitcnt lgkmcnt(0)" ::: "memory");
        const int q4 = (lane & 15) >> 2, p4 = lane & 3;
        lds_cptr hr = (lds_cptr)(lds_ptr)L + w * 15360 + (8 * (g & 1) + q4) * 176 + p4 * 8 + (g >> 1) * 32;
        const int s0 = tb * 64 + 16 * w + 8 * (g & 1);
        u16* hf = (u16*)(ws + OFF_H) + ((size_t)(5 * cg + (g >> 1)) * 64 + (s0 >> 5)) * 512 + (((s0 & 31) >> 3) * 16 + n16) * 8;
        const bf16x8 f0 = cat8(tr_read(hr), tr_read(hr + 704));
        const bf16x8 f1 = cat8(tr_read(hr + 64), tr_read(hr + 704 + 64));
        const bf16x8 f2 = cat8(tr_read(hr + (g < 2 ? 128 : 0)), tr_read(hr + 704 + (g < 2 ? 128 : 0)));
        *(bf16x8*)(hf) = f0;
        *(bf16x8*)(hf + 2 * 64 * 512) = f1;
        if (g < 2) *(bf16x8*)(hf + 4 * 64 * 512) = f2;
        u16* x1r = (u16*)(ws + OFF_X1) + (size_t)tok * 640 + 80 * cg + 4 * g;
#pragma unroll
        for (int nt = 0; nt < 5; ++nt) *(u32x2*)(x1r + 16 * nt) = x1o[nt];
    }
    SST(5); SST_FLUSHK(ws, XF ? 7 : 2);
}

template <int MODE, int NS, int PF>
DI void gemm_body(const bf16x8* __restrict__ po, const bf16x8* __restrict__ pf, const int (&ooff)[4], int fstride, f32x4 (&acc)[4][5]) {
    bf16x8 ro[PF][4], rf[PF][5];
#pragma unroll
    for (int s = 0; s < PF; ++s) {
#pragma unroll
        for (int j = 0; j < 4; ++j) ro[s][j] = po[ooff[j] + s * 64];
#pragma unroll
        for (int i = 0; i < 5; ++i) rf[s][i] = pf[i * fstride + s * 64];
        SBAR();
    }
#pragma unroll
    for (int s = 0; s < NS; ++s) {
        const int sl = s % PF;
        SBAR();
#pragma unroll
        for (int j = 0; j < 4; ++j)
#pragma unroll
            for (int i = 0; i < 5; ++i) {
                if (MODE == 1) acc[j][i] = MFMA16_32(ro[sl][j], rf[sl][i], acc[j][i]);
                else acc[j][i] = MFMA16_32(rf[sl][i], ro[sl][j], acc[j][i]);
            }
        SBAR();
        if (s + PF < NS) {
#pragma unroll
            for (int j = 0; j < 4; ++j) ro[sl][j] = po[ooff[j] + (s + PF) * 64];
#pragma unroll
            for (int i = 0; i < 5; ++i) rf[sl][i] = pf[i * fstride + (s + PF) * 64];
        }
    }
}

template <int MODE>
__global__ __launch_bounds__(256, 1) void k_gemm(char* __restrict__ ws, const u16* __restrict__ bias, u16* __restrict__ out) {
    __shared__ __attribute__((aligned(16))) f32x4 red[4 * 3 * 5 * 64];
    const int tid = threadIdx.x, lane = tid & 63, w = __builtin_amdgcn_readfirstlane(tid >> 6);
    const int b = blockIdx.x, xcd = b & 7, slot = b >> 3;
    const int n16 = lane & 15, g = lane >> 4;
    constexpr int KS = MODE == 1 ? 64 : 54;
    int ot, dt;
    if (MODE == 1) {
        ot = 7 * (xcd >> 1) + (slot >> 2); dt = 4 * (xcd & 1) + (slot & 3);
        if (slot >= 28 || ot >= 27) return;
    } else {
        ot = 8 * (xcd >> 1) + (slot >> 2); dt = 4 * (xcd & 1) + (slot & 3);
    }
    SST_DECL; SST(0);
    u32x2 bb = {0u, 0u}, xi[5]; float bs = 0.f;
    if (MODE == 1) bb = *(const u32x2*)(bias + 64 * ot + 16 * w + 4 * g);
    else {
        const int s = 64 * ot + 16 * w + n16;
        bs = bf2f(bias[s]);
        const u16* x1 = (const u16*)(ws + OFF_X1) + (size_t)s * 640 + 80 * dt + 4 * g;
#pragma unroll
        for (int i = 0; i < 5; ++i) xi[i] = *(const u32x2*)(x1 + 16 * i);
    }
    const int k0 = MODE == 1 ? 16 * w : (w < 2 ? 14 * w : 28 + 13 * (w - 2));
    const bf16x8* po = (const bf16x8*)(ws + (MODE == 1 ? OFF_W1F : OFF_W2F)) + ((size_t)(4 * ot) * KS + k0) * 64 + lane;
    const bf16x8* pf = (const bf16x8*)(ws + (MODE == 1 ? OFF_H : OFF_U)) + ((size_t)(5 * dt) * KS + k0) * 64 + lane;
    int ooff[4];
#pragma unroll
    for (int j = 0; j < 4; ++j) ooff[j] = ((j + w) & 3) * KS * 64;
    f32x4 acc[4][5];
#pragma unroll
    for (int j = 0; j < 4; ++j)
#pragma unroll
        for (int i = 0; i < 5; ++i) acc[j][i] = f32x4{0.f, 0.f, 0.f, 0.f};
    if (MODE == 1) gemm_body<1, 16, 6>(po, pf, ooff, KS * 64, acc);
    else if (w < 2) gemm_body<2, 14, 6>(po, pf, ooff, KS * 64, acc);
    else gemm_body<2, 13, 6>(po, pf, ooff, KS * 64, acc);
    SST(1);
#pragma unroll
    for (int j = 1; j < 4; ++j) {
        const int o = (w + j) & 3;
#pragma unroll
        for (int i = 0; i < 5; ++i) red[((o * 3 + (j - 1)) * 5 + i) * 64 + lane] = acc[j][i];
    }
    RAW_BARRIER();
#pragma unroll
    for (int j = 0; j < 3; ++j)
#pragma unroll
        for (int i = 0; i < 5; ++i) acc[1 + j][i] = red[((w * 3 + j) * 5 + i) * 64 + lane];
    SBAR();
#pragma unroll
    for (int i = 0; i < 5; ++i) acc[0][i] = (acc[0][i] + acc[1][i]) + (acc[2][i] + acc[3][i]);
    SST(2);
    if (MODE == 1) {
        const int f = 64 * ot + 16 * w + 4 * g;
        const float bv[4] = {bf_lo(bb[0]), bf_hi(bb[0]), bf_lo(bb[1]), bf_hi(bb[1])};
        u16* uf = (u16*)(ws + OFF_U);
#pragma unroll
        for (int i = 0; i < 5; ++i) {
            float r[4];
#pragma unroll
            for (int e = 0; e < 4; ++e) {
                const float u = acc[0][i][e] + bv[e];
                const float z = u * (1.f + 0.044715f * u * u) * (-2.f * 0.7978845608028654f * 1.4426950408889634f);
                r[e] = u * __builtin_amdgcn_rcpf(1.f + EX2(z));
            }
            *(u32x2*)(uf + ((size_t)((5 * dt + i) * 54 + (f >> 5)) * 64 + ((f & 31) >> 3) * 16 + n16) * 8 + (f & 7)) =
                u32x2{pk2(r[0], r[1]), pk2(r[2], r[3])};
        }
    } else {
        const int s = 64 * ot + 16 * w + n16;
        float xv[20]; float sum = 0.f;
#pragma unroll
        for (int i = 0; i < 5; ++i) {
            const int d = 80 * dt + 16 * i + 4 * g;
            u16* o = MODE == 3 ? out + (size_t)s * 640 + d
                               : out + ((size_t)((4 * ot + w) * 20 + (d >> 5)) * 64 + ((d & 31) >> 3) * 16 + n16) * 8 + (d & 7);
            const unsigned u0 = pk2(bf_lo(xi[i][0]) + acc[0][i][0] + bs, bf_hi(xi[i][0]) + acc[0][i][1] + bs);
            const unsigned u1 = pk2(bf_lo(xi[i][1]) + acc[0][i][2] + bs, bf_hi(xi[i][1]) + acc[0][i][3] + bs);
            *(u32x2*)o = u32x2{u0, u1};
            xv[4 * i] = bf_lo(u0); xv[4 * i + 1] = bf_hi(u0); xv[4 * i + 2] = bf_lo(u1); xv[4 * i + 3] = bf_hi(u1);
            sum += (xv[4 * i] + xv[4 * i + 1]) + (xv[4 * i + 2] + xv[4 * i + 3]);
        }
        if (MODE == 2) {
            const float mw = add32(add16(sum)) * (1.f / 80.f);
            float s2 = 0.f;
#pragma unroll
            for (int i = 0; i < 20; ++i) { const float dd = xv[i] - mw; s2 += dd * dd; }
            s2 = add32(add16(s2));
            if (g == 0) *(f32x2*)(ws + OFF_XS + ((size_t)s * 8 + dt) * 8) = f32x2{mw, s2};
        }
    }
    SST(3); SST_FLUSHK(ws, MODE == 1 ? (out ? 8 : 3) : (MODE == 2 ? 4 : 9));
}

typedef __attribute__((address_space(1))) unsigned gu32;
constexpr int AUXL = 16;
#define LD_SC1(off) __builtin_bit_cast(bf16x8, __builtin_amdgcn_raw_buffer_load_b128(rs, (int)(off), 0, 16))
DI void edge_publish(gu32* cnt) {
    RAW_BARRIER();
    if (threadIdx.x == 0) __hip_atomic_fetch_add(cnt, 1u, __ATOMIC_RELAXED, __HIP_MEMORY_SCOPE_AGENT);
}
DI void edge_wait(gu32* cnt, unsigned want) {
    if (threadIdx.x == 0) {
        for (int spin = 0; spin < (1 << 20); ++spin) {
            if (__hip_atomic_load(cnt, __ATOMIC_RELAXED, __HIP_MEMORY_SCOPE_AGENT) >= want) break;
            __builtin_amdgcn_s_sleep(1);
        }
        __builtin_amdgcn_fence(__ATOMIC_ACQUIRE, "wavefront");
    }
    RAW_BARRIER();
}
#define W1_A 6
#define W2_FIRST 7
constexpr int MLP_LDS = 73728;
template <bool R1>
__global__ __launch_bounds__(256, 1) void k_mlp(char* __restrict__ ws, const u16* __restrict__ xin, const u16* __restrict__ b1,
                                                const u16* __restrict__ b2, u16* __restrict__ out) {
    extern __shared__ __attribute__((aligned(16))) char lds_raw[];
    char* L = lds_raw;
    const int tid = threadIdx.x, lane = tid & 63, w = __builtin_amdgcn_readfirstlane(tid >> 6);
    const int b = blockIdx.x, slot = b >> 3, tb = ((b & 7) >> 1) * 8 + (slot >> 2), cg = (b & 1) * 4 + (slot & 3);
    const int ot1 = ((b & 7) >> 1) * 7 + (slot >> 2);
    const bool act1 = slot < 28 && ot1 < 27;
    const int n16 = lane & 15, g = lane >> 4;
    const __amdgpu_buffer_rsrc_t rs = __builtin_amdgcn_make_buffer_rsrc((void*)ws, 0, (int)WS_NEED, 0x00020000);
    gu32* cnt = (gu32*)(ws + OFF_CNT);
    SST_DECL; SST(0);
    f32x4 acc[4][5];
#define ACC_ZERO() do { _Pragma("unroll") for (int i_ = 0; i_ < 4; ++i_) _Pragma("unroll") for (int j_ = 0; j_ < 5; ++j_) acc[i_][j_] = f32x4{0.f, 0.f, 0.f, 0.f}; } while (0)
#define ACC_REDUCE() do { \
    _Pragma("unroll") for (int j_ = 1; j_ < 4; ++j_) { const int o_ = (w + j_) & 3; \
        _Pragma("unroll") for (int i_ = 0; i_ < 5; ++i_) *(f32x4*)(L + (((o_ * 3 + (j_ - 1)) * 5 + i_) * 64 + lane) * 16) = acc[j_][i_]; } \
    RAW_BARRIER(); \
    _Pragma("unroll") for (int j_ = 0; j_ < 3; ++j_) _Pragma("unroll") for (int i_ = 0; i_ < 5; ++i_) \
        acc[1 + j_][i_] = *(const f32x4*)(L + (((w * 3 + j_) * 5 + i_) * 64 + lane) * 16); \
    SBAR(); \
    _Pragma("unroll") for (int i_ = 0; i_ < 5; ++i_) acc[0][i_] = (acc[0][i_] + acc[1][i_]) + (acc[2][i_] + acc[3][i_]); } while (0)
    const int tok = tb * 64 + 16 * w + n16;
    bf16x8 wr[16][4];
    int ro1[4], ro2[4];
#pragma unroll
    for (int j = 0; j < 4; ++j) { ro1[j] = ((j + w) & 3) * 64 * 64; ro2[j] = ((j + w) & 3) * 54 * 64; }
    const bf16x8* pw1 = (const bf16x8*)(ws + OFF_W1F) + ((size_t)(4 * (act1 ? ot1 : 0)) * 64 + 16 * w) * 64 + lane;
    const int k2 = w < 2 ? 14 * w : 28 + 13 * (w - 2);
    const bf16x8* pw2 = (const bf16x8*)(ws + OFF_W2F) + ((size_t)(4 * tb) * 54 + k2) * 64 + lane;
#define W1_LD(lo, hi) do { _Pragma("unroll") for (int s_ = (lo); s_ < (hi); ++s_) _Pragma("unroll") for (int j_ = 0; j_ < 4; ++j_) wr[s_][j_] = pw1[ro1[j_] + s_ * 64]; SBAR(); } while (0)
#define W2_LD(lo, hi) do { _Pragma("unroll") for (int s_ = (lo); s_ < (hi); ++s_) _Pragma("unroll") for (int j_ = 0; j_ < 4; ++j_) wr[s_][j_] = pw2[ro2[j_] + s_ * 64]; SBAR(); } while (0)

    u32x2 x1o[5], bb; float bs;
    {
        ACC_ZERO();
        u32x2 xi[5];
        const bf16x8* xb = (const bf16x8*)(ws + OFF_O) + (size_t)(tb * 4 * 20 + 5 * w) * 64 + lane;
        const bf16x8* wb = (const bf16x8*)(ws + OFF_WOF) + (size_t)(5 * cg * 20 + 5 * w) * 64 + lane;
        bf16x8 xf[5][4], wf[5][5];
        WO_LD(0); WO_LD(1); WO_LD(2); WO_LD(3);
#pragma unroll
        for (int ks = 0; ks < 5; ++ks) {
#pragma unroll
            for (int nt = 0; nt < 5; ++nt)
#pragma unroll
                for (int i = 0; i < 4; ++i) acc[i][nt] = MFMA16_32(wf[ks][nt], xf[ks][i], acc[i][nt]);
            SBAR();
            if (ks == 0) {
                WO_LD(4);
#pragma unroll
                for (int nt = 0; nt < 5; ++nt) {
                    const int d = 80 * cg + 16 * nt + 4 * g;
                    if (!R1) xi[nt] = *(const u32x2*)(xin + ((size_t)((tok >> 4) * 20 + (d >> 5)) * 64 + ((d & 31) >> 3) * 16 + n16) * 8 + (d & 7));
                    else xi[nt] = *(const u32x2*)(xin + (size_t)tok * 640 + d);
                }
                bb = *(const u32x2*)(b1 + 64 * (act1 ? ot1 : 0) + 16 * w + 4 * g);
                bs = bf2f(b2[tok]);
                SBAR();
            }
            if (ks == 1) W1_LD(0, 2);
            if (ks == 2) W1_LD(2, 4);
            if (ks == 3) W1_LD(4, 6);
            if (ks == 4) W1_LD(6, W1_A);
        }
        SST(1);
        ACC_REDUCE();
        float xv[20], s = 0.f;
#pragma unroll
        for (int nt = 0; nt < 5; ++nt) {
            const unsigned u0 = pk2(bf_lo(xi[nt][0]) + acc[0][nt][0], bf_hi(xi[nt][0]) + acc[0][nt][1]);
            const unsigned u1 = pk2(bf_lo(xi[nt][1]) + acc[0][nt][2], bf_hi(xi[nt][1]) + acc[0][nt][3]);
            x1o[nt] = u32x2{u0, u1};
            xv[4 * nt] = bf_lo(u0); xv[4 * nt + 1] = bf_hi(u0); xv[4 * nt + 2] = bf_lo(u1); xv[4 * nt + 3] = bf_hi(u1);
            s += (xv[4 * nt] + xv[4 * nt + 1]) + (xv[4 * nt + 2] + xv[4 * nt + 3]);
        }
        const float mw = add32(add16(s)) * (1.f / 80.f);
        float s2 = 0.f;
#pragma unroll
        for (int i = 0; i < 20; ++i) { const float dd = xv[i] - mw; s2 += dd * dd; }
        s2 = add32(add16(s2));
        gu64* gp = (gu64*)(ws + OFF_XG) + (size_t)tok * 8;
        if (g == 0) __hip_atomic_store(gp + cg, ((unsigned long long)(__float_as_uint(s2) | 0x80000000u) << 32) | __float_as_uint(mw),
                                       __ATOMIC_RELAXED, __HIP_MEMORY_SCOPE_AGENT);
        SBAR();
        SST(2);
        unsigned long long v0, v1;
        for (int spin = 0; spin < (1 << 20); ++spin) {
            v0 = __hip_atomic_load(gp + 2 * g, __ATOMIC_RELAXED, __HIP_MEMORY_SCOPE_AGENT);
            v1 = __hip_atomic_load(gp + 2 * g + 1, __ATOMIC_RELAXED, __HIP_MEMORY_SCOPE_AGENT);
            if (__all((long long)(v0 & v1) < 0)) break;
            __builtin_amdgcn_s_sleep(4);
        }
        SST(3);
        const float m0 = __uint_as_float((unsigned)v0), m1 = __uint_as_float((unsigned)v1);
        const float mean = add32(add16(m0 + m1)) * 0.125f;
        const float d0 = m0 - mean, d1 = m1 - mean;
        const float var = add32(add16(__uint_as_float((unsigned)(v0 >> 32) & 0x7fffffffu) + __uint_as_float((unsigned)(v1 >> 32) & 0x7fffffffu) +
                                      80.f * (d0 * d0 + d1 * d1))) * (1.f / 640.f);
        float shf, r2; ln2_scalars(mean, var, shf, r2);
        char* hb = L + w * 15360;
#pragma unroll
        for (int nt = 0; nt < 5; ++nt)
            *(u32x2*)(hb + n16 * 176 + (16 * nt + 4 * g) * 2) =
                u32x2{pk2((xv[4 * nt] - shf) * r2, (xv[4 * nt + 1] - shf) * r2), pk2((xv[4 * nt + 2] - shf) * r2, (xv[4 * nt + 3] - shf) * r2)};
        asm volatile("s_waitcnt lgkmcnt(0)" ::: "memory");
        const int q4 = (lane & 15) >> 2, p4 = lane & 3;
        lds_cptr hr = (lds_cptr)(lds_ptr)L + w * 15360 + (8 * (g & 1) + q4) * 176 + p4 * 8 + (g >> 1) * 32;
        const int s0 = tb * 64 + 16 * w + 8 * (g & 1);
        const int hoff = (int)OFF_H + ((5 * cg + (g >> 1)) * 64 + (s0 >> 5)) * 1024 + (((s0 & 31) >> 3) * 16 + n16) * 16;
        const bf16x8 f0 = cat8(tr_read(hr), tr_read(hr + 704));
        const bf16x8 f1 = cat8(tr_read(hr + 64), tr_read(hr + 704 + 64));
        const bf16x8 f2 = cat8(tr_read(hr + (g < 2 ? 128 : 0)), tr_read(hr + 704 + (g < 2 ? 128 : 0)));
        __builtin_amdgcn_raw_buffer_store_b128(__builtin_bit_cast(u32x4, f0), rs, hoff, 0, 16);
        __builtin_amdgcn_raw_buffer_store_b128(__builtin_bit_cast(u32x4, f1), rs, hoff + 2 * 65536, 0, 16);
        if (g < 2) __builtin_amdgcn_raw_buffer_store_b128(__builtin_bit_cast(u32x4, f2), rs, hoff + 4 * 65536, 0, 16);
        SBAR();
        W1_LD(W1_A, W1_A + 4);
        VMCNT(16);
        SST(4);
        edge_publish(cnt + cg * 64);
    }
    if (act1) {
        W1_LD(W1_A + 4, 16);
        edge_wait(cnt + cg * 64, 32);
        SST(5);
        ACC_ZERO();
        const int ho = (int)OFF_H + ((5 * cg) * 64 + 16 * w) * 1024 + lane * 16;
        constexpr int PF = 6;
        bf16x8 rf[PF][5];
#pragma unroll
        for (int s = 0; s < PF; ++s) {
#pragma unroll
            for (int i = 0; i < 5; ++i) rf[s][i] = LD_SC1(ho + i * 65536 + s * 1024);
            SBAR();
        }
#pragma unroll
        for (int s = 0; s < 16; ++s) {
#pragma unroll
            for (int j = 0; j < 4; ++j)
#pragma unroll
                for (int i = 0; i < 5; ++i) acc[j][i] = MFMA16_32(wr[s][j], rf[s % PF][i], acc[j][i]);
            SBAR();
            if (s + PF < 16) {
#pragma unroll
                for (int i = 0; i < 5; ++i) rf[s % PF][i] = LD_SC1(ho + i * 65536 + (s + PF) * 1024);
                SBAR();
            }
            if (s >= W2_FIRST && s - W2_FIRST < 9) W2_LD(s - W2_FIRST, s - W2_FIRST + 1);
        }
        SST(6);
        ACC_REDUCE();
        const int f = 64 * ot1 + 16 * w + 4 * g;
        const float bv[4] = {bf_lo(bb[0]), bf_hi(bb[0]), bf_lo(bb[1]), bf_hi(bb[1])};
        const int uo = (int)OFF_U + ((5 * cg) * 54 + (f >> 5)) * 1024 + (((f & 31) >> 3) * 16 + n16) * 16 + (f & 7) * 2;
#pragma unroll
        for (int i = 0; i < 5; ++i) {
            float r[4];
#pragma unroll
            for (int e = 0; e < 4; ++e) {
                const float u = acc[0][i][e] + bv[e];
                const float z = u * (1.f + 0.044715f * u * u) * (-2.f * 0.7978845608028654f * 1.4426950408889634f);
                r[e] = u * __builtin_amdgcn_rcpf(1.f + EX2(z));
            }
            __builtin_amdgcn_raw_buffer_store_b64(u32x2{pk2(r[0], r[1]), pk2(r[2], r[3])}, rs, uo + i * 54 * 1024, 0, 16);
        }
        SBAR();
        W2_LD(9, 13);
        VMCNT(16);
        SST(7);
        edge_publish(cnt + (8 + cg) * 64);
    } else {
        W2_LD(0, 13);
    }
    {
        if (w < 2) W2_LD(13, 14);
        edge_wait(cnt + (8 + cg) * 64, 27);
        SST(8);
        ACC_ZERO();
        const int uo = (int)OFF_U + ((5 * cg) * 54 + k2) * 1024 + lane * 16;
        constexpr int PF = 6;
        bf16x8 rf[PF][5];
#pragma unroll
        for (int s = 0; s < PF; ++s) {
#pragma unroll
            for (int i = 0; i < 5; ++i) rf[s][i] = LD_SC1(uo + i * 54 * 1024 + s * 1024);
            SBAR();
        }
#pragma unroll
        for (int s = 0; s < 14; ++s) {
            if (s < 13 || w < 2) {
#pragma unroll
                for (int j = 0; j < 4; ++j)
#pragma unroll
                    for (int i = 0; i < 5; ++i) acc[j][i] = MFMA16_32(rf[s % PF][i], wr[s][j], acc[j][i]);
            }
            SBAR();
            if (s + PF < 13 || (s + PF == 13 && w < 2)) {
#pragma unroll
                for (int i = 0; i < 5; ++i) rf[s % PF][i] = LD_SC1(uo + i * 54 * 1024 + (s + PF) * 1024);
                SBAR();
            }
        }
        SST(9);
        ACC_REDUCE();
        float xv[20], sum = 0.f;
#pragma unroll
        for (int i = 0; i < 5; ++i) {
            const int d = 80 * cg + 16 * i + 4 * g;
            const unsigned u0 = pk2(bf_lo(x1o[i][0]) + acc[0][i][0] + bs, bf_hi(x1o[i][0]) + acc[0][i][1] + bs);
            const unsigned u1 = pk2(bf_lo(x1o[i][1]) + acc[0][i][2] + bs, bf_hi(x1o[i][1]) + acc[0][i][3] + bs);
            if (R1) __builtin_amdgcn_raw_buffer_store_b64(u32x2{u0, u1}, rs,
                        (int)OFF_X2 + ((4 * tb + w) * 20 + (d >> 5)) * 1024 + (((d & 31) >> 3) * 16 + n16) * 16 + (d & 7) * 2, 0, 16);
            else *(u32x2*)(out + (size_t)tok * 640 + d) = u32x2{u0, u1};
            xv[4 * i] = bf_lo(u0); xv[4 * i + 1] = bf_hi(u0); xv[4 * i + 2] = bf_lo(u1); xv[4 * i + 3] = bf_hi(u1);
            sum += (xv[4 * i] + xv[4 * i + 1]) + (xv[4 * i + 2] + xv[4 * i + 3]);
        }
        if (R1) {
            const float mw = add32(add16(sum)) * (1.f / 80.f);
            float s2 = 0.f;
#pragma unroll
            for (int i = 0; i < 20; ++i) { const float dd = xv[i] - mw; s2 += dd * dd; }
            s2 = add32(add16(s2));
            if (g == 0) __builtin_amdgcn_raw_buffer_store_b64(u32x2{__float_as_uint(mw), __float_as_uint(s2)}, rs, (int)OFF_XS + ((tb * 8 + cg) * 64 + 16 * w + n16) * 8, 0, 16);
        }
    }
    if (R1) {
        const int mt0 = cg >> 1, isv = cg & 1;
        const bf16x8* wb = (const bf16x8*)(ws + OFF_WQF) + (size_t)(5 * cg * 20 + 5 * w) * 64 + lane;
        const bf16x8* kb = (const bf16x8*)(ws + OFF_WQF) + (size_t)((40 + 2 * isv) * 20 + 5 * w) * 64 + lane;
        bf16x8 wf[5][5], wkf[5][2], xf[5][4], xk[5];
        SBAR();
#pragma unroll
        for (int i = 0; i < 16; ++i) wf[i / 5][i % 5] = wb[((i % 5) * 20 + i / 5) * 64];
        SBAR();
        VMCNT(16);
        SST(10);
        edge_publish(cnt + (16 + tb) * 64);
#pragma unroll
        for (int i = 16; i < 25; ++i) wf[i / 5][i % 5] = wb[((i % 5) * 20 + i / 5) * 64];
#pragma unroll
        for (int ks = 0; ks < 5; ++ks) { wkf[ks][0] = kb[ks * 64]; wkf[ks][1] = kb[(20 + ks) * 64]; }
        f32x4 csq[5], cs0, cs1, stq[4], stk[4], acck[2];
        {
            const float* csp = (const float*)(ws + OFF_CS);
#pragma unroll
            for (int nt = 0; nt < 5; ++nt) csq[nt] = *(const f32x4*)(csp + 80 * cg + 16 * nt + 4 * g);
            cs0 = *(const f32x4*)(csp + 640 + 32 * isv + 4 * g); cs1 = *(const f32x4*)(csp + 640 + 32 * isv + 16);
        }
        SBAR();
        edge_wait(cnt + (16 + tb) * 64, 8);
        SST(11);
        const int xo = (int)OFF_X2 + (tb * 4 * 20 + 5 * w) * 1024 + lane * 16;
#pragma unroll
        for (int ks = 0; ks < 5; ++ks) {
#pragma unroll
            for (int i = 0; i < 4; ++i) xf[ks][i] = LD_SC1(xo + (((i + w) & 3) * 20 + ks) * 1024);
            xk[ks] = LD_SC1(xo + (mt0 * 20 + ks) * 1024);
            SBAR();
        }
#pragma unroll
        for (int i = 0; i < 4; ++i) {
            const u32x2 a0 = __builtin_amdgcn_raw_buffer_load_b64(rs, (int)OFF_XS + ((tb * 8 + 2 * i) * 64 + 16 * w + n16) * 8, 0, AUXL);
            const u32x2 a1 = __builtin_amdgcn_raw_buffer_load_b64(rs, (int)OFF_XS + ((tb * 8 + 2 * i + 1) * 64 + 16 * w + n16) * 8, 0, AUXL);
            const u32x2 c0 = __builtin_amdgcn_raw_buffer_load_b64(rs, (int)OFF_XS + ((tb * 8 + 2 * i) * 64 + 16 * mt0 + n16) * 8, 0, AUXL);
            const u32x2 c1 = __builtin_amdgcn_raw_buffer_load_b64(rs, (int)OFF_XS + ((tb * 8 + 2 * i + 1) * 64 + 16 * mt0 + n16) * 8, 0, AUXL);
            stq[i] = __builtin_bit_cast(f32x4, u32x4{a0[0], a0[1], a1[0], a1[1]});
            stk[i] = __builtin_bit_cast(f32x4, u32x4{c0[0], c0[1], c1[0], c1[1]});
        }
        SBAR();
        ACC_ZERO();
        acck[0] = f32x4{0.f, 0.f, 0.f, 0.f}; acck[1] = acck[0];
#pragma unroll
        for (int ks = 0; ks < 5; ++ks) {
#pragma unroll
            for (int nt = 0; nt < 5; ++nt)
#pragma unroll
                for (int i = 0; i < 4; ++i) acc[i][nt] = MFMA16_32(wf[ks][nt], xf[ks][i], acc[i][nt]);
            acck[0] = MFMA16_32(wkf[ks][0], xk[ks], acck[0]);
            acck[1] = MFMA16_32(wkf[ks][1], xk[ks], acck[1]);
            SBAR();
        }
        SST(12);
        const float z4[4] = {0.f, 0.f, 0.f, 0.f}, z5[5] = {0.f, 0.f, 0.f, 0.f, 0.f}, z2[2] = {0.f, 0.f};
        qkv_finish<true>(ws, L, tb, cg, w, lane, acc, acck, csq, cs0, cs1, stq, stk, z4, z4, z5, z2);
    }
    SST_FLUSHK(ws, R1 ? 2 : 7);
}

template <int N> __global__ void k_end() {}

extern "C" void kernel_launch(void* const* d_in, const int* in_sizes, int n_in, void* d_out, int out_size, void* d_ws,
                              size_t ws_size, hipStream_t stream) {
    const u16* x = (const u16*)d_in[0];
    const u16* wq = (const u16*)d_in[1];
    const u16* wk = (const u16*)d_in[2];
    const u16* wv = (const u16*)d_in[3];
    const u16* wo = (const u16*)d_in[4];
    const u16* w1 = (const u16*)d_in[6];
    const u16* b1 = (const u16*)d_in[7];
    const u16* w2 = (const u16*)d_in[8];
    const u16* b2 = (const u16*)d_in[9];
    char* ws = (char*)d_ws;
    u16* x2 = (u16*)(ws + OFF_X2);
    (void)hipFuncSetAttribute((const void*)k_qkv<false>, hipFuncAttributeMaxDynamicSharedMemorySize, QKV_LDS);
    (void)hipFuncSetAttribute((const void*)k_attn<true>, hipFuncAttributeMaxDynamicSharedMemorySize, ATT_LDS);
    (void)hipFuncSetAttribute((const void*)k_attn<false>, hipFuncAttributeMaxDynamicSharedMemorySize, ATT_LDS);
    (void)hipFuncSetAttribute((const void*)k_mlp<true>, hipFuncAttributeMaxDynamicSharedMemorySize, MLP_LDS);
    (void)hipFuncSetAttribute((const void*)k_mlp<false>, hipFuncAttributeMaxDynamicSharedMemorySize, MLP_LDS);
    (void)hipFuncSetAttribute((const void*)k_end<0>, hipFuncAttributeMaxDynamicSharedMemorySize, 0);
    k_qkv<false><<<256, 256, QKV_LDS, stream>>>(x, wq, wk, wv, wo, ws, w1, w2);
    k_attn<true><<<256, 512, ATT_LDS, stream>>>(ws, w1, w2, wo, wq);
    k_mlp<true><<<256, 256, MLP_LDS, stream>>>(ws, x, b1, b2, nullptr);
    k_attn<false><<<256, 512, ATT_LDS, stream>>>(ws, w1, w2, wo, wq);
    k_mlp<false><<<256, 256, MLP_LDS, stream>>>(ws, x2, b1, b2, (u16*)d_out);
}
```

```cpp
#define PJA 0
#define PJB 6
#define PJC 5
#include <hip/hip_runtime.h>
#include <hip/hip_bf16.h>
#include <stdint.h>

typedef unsigned short u16;
typedef short bf16x8 __attribute__((ext_vector_type(8)));
typedef short bf16x4 __attribute__((ext_vector_type(4)));
typedef float f32x16 __attribute__((ext_vector_type(16)));
typedef float f32x4 __attribute__((ext_vector_type(4)));
typedef float f32x2 __attribute__((ext_vector_type(2)));
typedef unsigned u32x4 __attribute__((ext_vector_type(4)));
typedef unsigned u32x2 __attribute__((ext_vector_type(2)));
typedef __bf16 bf2_t __attribute__((ext_vector_type(2)));
typedef __attribute__((address_space(3))) char* lds_ptr;
typedef __attribute__((address_space(3))) const char* lds_cptr;
typedef __attribute__((address_space(1))) unsigned long long gu64;

#define DI __device__ __forceinline__
#define SBAR() __builtin_amdgcn_sched_barrier(0)
#define PIN(x) asm volatile("" : "+v"(x))
#define MFMA32_16(a, b, c) __builtin_amdgcn_mfma_f32_32x32x16_bf16(a, b, c, 0, 0, 0)
#define MFMA32_8(a, b, c) __builtin_amdgcn_mfma_f32_32x32x8bf16_1k(a, b, c, 0, 0, 0)
#define MFMA16_32(a, b, c) __builtin_amdgcn_mfma_f32_16x16x32_bf16(a, b, c, 0, 0, 0)
#define EX2(x) __builtin_amdgcn_exp2f(x)
#define RAW_BARRIER() asm volatile("s_waitcnt lgkmcnt(0)\n\ts_barrier" ::: "memory")
#define VMCNT(n) asm volatile("s_waitcnt vmcnt(%0)" :: "i"(n) : "memory")

#define SST_DECL do {} while (0)
#define SST(i) do {} while (0)
#define SST_FLUSHK(ws, kid) do {} while (0)
#define PREP_STORE(p, v) asm volatile("global_store_dwordx4 %0, %1, off sc1\n\ts_nop 1" :: "v"(p), "v"(v) : "memory")
#define ST8(p, v) (*(u32x2*)(p) = (v))
#define KADDR(dst, sym) asm volatile("s_getpc_b64 s[40:41]\n\ts_add_u32 s40, s40, " sym "@rel32@lo+4\n\ts_addc_u32 s41, s41, " sym "@rel32@hi+12\n\ts_mov_b64 %0, s[40:41]" : "=s"(dst) :: "s40", "s41", "scc")
constexpr int S_ = 2048, D_ = 640, F_ = 1728;
constexpr float LN_EPS = 1e-5f;
constexpr float QSCALE = 0.22360679774997896f * 1.4426950408889634f;
constexpr float SAFE_BOUND = 60.0f;

DI unsigned pk2(float a, float b) { f32x2 v = {a, b}; bf2_t r = __builtin_convertvector(v, bf2_t); return __builtin_bit_cast(unsigned, r); }
DI float bf_lo(unsigned u) { return __uint_as_float(u << 16); }
DI float bf_hi(unsigned u) { return __uint_as_float(u & 0xffff0000u); }
DI float bf2f(u16 v) { return __uint_as_float((unsigned)v << 16); }
DI float dot2(unsigned a, unsigned b, float c) { return __builtin_amdgcn_fdot2_f32_bf16(__builtin_bit_cast(bf2_t, a), __builtin_bit_cast(bf2_t, b), c, false); }
DI bf16x4 tr_read(lds_cptr p) { return __builtin_amdgcn_ds_read_tr16_b64_v4i16((__attribute__((address_space(3))) bf16x4*)p); }
DI bf16x8 cat8(bf16x4 a, bf16x4 b) { return __builtin_shufflevector(a, b, 0, 1, 2, 3, 4, 5, 6, 7); }
DI void glds16(const void* g, lds_ptr l) {
    unsigned keep;
    asm volatile("s_mov_b32 %0, m0\n\ts_mov_b32 m0, %2\n\ts_nop 0\n\tglobal_load_lds_dwordx4 %1, off\n\ts_mov_b32 m0, %0"
                 : "=&s"(keep) : "v"(g), "s"((unsigned)(uintptr_t)l) : "memory");
}
DI float xor32(float v) { return __shfl_xor(v, 32); }
DI float add32(float v) {
    auto r = __builtin_amdgcn_permlane32_swap(__float_as_uint(v), __float_as_uint(v), false, false);
    return __uint_as_float(r[0]) + __uint_as_float(r[1]);
}
DI float add16(float v) {
    auto r = __builtin_amdgcn_permlane16_swap(__float_as_uint(v), __float_as_uint(v), false, false);
    return __uint_as_float(r[0]) + __uint_as_float(r[1]);
}
DI float max16x(float v) { auto r = __builtin_amdgcn_permlane16_swap(__float_as_uint(v), __float_as_uint(v), false, false); return fmaxf(__uint_as_float(r[0]), __uint_as_float(r[1])); }
DI float max32x(float v) { auto r = __builtin_amdgcn_permlane32_swap(__float_as_uint(v), __float_as_uint(v), false, false); return fmaxf(__uint_as_float(r[0]), __uint_as_float(r[1])); }
template <int CTRL> DI float dppf(float v) { return __int_as_float(__builtin_amdgcn_update_dpp(0, __float_as_int(v), CTRL, 0xf, 0xf, false)); }
DI float rowmax16(float v) {
    v = fmaxf(v, dppf<0x128>(v)); v = fmaxf(v, dppf<0x124>(v)); v = fmaxf(v, dppf<0x122>(v)); v = fmaxf(v, dppf<0x121>(v)); return v;
}
DI float wavemax(float v) { return max32x(max16x(rowmax16(v))); }
DI u32x4 gather8(const u16* s, int ld) {
    u32x4 r;
#pragma unroll
    for (int e = 0; e < 4; ++e) r[e] = (unsigned)s[(2 * e) * ld] | ((unsigned)s[(2 * e + 1) * ld] << 16);
    return r;
}

constexpr size_t OFF_Q = 0;
constexpr size_t OFF_K1 = OFF_Q + (size_t)2048 * 32 * 48;
constexpr size_t OFF_K2 = OFF_K1 + 2048 * 32;
constexpr size_t OFF_V = OFF_K2 + 2048 * 8;
constexpr size_t OFF_KN = OFF_V + 128 * 640;
constexpr size_t OFF_QN = OFF_KN + 512;
constexpr size_t OFF_CS = OFF_QN + 4096;
constexpr size_t OFF_CONST = OFF_CS + 4096;
constexpr size_t OFF_CNT = OFF_CONST + 256;
constexpr size_t OFF_XG = OFF_CNT + 48 * 256;
constexpr size_t OFF_XS = OFF_XG + 262144;
constexpr size_t OFF_X1 = OFF_XS + 131072;
constexpr size_t OFF_H = OFF_X1 + (size_t)S_ * D_ * 2;
constexpr size_t OFF_U = OFF_H + (size_t)S_ * D_ * 2;
constexpr size_t OFF_X2 = OFF_U + (size_t)D_ * F_ * 2;
constexpr size_t OFF_O = OFF_X2 + (size_t)S_ * D_ * 2;
constexpr size_t OFF_W1F = OFF_O + (size_t)S_ * D_ * 2;
constexpr size_t OFF_W2F = OFF_W1F + (size_t)S_ * F_ * 2;
constexpr size_t OFF_WQF = OFF_W2F + (size_t)S_ * F_ * 2;
constexpr size_t OFF_WOF = OFF_WQF + (size_t)44 * 20 * 1024;
constexpr size_t WS_NEED = OFF_WOF + (size_t)800 * 1024;
#define TLB_STRIDE 32768
constexpr int NPREP = 1728 + 1728 + 200 + 200;

DI float rbf(float v) { return bf_lo(pk2(v, 0.f)); }
DI float ln_sd(float var) { return rbf(__builtin_amdgcn_sqrtf(rbf(rbf(var) + LN_EPS))); }
DI void ln2_scalars(float mu, float var, float& shift, float& scale) {
    const float m1 = rbf(mu), sd1 = ln_sd(var), i1 = 1.0f / sd1;
    const float m2 = rbf((mu - m1) * i1), sd2 = ln_sd(var * i1 * i1);
    shift = m1 + m2 * sd1; scale = i1 / sd2;
}
DI void chan8(const f32x4 (&st)[4], float& mean, float& var) {
    float m = 0.f, m2 = 0.f;
#pragma unroll
    for (int i = 0; i < 4; ++i) { m += st[i][0] + st[i][2]; m2 += st[i][1] + st[i][3]; }
    m *= 0.125f;
#pragma unroll
    for (int i = 0; i < 4; ++i) { const float d0 = st[i][0] - m, d1 = st[i][2] - m; m2 += 80.f * (d0 * d0 + d1 * d1); }
    mean = m; var = m2 * (1.f / 640.f);
}

template <bool FAST>
DI void qkv_finish(char* __restrict__ ws, char* L, int tb, int cg, int w, int lane, f32x4 (&acc)[4][5], f32x4 (&acck)[2],
                   const f32x4 (&csq)[5], const f32x4& cs0, const f32x4& cs1, const f32x4 (&stq)[4], const f32x4 (&stk)[4],
                   const float (&sm)[4], const float (&sq)[4], const float (&cs)[5], const float (&csk)[2]) {
    const int n16 = lane & 15, g = lane >> 4, mt0 = cg >> 1, isv = cg & 1;
    constexpr int R_OFF = 0, PK_OFF = 61440, ST_OFF = 69632, CS_OFF = 71680;
#pragma unroll
    for (int j = 1; j < 4; ++j) {
        const int o = (w + j) & 3;
#pragma unroll
        for (int nt = 0; nt < 5; ++nt) *(f32x4*)(L + R_OFF + (((o * 3 + (j - 1)) * 5 + nt) * 64 + lane) * 16) = acc[j][nt];
    }
    *(f32x4*)(L + PK_OFF + ((w * 2 + 0) * 64 + lane) * 16) = acck[0];
    *(f32x4*)(L + PK_OFF + ((w * 2 + 1) * 64 + lane) * 16) = acck[1];
    if (!FAST && g == 0) {
#pragma unroll
        for (int i = 0; i < 4; ++i) *(f32x2*)(L + ST_OFF + (w * 64 + ((i + w) & 3) * 16 + n16) * 8) = f32x2{sm[i], sq[i]};
#pragma unroll
        for (int nt = 0; nt < 5; ++nt) *(float*)(L + CS_OFF + w * 448 + (nt * 16 + n16) * 4) = cs[nt];
#pragma unroll
        for (int nt = 0; nt < 2; ++nt) *(float*)(L + CS_OFF + w * 448 + (80 + nt * 16 + n16) * 4) = csk[nt];
    }
    char* qrow = ws + OFF_Q + (size_t)(tb * 8 + cg) * 12288 + (16 * w + n16) * 48;
    int qoff[5];
#pragma unroll
    for (int nt = 0; nt < 5; ++nt) { const int G = 4 * nt + g, hl = G / 5; qoff[nt] = hl * 3072 + (G - 5 * hl) * 8; PIN(qoff[nt]); }
    RAW_BARRIER();
    auto stats = [&](int tl, const f32x4 (&st)[4], float& mean, float& rstd) {
        float var;
        if (FAST) chan8(st, mean, var);
        else {
            float s = 0.f, s2 = 0.f;
#pragma unroll
            for (int ww = 0; ww < 4; ++ww) { f32x2 v = *(const f32x2*)(L + ST_OFF + (ww * 64 + tl) * 8); s += v[0]; s2 += v[1]; }
            mean = s * (1.f / 640.f);
            var = fmaxf(s2 * (1.f / 640.f) - mean * mean, 0.f);
        }
        mean = rbf(mean);
        rstd = 1.0f / ln_sd(var);
    };
    {
        f32x4 rr[3][5], cc[4][5];
#pragma unroll
        for (int j = 0; j < 3; ++j)
#pragma unroll
            for (int nt = 0; nt < 5; ++nt) rr[j][nt] = *(const f32x4*)(L + R_OFF + (((w * 3 + j) * 5 + nt) * 64 + lane) * 16);
        if (!FAST) {
#pragma unroll
            for (int ww = 0; ww < 4; ++ww)
#pragma unroll
                for (int nt = 0; nt < 5; ++nt) cc[ww][nt] = *(const f32x4*)(L + CS_OFF + ww * 448 + (nt * 16 + 4 * g) * 4);
        }
        float mean, rstd; stats(16 * w + n16, stq, mean, rstd);
        SBAR();
        const float sc = rstd * QSCALE;
        float n2[5];
#pragma unroll
        for (int nt = 0; nt < 5; ++nt) {
            const f32x4 a = (acc[0][nt] + rr[0][nt]) + (rr[1][nt] + rr[2][nt]);
            f32x4 s;
            if (FAST) s = csq[nt];
            else {
                s = (cc[0][nt] + cc[1][nt]) + (cc[2][nt] + cc[3][nt]);
                if (tb == 0 && w == 0 && n16 == 0) *(f32x4*)(ws + OFF_CS + (80 * cg + 16 * nt + 4 * g) * 4) = s;
            }
            const unsigned u0 = pk2((a[0] - mean * s[0]) * sc, (a[1] - mean * s[1]) * sc);
            const unsigned u1 = pk2((a[2] - mean * s[2]) * sc, (a[3] - mean * s[3]) * sc);
            { const u32x2 v_ = u32x2{u0, u1}; ST8(qrow + qoff[nt], v_); }
            n2[nt] = dot2(u1, u1, dot2(u0, u0, 0.f));
        }
        float hn[4];
        hn[0] = n2[0] + (g == 0 ? n2[1] : 0.f);
        hn[1] = (g > 0 ? n2[1] : 0.f) + (g < 2 ? n2[2] : 0.f);
        hn[2] = (g > 1 ? n2[2] : 0.f) + (g < 3 ? n2[3] : 0.f);
        hn[3] = (g == 3 ? n2[3] : 0.f) + n2[4];
#pragma unroll
        for (int hh = 0; hh < 4; ++hh) hn[hh] = add32(add16(hn[hh]));
        const float n2a = (g & 1) ? hn[1] : hn[0], n2b = (g & 1) ? hn[3] : hn[2];
        const unsigned qb = pk2(__builtin_amdgcn_sqrtf((g & 2) ? n2b : n2a), 0.f);
        { const u32x2 v_ = u32x2{qb, 0u}; ST8(qrow + g * 3072 + 40, v_); }
        float m = bf_lo(qb);
        if (!(m == m)) m = INFINITY;
        m = wavemax(m);
        if (lane == 0) *(float*)(ws + OFF_QN + ((tb * 8 + cg) * 4 + w) * 4) = m;
    }
    if (w == 3) {
        const int t16 = tb * 4 + mt0;
        float mean, rstd; stats(mt0 * 16 + n16, stk, mean, rstd);
        f32x4 a0 = {0.f, 0.f, 0.f, 0.f}, a1 = a0, s0 = a0, s1 = a0;
#pragma unroll
        for (int ww = 0; ww < 4; ++ww) {
            a0 += *(const f32x4*)(L + PK_OFF + ((ww * 2 + 0) * 64 + lane) * 16);
            a1 += *(const f32x4*)(L + PK_OFF + ((ww * 2 + 1) * 64 + n16) * 16);
            if (!FAST) {
                s0 += *(const f32x4*)(L + CS_OFF + ww * 448 + (80 + 4 * g) * 4);
                s1 += *(const f32x4*)(L + CS_OFF + ww * 448 + 96 * 4);
            }
        }
        if (FAST) { s0 = cs0; s1 = cs1; }
        else if (tb == 0 && cg < 2 && n16 == 0) {
            *(f32x4*)(ws + OFF_CS + (640 + 32 * isv + 4 * g) * 4) = s0;
            if (g == 0) *(f32x4*)(ws + OFF_CS + (640 + 32 * isv + 16) * 4) = s1;
        }
        const unsigned u0 = pk2((a0[0] - mean * s0[0]) * rstd, (a0[1] - mean * s0[1]) * rstd);
        const unsigned u1 = pk2((a0[2] - mean * s0[2]) * rstd, (a0[3] - mean * s0[3]) * rstd);
        const unsigned u2 = pk2((a1[0] - mean * s1[0]) * rstd, (a1[1] - mean * s1[1]) * rstd);
        const unsigned u3 = pk2((a1[2] - mean * s1[2]) * rstd, (a1[3] - mean * s1[3]) * rstd);
        if (!isv) {
            const int key = t16 * 16 + n16;
            *(u32x2*)(ws + OFF_K1 + key * 32 + g * 8) = u32x2{u0, u1};
            if (g == 0) *(u32x2*)(ws + OFF_K2 + key * 8) = u32x2{u2, u3};
            float n2 = dot2(u1, u1, dot2(u0, u0, 0.f));
            n2 = add32(add16(n2));
            n2 = dot2(u3, u3, dot2(u2, u2, n2));
            if (!(n2 == n2)) n2 = INFINITY;
            n2 = rowmax16(n2);
            if (lane == 0) *(float*)(ws + OFF_KN + t16 * 4) = n2;
        } else {
            u16* vb = (u16*)(ws + OFF_V + t16 * 640 + (n16 >> 3) * 320) + (n16 & 7);
            vb[(4 * g + 0) * 8] = (u16)u0; vb[(4 * g + 1) * 8] = (u16)(u0 >> 16);
            vb[(4 * g + 2) * 8] = (u16)u1; vb[(4 * g + 3) * 8] = (u16)(u1 >> 16);
            if (g == 0) { vb[16 * 8] = (u16)u2; vb[17 * 8] = (u16)(u2 >> 16); vb[18 * 8] = (u16)u3; vb[19 * 8] = (u16)(u3 >> 16); }
        }
    }
    if (w == 0) *(u32x2*)(ws + OFF_XG + ((size_t)(tb * 64 + lane) * 8 + cg) * 8) = u32x2{0u, 0u};
}

constexpr int QKV_LDS = 163840;
template <bool FAST>
__global__ __launch_bounds__(256, 1) void k_qkv(const u16* __restrict__ x, const u16* __restrict__ wq, const u16* __restrict__ wk,
                                                const u16* __restrict__ wv, const u16* __restrict__ wo, char* __restrict__ ws,
                                                const u16* __restrict__ w1, const u16* __restrict__ w2) {
    extern __shared__ __attribute__((aligned(16))) char lds_raw[];
    lds_ptr lds = (lds_ptr)lds_raw;
    char* L = lds_raw;
    const int tid = threadIdx.x, lane = tid & 63, w = __builtin_amdgcn_readfirstlane(tid >> 6);
    const int b = blockIdx.x, slot = b >> 3, tb = ((b & 7) >> 1) * 8 + (slot >> 2), cg = (b & 1) * 4 + (slot & 3);
    const int mt0 = cg >> 1, isv = cg & 1;
    const int n16 = lane & 15, g = lane >> 4;
    SST_DECL; SST(0);
    unsigned code_touch = 0u, wtouch[2] = {0u, 0u};
    if (!FAST && w == 2) {
        uint64_t a[5];
        KADDR(a[0], "_Z6k_attnILb1EEvPcPKtS2_S2_S2_"); KADDR(a[1], "_Z6k_attnILb0EEvPcPKtS2_S2_S2_");
        KADDR(a[2], "_Z5k_mlpILb1EEvPcPKtS2_S2_Pt"); KADDR(a[3], "_Z5k_mlpILb0EEvPcPKtS2_S2_Pt"); KADDR(a[4], "_Z5k_endILi0EEvv");
        uint64_t lo = a[0], hi = a[0];
#pragma unroll
        for (int i = 1; i < 5; ++i) { lo = a[i] < lo ? a[i] : lo; hi = a[i] > hi ? a[i] : hi; }
        const uint64_t pa = lo + (uint64_t)(slot * 64 + lane) * 128;
        if (pa < hi) code_touch = *(const __attribute__((address_space(1))) unsigned*)pa;
        SBAR();
    }
    f32x4 acc[4][5], acck[2];
#pragma unroll
    for (int i = 0; i < 4; ++i)
#pragma unroll
        for (int j = 0; j < 5; ++j) acc[i][j] = f32x4{0.f, 0.f, 0.f, 0.f};
    acck[0] = f32x4{0.f, 0.f, 0.f, 0.f}; acck[1] = acck[0];
    f32x4 csq[5], cs0, cs1, stq[4], stk[4];
    float sm[4] = {0.f, 0.f, 0.f, 0.f}, sq[4] = {0.f, 0.f, 0.f, 0.f};
    float cs[5] = {0.f, 0.f, 0.f, 0.f, 0.f}, csk[2] = {0.f, 0.f};
    if (FAST) {
        const bf16x8* xb = (const bf16x8*)x + (size_t)(tb * 4 * 20 + 5 * w) * 64 + lane;
        const bf16x8* wb = (const bf16x8*)(ws + OFF_WQF) + (size_t)(5 * cg * 20 + 5 * w) * 64 + lane;
        const bf16x8* kb = (const bf16x8*)(ws + OFF_WQF) + (size_t)((40 + 2 * isv) * 20 + 5 * w) * 64 + lane;
        bf16x8 xf[5][4], xk[5], wf[5][5], wkf[5][2];
#define QKV_LD(ks) do { _Pragma("unroll") for (int i = 0; i < 4; ++i) xf[ks][i] = xb[(((i + w) & 3) * 20 + (ks)) * 64]; \
        _Pragma("unroll") for (int nt = 0; nt < 5; ++nt) wf[ks][nt] = wb[(nt * 20 + (ks)) * 64]; \
        xk[ks] = xb[(mt0 * 20 + (ks)) * 64]; wkf[ks][0] = kb[(ks) * 64]; wkf[ks][1] = kb[(20 + (ks)) * 64]; SBAR(); } while (0)
        QKV_LD(0); QKV_LD(1); QKV_LD(2); QKV_LD(3);
#pragma unroll
        for (int ks = 0; ks < 5; ++ks) {
#pragma unroll
            for (int nt = 0; nt < 5; ++nt)
#pragma unroll
                for (int i = 0; i < 4; ++i) acc[i][nt] = MFMA16_32(wf[ks][nt], xf[ks][i], acc[i][nt]);
            acck[0] = MFMA16_32(wkf[ks][0], xk[ks], acck[0]);
            acck[1] = MFMA16_32(wkf[ks][1], xk[ks], acck[1]);
            SBAR();
            if (ks == 0) {
                QKV_LD(4);
                const float* csp = (const float*)(ws + OFF_CS);
#pragma unroll
                for (int nt = 0; nt < 5; ++nt) csq[nt] = *(const f32x4*)(csp + 80 * cg + 16 * nt + 4 * g);
                cs0 = *(const f32x4*)(csp + 640 + 32 * isv + 4 * g); cs1 = *(const f32x4*)(csp + 640 + 32 * isv + 16);
                const f32x4* sp = (const f32x4*)(ws + OFF_XS) + (size_t)(tb * 64 + n16) * 4;
#pragma unroll
                for (int i = 0; i < 4; ++i) { stq[i] = sp[16 * w * 4 + i]; stk[i] = sp[16 * mt0 * 4 + i]; }
                SBAR();
            }
        }
    } else {
        lds_ptr ra = lds + w * 40960, rb = ra + 20480;
        const char* xs = (const char*)(x + (size_t)(tb * 64) * 640 + 160 * w);
#pragma unroll
        for (int j = 0; j < 5; ++j) {
            const int c = 64 * j + lane;
            const unsigned xo = (unsigned)(c / 20) * 1280u + (unsigned)(c % 20) * 16u;
#pragma unroll
            for (int i = 0; i < 4; ++i) glds16(xs + (size_t)i * 20480 + xo, ra + i * 5120 + j * 1024);
        }
        const char* src = (const char*)(wq + (size_t)(160 * w) * 640 + 80 * cg);
        const unsigned loff = (unsigned)(lane / 10) * 1280u + (unsigned)(lane % 10) * 16u;
        const char* src2 = (const char*)((isv ? wv : wk) + (size_t)(160 * w) * 20);
#pragma unroll
        for (int i = 0; i < 16; ++i) glds16(src + (size_t)i * 7680 + loff, rb + i * 960);
#pragma unroll
        for (int i = 0; i < 3; ++i) glds16(src2 + i * 1024 + lane * 16, rb + 15488 + i * 1024);
        if (lane < 48) glds16(src2 + 3 * 1024 + lane * 16, rb + 15488 + 3 * 1024);
        SST(1);
        VMCNT(20);
        bf16x8 xf[4][5], xk[5];
        {
            const char* xl = L + w * 40960 + n16 * 320 + g * 16;
#pragma unroll
            for (int i = 0; i < 4; ++i)
#pragma unroll
                for (int ks = 0; ks < 5; ++ks) xf[i][ks] = *(const bf16x8*)(xl + ((i + w) & 3) * 5120 + ks * 64);
#pragma unroll
            for (int ks = 0; ks < 5; ++ks) xk[ks] = *(const bf16x8*)(xl + mt0 * 5120 + ks * 64);
        }
        asm volatile("s_waitcnt lgkmcnt(0)" ::: "memory");
        SBAR();
#pragma unroll
        for (int i = 0; i < 10; ++i) glds16(src + (size_t)(16 + i) * 7680 + loff, ra + i * 960);
        if (lane < 40) glds16(src + (size_t)26 * 7680 + loff, ra + 10 * 960);
#pragma unroll
        for (int i = 0; i < 2; ++i) glds16(src2 + 3840 + i * 1024 + lane * 16, ra + 10240 + i * 1024);
        if (lane < 32) glds16(src2 + 3840 + 2 * 1024 + lane * 16, ra + 10240 + 2 * 1024);
        SST(2);
        const unsigned ONES = 0x3f803f80u;
#pragma unroll
        for (int i = 0; i < 4; ++i)
#pragma unroll
            for (int ks = 0; ks < 5; ++ks) {
                u32x4 u = __builtin_bit_cast(u32x4, xf[i][ks]);
#pragma unroll
                for (int e = 0; e < 4; ++e) { sm[i] = dot2(u[e], ONES, sm[i]); sq[i] = dot2(u[e], u[e], sq[i]); }
            }
        SBAR();
        VMCNT(14);
        SST(3);
        const int q4 = (lane & 15) >> 2, p4 = lane & 3;
        lds_cptr wb0 = (lds_cptr)rb + (8 * g + q4) * 160 + p4 * 8, wb1 = (lds_cptr)ra + (8 * g + q4) * 160 + p4 * 8 - 3 * 5120;
        lds_cptr kb0 = (lds_cptr)rb + 15488 + (8 * g + q4) * 40 + p4 * 8, kb1 = (lds_cptr)ra + 10240 + (8 * g + q4) * 40 + p4 * 8 - 3 * 1280;
        bf16x8 wf[2][7];
#define QKV_TR(ks, s_) do { lds_cptr wb = (ks) < 3 ? wb0 : wb1, kb = (ks) < 3 ? kb0 : kb1; \
        _Pragma("unroll") for (int nt = 0; nt < 5; ++nt) wf[s_][nt] = cat8(tr_read(wb + (ks) * 5120 + nt * 32), tr_read(wb + (ks) * 5120 + 640 + nt * 32)); \
        _Pragma("unroll") for (int nt = 0; nt < 2; ++nt) wf[s_][5 + nt] = cat8(tr_read(kb + (ks) * 1280 + nt * 32), tr_read(kb + (ks) * 1280 + 160 + nt * 32)); SBAR(); } while (0)
        QKV_TR(0, 0);
#pragma unroll
        for (int ks = 0; ks < 5; ++ks) {
            if (ks == 2) {
                VMCNT(0); SBAR();
            }
            if (ks < 4) QKV_TR(ks + 1, (ks + 1) & 1);
#pragma unroll
            for (int nt = 0; nt < 5; ++nt) {
                u32x4 u = __builtin_bit_cast(u32x4, wf[ks & 1][nt]);
#pragma unroll
                for (int e = 0; e < 4; ++e) cs[nt] = dot2(u[e], ONES, cs[nt]);
#pragma unroll
                for (int i = 0; i < 4; ++i) acc[i][nt] = MFMA16_32(wf[ks & 1][nt], xf[i][ks], acc[i][nt]);
            }
#pragma unroll
            for (int nt = 0; nt < 2; ++nt) {
                u32x4 u = __builtin_bit_cast(u32x4, wf[ks & 1][5 + nt]);
#pragma unroll
                for (int e = 0; e < 4; ++e) csk[nt] = dot2(u[e], ONES, csk[nt]);
                acck[nt] = MFMA16_32(wf[ks & 1][5 + nt], xk[ks], acck[nt]);
            }
            SBAR();
        }
#pragma unroll
        for (int i = 0; i < 4; ++i) { sm[i] = add32(add16(sm[i])); sq[i] = add32(add16(sq[i])); }
#pragma unroll
        for (int i = 0; i < 5; ++i) cs[i] = add32(add16(cs[i]));
#pragma unroll
        for (int i = 0; i < 2; ++i) csk[i] = add32(add16(csk[i]));
    }
    SST(4);
    if (!FAST) RAW_BARRIER();
    qkv_finish<FAST>(ws, L, tb, cg, w, lane, acc, acck, csq, cs0, cs1, stq, stk, sm, sq, cs, csk);
    if (!FAST && w == 1) {
        if (b < 80) {
            const int ks = b >> 2, ct = b & 3; int cn = 16 * (ct & 1) + n16; if (cn >= 20) cn = 0;
            *(u32x4*)(ws + OFF_WQF + (size_t)((40 + ct) * 20 + ks) * 1024 + lane * 16) =
                gather8(((ct & 2) ? wv : wk) + (size_t)(32 * ks + 8 * g) * 20 + cn, 20);
        } else if (b == 80 && lane < 4) {
            const unsigned v = (lane & 2) ? 0x3f803f80u : 0u;
            *(u32x4*)(ws + OFF_CONST + lane * 16) = u32x4{v, v, v, v};
        }
    }
    PIN(code_touch); PIN(wtouch[0]); PIN(wtouch[1]);
    SST(7); SST_FLUSHK(ws, FAST ? 5 : 0);
}

constexpr int PJ0 = PJA, PJ1 = PJB, PJD = PJC;
constexpr int PBW = 2688, ATT_LDS = 32 * PBW + 16384;
template <bool PREP>
__global__ __launch_bounds__(512, 2) void k_attn(char* __restrict__ ws, const u16* __restrict__ w1, const u16* __restrict__ w2,
                                                 const u16* __restrict__ wo, const u16* __restrict__ wq) {
    extern __shared__ __attribute__((aligned(16))) char lds_raw[];
    char* L = lds_raw;
    const int tid = threadIdx.x, lane = tid & 63, w = __builtin_amdgcn_readfirstlane(tid >> 6);
    const int b = blockIdx.x, slot = b >> 3, tb = ((b & 7) >> 1) * 8 + (slot >> 2), cg = (b & 1) * 4 + (slot & 3);
    const int r32 = lane & 31, h = lane >> 5, n16 = lane & 15, g = lane >> 4;
    const int cgp = w & 1, kq = w >> 1;
    SST_DECL; SST(0);
    float kn0 = *(const float*)(ws + OFF_KN + lane * 4), kn1 = *(const float*)(ws + OFF_KN + 256 + lane * 4);
    float qmax = *(const float*)(ws + OFF_QN + ((tb * 8 + cg) * 4 + (lane & 3)) * 4);
    bf16x8 q1[4]; bf16x4 q2[4];
    {
        const char* qg = ws + OFF_Q + (size_t)(tb * 8 + cg) * 12288 + cgp * 6144 + r32 * 48;
#pragma unroll
        for (int j = 0; j < 4; ++j) { q1[j] = *(const bf16x8*)(qg + j * 1536 + 16 * h); q2[j] = *(const bf16x4*)(qg + j * 1536 + 32 + 8 * h); }
    }
    SBAR();
    const int kr = (r32 & 19) | ((r32 & 4) << 1) | ((r32 & 8) >> 1);
    const unsigned ko1 = (unsigned)OFF_K1 + (unsigned)(512 * kq + kr) * 32u + 16u * h;
    const unsigned ko2 = h ? (unsigned)OFF_CONST + 32u : (unsigned)OFF_K2 + (unsigned)(512 * kq + kr) * 8u;
    const unsigned ks2 = h ? 0u : 256u;
    const unsigned vo = r32 < 20 ? (unsigned)OFF_V + (unsigned)(32 * kq) * 640u + (unsigned)(h * 20 + r32) * 16u
                                 : (unsigned)OFF_CONST + (r32 == 20 ? 32u : 0u);
    const unsigned vs = r32 < 20 ? 640u : 0u;
    bf16x8 k1[3], v0[3], v1[3]; bf16x4 k2[3];
#define K_LOAD(t) do { k1[(t) % 3] = *(const bf16x8*)(ws + ko1 + (t) * 1024); k2[(t) % 3] = *(const bf16x4*)(ws + (ko2 + (t) * ks2)); } while (0)
#define V_LOAD(t) do { v0[(t) % 3] = *(const bf16x8*)(ws + (vo + (2 * (t)) * vs)); v1[(t) % 3] = *(const bf16x8*)(ws + (vo + (2 * (t) + 1) * vs)); } while (0)
    K_LOAD(0); V_LOAD(0); SBAR(); K_LOAD(1); V_LOAD(1); SBAR();
    PIN(kn0); PIN(kn1);
    const float kmax2 = wavemax(fmaxf(kn0, kn1));
    qmax = rowmax16(qmax);
    const float kb = __builtin_amdgcn_sqrtf(kmax2);
    const bool safe = __builtin_amdgcn_readfirstlane((int)(qmax * kb <= SAFE_BOUND)) != 0;
    const f32x16 zero16 = {0.f, 0.f, 0.f, 0.f, 0.f, 0.f, 0.f, 0.f, 0.f, 0.f, 0.f, 0.f, 0.f, 0.f, 0.f, 0.f};
    if (safe) {
        if (h) {
#pragma unroll
            for (int j = 0; j < 4; ++j) q2[j][0] = (short)pk2(-bf2f((u16)q2[j][0]) * kb, 0.f);
        }
    } else {
        float m[4];
#pragma unroll
        for (int j = 0; j < 4; ++j) { m[j] = -INFINITY; if (h) q2[j] = bf16x4{0, 0, 0, 0}; }
        for (int t = 0; t < 16; ++t) {
            const bf16x8 ka = *(const bf16x8*)(ws + ko1 + t * 1024);
            const bf16x4 kc = *(const bf16x4*)(ws + (ko2 + t * ks2));
#pragma unroll
            for (int j = 0; j < 4; ++j) {
                f32x16 s = MFMA32_16(ka, q1[j], zero16);
                SBAR(); asm volatile("s_nop 15" ::: "memory"); SBAR();
                s = MFMA32_8(kc, q2[j], s);
#pragma unroll
                for (int e = 0; e < 16; ++e) m[j] = fmaxf(m[j], s[e]);
                SBAR();
            }
        }
#pragma unroll
        for (int j = 0; j < 4; ++j) {
            const float mm = fmaxf(m[j], xor32(m[j]));
            if (h == 0) *(float*)(L + ((w * 4 + j) * 32 + r32) * 4) = mm;
        }
        RAW_BARRIER();
#pragma unroll
        for (int j = 0; j < 4; ++j) {
            float mm = -INFINITY;
#pragma unroll
            for (int qq = 0; qq < 4; ++qq) mm = fmaxf(mm, *(const float*)(L + (((2 * qq + cgp) * 4 + j) * 32 + r32) * 4));
            const unsigned hi = __float_as_uint(mm) & 0xffff0000u;
            const float r1 = mm - __uint_as_float(hi);
            const unsigned mid = __float_as_uint(r1) & 0xffff0000u;
            const float r2 = r1 - __uint_as_float(mid);
            const unsigned lo = __float_as_uint(r2) & 0xffff0000u;
            if (h) q2[j] = __builtin_bit_cast(bf16x4, u32x2{hi ^ 0x80000000u, ((mid ^ 0x80000000u) >> 16) | (lo ^ 0x80000000u)});
        }
        RAW_BARRIER();
    }
    SST(1);
    f32x16 o[4], sa, sb;
#pragma unroll
    for (int j = 0; j < 4; ++j) o[j] = zero16;
    u32x4 p1prev = {0u, 0u, 0u, 0u};
    f32x4 ea, eb;
    sa = MFMA32_16(k1[0], q1[0], zero16);
    SBAR(); asm volatile("s_nop 15" ::: "memory"); SBAR();
    sa = MFMA32_8(k2[0], q2[0], sa);
    SBAR(); asm volatile("s_nop 15" ::: "memory"); SBAR();
    u32x4 prep[4]; char* prep_dst = nullptr; int prep_st = 0;
    unsigned l2t[3] = {0u, 0u, 0u};
#pragma unroll
    for (int t = 0; t < 16; ++t) {
#pragma unroll
        for (int j = 0; j < 4; ++j) {
            const int it = 4 * t + j;
            f32x16& cur = (it & 1) ? sb : sa; f32x16& nxt = (it & 1) ? sa : sb;
            const int tn = j < 3 ? t : t + 1, jn = (j + 1) & 3;
            const int tp = j > 0 ? t : t - 1, jp = (j + 3) & 3;
            u32x4 p0, p1;
            if (j == 0 && t + 2 < 16) K_LOAD(t + 2);
            if (j == 1 && t + 2 < 16) V_LOAD(t + 2);
            if (PREP && j == 2 && (t == PJ0 || t == PJ1)) {
                int id = (t == PJ1 ? 2048 : 0) + b * 8 + w; if (id >= NPREP) id -= 2048;
                const u16* src; int ld;
                if (id < 1728) { const int ks = id / 27, c4 = id - ks * 27; src = w1 + (size_t)(32 * ks) * F_ + 64 * c4; ld = F_;
                                 prep_dst = ws + OFF_W1F + (size_t)(4 * c4 * 64 + ks) * 1024; prep_st = 64 * 1024; }
                else if (id < 3456) { const int jj = id - 1728, ks = jj >> 5, c4 = jj & 31; src = w2 + (size_t)(32 * ks) * S_ + 64 * c4; ld = S_;
                                      prep_dst = ws + OFF_W2F + (size_t)(4 * c4 * 54 + ks) * 1024; prep_st = 54 * 1024; }
                else { int jj = id - 3456; const bool isq = jj >= 200; if (isq) jj -= 200; const int ks = jj / 10, c4 = jj - ks * 10;
                       src = (isq ? wq : wo) + (size_t)(32 * ks) * 640 + 64 * c4; ld = 640;
                       prep_dst = ws + (isq ? OFF_WQF : OFF_WOF) + (size_t)(4 * c4 * 20 + ks) * 1024; prep_st = 20 * 1024; }
                src += (size_t)(lane >> 3) * ld + (lane & 7) * 8;
#pragma unroll
                for (int i = 0; i < 4; ++i) prep[i] = *(const u32x4*)(src + (size_t)(8 * i) * ld);
            }
            if (PREP && j == 2 && (t == PJ0 + PJD || t == PJ1 + PJD)) {
#pragma unroll
                for (int i = 0; i < 4; ++i) *(u32x4*)(L + w * 4096 + i * 1024 + lane * 16) = prep[i];
            }
            if (PREP && j == 3 && (t == PJ0 + PJD || t == PJ1 + PJD)) {
                lds_cptr tp_ = (lds_cptr)(lds_ptr)L + w * 4096 + (8 * g + (n16 >> 2)) * 128 + (lane & 3) * 8;
#pragma unroll
                for (int i = 0; i < 4; ++i) prep[i] = __builtin_bit_cast(u32x4, cat8(tr_read(tp_ + i * 32), tr_read(tp_ + 512 + i * 32)));
            }
            if (PREP && j == 0 && (t == PJ0 + PJD + 1 || t == PJ1 + PJD + 1)) {
#pragma unroll
                for (int i = 0; i < 4; ++i) PREP_STORE((u32x4*)(prep_dst + (size_t)i * prep_st + lane * 16), prep[i]);
            }
            SBAR();
            if (it < 63) nxt = MFMA32_16(k1[tn % 3], q1[jn], zero16);
            SBAR();
            p0[0] = pk2(EX2(cur[0]), EX2(cur[1])); p0[1] = pk2(EX2(cur[2]), EX2(cur[3]));
            PIN(p0); SBAR();
            if (it > 0) o[jp] = MFMA32_16(v1[(tp + 3) % 3], __builtin_bit_cast(bf16x8, p1prev), o[jp]);
            SBAR();
            p0[2] = pk2(EX2(cur[4]), EX2(cur[5])); p0[3] = pk2(EX2(cur[6]), EX2(cur[7]));
            PIN(p0); SBAR();
            if (it < 63) nxt = MFMA32_8(k2[tn % 3], q2[jn], nxt);
            SBAR();
            p1[0] = pk2(EX2(cur[8]), EX2(cur[9])); p1[1] = pk2(EX2(cur[10]), EX2(cur[11]));
            PIN(p1); SBAR();
            o[j] = MFMA32_16(v0[t % 3], __builtin_bit_cast(bf16x8, p0), o[j]);
            SBAR();
            p1[2] = pk2(EX2(cur[12]), EX2(cur[13])); p1[3] = pk2(EX2(cur[14]), EX2(cur[15]));
            PIN(p1); SBAR();
            p1prev = p1;
        }
        if (t == 7) SST(2);
    }
    o[3] = MFMA32_16(v1[15 % 3], __builtin_bit_cast(bf16x8, p1prev), o[3]);
    SST(3);
    if (PREP) RAW_BARRIER();
#pragma unroll
    for (int j = 0; j < 4; ++j) {
        char* pb = L + (kq * 8 + cgp * 4 + j) * PBW;
        *(f32x4*)(pb + lane * 16) = f32x4{o[j][0], o[j][1], o[j][2], o[j][3]};
        *(f32x4*)(pb + 1024 + lane * 16) = f32x4{o[j][4], o[j][5], o[j][6], o[j][7]};
        if (h == 0) *(f32x4*)(pb + 2048 + r32 * 16) = f32x4{o[j][8], o[j][9], o[j][10], o[j][11]};
        else *(float*)(pb + 2560 + r32 * 4) = o[j][8];
    }
    RAW_BARRIER();
    SST(4);
    {
        const char* pb = L + w * PBW;
        f32x4 a0 = {0.f, 0.f, 0.f, 0.f}, a1 = a0, a2 = a0; float l = 0.f;
#pragma unroll
        for (int qq = 0; qq < 4; ++qq) {
            a0 += *(const f32x4*)(pb + qq * 8 * PBW + lane * 16);
            a1 += *(const f32x4*)(pb + qq * 8 * PBW + 1024 + lane * 16);
            a2 += *(const f32x4*)(pb + qq * 8 * PBW + 2048 + r32 * 16);
            l += *(const float*)(pb + qq * 8 * PBW + 2560 + r32 * 4);
        }
        const float inv = __builtin_amdgcn_rcpf(l);
        const int tl = 32 * (w & 1) + r32, hd0 = 80 * cg + 20 * (w >> 1);
        char* ob = ws + OFF_O + (size_t)(tb * 4 + (tl >> 4)) * 20480 + (tl & 15) * 16;
        auto st = [&](int hd, const f32x4& a) {
            const u32x2 v_ = u32x2{pk2(a[0] * inv, a[1] * inv), pk2(a[2] * inv, a[3] * inv)};
            ST8(ob + (hd >> 5) * 1024 + ((hd & 31) >> 3) * 256 + (hd & 7) * 2, v_);
        };
        st(hd0 + 4 * h, a0); st(hd0 + 8 + 4 * h, a1);
        if (h == 0) st(hd0 + 16, a2);
    }
    if (b == 0 && tid < 48) *(unsigned*)(ws + OFF_CNT + tid * 256) = 0u;
    PIN(l2t[0]); PIN(l2t[1]); PIN(l2t[2]);
    SST(5); SST_FLUSHK(ws, PREP ? 1 : 6);
}

template <bool XF>
__global__ __launch_bounds__(256, 1) void k_wo(char* __restrict__ ws, const u16* __restrict__ xin) {
    __shared__ __attribute__((aligned(16))) char L[61440];
    const int tid = threadIdx.x, lane = tid & 63, w = __builtin_amdgcn_readfirstlane(tid >> 6);
    const int b = blockIdx.x, slot = b >> 3, tb = ((b & 7) >> 1) * 8 + (slot >> 2), cg = (b & 1) * 4 + (slot & 3);
    const int n16 = lane & 15, g = lane >> 4;
    SST_DECL; SST(0);
    f32x4 acc[4][5];
#pragma unroll
    for (int i = 0; i < 4; ++i)
#pragma unroll
        for (int j = 0; j < 5; ++j) acc[i][j] = f32x4{0.f, 0.f, 0.f, 0.f};
    const int tok = tb * 64 + 16 * w + n16;
    u32x2 xi[5];
    {
        const bf16x8* xb = (const bf16x8*)(ws + OFF_O) + (size_t)(tb * 4 * 20 + 5 * w) * 64 + lane;
        const bf16x8* wb = (const bf16x8*)(ws + OFF_WOF) + (size_t)(5 * cg * 20 + 5 * w) * 64 + lane;
        bf16x8 xf[5][4], wf[5][5];
#define WO_LD(ks) do { _Pragma("unroll") for (int i = 0; i < 4; ++i) xf[ks][i] = xb[(((i + w) & 3) * 20 + (ks)) * 64]; \
        _Pragma("unroll") for (int nt = 0; nt < 5; ++nt) wf[ks][nt] = wb[(nt * 20 + (ks)) * 64]; SBAR(); } while (0)
        WO_LD(0); WO_LD(1); WO_LD(2); WO_LD(3);
#pragma unroll
        for (int ks = 0; ks < 5; ++ks) {
#pragma unroll
            for (int nt = 0; nt < 5; ++nt)
#pragma unroll
                for (int i = 0; i < 4; ++i) acc[i][nt] = MFMA16_32(wf[ks][nt], xf[ks][i], acc[i][nt]);
            SBAR();
            if (ks == 0) {
                WO_LD(4);
#pragma unroll
                for (int nt = 0; nt < 5; ++nt) {
                    const int d = 80 * cg + 16 * nt + 4 * g;
                    if (XF) xi[nt] = *(const u32x2*)(xin + ((size_t)((tok >> 4) * 20 + (d >> 5)) * 64 + ((d & 31) >> 3) * 16 + n16) * 8 + (d & 7));
                    else xi[nt] = *(const u32x2*)(xin + (size_t)tok * 640 + d);
                }
                SBAR();
            }
        }
    }
    SST(1);
#pragma unroll
    for (int j = 1; j < 4; ++j) {
        const int o = (w + j) & 3;
#pragma unroll
        for (int nt = 0; nt < 5; ++nt) *(f32x4*)(L + (((o * 3 + (j - 1)) * 5 + nt) * 64 + lane) * 16) = acc[j][nt];
    }
    RAW_BARRIER();
    SST(2);
    float xv[20]; u32x2 x1o[5];
    {
        f32x4 rr[3][5];
#pragma unroll
        for (int j = 0; j < 3; ++j)
#pragma unroll
            for (int nt = 0; nt < 5; ++nt) rr[j][nt] = *(const f32x4*)(L + (((w * 3 + j) * 5 + nt) * 64 + lane) * 16);
        SBAR();
        float s = 0.f;
#pragma unroll
        for (int nt = 0; nt < 5; ++nt) {
            const f32x4 a = (acc[0][nt] + rr[0][nt]) + (rr[1][nt] + rr[2][nt]);
            const unsigned u0 = pk2(bf_lo(xi[nt][0]) + a[0], bf_hi(xi[nt][0]) + a[1]);
            const unsigned u1 = pk2(bf_lo(xi[nt][1]) + a[2], bf_hi(xi[nt][1]) + a[3]);
            x1o[nt] = u32x2{u0, u1};
            xv[4 * nt] = bf_lo(u0); xv[4 * nt + 1] = bf_hi(u0); xv[4 * nt + 2] = bf_lo(u1); xv[4 * nt + 3] = bf_hi(u1);
            s += (xv[4 * nt] + xv[4 * nt + 1]) + (xv[4 * nt + 2] + xv[4 * nt + 3]);
        }
        const float mw = add32(add16(s)) * (1.f / 80.f);
        float s2 = 0.f;
#pragma unroll
        for (int i = 0; i < 20; ++i) { const float dd = xv[i] - mw; s2 += dd * dd; }
        s2 = add32(add16(s2));
        gu64* gp = (gu64*)(ws + OFF_XG) + (size_t)tok * 8 + cg;
        if (g == 0) __hip_atomic_store(gp, ((unsigned long long)(__float_as_uint(s2) | 0x80000000u) << 32) | __float_as_uint(mw),
                                       __ATOMIC_RELAXED, __HIP_MEMORY_SCOPE_AGENT);
    }
    SST(3);
    float mean, var;
    {
        gu64* gp = (gu64*)(ws + OFF_XG) + (size_t)tok * 8 + 2 * g;
        unsigned long long v0, v1;
        for (int spin = 0; spin < (1 << 20); ++spin) {
            v0 = __hip_atomic_load(gp, __ATOMIC_RELAXED, __HIP_MEMORY_SCOPE_AGENT);
            v1 = __hip_atomic_load(gp + 1, __ATOMIC_RELAXED, __HIP_MEMORY_SCOPE_AGENT);
            if (__all((long long)(v0 & v1) < 0)) break;
            __builtin_amdgcn_s_sleep(4);
        }
        const float m0 = __uint_as_float((unsigned)v0), m1 = __uint_as_float((unsigned)v1);
        mean = add32(add16(m0 + m1)) * 0.125f;
        const float d0 = m0 - mean, d1 = m1 - mean;
        var = add32(add16(__uint_as_float((unsigned)(v0 >> 32) & 0x7fffffffu) + __uint_as_float((unsigned)(v1 >> 32) & 0x7fffffffu) +
                          80.f * (d0 * d0 + d1 * d1))) * (1.f / 640.f);
    }
    SST(4);
    {
        float shf, r2; ln2_scalars(mean, var, shf, r2);
        char* hb = L + w * 15360;
#pragma unroll
        for (int nt = 0; nt < 5; ++nt)
            *(u32x2*)(hb + n16 * 176 + (16 * nt + 4 * g) * 2) =
                u32x2{pk2((xv[4 * nt] - shf) * r2, (xv[4 * nt + 1] - shf) * r2), pk2((xv[4 * nt + 2] - shf) * r2, (xv[4 * nt + 3] - shf) * r2)};
        asm volatile("s_waitcnt lgkmcnt(0)" ::: "memory");
        const int q4 = (lane & 15) >> 2, p4 = lane & 3;
        lds_cptr hr = (lds_cptr)(lds_ptr)L + w * 15360 + (8 * (g & 1) + q4) * 176 + p4 * 8 + (g >> 1) * 32;
        const int s0 = tb * 64 + 16 * w + 8 * (g & 1);
        u16* hf = (u16*)(ws + OFF_H) + ((size_t)(5 * cg + (g >> 1)) * 64 + (s0 >> 5)) * 512 + (((s0 & 31) >> 3) * 16 + n16) * 8;
        const bf16x8 f0 = cat8(tr_read(hr), tr_read(hr + 704));
        const bf16x8 f1 = cat8(tr_read(hr + 64), tr_read(hr + 704 + 64));
        const bf16x8 f2 = cat8(tr_read(hr + (g < 2 ? 128 : 0)), tr_read(hr + 704 + (g < 2 ? 128 : 0)));
        *(bf16x8*)(hf) = f0;
        *(bf16x8*)(hf + 2 * 64 * 512) = f1;
        if (g < 2) *(bf16x8*)(hf + 4 * 64 * 512) = f2;
        u16* x1r = (u16*)(ws + OFF_X1) + (size_t)tok * 640 + 80 * cg + 4 * g;
#pragma unroll
        for (int nt = 0; nt < 5; ++nt) *(u32x2*)(x1r + 16 * nt) = x1o[nt];
    }
    SST(5); SST_FLUSHK(ws, XF ? 7 : 2);
}

template <int MODE, int NS, int PF>
DI void gemm_body(const bf16x8* __restrict__ po, const bf16x8* __restrict__ pf, const int (&ooff)[4], int fstride, f32x4 (&acc)[4][5]) {
    bf16x8 ro[PF][4], rf[PF][5];
#pragma unroll
    for (int s = 0; s < PF; ++s) {
#pragma unroll
        for (int j = 0; j < 4; ++j) ro[s][j] = po[ooff[j] + s * 64];
#pragma unroll
        for (int i = 0; i < 5; ++i) rf[s][i] = pf[i * fstride + s * 64];
        SBAR();
    }
#pragma unroll
    for (int s = 0; s < NS; ++s) {
        const int sl = s % PF;
        SBAR();
#pragma unroll
        for (int j = 0; j < 4; ++j)
#pragma unroll
            for (int i = 0; i < 5; ++i) {
                if (MODE == 1) acc[j][i] = MFMA16_32(ro[sl][j], rf[sl][i], acc[j][i]);
                else acc[j][i] = MFMA16_32(rf[sl][i], ro[sl][j], acc[j][i]);
            }
        SBAR();
        if (s + PF < NS) {
#pragma unroll
            for (int j = 0; j < 4; ++j) ro[sl][j] = po[ooff[j] + (s + PF) * 64];
#pragma unroll
            for (int i = 0; i < 5; ++i) rf[sl][i] = pf[i * fstride + (s + PF) * 64];
        }
    }
}

template <int MODE>
__global__ __launch_bounds__(256, 1) void k_gemm(char* __restrict__ ws, const u16* __restrict__ bias, u16* __restrict__ out) {
    __shared__ __attribute__((aligned(16))) f32x4 red[4 * 3 * 5 * 64];
    const int tid = threadIdx.x, lane = tid & 63, w = __builtin_amdgcn_readfirstlane(tid >> 6);
    const int b = blockIdx.x, xcd = b & 7, slot = b >> 3;
    const int n16 = lane & 15, g = lane >> 4;
    constexpr int KS = MODE == 1 ? 64 : 54;
    int ot, dt;
    if (MODE == 1) {
        ot = 7 * (xcd >> 1) + (slot >> 2); dt = 4 * (xcd & 1) + (slot & 3);
        if (slot >= 28 || ot >= 27) return;
    } else {
        ot = 8 * (xcd >> 1) + (slot >> 2); dt = 4 * (xcd & 1) + (slot & 3);
    }
    SST_DECL; SST(0);
    u32x2 bb = {0u, 0u}, xi[5]; float bs = 0.f;
    if (MODE == 1) bb = *(const u32x2*)(bias + 64 * ot + 16 * w + 4 * g);
    else {
        const int s = 64 * ot + 16 * w + n16;
        bs = bf2f(bias[s]);
        const u16* x1 = (const u16*)(ws + OFF_X1) + (size_t)s * 640 + 80 * dt + 4 * g;
#pragma unroll
        for (int i = 0; i < 5; ++i) xi[i] = *(const u32x2*)(x1 + 16 * i);
    }
    const int k0 = MODE == 1 ? 16 * w : (w < 2 ? 14 * w : 28 + 13 * (w - 2));
    const bf16x8* po = (const bf16x8*)(ws + (MODE == 1 ? OFF_W1F : OFF_W2F)) + ((size_t)(4 * ot) * KS + k0) * 64 + lane;
    const bf16x8* pf = (const bf16x8*)(ws + (MODE == 1 ? OFF_H : OFF_U)) + ((size_t)(5 * dt) * KS + k0) * 64 + lane;
    int ooff[4];
#pragma unroll
    for (int j = 0; j < 4; ++j) ooff[j] = ((j + w) & 3) * KS * 64;
    f32x4 acc[4][5];
#pragma unroll
    for (int j = 0; j < 4; ++j)
#pragma unroll
        for (int i = 0; i < 5; ++i) acc[j][i] = f32x4{0.f, 0.f, 0.f, 0.f};
    if (MODE == 1) gemm_body<1, 16, 6>(po, pf, ooff, KS * 64, acc);
    else if (w < 2) gemm_body<2, 14, 6>(po, pf, ooff, KS * 64, acc);
    else gemm_body<2, 13, 6>(po, pf, ooff, KS * 64, acc);
    SST(1);
#pragma unroll
    for (int j = 1; j < 4; ++j) {
        const int o = (w + j) & 3;
#pragma unroll
        for (int i = 0; i < 5; ++i) red[((o * 3 + (j - 1)) * 5 + i) * 64 + lane] = acc[j][i];
    }
    RAW_BARRIER();
#pragma unroll
    for (int j = 0; j < 3; ++j)
#pragma unroll
        for (int i = 0; i < 5; ++i) acc[1 + j][i] = red[((w * 3 + j) * 5 + i) * 64 + lane];
    SBAR();
#pragma unroll
    for (int i = 0; i < 5; ++i) acc[0][i] = (acc[0][i] + acc[1][i]) + (acc[2][i] + acc[3][i]);
    SST(2);
    if (MODE == 1) {
        const int f = 64 * ot + 16 * w + 4 * g;
        const float bv[4] = {bf_lo(bb[0]), bf_hi(bb[0]), bf_lo(bb[1]), bf_hi(bb[1])};
        u16* uf = (u16*)(ws + OFF_U);
#pragma unroll
        for (int i = 0; i < 5; ++i) {
            float r[4];
#pragma unroll
            for (int e = 0; e < 4; ++e) {
                const float u = acc[0][i][e] + bv[e];
                const float z = u * (1.f + 0.044715f * u * u) * (-2.f * 0.7978845608028654f * 1.4426950408889634f);
                r[e] = u * __builtin_amdgcn_rcpf(1.f + EX2(z));
            }
            *(u32x2*)(uf + ((size_t)((5 * dt + i) * 54 + (f >> 5)) * 64 + ((f & 31) >> 3) * 16 + n16) * 8 + (f & 7)) =
                u32x2{pk2(r[0], r[1]), pk2(r[2], r[3])};
        }
    } else {
        const int s = 64 * ot + 16 * w + n16;
        float xv[20]; float sum = 0.f;
#pragma unroll
        for (int i = 0; i < 5; ++i) {
            const int d = 80 * dt + 16 * i + 4 * g;
            u16* o = MODE == 3 ? out + (size_t)s * 640 + d
                               : out + ((size_t)((4 * ot + w) * 20 + (d >> 5)) * 64 + ((d & 31) >> 3) * 16 + n16) * 8 + (d & 7);
            const unsigned u0 = pk2(bf_lo(xi[i][0]) + acc[0][i][0] + bs, bf_hi(xi[i][0]) + acc[0][i][1] + bs);
            const unsigned u1 = pk2(bf_lo(xi[i][1]) + acc[0][i][2] + bs, bf_hi(xi[i][1]) + acc[0][i][3] + bs);
            *(u32x2*)o = u32x2{u0, u1};
            xv[4 * i] = bf_lo(u0); xv[4 * i + 1] = bf_hi(u0); xv[4 * i + 2] = bf_lo(u1); xv[4 * i + 3] = bf_hi(u1);
            sum += (xv[4 * i] + xv[4 * i + 1]) + (xv[4 * i + 2] + xv[4 * i + 3]);
        }
        if (MODE == 2) {
            const float mw = add32(add16(sum)) * (1.f / 80.f);
            float s2 = 0.f;
#pragma unroll
            for (int i = 0; i < 20; ++i) { const float dd = xv[i] - mw; s2 += dd * dd; }
            s2 = add32(add16(s2));
            if (g == 0) *(f32x2*)(ws + OFF_XS + ((size_t)s * 8 + dt) * 8) = f32x2{mw, s2};
        }
    }
    SST(3); SST_FLUSHK(ws, MODE == 1 ? (out ? 8 : 3) : (MODE == 2 ? 4 : 9));
}

typedef __attribute__((address_space(1))) unsigned gu32;
constexpr int AUXL = 16;
#define LD_SC1(off) __builtin_bit_cast(bf16x8, __builtin_amdgcn_raw_buffer_load_b128(rs, (int)(off), 0, 16))
DI void edge_publish(gu32* cnt) {
    RAW_BARRIER();
    if (threadIdx.x == 0) __hip_atomic_fetch_add(cnt, 1u, __ATOMIC_RELAXED, __HIP_MEMORY_SCOPE_AGENT);
}
DI void edge_wait(gu32* cnt, unsigned want) {
    if (threadIdx.x == 0) {
        for (int spin = 0; spin < (1 << 20); ++spin) {
            if (__hip_atomic_load(cnt, __ATOMIC_RELAXED, __HIP_MEMORY_SCOPE_AGENT) >= want) break;
            __builtin_amdgcn_s_sleep(1);
        }
        __builtin_amdgcn_fence(__ATOMIC_ACQUIRE, "wavefront");
    }
    RAW_BARRIER();
}
#define W1_A 6
#define W2_FIRST 7
constexpr int MLP_LDS = 73728;
template <bool R1>
__global__ __launch_bounds__(256, 1) void k_mlp(char* __restrict__ ws, const u16* __restrict__ xin, const u16* __restrict__ b1,
                                                const u16* __restrict__ b2, u16* __restrict__ out) {
    extern __shared__ __attribute__((aligned(16))) char lds_raw[];
    char* L = lds_raw;
    const int tid = threadIdx.x, lane = tid & 63, w = __builtin_amdgcn_readfirstlane(tid >> 6);
    const int b = blockIdx.x, slot = b >> 3, tb = ((b & 7) >> 1) * 8 + (slot >> 2), cg = (b & 1) * 4 + (slot & 3);
    const int ot1 = ((b & 7) >> 1) * 7 + (slot >> 2);
    const bool act1 = slot < 28 && ot1 < 27;
    const int n16 = lane & 15, g = lane >> 4;
    const __amdgpu_buffer_rsrc_t rs = __builtin_amdgcn_make_buffer_rsrc((void*)ws, 0, (int)WS_NEED, 0x00020000);
    gu32* cnt = (gu32*)(ws + OFF_CNT);
    SST_DECL; SST(0);
    f32x4 acc[4][5];
#define ACC_ZERO() do { _Pragma("unroll") for (int i_ = 0; i_ < 4; ++i_) _Pragma("unroll") for (int j_ = 0; j_ < 5; ++j_) acc[i_][j_] = f32x4{0.f, 0.f, 0.f, 0.f}; } while (0)
#define ACC_REDUCE() do { \
    _Pragma("unroll") for (int j_ = 1; j_ < 4; ++j_) { const int o_ = (w + j_) & 3; \
        _Pragma("unroll") for (int i_ = 0; i_ < 5; ++i_) *(f32x4*)(L + (((o_ * 3 + (j_ - 1)) * 5 + i_) * 64 + lane) * 16) = acc[j_][i_]; } \
    RAW_BARRIER(); \
    _Pragma("unroll") for (int j_ = 0; j_ < 3; ++j_) _Pragma("unroll") for (int i_ = 0; i_ < 5; ++i_) \
        acc[1 + j_][i_] = *(const f32x4*)(L + (((w * 3 + j_) * 5 + i_) * 64 + lane) * 16); \
    SBAR(); \
    _Pragma("unroll") for (int i_ = 0; i_ < 5; ++i_) acc[0][i_] = (acc[0][i_] + acc[1][i_]) + (acc[2][i_] + acc[3][i_]); } while (0)
    const int tok = tb * 64 + 16 * w + n16;
    bf16x8 wr[16][4];
    int ro1[4], ro2[4];
#pragma unroll
    for (int j = 0; j < 4; ++j) { ro1[j] = ((j + w) & 3) * 64 * 64; ro2[j] = ((j + w) & 3) * 54 * 64; }
    const bf16x8* pw1 = (const bf16x8*)(ws + OFF_W1F) + ((size_t)(4 * (act1 ? ot1 : 0)) * 64 + 16 * w) * 64 + lane;
    const int k2 = w < 2 ? 14 * w : 28 + 13 * (w - 2);
    const bf16x8* pw2 = (const bf16x8*)(ws + OFF_W2F) + ((size_t)(4 * tb) * 54 + k2) * 64 + lane;
#define W1_LD(lo, hi) do { _Pragma("unroll") for (int s_ = (lo); s_ < (hi); ++s_) _Pragma("unroll") for (int j_ = 0; j_ < 4; ++j_) wr[s_][j_] = pw1[ro1[j_] + s_ * 64]; SBAR(); } while (0)
#define W2_LD(lo, hi) do { _Pragma("unroll") for (int s_ = (lo); s_ < (hi); ++s_) _Pragma("unroll") for (int j_ = 0; j_ < 4; ++j_) wr[s_][j_] = pw2[ro2[j_] + s_ * 64]; SBAR(); } while (0)

    u32x2 x1o[5], bb; float bs;
    {
        ACC_ZERO();
        u32x2 xi[5];
        const bf16x8* xb = (const bf16x8*)(ws + OFF_O) + (size_t)(tb * 4 * 20 + 5 * w) * 64 + lane;
        const bf16x8* wb = (const bf16x8*)(ws + OFF_WOF) + (size_t)(5 * cg * 20 + 5 * w) * 64 + lane;
        bf16x8 xf[5][4], wf[5][5];
        WO_LD(0); WO_LD(1); WO_LD(2); WO_LD(3);
#pragma unroll
        for (int ks = 0; ks < 5; ++ks) {
#pragma unroll
            for (int nt = 0; nt < 5; ++nt)
#pragma unroll
                for (int i = 0; i < 4; ++i) acc[i][nt] = MFMA16_32(wf[ks][nt], xf[ks][i], acc[i][nt]);
            SBAR();
            if (ks == 0) {
                WO_LD(4);
#pragma unroll
                for (int nt = 0; nt < 5; ++nt) {
                    const int d = 80 * cg + 16 * nt + 4 * g;
                    if (!R1) xi[nt] = *(const u32x2*)(xin + ((size_t)((tok >> 4) * 20 + (d >> 5)) * 64 + ((d & 31) >> 3) * 16 + n16) * 8 + (d & 7));
                    else xi[nt] = *(const u32x2*)(xin + (size_t)tok * 640 + d);
                }
                bb = *(const u32x2*)(b1 + 64 * (act1 ? ot1 : 0) + 16 * w + 4 * g);
                bs = bf2f(b2[tok]);
                SBAR();
            }
            if (ks == 1) W1_LD(0, 2);
            if (ks == 2) W1_LD(2, 4);
            if (ks == 3) W1_LD(4, 6);
            if (ks == 4) W1_LD(6, W1_A);
        }
        SST(1);
        ACC_REDUCE();
        float xv[20], s = 0.f;
#pragma unroll
        for (int nt = 0; nt < 5; ++nt) {
            const unsigned u0 = pk2(bf_lo(xi[nt][0]) + acc[0][nt][0], bf_hi(xi[nt][0]) + acc[0][nt][1]);
            const unsigned u1 = pk2(bf_lo(xi[nt][1]) + acc[0][nt][2], bf_hi(xi[nt][1]) + acc[0][nt][3]);
            x1o[nt] = u32x2{u0, u1};
            xv[4 * nt] = bf_lo(u0); xv[4 * nt + 1] = bf_hi(u0); xv[4 * nt + 2] = bf_lo(u1); xv[4 * nt + 3] = bf_hi(u1);
            s += (xv[4 * nt] + xv[4 * nt + 1]) + (xv[4 * nt + 2] + xv[4 * nt + 3]);
        }
        const float mw = add32(add16(s)) * (1.f / 80.f);
        float s2 = 0.f;
#pragma unroll
        for (int i = 0; i < 20; ++i) { const float dd = xv[i] - mw; s2 += dd * dd; }
        s2 = add32(add16(s2));
        gu64* gp = (gu64*)(ws + OFF_XG) + (size_t)tok * 8;
        if (g == 0) __hip_atomic_store(gp + cg, ((unsigned long long)(__float_as_uint(s2) | 0x80000000u) << 32) | __float_as_uint(mw),
                                       __ATOMIC_RELAXED, __HIP_MEMORY_SCOPE_AGENT);
        SBAR();
        SST(2);
        unsigned long long v0, v1;
        for (int spin = 0; spin < (1 << 20); ++spin) {
            v0 = __hip_atomic_load(gp + 2 * g, __ATOMIC_RELAXED, __HIP_MEMORY_SCOPE_AGENT);
            v1 = __hip_atomic_load(gp + 2 * g + 1, __ATOMIC_RELAXED, __HIP_MEMORY_SCOPE_AGENT);
            if (__all((long long)(v0 & v1) < 0)) break;
            __builtin_amdgcn_s_sleep(4);
        }
        SST(3);
        const float m0 = __uint_as_float((unsigned)v0), m1 = __uint_as_float((unsigned)v1);
        const float mean = add32(add16(m0 + m1)) * 0.125f;
        const float d0 = m0 - mean, d1 = m1 - mean;
        const float var = add32(add16(__uint_as_float((unsigned)(v0 >> 32) & 0x7fffffffu) + __uint_as_float((unsigned)(v1 >> 32) & 0x7fffffffu) +
                                      80.f * (d0 * d0 + d1 * d1))) * (1.f / 640.f);
        float shf, r2; ln2_scalars(mean, var, shf, r2);
        char* hb = L + w * 15360;
#pragma unroll
        for (int nt = 0; nt < 5; ++nt)
            *(u32x2*)(hb + n16 * 176 + (16 * nt + 4 * g) * 2) =
                u32x2{pk2((xv[4 * nt] - shf) * r2, (xv[4 * nt + 1] - shf) * r2), pk2((xv[4 * nt + 2] - shf) * r2, (xv[4 * nt + 3] - shf) * r2)};
        asm volatile("s_waitcnt lgkmcnt(0)" ::: "memory");
        const int q4 = (lane & 15) >> 2, p4 = lane & 3;
        lds_cptr hr = (lds_cptr)(lds_ptr)L + w * 15360 + (8 * (g & 1) + q4) * 176 + p4 * 8 + (g >> 1) * 32;
        const int s0 = tb * 64 + 16 * w + 8 * (g & 1);
        const int hoff = (int)OFF_H + ((5 * cg + (g >> 1)) * 64 + (s0 >> 5)) * 1024 + (((s0 & 31) >> 3) * 16 + n16) * 16;
        const bf16x8 f0 = cat8(tr_read(hr), tr_read(hr + 704));
        const bf16x8 f1 = cat8(tr_read(hr + 64), tr_read(hr + 704 + 64));
        const bf16x8 f2 = cat8(tr_read(hr + (g < 2 ? 128 : 0)), tr_read(hr + 704 + (g < 2 ? 128 : 0)));
        __builtin_amdgcn_raw_buffer_store_b128(__builtin_bit_cast(u32x4, f0), rs, hoff, 0, 16);
        __builtin_amdgcn_raw_buffer_store_b128(__builtin_bit_cast(u32x4, f1), rs, hoff + 2 * 65536, 0, 16);
        if (g < 2) __builtin_amdgcn_raw_buffer_store_b128(__builtin_bit_cast(u32x4, f2), rs, hoff + 4 * 65536, 0, 16);
        SBAR();
        W1_LD(W1_A, W1_A + 4);
        VMCNT(16);
        SST(4);
        edge_publish(cnt + cg * 64);
    }
    if (act1) {
        W1_LD(W1_A + 4, 16);
        edge_wait(cnt + cg * 64, 32);
        SST(5);
        ACC_ZERO();
        const int ho = (int)OFF_H + ((5 * cg) * 64 + 16 * w) * 1024 + lane * 16;
        constexpr int PF = 6;
        bf16x8 rf[PF][5];
#pragma unroll
        for (int s = 0; s < PF; ++s) {
#pragma unroll
            for (int i = 0; i < 5; ++i) rf[s][i] = LD_SC1(ho + i * 65536 + s * 1024);
            SBAR();
        }
#pragma unroll
        for (int s = 0; s < 16; ++s) {
#pragma unroll
            for (int j = 0; j < 4; ++j)
#pragma unroll
                for (int i = 0; i < 5; ++i) acc[j][i] = MFMA16_32(wr[s][j], rf[s % PF][i], acc[j][i]);
            SBAR();
            if (s + PF < 16) {
#pragma unroll
                for (int i = 0; i < 5; ++i) rf[s % PF][i] = LD_SC1(ho + i * 65536 + (s + PF) * 1024);
                SBAR();
            }
            if (s >= W2_FIRST && s - W2_FIRST < 9) W2_LD(s - W2_FIRST, s - W2_FIRST + 1);
        }
        SST(6);
        ACC_REDUCE();
        const int f = 64 * ot1 + 16 * w + 4 * g;
        const float bv[4] = {bf_lo(bb[0]), bf_hi(bb[0]), bf_lo(bb[1]), bf_hi(bb[1])};
        const int uo = (int)OFF_U + ((5 * cg) * 54 + (f >> 5)) * 1024 + (((f & 31) >> 3) * 16 + n16) * 16 + (f & 7) * 2;
#pragma unroll
        for (int i = 0; i < 5; ++i) {
            float r[4];
#pragma unroll
            for (int e = 0; e < 4; ++e) {
                const float u = acc[0][i][e] + bv[e];
                const float z = u * (1.f + 0.044715f * u * u) * (-2.f * 0.7978845608028654f * 1.4426950408889634f);
                r[e] = u * __builtin_amdgcn_rcpf(1.f + EX2(z));
            }
            __builtin_amdgcn_raw_buffer_store_b64(u32x2{pk2(r[0], r[1]), pk2(r[2], r[3])}, rs, uo + i * 54 * 1024, 0, 16);
        }
        SBAR();
        W2_LD(9, 13);
        VMCNT(16);
        SST(7);
        edge_publish(cnt + (8 + cg) * 64);
    } else {
        W2_LD(0, 13);
    }
    {
        if (w < 2) W2_LD(13, 14);
        edge_wait(cnt + (8 + cg) * 64, 27);
        SST(8);
        ACC_ZERO();
        const int uo = (int)OFF_U + ((5 * cg) * 54 + k2) * 1024 + lane * 16;
        constexpr int PF = 6;
        bf16x8 rf[PF][5];
#pragma unroll
        for (int s = 0; s < PF; ++s) {
#pragma unroll
            for (int i = 0; i < 5; ++i) rf[s][i] = LD_SC1(uo + i * 54 * 1024 + s * 1024);
            SBAR();
        }
#pragma unroll
        for (int s = 0; s < 14; ++s) {
            if (s < 13 || w < 2) {
#pragma unroll
                for (int j = 0; j < 4; ++j)
#pragma unroll
                    for (int i = 0; i < 5; ++i) acc[j][i] = MFMA16_32(rf[s % PF][i], wr[s][j], acc[j][i]);
            }
            SBAR();
            if (s + PF < 13 || (s + PF == 13 && w < 2)) {
#pragma unroll
                for (int i = 0; i < 5; ++i) rf[s % PF][i] = LD_SC1(uo + i * 54 * 1024 + (s + PF) * 1024);
                SBAR();
            }
        }
        SST(9);
        ACC_REDUCE();
        float xv[20], sum = 0.f;
#pragma unroll
        for (int i = 0; i < 5; ++i) {
            const int d = 80 * cg + 16 * i + 4 * g;
            const unsigned u0 = pk2(bf_lo(x1o[i][0]) + acc[0][i][0] + bs, bf_hi(x1o[i][0]) + acc[0][i][1] + bs);
            const unsigned u1 = pk2(bf_lo(x1o[i][1]) + acc[0][i][2] + bs, bf_hi(x1o[i][1]) + acc[0][i][3] + bs);
            if (R1) __builtin_amdgcn_raw_buffer_store_b64(u32x2{u0, u1}, rs,
                        (int)OFF_X2 + ((4 * tb + w) * 20 + (d >> 5)) * 1024 + (((d & 31) >> 3) * 16 + n16) * 16 + (d & 7) * 2, 0, 16);
            else *(u32x2*)(out + (size_t)tok * 640 + d) = u32x2{u0, u1};
            xv[4 * i] = bf_lo(u0); xv[4 * i + 1] = bf_hi(u0); xv[4 * i + 2] = bf_lo(u1); xv[4 * i + 3] = bf_hi(u1);
            sum += (xv[4 * i] + xv[4 * i + 1]) + (xv[4 * i + 2] + xv[4 * i + 3]);
        }
        if (R1) {
            const float mw = add32(add16(sum)) * (1.f / 80.f);
            float s2 = 0.f;
#pragma unroll
            for (int i = 0; i < 20; ++i) { const float dd = xv[i] - mw; s2 += dd * dd; }
            s2 = add32(add16(s2));
            if (g == 0) __builtin_amdgcn_raw_buffer_store_b64(u32x2{__float_as_uint(mw), __float_as_uint(s2)}, rs, (int)OFF_XS + ((tb * 8 + cg) * 64 + 16 * w + n16) * 8, 0, 16);
        }
    }
    if (R1) {
        const int mt0 = cg >> 1, isv = cg & 1;
        const bf16x8* wb = (const bf16x8*)(ws + OFF_WQF) + (size_t)(5 * cg * 20 + 5 * w) * 64 + lane;
        const bf16x8* kb = (const bf16x8*)(ws + OFF_WQF) + (size_t)((40 + 2 * isv) * 20 + 5 * w) * 64 + lane;
        bf16x8 wf[5][5], wkf[5][2], xf[5][4], xk[5];
        SBAR();
#pragma unroll
        for (int i = 0; i < 16; ++i) wf[i / 5][i % 5] = wb[((i % 5) * 20 + i / 5) * 64];
        SBAR();
        VMCNT(16);
        SST(10);
        edge_publish(cnt + (16 + tb) * 64);
#pragma unroll
        for (int i = 16; i < 25; ++i) wf[i / 5][i % 5] = wb[((i % 5) * 20 + i / 5) * 64];
#pragma unroll
        for (int ks = 0; ks < 5; ++ks) { wkf[ks][0] = kb[ks * 64]; wkf[ks][1] = kb[(20 + ks) * 64]; }
        f32x4 csq[5], cs0, cs1, stq[4], stk[4], acck[2];
        {
            const float* csp = (const float*)(ws + OFF_CS);
#pragma unroll
            for (int nt = 0; nt < 5; ++nt) csq[nt] = *(const f32x4*)(csp + 80 * cg + 16 * nt + 4 * g);
            cs0 = *(const f32x4*)(csp + 640 + 32 * isv + 4 * g); cs1 = *(const f32x4*)(csp + 640 + 32 * isv + 16);
        }
        SBAR();
        edge_wait(cnt + (16 + tb) * 64, 8);
        SST(11);
        const int xo = (int)OFF_X2 + (tb * 4 * 20 + 5 * w) * 1024 + lane * 16;
#pragma unroll
        for (int ks = 0; ks < 5; ++ks) {
#pragma unroll
            for (int i = 0; i < 4; ++i) xf[ks][i] = LD_SC1(xo + (((i + w) & 3) * 20 + ks) * 1024);
            xk[ks] = LD_SC1(xo + (mt0 * 20 + ks) * 1024);
            SBAR();
        }
#pragma unroll
        for (int i = 0; i < 4; ++i) {
            const u32x2 a0 = __builtin_amdgcn_raw_buffer_load_b64(rs, (int)OFF_XS + ((tb * 8 + 2 * i) * 64 + 16 * w + n16) * 8, 0, AUXL);
            const u32x2 a1 = __builtin_amdgcn_raw_buffer_load_b64(rs, (int)OFF_XS + ((tb * 8 + 2 * i + 1) * 64 + 16 * w + n16) * 8, 0, AUXL);
            const u32x2 c0 = __builtin_amdgcn_raw_buffer_load_b64(rs, (int)OFF_XS + ((tb * 8 + 2 * i) * 64 + 16 * mt0 + n16) * 8, 0, AUXL);
            const u32x2 c1 = __builtin_amdgcn_raw_buffer_load_b64(rs, (int)OFF_XS + ((tb * 8 + 2 * i + 1) * 64 + 16 * mt0 + n16) * 8, 0, AUXL);
            stq[i] = __builtin_bit_cast(f32x4, u32x4{a0[0], a0[1], a1[0], a1[1]});
            stk[i] = __builtin_bit_cast(f32x4, u32x4{c0[0], c0[1], c1[0], c1[1]});
        }
        SBAR();
        ACC_ZERO();
        acck[0] = f32x4{0.f, 0.f, 0.f, 0.f}; acck[1] = acck[0];
#pragma unroll
        for (int ks = 0; ks < 5; ++ks) {
#pragma unroll
            for (int nt = 0; nt < 5; ++nt)
#pragma unroll
                for (int i = 0; i < 4; ++i) acc[i][nt] = MFMA16_32(wf[ks][nt], xf[ks][i], acc[i][nt]);
            acck[0] = MFMA16_32(wkf[ks][0], xk[ks], acck[0]);
            acck[1] = MFMA16_32(wkf[ks][1], xk[ks], acck[1]);
            SBAR();
        }
        SST(12);
        const float z4[4] = {0.f, 0.f, 0.f, 0.f}, z5[5] = {0.f, 0.f, 0.f, 0.f, 0.f}, z2[2] = {0.f, 0.f};
        qkv_finish<true>(ws, L, tb, cg, w, lane, acc, acck, csq, cs0, cs1, stq, stk, z4, z4, z5, z2);
    }
    SST_FLUSHK(ws, R1 ? 2 : 7);
}

template <int N> __global__ void k_end() {}

extern "C" void kernel_launch(void* const* d_in, const int* in_sizes, int n_in, void* d_out, int out_size, void* d_ws,
                              size_t ws_size, hipStream_t stream) {
    const u16* x = (const u16*)d_in[0];
    const u16* wq = (const u16*)d_in[1];
    const u16* wk = (const u16*)d_in[2];
    const u16* wv = (const u16*)d_in[3];
    const u16* wo = (const u16*)d_in[4];
    const u16* w1 = (const u16*)d_in[6];
    const u16* b1 = (const u16*)d_in[7];
    const u16* w2 = (const u16*)d_in[8];
    const u16* b2 = (const u16*)d_in[9];
    char* ws = (char*)d_ws;
    u16* x2 = (u16*)(ws + OFF_X2);
    (void)hipFuncSetAttribute((const void*)k_qkv<false>, hipFuncAttributeMaxDynamicSharedMemorySize, QKV_LDS);
    (void)hipFuncSetAttribute((const void*)k_attn<true>, hipFuncAttributeMaxDynamicSharedMemorySize, ATT_LDS);
    (void)hipFuncSetAttribute((const void*)k_attn<false>, hipFuncAttributeMaxDynamicSharedMemorySize, ATT_LDS);
    (void)hipFuncSetAttribute((const void*)k_mlp<true>, hipFuncAttributeMaxDynamicSharedMemorySize, MLP_LDS);
    (void)hipFuncSetAttribute((const void*)k_mlp<false>, hipFuncAttributeMaxDynamicSharedMemorySize, MLP_LDS);
    (void)hipFuncSetAttribute((const void*)k_end<0>, hipFuncAttributeMaxDynamicSharedMemorySize, 0);
    k_qkv<false><<<256, 256, QKV_LDS, stream>>>(x, wq, wk, wv, wo, ws, w1, w2);
    k_attn<true><<<256, 512, ATT_LDS, stream>>>(ws, w1, w2, wo, wq);
    k_mlp<true><<<256, 256, MLP_LDS, stream>>>(ws, x, b1, b2, nullptr);
    k_attn<false><<<256, 512, ATT_LDS, stream>>>(ws, w1, w2, wo, wq);
    k_mlp<false><<<256, 256, MLP_LDS, stream>>>(ws, x2, b1, b2, (u16*)d_out);
}
```

```cpp
#include <hip/hip_runtime.h>
#include <hip/hip_bf16.h>
#include <stdint.h>


typedef unsigned short u16;
typedef short bf16x8 __attribute__((ext_vector_type(8)));
typedef short bf16x4 __attribute__((ext_vector_type(4)));
typedef float f32x16 __attribute__((ext_vector_type(16)));
typedef float f32x4 __attribute__((ext_vector_type(4)));
typedef float f32x2 __attribute__((ext_vector_type(2)));
typedef unsigned u32x4 __attribute__((ext_vector_type(4)));
typedef unsigned u32x2 __attribute__((ext_vector_type(2)));
typedef __bf16 bf2_t __attribute__((ext_vector_type(2)));
typedef __attribute__((address_space(3))) char* lds_ptr;
typedef __attribute__((address_space(3))) const char* lds_cptr;
typedef __attribute__((address_space(1))) unsigned long long gu64;

#define DI __device__ __forceinline__
#define SBAR() __builtin_amdgcn_sched_barrier(0)
#define PIN(x) asm volatile("" : "+v"(x))
#define MFMA32_16(a, b, c) __builtin_amdgcn_mfma_f32_32x32x16_bf16(a, b, c, 0, 0, 0)
#define MFMA32_8(a, b, c) __builtin_amdgcn_mfma_f32_32x32x8bf16_1k(a, b, c, 0, 0, 0)
#define MFMA16_32(a, b, c) __builtin_amdgcn_mfma_f32_16x16x32_bf16(a, b, c, 0, 0, 0)
#define EX2(x) __builtin_amdgcn_exp2f(x)
#define RAW_BARRIER() asm volatile("s_waitcnt lgkmcnt(0)\n\ts_barrier" ::: "memory")
#define VMCNT(n) asm volatile("s_waitcnt vmcnt(%0)" :: "i"(n) : "memory")

#define PREP_STORE(p, v) asm volatile("global_store_dwordx4 %0, %1, off sc1\n\ts_nop 1" :: "v"(p), "v"(v) : "memory")
#define KADDR(dst, sym) asm volatile("s_getpc_b64 s[40:41]\n\ts_add_u32 s40, s40, " sym "@rel32@lo+4\n\ts_addc_u32 s41, s41, " sym "@rel32@hi+12\n\ts_mov_b64 %0, s[40:41]" : "=s"(dst) :: "s40", "s41", "scc")
constexpr int S_ = 2048, D_ = 640, F_ = 1728;
constexpr float LN_EPS = 1e-5f;
constexpr float QSCALE = 0.22360679774997896f * 1.4426950408889634f;
constexpr float SAFE_BOUND = 48.0f;

DI unsigned pk2(float a, float b) { f32x2 v = {a, b}; bf2_t r = __builtin_convertvector(v, bf2_t); return __builtin_bit_cast(unsigned, r); }
DI float bf_lo(unsigned u) { return __uint_as_float(u << 16); }
DI float bf_hi(unsigned u) { return __uint_as_float(u & 0xffff0000u); }
DI float bf2f(u16 v) { return __uint_as_float((unsigned)v << 16); }
DI float dot2(unsigned a, unsigned b, float c) { return __builtin_amdgcn_fdot2_f32_bf16(__builtin_bit_cast(bf2_t, a), __builtin_bit_cast(bf2_t, b), c, false); }
DI bf16x4 tr_read(lds_cptr p) { return __builtin_amdgcn_ds_read_tr16_b64_v4i16((__attribute__((address_space(3))) bf16x4*)p); }
DI bf16x8 cat8(bf16x4 a, bf16x4 b) { return __builtin_shufflevector(a, b, 0, 1, 2, 3, 4, 5, 6, 7); }
DI void glds16(const void* g, lds_ptr l) {
    unsigned keep;
    asm volatile("s_mov_b32 %0, m0\n\ts_mov_b32 m0, %2\n\ts_nop 0\n\tglobal_load_lds_dwordx4 %1, off\n\ts_mov_b32 m0, %0"
                 : "=&s"(keep) : "v"(g), "s"((unsigned)(uintptr_t)l) : "memory");
}
DI float xor32(float v) { return __shfl_xor(v, 32); }
DI float add32(float v) {
    auto r = __builtin_amdgcn_permlane32_swap(__float_as_uint(v), __float_as_uint(v), false, false);
    return __uint_as_float(r[0]) + __uint_as_float(r[1]);
}
DI float add16(float v) {
    auto r = __builtin_amdgcn_permlane16_swap(__float_as_uint(v), __float_as_uint(v), false, false);
    return __uint_as_float(r[0]) + __uint_as_float(r[1]);
}
DI float max16x(float v) { auto r = __builtin_amdgcn_permlane16_swap(__float_as_uint(v), __float_as_uint(v), false, false); return fmaxf(__uint_as_float(r[0]), __uint_as_float(r[1])); }
DI float max32x(float v) { auto r = __builtin_amdgcn_permlane32_swap(__float_as_uint(v), __float_as_uint(v), false, false); return fmaxf(__uint_as_float(r[0]), __uint_as_float(r[1])); }
template <int CTRL> DI float dppf(float v) { return __int_as_float(__builtin_amdgcn_update_dpp(0, __float_as_int(v), CTRL, 0xf, 0xf, false)); }
DI float rowmax16(float v) {
    v = fmaxf(v, dppf<0x128>(v)); v = fmaxf(v, dppf<0x124>(v)); v = fmaxf(v, dppf<0x122>(v)); v = fmaxf(v, dppf<0x121>(v)); return v;
}
DI float wavemax(float v) { return max32x(max16x(rowmax16(v))); }

constexpr size_t OFF_Q = 0;
constexpr size_t OFF_K1 = OFF_Q + (size_t)2048 * 32 * 48;
constexpr size_t OFF_K2 = OFF_K1 + 2048 * 32;
constexpr size_t OFF_V = OFF_K2 + 2048 * 8;
constexpr size_t OFF_KN = OFF_V + 128 * 640;
constexpr size_t OFF_QN = OFF_KN + 512;
constexpr size_t OFF_CS = OFF_QN + 4096;
constexpr size_t OFF_CONST = OFF_CS + 4096;
constexpr size_t OFF_CNT = OFF_CONST + 256;
constexpr size_t OFF_XG = OFF_CNT + 48 * 256;
constexpr size_t OFF_XS = OFF_XG + 131072;
constexpr size_t OFF_H = OFF_XS + 131072 + 256;
constexpr size_t OFF_U = OFF_H + (size_t)S_ * D_ * 2;
constexpr size_t OFF_X2 = OFF_U + (size_t)D_ * F_ * 2;
constexpr size_t OFF_O = OFF_X2 + (size_t)S_ * D_ * 2;
constexpr size_t OFF_W1F = OFF_O + (size_t)S_ * D_ * 2;
constexpr size_t OFF_W2F = OFF_W1F + (size_t)S_ * F_ * 2;
constexpr size_t OFF_WQF = OFF_W2F + (size_t)S_ * F_ * 2;
constexpr size_t OFF_WOF = OFF_WQF + (size_t)44 * 20 * 1024;
static_assert(OFF_H % 1024 == 0, "fragment buffers are 1 KiB aligned");
constexpr size_t WS_NEED = OFF_WOF + (size_t)800 * 1024;
constexpr int NPREP = 1728 + 1728 + 200 + 200;

DI float rbf(float v) { return bf_lo(pk2(v, 0.f)); }
DI float ln_sd(float var) { return rbf(__builtin_amdgcn_sqrtf(rbf(rbf(var) + LN_EPS))); }
DI void ln2_scalars(float mu, float var, float& shift, float& scale) {
    const float m1 = rbf(mu), sd1 = ln_sd(var), i1 = 1.0f / sd1;
    const float m2 = rbf((mu - m1) * i1), sd2 = ln_sd(var * i1 * i1);
    shift = m1 + m2 * sd1; scale = i1 / sd2;
}
DI void chan8(const f32x4 (&st)[4], float& mean, float& var) {
    float m = 0.f, m2 = 0.f;
#pragma unroll
    for (int i = 0; i < 4; ++i) { m += st[i][0] + st[i][2]; m2 += st[i][1] + st[i][3]; }
    m *= 0.125f;
#pragma unroll
    for (int i = 0; i < 4; ++i) { const float d0 = st[i][0] - m, d1 = st[i][2] - m; m2 += 80.f * (d0 * d0 + d1 * d1); }
    mean = m; var = m2 * (1.f / 640.f);
}

template <bool FAST>
DI void qkv_finish(char* __restrict__ ws, char* L, int tb, int cg, int w, int lane, f32x4 (&acc)[4][5], f32x4 (&acck)[2],
                   const f32x4 (&csq)[5], const f32x4& cs0, const f32x4& cs1, const f32x4 (&stq)[4], const f32x4 (&stk)[4],
                   const float (&sm)[4], const float (&sq)[4], const float (&cs)[5], const float (&csk)[2]) {
    const int n16 = lane & 15, g = lane >> 4, mt0 = cg >> 1, isv = cg & 1;
    constexpr int R_OFF = 0, PK_OFF = 61440, ST_OFF = 69632, CS_OFF = 71680;
#pragma unroll
    for (int j = 1; j < 4; ++j) {
        const int o = (w + j) & 3;
#pragma unroll
        for (int nt = 0; nt < 5; ++nt) *(f32x4*)(L + R_OFF + (((o * 3 + (j - 1)) * 5 + nt) * 64 + lane) * 16) = acc[j][nt];
    }
    *(f32x4*)(L + PK_OFF + ((w * 2 + 0) * 64 + lane) * 16) = acck[0];
    *(f32x4*)(L + PK_OFF + ((w * 2 + 1) * 64 + lane) * 16) = acck[1];
    if (!FAST && g == 0) {
#pragma unroll
        for (int i = 0; i < 4; ++i) *(f32x2*)(L + ST_OFF + (w * 64 + ((i + w) & 3) * 16 + n16) * 8) = f32x2{sm[i], sq[i]};
#pragma unroll
        for (int nt = 0; nt < 5; ++nt) *(float*)(L + CS_OFF + w * 448 + (nt * 16 + n16) * 4) = cs[nt];
#pragma unroll
        for (int nt = 0; nt < 2; ++nt) *(float*)(L + CS_OFF + w * 448 + (80 + nt * 16 + n16) * 4) = csk[nt];
    }
    char* qrow = ws + OFF_Q + (size_t)(tb * 8 + cg) * 12288 + (16 * w + n16) * 48;
    int qoff[5];
#pragma unroll
    for (int nt = 0; nt < 5; ++nt) { const int G = 4 * nt + g, hl = G / 5; qoff[nt] = hl * 3072 + (G - 5 * hl) * 8; PIN(qoff[nt]); }
    RAW_BARRIER();
    auto stats = [&](int tl, const f32x4 (&st)[4], float& mean, float& rstd) {
        float var;
        if (FAST) chan8(st, mean, var);
        else {
            float s = 0.f, s2 = 0.f;
#pragma unroll
            for (int ww = 0; ww < 4; ++ww) { f32x2 v = *(const f32x2*)(L + ST_OFF + (ww * 64 + tl) * 8); s += v[0]; s2 += v[1]; }
            mean = s * (1.f / 640.f);
            var = fmaxf(s2 * (1.f / 640.f) - mean * mean, 0.f);
        }
        mean = rbf(mean);
        rstd = 1.0f / ln_sd(var);
    };
    {
        f32x4 rr[3][5], cc[4][5];
#pragma unroll
        for (int j = 0; j < 3; ++j)
#pragma unroll
            for (int nt = 0; nt < 5; ++nt) rr[j][nt] = *(const f32x4*)(L + R_OFF + (((w * 3 + j) * 5 + nt) * 64 + lane) * 16);
        if (!FAST) {
#pragma unroll
            for (int ww = 0; ww < 4; ++ww)
#pragma unroll
                for (int nt = 0; nt < 5; ++nt) cc[ww][nt] = *(const f32x4*)(L + CS_OFF + ww * 448 + (nt * 16 + 4 * g) * 4);
        }
        float mean, rstd; stats(16 * w + n16, stq, mean, rstd);
        SBAR();
        const float sc = rstd * QSCALE;
        float n2[5];
#pragma unroll
        for (int nt = 0; nt < 5; ++nt) {
            const f32x4 a = (acc[0][nt] + rr[0][nt]) + (rr[1][nt] + rr[2][nt]);
            f32x4 s;
            if (FAST) s = csq[nt];
            else {
                s = (cc[0][nt] + cc[1][nt]) + (cc[2][nt] + cc[3][nt]);
                if (tb == 0 && w == 0 && n16 == 0) *(f32x4*)(ws + OFF_CS + (80 * cg + 16 * nt + 4 * g) * 4) = s;
            }
            const unsigned u0 = pk2((a[0] - mean * s[0]) * sc, (a[1] - mean * s[1]) * sc);
            const unsigned u1 = pk2((a[2] - mean * s[2]) * sc, (a[3] - mean * s[3]) * sc);
            *(u32x2*)(qrow + qoff[nt]) = u32x2{u0, u1};
            n2[nt] = dot2(u1, u1, dot2(u0, u0, 0.f));
        }
        float hn[4];
        hn[0] = n2[0] + (g == 0 ? n2[1] : 0.f);
        hn[1] = (g > 0 ? n2[1] : 0.f) + (g < 2 ? n2[2] : 0.f);
        hn[2] = (g > 1 ? n2[2] : 0.f) + (g < 3 ? n2[3] : 0.f);
        hn[3] = (g == 3 ? n2[3] : 0.f) + n2[4];
#pragma unroll
        for (int hh = 0; hh < 4; ++hh) hn[hh] = add32(add16(hn[hh]));
        const float n2a = (g & 1) ? hn[1] : hn[0], n2b = (g & 1) ? hn[3] : hn[2];
        const unsigned qb = pk2(__builtin_amdgcn_sqrtf((g & 2) ? n2b : n2a), 0.f);
        *(u32x2*)(qrow + g * 3072 + 40) = u32x2{qb, 0u};
        float m = bf_lo(qb);
        if (!(m == m)) m = INFINITY;
        m = wavemax(m);
        if (lane == 0) *(float*)(ws + OFF_QN + ((tb * 8 + cg) * 4 + w) * 4) = m;
    }
    if (w == 3) {
        const int t16 = tb * 4 + mt0;
        float mean, rstd; stats(mt0 * 16 + n16, stk, mean, rstd);
        f32x4 a0 = {0.f, 0.f, 0.f, 0.f}, a1 = a0, s0 = a0, s1 = a0;
#pragma unroll
        for (int ww = 0; ww < 4; ++ww) {
            a0 += *(const f32x4*)(L + PK_OFF + ((ww * 2 + 0) * 64 + lane) * 16);
            a1 += *(const f32x4*)(L + PK_OFF + ((ww * 2 + 1) * 64 + n16) * 16);
            if (!FAST) {
                s0 += *(const f32x4*)(L + CS_OFF + ww * 448 + (80 + 4 * g) * 4);
                s1 += *(const f32x4*)(L + CS_OFF + ww * 448 + 96 * 4);
            }
        }
        if (FAST) { s0 = cs0; s1 = cs1; }
        else if (tb == 0 && cg < 2 && n16 == 0) {
            *(f32x4*)(ws + OFF_CS + (640 + 32 * isv + 4 * g) * 4) = s0;
            if (g == 0) *(f32x4*)(ws + OFF_CS + (640 + 32 * isv + 16) * 4) = s1;
        }
        const unsigned u0 = pk2((a0[0] - mean * s0[0]) * rstd, (a0[1] - mean * s0[1]) * rstd);
        const unsigned u1 = pk2((a0[2] - mean * s0[2]) * rstd, (a0[3] - mean * s0[3]) * rstd);
        const unsigned u2 = pk2((a1[0] - mean * s1[0]) * rstd, (a1[1] - mean * s1[1]) * rstd);
        const unsigned u3 = pk2((a1[2] - mean * s1[2]) * rstd, (a1[3] - mean * s1[3]) * rstd);
        if (!isv) {
            const int key = t16 * 16 + n16;
            *(u32x2*)(ws + OFF_K1 + key * 32 + g * 8) = u32x2{u0, u1};
            if (g == 0) *(u32x2*)(ws + OFF_K2 + key * 8) = u32x2{u2, u3};
            float n2 = dot2(u1, u1, dot2(u0, u0, 0.f));
            n2 = add32(add16(n2));
            n2 = dot2(u3, u3, dot2(u2, u2, n2));
            if (!(n2 == n2)) n2 = INFINITY;
            n2 = rowmax16(n2);
            if (lane == 0) *(float*)(ws + OFF_KN + t16 * 4) = n2;
        } else {
            u16* vb = (u16*)(ws + OFF_V + t16 * 640 + (n16 >> 3) * 320) + (n16 & 7);
            vb[(4 * g + 0) * 8] = (u16)u0; vb[(4 * g + 1) * 8] = (u16)(u0 >> 16);
            vb[(4 * g + 2) * 8] = (u16)u1; vb[(4 * g + 3) * 8] = (u16)(u1 >> 16);
            if (g == 0) { vb[16 * 8] = (u16)u2; vb[17 * 8] = (u16)(u2 >> 16); vb[18 * 8] = (u16)u3; vb[19 * 8] = (u16)(u3 >> 16); }
        }
    }
    if (w == 0) *(u32x2*)(ws + OFF_XG + ((size_t)(tb * 64 + lane) * 8 + cg) * 8) = u32x2{0u, 0u};
}

constexpr int QKV_LDS = 163840;
__global__ __launch_bounds__(256, 1) void k_qkv(const u16* __restrict__ x, const u16* __restrict__ wq, const u16* __restrict__ wk,
                                                const u16* __restrict__ wv, char* __restrict__ ws) {
    extern __shared__ __attribute__((aligned(16))) char lds_raw[];
    lds_ptr lds = (lds_ptr)lds_raw;
    char* L = lds_raw;
    const int tid = threadIdx.x, lane = tid & 63, w = __builtin_amdgcn_readfirstlane(tid >> 6);
    const int b = blockIdx.x, slot = b >> 3, tb = ((b & 7) >> 1) * 8 + (slot >> 2), cg = (b & 1) * 4 + (slot & 3);
    const int mt0 = cg >> 1, isv = cg & 1;
    const int n16 = lane & 15, g = lane >> 4;
    unsigned code_touch = 0u;
    if (w == 2) {
        uint64_t a[5];
        KADDR(a[0], "_Z6k_attnILb1EEvPcPKtS2_S2_S2_S2_S2_"); KADDR(a[1], "_Z6k_attnILb0EEvPcPKtS2_S2_S2_S2_S2_");
        KADDR(a[2], "_Z5k_mlpILb1EEvPcPKtS2_S2_Pt"); KADDR(a[3], "_Z5k_mlpILb0EEvPcPKtS2_S2_Pt"); KADDR(a[4], "_Z5k_endILi0EEvv");
        uint64_t lo = a[0], hi = a[0];
#pragma unroll
        for (int i = 1; i < 5; ++i) { lo = a[i] < lo ? a[i] : lo; hi = a[i] > hi ? a[i] : hi; }
        const uint64_t pa = lo + (uint64_t)(slot * 64 + lane) * 128;
        if (pa < hi) code_touch = *(const __attribute__((address_space(1))) unsigned*)pa;
    }
    SBAR();
    f32x4 acc[4][5], acck[2];
#pragma unroll
    for (int i = 0; i < 4; ++i)
#pragma unroll
        for (int j = 0; j < 5; ++j) acc[i][j] = f32x4{0.f, 0.f, 0.f, 0.f};
    acck[0] = f32x4{0.f, 0.f, 0.f, 0.f}; acck[1] = acck[0];
    f32x4 csq[5], cs0, cs1, stq[4], stk[4];
    float sm[4] = {0.f, 0.f, 0.f, 0.f}, sq[4] = {0.f, 0.f, 0.f, 0.f};
    float cs[5] = {0.f, 0.f, 0.f, 0.f, 0.f}, csk[2] = {0.f, 0.f};
    {
        lds_ptr ra = lds + w * 40960, rb = ra + 20480;
        const char* xs = (const char*)(x + (size_t)(tb * 64) * 640 + 160 * w);
#pragma unroll
        for (int j = 0; j < 5; ++j) {
            const int c = 64 * j + lane;
            const unsigned xo = (unsigned)(c / 20) * 1280u + (unsigned)(c % 20) * 16u;
#pragma unroll
            for (int i = 0; i < 4; ++i) glds16(xs + (size_t)i * 20480 + xo, ra + i * 5120 + j * 1024);
        }
        const char* src = (const char*)(wq + (size_t)(160 * w) * 640 + 80 * cg);
        const unsigned loff = (unsigned)(lane / 10) * 1280u + (unsigned)(lane % 10) * 16u;
        const char* src2 = (const char*)((isv ? wv : wk) + (size_t)(160 * w) * 20);
#pragma unroll
        for (int i = 0; i < 16; ++i) glds16(src + (size_t)i * 7680 + loff, rb + i * 960);
#pragma unroll
        for (int i = 0; i < 3; ++i) glds16(src2 + i * 1024 + lane * 16, rb + 15488 + i * 1024);
        if (lane < 48) glds16(src2 + 3 * 1024 + lane * 16, rb + 15488 + 3 * 1024);
        VMCNT(20);
        bf16x8 xf[4][5], xk[5];
        {
            const char* xl = L + w * 40960 + n16 * 320 + g * 16;
#pragma unroll
            for (int i = 0; i < 4; ++i)
#pragma unroll
                for (int ks = 0; ks < 5; ++ks) xf[i][ks] = *(const bf16x8*)(xl + ((i + w) & 3) * 5120 + ks * 64);
#pragma unroll
            for (int ks = 0; ks < 5; ++ks) xk[ks] = *(const bf16x8*)(xl + mt0 * 5120 + ks * 64);
        }
        asm volatile("s_waitcnt lgkmcnt(0)" ::: "memory");
        SBAR();
#pragma unroll
        for (int i = 0; i < 10; ++i) glds16(src + (size_t)(16 + i) * 7680 + loff, ra + i * 960);
        if (lane < 40) glds16(src + (size_t)26 * 7680 + loff, ra + 10 * 960);
#pragma unroll
        for (int i = 0; i < 2; ++i) glds16(src2 + 3840 + i * 1024 + lane * 16, ra + 10240 + i * 1024);
        if (lane < 32) glds16(src2 + 3840 + 2 * 1024 + lane * 16, ra + 10240 + 2 * 1024);
        const unsigned ONES = 0x3f803f80u;
#pragma unroll
        for (int i = 0; i < 4; ++i)
#pragma unroll
            for (int ks = 0; ks < 5; ++ks) {
                u32x4 u = __builtin_bit_cast(u32x4, xf[i][ks]);
#pragma unroll
                for (int e = 0; e < 4; ++e) { sm[i] = dot2(u[e], ONES, sm[i]); sq[i] = dot2(u[e], u[e], sq[i]); }
            }
        SBAR();
        VMCNT(14);
        const int q4 = (lane & 15) >> 2, p4 = lane & 3;
        lds_cptr wb0 = (lds_cptr)rb + (8 * g + q4) * 160 + p4 * 8, wb1 = (lds_cptr)ra + (8 * g + q4) * 160 + p4 * 8 - 3 * 5120;
        lds_cptr kb0 = (lds_cptr)rb + 15488 + (8 * g + q4) * 40 + p4 * 8, kb1 = (lds_cptr)ra + 10240 + (8 * g + q4) * 40 + p4 * 8 - 3 * 1280;
        bf16x8 wf[2][7];
#define QKV_TR(ks, s_) do { lds_cptr wb = (ks) < 3 ? wb0 : wb1, kb = (ks) < 3 ? kb0 : kb1; \
        _Pragma("unroll") for (int nt = 0; nt < 5; ++nt) wf[s_][nt] = cat8(tr_read(wb + (ks) * 5120 + nt * 32), tr_read(wb + (ks) * 5120 + 640 + nt * 32)); \
        _Pragma("unroll") for (int nt = 0; nt < 2; ++nt) wf[s_][5 + nt] = cat8(tr_read(kb + (ks) * 1280 + nt * 32), tr_read(kb + (ks) * 1280 + 160 + nt * 32)); SBAR(); } while (0)
        QKV_TR(0, 0);
#pragma unroll
        for (int ks = 0; ks < 5; ++ks) {
            if (ks == 2) {
                VMCNT(0); SBAR();
            }
            if (ks < 4) QKV_TR(ks + 1, (ks + 1) & 1);
#pragma unroll
            for (int nt = 0; nt < 5; ++nt) {
                u32x4 u = __builtin_bit_cast(u32x4, wf[ks & 1][nt]);
#pragma unroll
                for (int e = 0; e < 4; ++e) cs[nt] = dot2(u[e], ONES, cs[nt]);
#pragma unroll
                for (int i = 0; i < 4; ++i) acc[i][nt] = MFMA16_32(wf[ks & 1][nt], xf[i][ks], acc[i][nt]);
            }
#pragma unroll
            for (int nt = 0; nt < 2; ++nt) {
                u32x4 u = __builtin_bit_cast(u32x4, wf[ks & 1][5 + nt]);
#pragma unroll
                for (int e = 0; e < 4; ++e) csk[nt] = dot2(u[e], ONES, csk[nt]);
                acck[nt] = MFMA16_32(wf[ks & 1][5 + nt], xk[ks], acck[nt]);
            }
            SBAR();
        }
#pragma unroll
        for (int i = 0; i < 4; ++i) { sm[i] = add32(add16(sm[i])); sq[i] = add32(add16(sq[i])); }
#pragma unroll
        for (int i = 0; i < 5; ++i) cs[i] = add32(add16(cs[i]));
#pragma unroll
        for (int i = 0; i < 2; ++i) csk[i] = add32(add16(csk[i]));
    }
    RAW_BARRIER();
    qkv_finish<false>(ws, L, tb, cg, w, lane, acc, acck, csq, cs0, cs1, stq, stk, sm, sq, cs, csk);
    PIN(code_touch);
    if (b == 0 && tid < 4) {
        const unsigned v = (tid & 2) ? 0x3f803f80u : 0u;
        *(u32x4*)(ws + OFF_CONST + tid * 16) = u32x4{v, v, v, v};
    }
}

constexpr int PJ0 = 0, PJ1 = 4, PJD = 3;
constexpr int PBW = 2688, ATT_LDS = 32 * PBW;
template <bool PREP>
__global__ __launch_bounds__(512, 2) void k_attn(char* __restrict__ ws, const u16* __restrict__ w1, const u16* __restrict__ w2,
                                                 const u16* __restrict__ wo, const u16* __restrict__ wq,
                                                 const u16* __restrict__ wk, const u16* __restrict__ wv) {
    extern __shared__ __attribute__((aligned(16))) char lds_raw[];
    char* L = lds_raw;
    const int tid = threadIdx.x, lane = tid & 63, w = __builtin_amdgcn_readfirstlane(tid >> 6);
    const int b = blockIdx.x, slot = b >> 3, tb = ((b & 7) >> 1) * 8 + (slot >> 2), cg = (b & 1) * 4 + (slot & 3);
    const int r32 = lane & 31, h = lane >> 5, n16 = lane & 15, g = lane >> 4;
    const int cgp = w & 1, kq = w >> 1;
    float kn0 = *(const float*)(ws + OFF_KN + lane * 4), kn1 = *(const float*)(ws + OFF_KN + 256 + lane * 4);
    float qmax = *(const float*)(ws + OFF_QN + ((tb * 8 + cg) * 4 + (lane & 3)) * 4);
    bf16x8 q1[4]; bf16x4 q2[4];
    {
        const char* qg = ws + OFF_Q + (size_t)(tb * 8 + cg) * 12288 + cgp * 6144 + r32 * 48;
#pragma unroll
        for (int j = 0; j < 4; ++j) { q1[j] = *(const bf16x8*)(qg + j * 1536 + 16 * h); q2[j] = *(const bf16x4*)(qg + j * 1536 + 32 + 8 * h); }
    }
    SBAR();
    const int kr = (r32 & 19) | ((r32 & 4) << 1) | ((r32 & 8) >> 1);
    const unsigned ko1 = (unsigned)OFF_K1 + (unsigned)(512 * kq + kr) * 32u + 16u * h;
    const unsigned ko2 = h ? (unsigned)OFF_CONST + 32u : (unsigned)OFF_K2 + (unsigned)(512 * kq + kr) * 8u;
    const unsigned ks2 = h ? 0u : 256u;
    const unsigned vo = r32 < 20 ? (unsigned)OFF_V + (unsigned)(32 * kq) * 640u + (unsigned)(h * 20 + r32) * 16u
                                 : (unsigned)OFF_CONST + (r32 == 20 ? 32u : 0u);
    const unsigned vs = r32 < 20 ? 640u : 0u;
    bf16x8 k1[3], v0[3], v1[3]; bf16x4 k2[3];
#define K_LOAD(t) do { k1[(t) % 3] = *(const bf16x8*)(ws + ko1 + (t) * 1024); k2[(t) % 3] = *(const bf16x4*)(ws + (ko2 + (t) * ks2)); } while (0)
#define V_LOAD(t) do { v0[(t) % 3] = *(const bf16x8*)(ws + (vo + (2 * (t)) * vs)); v1[(t) % 3] = *(const bf16x8*)(ws + (vo + (2 * (t) + 1) * vs)); } while (0)
    K_LOAD(0); V_LOAD(0); SBAR(); K_LOAD(1); V_LOAD(1); SBAR();
    PIN(kn0); PIN(kn1);
    const float kmax2 = wavemax(fmaxf(kn0, kn1));
    qmax = rowmax16(qmax);
    const float kb = __builtin_amdgcn_sqrtf(kmax2);
    const bool safe = __builtin_amdgcn_readfirstlane((int)(qmax * kb <= SAFE_BOUND)) != 0;
    const f32x16 zero16 = {0.f, 0.f, 0.f, 0.f, 0.f, 0.f, 0.f, 0.f, 0.f, 0.f, 0.f, 0.f, 0.f, 0.f, 0.f, 0.f};
    if (safe) {
        if (h) {
#pragma unroll
            for (int j = 0; j < 4; ++j) q2[j][0] = (short)pk2(-bf2f((u16)q2[j][0]) * kb, 0.f);
        }
    } else {
        float m[4];
#pragma unroll
        for (int j = 0; j < 4; ++j) { m[j] = -INFINITY; if (h) q2[j] = bf16x4{0, 0, 0, 0}; }
        for (int t = 0; t < 16; ++t) {
            const bf16x8 ka = *(const bf16x8*)(ws + ko1 + t * 1024);
            const bf16x4 kc = *(const bf16x4*)(ws + (ko2 + t * ks2));
#pragma unroll
            for (int j = 0; j < 4; ++j) {
                f32x16 s = MFMA32_16(ka, q1[j], zero16);
                SBAR(); asm volatile("s_nop 15" ::: "memory"); SBAR();
                s = MFMA32_8(kc, q2[j], s);
#pragma unroll
                for (int e = 0; e < 16; ++e) m[j] = fmaxf(m[j], s[e]);
                SBAR();
            }
        }
#pragma unroll
        for (int j = 0; j < 4; ++j) {
            const float mm = fmaxf(m[j], xor32(m[j]));
            if (h == 0) *(float*)(L + ((w * 4 + j) * 32 + r32) * 4) = mm;
        }
        RAW_BARRIER();
#pragma unroll
        for (int j = 0; j < 4; ++j) {
            float mm = -INFINITY;
#pragma unroll
            for (int qq = 0; qq < 4; ++qq) mm = fmaxf(mm, *(const float*)(L + (((2 * qq + cgp) * 4 + j) * 32 + r32) * 4));
            const unsigned hi = __float_as_uint(mm) & 0xffff0000u;
            const float r1 = mm - __uint_as_float(hi);
            const unsigned mid = __float_as_uint(r1) & 0xffff0000u;
            const float r2 = r1 - __uint_as_float(mid);
            const unsigned lo = __float_as_uint(r2) & 0xffff0000u;
            if (h) q2[j] = __builtin_bit_cast(bf16x4, u32x2{hi ^ 0x80000000u, ((mid ^ 0x80000000u) >> 16) | (lo ^ 0x80000000u)});
        }
        RAW_BARRIER();
    }
    f32x16 o[4], sa, sb;
#pragma unroll
    for (int j = 0; j < 4; ++j) o[j] = zero16;
    u32x4 p1prev = {0u, 0u, 0u, 0u};
    sa = MFMA32_16(k1[0], q1[0], zero16);
    SBAR(); asm volatile("s_nop 15" ::: "memory"); SBAR();
    sa = MFMA32_8(k2[0], q2[0], sa);
    SBAR(); asm volatile("s_nop 15" ::: "memory"); SBAR();
    u32x4 prep[4]; char* prep_dst = nullptr; int prep_st = 0;
#pragma unroll
    for (int t = 0; t < 16; ++t) {
#pragma unroll
        for (int j = 0; j < 4; ++j) {
            const int it = 4 * t + j;
            f32x16& cur = (it & 1) ? sb : sa; f32x16& nxt = (it & 1) ? sa : sb;
            const int tn = j < 3 ? t : t + 1, jn = (j + 1) & 3;
            const int tp = j > 0 ? t : t - 1, jp = (j + 3) & 3;
            u32x4 p0, p1;
            if (j == 0 && t + 2 < 16) K_LOAD(t + 2);
            if (j == 1 && t + 2 < 16) V_LOAD(t + 2);
            if (PREP && j == 2 && (t == PJ0 || t == PJ1)) {
                int id = (t == PJ1 ? 2048 : 0) + b * 8 + w; if (id >= NPREP) id -= 2048;
                const u16* src; int ld;
                if (id < 1728) { const int ks = id / 27, c4 = id - ks * 27; src = w1 + (size_t)(32 * ks) * F_ + 64 * c4; ld = F_;
                                 prep_dst = ws + OFF_W1F + (size_t)(4 * c4 * 64 + ks) * 1024; prep_st = 64 * 1024; }
                else if (id < 3456) { const int jj = id - 1728, ks = jj >> 5, c4 = jj & 31; src = w2 + (size_t)(32 * ks) * S_ + 64 * c4; ld = S_;
                                      prep_dst = ws + OFF_W2F + (size_t)(4 * c4 * 54 + ks) * 1024; prep_st = 54 * 1024; }
                else { int jj = id - 3456; const bool isq = jj >= 200; if (isq) jj -= 200; const int ks = jj / 10, c4 = jj - ks * 10;
                       src = (isq ? wq : wo) + (size_t)(32 * ks) * 640 + 64 * c4; ld = 640;
                       prep_dst = ws + (isq ? OFF_WQF : OFF_WOF) + (size_t)(4 * c4 * 20 + ks) * 1024; prep_st = 20 * 1024; }
                src += (size_t)(lane >> 3) * ld + (lane & 7) * 8;
#pragma unroll
                for (int i = 0; i < 4; ++i) prep[i] = *(const u32x4*)(src + (size_t)(8 * i) * ld);
            }
            if (PREP && j == 2 && t == 9) {
                const int id = (b * 8 + w) % 80, ks = id >> 2, ct = id & 3; int cn = 16 * (ct & 1) + n16; if (cn >= 20) cn = 0;
                const u16* sp = ((ct & 2) ? wv : wk) + (size_t)(32 * ks + 8 * g) * 20 + cn;
                if (b < 10) {
#pragma unroll
                    for (int e = 0; e < 8; ++e) prep[e >> 2][e & 3] = sp[e * 20];
                }
                prep_dst = ws + OFF_WQF + (size_t)((40 + ct) * 20 + ks) * 1024;
            }
            if (PREP && j == 2 && t == 12 && b < 10)
                *(u32x4*)(prep_dst + lane * 16) = u32x4{prep[0][0] | (prep[0][1] << 16), prep[0][2] | (prep[0][3] << 16),
                                                        prep[1][0] | (prep[1][1] << 16), prep[1][2] | (prep[1][3] << 16)};
            if (PREP && j == 2 && (t == PJ0 + PJD || t == PJ1 + PJD)) {
#pragma unroll
                for (int i = 0; i < 4; ++i) *(u32x4*)(L + w * 4096 + i * 1024 + lane * 16) = prep[i];
            }
            if (PREP && j == 3 && (t == PJ0 + PJD || t == PJ1 + PJD)) {
                lds_cptr tp_ = (lds_cptr)(lds_ptr)L + w * 4096 + (8 * g + (n16 >> 2)) * 128 + (lane & 3) * 8;
#pragma unroll
                for (int i = 0; i < 4; ++i) prep[i] = __builtin_bit_cast(u32x4, cat8(tr_read(tp_ + i * 32), tr_read(tp_ + 512 + i * 32)));
            }
            if (PREP && j == 0 && (t == PJ0 + PJD + 1 || t == PJ1 + PJD + 1)) {
#pragma unroll
                for (int i = 0; i < 4; ++i) PREP_STORE((u32x4*)(prep_dst + (size_t)i * prep_st + lane * 16), prep[i]);
            }
            SBAR();
            if (it < 63) nxt = MFMA32_16(k1[tn % 3], q1[jn], zero16);
            SBAR();
            p0[0] = pk2(EX2(cur[0]), EX2(cur[1])); p0[1] = pk2(EX2(cur[2]), EX2(cur[3]));
            PIN(p0); SBAR();
            if (it > 0) o[jp] = MFMA32_16(v1[(tp + 3) % 3], __builtin_bit_cast(bf16x8, p1prev), o[jp]);
            SBAR();
            p0[2] = pk2(EX2(cur[4]), EX2(cur[5])); p0[3] = pk2(EX2(cur[6]), EX2(cur[7]));
            PIN(p0); SBAR();
            if (it < 63) nxt = MFMA32_8(k2[tn % 3], q2[jn], nxt);
            SBAR();
            p1[0] = pk2(EX2(cur[8]), EX2(cur[9])); p1[1] = pk2(EX2(cur[10]), EX2(cur[11]));
            PIN(p1); SBAR();
            o[j] = MFMA32_16(v0[t % 3], __builtin_bit_cast(bf16x8, p0), o[j]);
            SBAR();
            p1[2] = pk2(EX2(cur[12]), EX2(cur[13])); p1[3] = pk2(EX2(cur[14]), EX2(cur[15]));
            PIN(p1); SBAR();
            p1prev = p1;
        }
    }
    o[3] = MFMA32_16(v1[15 % 3], __builtin_bit_cast(bf16x8, p1prev), o[3]);
    if (PREP) RAW_BARRIER();
#pragma unroll
    for (int j = 0; j < 4; ++j) {
        char* pb = L + (kq * 8 + cgp * 4 + j) * PBW;
        *(f32x4*)(pb + lane * 16) = f32x4{o[j][0], o[j][1], o[j][2], o[j][3]};
        *(f32x4*)(pb + 1024 + lane * 16) = f32x4{o[j][4], o[j][5], o[j][6], o[j][7]};
        if (h == 0) *(f32x4*)(pb + 2048 + r32 * 16) = f32x4{o[j][8], o[j][9], o[j][10], o[j][11]};
        else *(float*)(pb + 2560 + r32 * 4) = o[j][8];
    }
    RAW_BARRIER();
    {
        const char* pb = L + w * PBW;
        f32x4 a0 = {0.f, 0.f, 0.f, 0.f}, a1 = a0, a2 = a0; float l = 0.f;
#pragma unroll
        for (int qq = 0; qq < 4; ++qq) {
            a0 += *(const f32x4*)(pb + qq * 8 * PBW + lane * 16);
            a1 += *(const f32x4*)(pb + qq * 8 * PBW + 1024 + lane * 16);
            a2 += *(const f32x4*)(pb + qq * 8 * PBW + 2048 + r32 * 16);
            l += *(const float*)(pb + qq * 8 * PBW + 2560 + r32 * 4);
        }
        const float inv = __builtin_amdgcn_rcpf(l);
        const int tl = 32 * (w & 1) + r32, hd0 = 80 * cg + 20 * (w >> 1);
        char* ob = ws + OFF_O + (size_t)(tb * 4 + (tl >> 4)) * 20480 + (tl & 15) * 16;
        auto st = [&](int hd, const f32x4& a) {
            *(u32x2*)(ob + (hd >> 5) * 1024 + ((hd & 31) >> 3) * 256 + (hd & 7) * 2) = u32x2{pk2(a[0] * inv, a[1] * inv), pk2(a[2] * inv, a[3] * inv)};
        };
        st(hd0 + 4 * h, a0); st(hd0 + 8 + 4 * h, a1);
        if (h == 0) st(hd0 + 16, a2);
    }
    if (b == 0 && tid < 48) *(unsigned*)(ws + OFF_CNT + tid * 256) = 0u;
}

typedef __attribute__((address_space(1))) unsigned gu32;
constexpr int AUXL = 16;
#define LD_SC1(off) __builtin_bit_cast(bf16x8, __builtin_amdgcn_raw_buffer_load_b128(rs, (int)(off), 0, 16))
DI void edge_publish(gu32* cnt) {
    RAW_BARRIER();
    if (threadIdx.x == 0) __hip_atomic_fetch_add(cnt, 1u, __ATOMIC_RELAXED, __HIP_MEMORY_SCOPE_AGENT);
}
DI void edge_wait(gu32* cnt, unsigned want) {
    if (threadIdx.x == 0) {
        for (int spin = 0; spin < (1 << 20); ++spin) {
            if (__hip_atomic_load(cnt, __ATOMIC_RELAXED, __HIP_MEMORY_SCOPE_AGENT) >= want) break;
            __builtin_amdgcn_s_sleep(1);
        }
        __builtin_amdgcn_fence(__ATOMIC_ACQUIRE, "wavefront");
    }
    RAW_BARRIER();
}
#define W1_A 6
#define W2_FIRST 7
constexpr int MLP_LDS = 73728;
#define WO_LD(ks) do { _Pragma("unroll") for (int i = 0; i < 4; ++i) xf[ks][i] = xb[(((i + w) & 3) * 20 + (ks)) * 64]; \
        _Pragma("unroll") for (int nt = 0; nt < 5; ++nt) wf[ks][nt] = wb[(nt * 20 + (ks)) * 64]; SBAR(); } while (0)
template <bool R1>
__global__ __launch_bounds__(256, 1) void k_mlp(char* __restrict__ ws, const u16* __restrict__ xin, const u16* __restrict__ b1,
                                                const u16* __restrict__ b2, u16* __restrict__ out) {
    extern __shared__ __attribute__((aligned(16))) char lds_raw[];
    char* L = lds_raw;
    const int tid = threadIdx.x, lane = tid & 63, w = __builtin_amdgcn_readfirstlane(tid >> 6);
    const int b = blockIdx.x, slot = b >> 3, tb = ((b & 7) >> 1) * 8 + (slot >> 2), cg = (b & 1) * 4 + (slot & 3);
    const int ot1 = ((b & 7) >> 1) * 7 + (slot >> 2);
    const bool act1 = slot < 28 && ot1 < 27;
    const int n16 = lane & 15, g = lane >> 4;
    const __amdgpu_buffer_rsrc_t rs = __builtin_amdgcn_make_buffer_rsrc((void*)ws, 0, (int)WS_NEED, 0x00020000);
    gu32* cnt = (gu32*)(ws + OFF_CNT);
    f32x4 acc[4][5];
#define ACC_ZERO() do { _Pragma("unroll") for (int i_ = 0; i_ < 4; ++i_) _Pragma("unroll") for (int j_ = 0; j_ < 5; ++j_) acc[i_][j_] = f32x4{0.f, 0.f, 0.f, 0.f}; } while (0)
#define ACC_REDUCE() do { \
    _Pragma("unroll") for (int j_ = 1; j_ < 4; ++j_) { const int o_ = (w + j_) & 3; \
        _Pragma("unroll") for (int i_ = 0; i_ < 5; ++i_) *(f32x4*)(L + (((o_ * 3 + (j_ - 1)) * 5 + i_) * 64 + lane) * 16) = acc[j_][i_]; } \
    RAW_BARRIER(); \
    _Pragma("unroll") for (int j_ = 0; j_ < 3; ++j_) _Pragma("unroll") for (int i_ = 0; i_ < 5; ++i_) \
        acc[1 + j_][i_] = *(const f32x4*)(L + (((w * 3 + j_) * 5 + i_) * 64 + lane) * 16); \
    SBAR(); \
    _Pragma("unroll") for (int i_ = 0; i_ < 5; ++i_) acc[0][i_] = (acc[0][i_] + acc[1][i_]) + (acc[2][i_] + acc[3][i_]); } while (0)
    const int tok = tb * 64 + 16 * w + n16;
    bf16x8 wr[16][4];
    int ro1[4], ro2[4];
#pragma unroll
    for (int j = 0; j < 4; ++j) { ro1[j] = ((j + w) & 3) * 64 * 64; ro2[j] = ((j + w) & 3) * 54 * 64; }
    const bf16x8* pw1 = (const bf16x8*)(ws + OFF_W1F) + ((size_t)(4 * (act1 ? ot1 : 0)) * 64 + 16 * w) * 64 + lane;
    const int k2 = w < 2 ? 14 * w : 28 + 13 * (w - 2);
    const bf16x8* pw2 = (const bf16x8*)(ws + OFF_W2F) + ((size_t)(4 * tb) * 54 + k2) * 64 + lane;
#define W1_LD(lo, hi) do { _Pragma("unroll") for (int s_ = (lo); s_ < (hi); ++s_) _Pragma("unroll") for (int j_ = 0; j_ < 4; ++j_) wr[s_][j_] = pw1[ro1[j_] + s_ * 64]; SBAR(); } while (0)
#define W2_LD(lo, hi) do { _Pragma("unroll") for (int s_ = (lo); s_ < (hi); ++s_) _Pragma("unroll") for (int j_ = 0; j_ < 4; ++j_) wr[s_][j_] = pw2[ro2[j_] + s_ * 64]; SBAR(); } while (0)

    u32x2 x1o[5], bb; float bs;
    {
        ACC_ZERO();
        u32x2 xi[5];
        const bf16x8* xb = (const bf16x8*)(ws + OFF_O) + (size_t)(tb * 4 * 20 + 5 * w) * 64 + lane;
        const bf16x8* wb = (const bf16x8*)(ws + OFF_WOF) + (size_t)(5 * cg * 20 + 5 * w) * 64 + lane;
        bf16x8 xf[5][4], wf[5][5];
        WO_LD(0); WO_LD(1); WO_LD(2); WO_LD(3);
#pragma unroll
        for (int ks = 0; ks < 5; ++ks) {
#pragma unroll
            for (int nt = 0; nt < 5; ++nt)
#pragma unroll
                for (int i = 0; i < 4; ++i) acc[i][nt] = MFMA16_32(wf[ks][nt], xf[ks][i], acc[i][nt]);
            SBAR();
            if (ks == 0) {
                WO_LD(4);
#pragma unroll
                for (int nt = 0; nt < 5; ++nt) {
                    const int d = 80 * cg + 16 * nt + 4 * g;
                    if (!R1) xi[nt] = *(const u32x2*)(xin + ((size_t)((tok >> 4) * 20 + (d >> 5)) * 64 + ((d & 31) >> 3) * 16 + n16) * 8 + (d & 7));
                    else xi[nt] = *(const u32x2*)(xin + (size_t)tok * 640 + d);
                }
                bb = *(const u32x2*)(b1 + 64 * (act1 ? ot1 : 0) + 16 * w + 4 * g);
                bs = bf2f(b2[tok]);
                SBAR();
            }
            if (ks == 1) W1_LD(0, 2);
            if (ks == 2) W1_LD(2, 4);
            if (ks == 3) W1_LD(4, 6);
            if (ks == 4) W1_LD(6, W1_A);
        }
        ACC_REDUCE();
        float xv[20], s = 0.f;
#pragma unroll
        for (int nt = 0; nt < 5; ++nt) {
            const unsigned u0 = pk2(bf_lo(xi[nt][0]) + acc[0][nt][0], bf_hi(xi[nt][0]) + acc[0][nt][1]);
            const unsigned u1 = pk2(bf_lo(xi[nt][1]) + acc[0][nt][2], bf_hi(xi[nt][1]) + acc[0][nt][3]);
            x1o[nt] = u32x2{u0, u1};
            xv[4 * nt] = bf_lo(u0); xv[4 * nt + 1] = bf_hi(u0); xv[4 * nt + 2] = bf_lo(u1); xv[4 * nt + 3] = bf_hi(u1);
            s += (xv[4 * nt] + xv[4 * nt + 1]) + (xv[4 * nt + 2] + xv[4 * nt + 3]);
        }
        const float mw = add32(add16(s)) * (1.f / 80.f);
        float s2 = 0.f;
#pragma unroll
        for (int i = 0; i < 20; ++i) { const float dd = xv[i] - mw; s2 += dd * dd; }
        s2 = add32(add16(s2));
        gu64* gp = (gu64*)(ws + OFF_XG) + (size_t)tok * 8;
        if (g == 0) __hip_atomic_store(gp + cg, ((unsigned long long)(__float_as_uint(s2) | 0x80000000u) << 32) | __float_as_uint(mw),
                                       __ATOMIC_RELAXED, __HIP_MEMORY_SCOPE_AGENT);
        SBAR();
        unsigned long long v0, v1;
        for (int spin = 0; spin < (1 << 20); ++spin) {
            v0 = __hip_atomic_load(gp + 2 * g, __ATOMIC_RELAXED, __HIP_MEMORY_SCOPE_AGENT);
            v1 = __hip_atomic_load(gp + 2 * g + 1, __ATOMIC_RELAXED, __HIP_MEMORY_SCOPE_AGENT);
            if (__all((long long)(v0 & v1) < 0)) break;
            __builtin_amdgcn_s_sleep(4);
        }
        const float m0 = __uint_as_float((unsigned)v0), m1 = __uint_as_float((unsigned)v1);
        const float mean = add32(add16(m0 + m1)) * 0.125f;
        const float d0 = m0 - mean, d1 = m1 - mean;
        const float var = add32(add16(__uint_as_float((unsigned)(v0 >> 32) & 0x7fffffffu) + __uint_as_float((unsigned)(v1 >> 32) & 0x7fffffffu) +
                                      80.f * (d0 * d0 + d1 * d1))) * (1.f / 640.f);
        float shf, r2; ln2_scalars(mean, var, shf, r2);
        char* hb = L + w * 15360;
#pragma unroll
        for (int nt = 0; nt < 5; ++nt)
            *(u32x2*)(hb + n16 * 176 + (16 * nt + 4 * g) * 2) =
                u32x2{pk2((xv[4 * nt] - shf) * r2, (xv[4 * nt + 1] - shf) * r2), pk2((xv[4 * nt + 2] - shf) * r2, (xv[4 * nt + 3] - shf) * r2)};
        asm volatile("s_waitcnt lgkmcnt(0)" ::: "memory");
        const int q4 = (lane & 15) >> 2, p4 = lane & 3;
        lds_cptr hr = (lds_cptr)(lds_ptr)L + w * 15360 + (8 * (g & 1) + q4) * 176 + p4 * 8 + (g >> 1) * 32;
        const int s0 = tb * 64 + 16 * w + 8 * (g & 1);
        const int hoff = (int)OFF_H + ((5 * cg + (g >> 1)) * 64 + (s0 >> 5)) * 1024 + (((s0 & 31) >> 3) * 16 + n16) * 16;
        const bf16x8 f0 = cat8(tr_read(hr), tr_read(hr + 704));
        const bf16x8 f1 = cat8(tr_read(hr + 64), tr_read(hr + 704 + 64));
        const bf16x8 f2 = cat8(tr_read(hr + (g < 2 ? 128 : 0)), tr_read(hr + 704 + (g < 2 ? 128 : 0)));
        __builtin_amdgcn_raw_buffer_store_b128(__builtin_bit_cast(u32x4, f0), rs, hoff, 0, 16);
        __builtin_amdgcn_raw_buffer_store_b128(__builtin_bit_cast(u32x4, f1), rs, hoff + 2 * 65536, 0, 16);
        if (g < 2) __builtin_amdgcn_raw_buffer_store_b128(__builtin_bit_cast(u32x4, f2), rs, hoff + 4 * 65536, 0, 16);
        SBAR();
        W1_LD(W1_A, W1_A + 4);
        VMCNT(16);
        edge_publish(cnt + cg * 64);
    }
    if (act1) {
        W1_LD(W1_A + 4, 16);
        edge_wait(cnt + cg * 64, 32);
        ACC_ZERO();
        const int ho = (int)OFF_H + ((5 * cg) * 64 + 16 * w) * 1024 + lane * 16;
        constexpr int PF = 6;
        bf16x8 rf[PF][5];
#pragma unroll
        for (int s = 0; s < PF; ++s) {
#pragma unroll
            for (int i = 0; i < 5; ++i) rf[s][i] = LD_SC1(ho + i * 65536 + s * 1024);
            SBAR();
        }
#pragma unroll
        for (int s = 0; s < 16; ++s) {
#pragma unroll
            for (int j = 0; j < 4; ++j)
#pragma unroll
                for (int i = 0; i < 5; ++i) acc[j][i] = MFMA16_32(wr[s][j], rf[s % PF][i], acc[j][i]);
            SBAR();
            if (s + PF < 16) {
#pragma unroll
                for (int i = 0; i < 5; ++i) rf[s % PF][i] = LD_SC1(ho + i * 65536 + (s + PF) * 1024);
                SBAR();
            }
            if (s >= W2_FIRST && s - W2_FIRST < 9) W2_LD(s - W2_FIRST, s - W2_FIRST + 1);
        }
        ACC_REDUCE();
        const int f = 64 * ot1 + 16 * w + 4 * g;
        const float bv[4] = {bf_lo(bb[0]), bf_hi(bb[0]), bf_lo(bb[1]), bf_hi(bb[1])};
        const int uo = (int)OFF_U + ((5 * cg) * 54 + (f >> 5)) * 1024 + (((f & 31) >> 3) * 16 + n16) * 16 + (f & 7) * 2;
#pragma unroll
        for (int i = 0; i < 5; ++i) {
            float r[4];
#pragma unroll
            for (int e = 0; e < 4; ++e) {
                const float u = acc[0][i][e] + bv[e];
                const float z = u * (1.f + 0.044715f * u * u) * (-2.f * 0.7978845608028654f * 1.4426950408889634f);
                r[e] = u * __builtin_amdgcn_rcpf(1.f + EX2(z));
            }
            __builtin_amdgcn_raw_buffer_store_b64(u32x2{pk2(r[0], r[1]), pk2(r[2], r[3])}, rs, uo + i * 54 * 1024, 0, 16);
        }
        SBAR();
        W2_LD(9, 13);
        VMCNT(16);
        edge_publish(cnt + (8 + cg) * 64);
    } else {
        W2_LD(0, 13);
    }
    {
        if (w < 2) W2_LD(13, 14);
        edge_wait(cnt + (8 + cg) * 64, 27);
        ACC_ZERO();
        const int uo = (int)OFF_U + ((5 * cg) * 54 + k2) * 1024 + lane * 16;
        constexpr int PF = 6;
        bf16x8 rf[PF][5];
#pragma unroll
        for (int s = 0; s < PF; ++s) {
#pragma unroll
            for (int i = 0; i < 5; ++i) rf[s][i] = LD_SC1(uo + i * 54 * 1024 + s * 1024);
            SBAR();
        }
#pragma unroll
        for (int s = 0; s < 14; ++s) {
            if (s < 13 || w < 2) {
#pragma unroll
                for (int j = 0; j < 4; ++j)
#pragma unroll
                    for (int i = 0; i < 5; ++i) acc[j][i] = MFMA16_32(rf[s % PF][i], wr[s][j], acc[j][i]);
            }
            SBAR();
            if (s + PF < 13 || (s + PF == 13 && w < 2)) {
#pragma unroll
                for (int i = 0; i < 5; ++i) rf[s % PF][i] = LD_SC1(uo + i * 54 * 1024 + (s + PF) * 1024);
                SBAR();
            }
        }
        ACC_REDUCE();
        float xv[20], sum = 0.f;
#pragma unroll
        for (int i = 0; i < 5; ++i) {
            const int d = 80 * cg + 16 * i + 4 * g;
            const unsigned u0 = pk2(bf_lo(x1o[i][0]) + acc[0][i][0] + bs, bf_hi(x1o[i][0]) + acc[0][i][1] + bs);
            const unsigned u1 = pk2(bf_lo(x1o[i][1]) + acc[0][i][2] + bs, bf_hi(x1o[i][1]) + acc[0][i][3] + bs);
            if (R1) __builtin_amdgcn_raw_buffer_store_b64(u32x2{u0, u1}, rs,
                        (int)OFF_X2 + ((4 * tb + w) * 20 + (d >> 5)) * 1024 + (((d & 31) >> 3) * 16 + n16) * 16 + (d & 7) * 2, 0, 16);
            else *(u32x2*)(out + (size_t)tok * 640 + d) = u32x2{u0, u1};
            xv[4 * i] = bf_lo(u0); xv[4 * i + 1] = bf_hi(u0); xv[4 * i + 2] = bf_lo(u1); xv[4 * i + 3] = bf_hi(u1);
            sum += (xv[4 * i] + xv[4 * i + 1]) + (xv[4 * i + 2] + xv[4 * i + 3]);
        }
        if (R1) {
            const float mw = add32(add16(sum)) * (1.f / 80.f);
            float s2 = 0.f;
#pragma unroll
            for (int i = 0; i < 20; ++i) { const float dd = xv[i] - mw; s2 += dd * dd; }
            s2 = add32(add16(s2));
            if (g == 0) __builtin_amdgcn_raw_buffer_store_b64(u32x2{__float_as_uint(mw), __float_as_uint(s2)}, rs, (int)OFF_XS + ((tb * 8 + cg) * 64 + 16 * w + n16) * 8, 0, 16);
        }
    }
    if (R1) {
        const int mt0 = cg >> 1, isv = cg & 1;
        const bf16x8* wb = (const bf16x8*)(ws + OFF_WQF) + (size_t)(5 * cg * 20 + 5 * w) * 64 + lane;
        const bf16x8* kb = (const bf16x8*)(ws + OFF_WQF) + (size_t)((40 + 2 * isv) * 20 + 5 * w) * 64 + lane;
        bf16x8 wf[5][5], wkf[5][2], xf[5][4], xk[5];
        SBAR();
#pragma unroll
        for (int i = 0; i < 16; ++i) wf[i / 5][i % 5] = wb[((i % 5) * 20 + i / 5) * 64];
        SBAR();
        VMCNT(16);
        edge_publish(cnt + (16 + tb) * 64);
#pragma unroll
        for (int i = 16; i < 25; ++i) wf[i / 5][i % 5] = wb[((i % 5) * 20 + i / 5) * 64];
#pragma unroll
        for (int ks = 0; ks < 5; ++ks) { wkf[ks][0] = kb[ks * 64]; wkf[ks][1] = kb[(20 + ks) * 64]; }
        f32x4 csq[5], cs0, cs1, stq[4], stk[4], acck[2];
        {
            const float* csp = (const float*)(ws + OFF_CS);
#pragma unroll
            for (int nt = 0; nt < 5; ++nt) csq[nt] = *(const f32x4*)(csp + 80 * cg + 16 * nt + 4 * g);
            cs0 = *(const f32x4*)(csp + 640 + 32 * isv + 4 * g); cs1 = *(const f32x4*)(csp + 640 + 32 * isv + 16);
        }
        SBAR();
        edge_wait(cnt + (16 + tb) * 64, 8);
        const int xo = (int)OFF_X2 + (tb * 4 * 20 + 5 * w) * 1024 + lane * 16;
#pragma unroll
        for (int ks = 0; ks < 5; ++ks) {
#pragma unroll
            for (int i = 0; i < 4; ++i) xf[ks][i] = LD_SC1(xo + (((i + w) & 3) * 20 + ks) * 1024);
            xk[ks] = LD_SC1(xo + (mt0 * 20 + ks) * 1024);
            SBAR();
        }
#pragma unroll
        for (int i = 0; i < 4; ++i) {
            const u32x2 a0 = __builtin_amdgcn_raw_buffer_load_b64(rs, (int)OFF_XS + ((tb * 8 + 2 * i) * 64 + 16 * w + n16) * 8, 0, AUXL);
            const u32x2 a1 = __builtin_amdgcn_raw_buffer_load_b64(rs, (int)OFF_XS + ((tb * 8 + 2 * i + 1) * 64 + 16 * w + n16) * 8, 0, AUXL);
            const u32x2 c0 = __builtin_amdgcn_raw_buffer_load_b64(rs, (int)OFF_XS + ((tb * 8 + 2 * i) * 64 + 16 * mt0 + n16) * 8, 0, AUXL);
            const u32x2 c1 = __builtin_amdgcn_raw_buffer_load_b64(rs, (int)OFF_XS + ((tb * 8 + 2 * i + 1) * 64 + 16 * mt0 + n16) * 8, 0, AUXL);
            stq[i] = __builtin_bit_cast(f32x4, u32x4{a0[0], a0[1], a1[0], a1[1]});
            stk[i] = __builtin_bit_cast(f32x4, u32x4{c0[0], c0[1], c1[0], c1[1]});
        }
        SBAR();
        ACC_ZERO();
        acck[0] = f32x4{0.f, 0.f, 0.f, 0.f}; acck[1] = acck[0];
#pragma unroll
        for (int ks = 0; ks < 5; ++ks) {
#pragma unroll
            for (int nt = 0; nt < 5; ++nt)
#pragma unroll
                for (int i = 0; i < 4; ++i) acc[i][nt] = MFMA16_32(wf[ks][nt], xf[ks][i], acc[i][nt]);
            acck[0] = MFMA16_32(wkf[ks][0], xk[ks], acck[0]);
            acck[1] = MFMA16_32(wkf[ks][1], xk[ks], acck[1]);
            SBAR();
        }
        const float z4[4] = {0.f, 0.f, 0.f, 0.f}, z5[5] = {0.f, 0.f, 0.f, 0.f, 0.f}, z2[2] = {0.f, 0.f};
        qkv_finish<true>(ws, L, tb, cg, w, lane, acc, acck, csq, cs0, cs1, stq, stk, z4, z4, z5, z2);
    }
}

template <int N> __global__ void k_end() {}

extern "C" void kernel_launch(void* const* d_in, const int* in_sizes, int n_in, void* d_out, int out_size, void* d_ws,
                              size_t ws_size, hipStream_t stream) {
    const u16* x = (const u16*)d_in[0];
    const u16* wq = (const u16*)d_in[1];
    const u16* wk = (const u16*)d_in[2];
    const u16* wv = (const u16*)d_in[3];
    const u16* wo = (const u16*)d_in[4];
    const u16* w1 = (const u16*)d_in[6];
    const u16* b1 = (const u16*)d_in[7];
    const u16* w2 = (const u16*)d_in[8];
    const u16* b2 = (const u16*)d_in[9];
    char* ws = (char*)d_ws;
    u16* x2 = (u16*)(ws + OFF_X2);
    (void)hipFuncSetAttribute((const void*)k_qkv, hipFuncAttributeMaxDynamicSharedMemorySize, QKV_LDS);
    (void)hipFuncSetAttribute((const void*)k_attn<true>, hipFuncAttributeMaxDynamicSharedMemorySize, ATT_LDS);
    (void)hipFuncSetAttribute((const void*)k_attn<false>, hipFuncAttributeMaxDynamicSharedMemorySize, ATT_LDS);
    (void)hipFuncSetAttribute((const void*)k_mlp<true>, hipFuncAttributeMaxDynamicSharedMemorySize, MLP_LDS);
    (void)hipFuncSetAttribute((const void*)k_mlp<false>, hipFuncAttributeMaxDynamicSharedMemorySize, MLP_LDS);
    (void)hipFuncSetAttribute((const void*)k_end<0>, hipFuncAttributeMaxDynamicSharedMemorySize, 0);
    k_qkv<<<256, 256, QKV_LDS, stream>>>(x, wq, wk, wv, ws);
    k_attn<true><<<256, 512, ATT_LDS, stream>>>(ws, w1, w2, wo, wq, wk, wv);
    k_mlp<true><<<256, 256, MLP_LDS, stream>>>(ws, x, b1, b2, nullptr);
    k_attn<false><<<256, 512, ATT_LDS, stream>>>(ws, w1, w2, wo, wq, wk, wv);
    k_mlp<false><<<256, 256, MLP_LDS, stream>>>(ws, x2, b1, b2, (u16*)d_out);
}
```

```cpp
#include <hip/hip_runtime.h>
#include <hip/hip_bf16.h>
#include <stdint.h>


typedef unsigned short u16;
typedef short bf16x8 __attribute__((ext_vector_type(8)));
typedef short bf16x4 __attribute__((ext_vector_type(4)));
typedef float f32x16 __attribute__((ext_vector_type(16)));
typedef float f32x4 __attribute__((ext_vector_type(4)));
typedef float f32x2 __attribute__((ext_vector_type(2)));
typedef unsigned u32x4 __attribute__((ext_vector_type(4)));
typedef unsigned u32x2 __attribute__((ext_vector_type(2)));
typedef __bf16 bf2_t __attribute__((ext_vector_type(2)));
typedef __attribute__((address_space(3))) char* lds_ptr;
typedef __attribute__((address_space(3))) const char* lds_cptr;
typedef __attribute__((address_space(1))) unsigned long long gu64;

#define DI __device__ __forceinline__
#define SBAR() __builtin_amdgcn_sched_barrier(0)
#define PIN(x) asm volatile("" : "+v"(x))
#define MFMA32_16(a, b, c) __builtin_amdgcn_mfma_f32_32x32x16_bf16(a, b, c, 0, 0, 0)
#define MFMA32_8(a, b, c) __builtin_amdgcn_mfma_f32_32x32x8bf16_1k(a, b, c, 0, 0, 0)
#define MFMA16_32(a, b, c) __builtin_amdgcn_mfma_f32_16x16x32_bf16(a, b, c, 0, 0, 0)
#define EX2(x) __builtin_amdgcn_exp2f(x)
#define RAW_BARRIER() asm volatile("s_waitcnt lgkmcnt(0)\n\ts_barrier" ::: "memory")
#define VMCNT(n) asm volatile("s_waitcnt vmcnt(%0)" :: "i"(n) : "memory")

#define PREP_STORE(p, v) asm volatile("global_store_dwordx4 %0, %1, off sc1\n\ts_nop 1" :: "v"(p), "v"(v) : "memory")
#define KADDR(dst, sym) asm volatile("s_getpc_b64 s[40:41]\n\ts_add_u32 s40, s40, " sym "@rel32@lo+4\n\ts_addc_u32 s41, s41, " sym "@rel32@hi+12\n\ts_mov_b64 %0, s[40:41]" : "=s"(dst) :: "s40", "s41", "scc")
constexpr int S_ = 2048, D_ = 640, F_ = 1728;
constexpr float LN_EPS = 1e-5f;
constexpr float QSCALE = 0.22360679774997896f * 1.4426950408889634f;
constexpr float SAFE_BOUND = 48.0f;

DI unsigned pk2(float a, float b) { f32x2 v = {a, b}; bf2_t r = __builtin_convertvector(v, bf2_t); return __builtin_bit_cast(unsigned, r); }
DI float bf_lo(unsigned u) { return __uint_as_float(u << 16); }
DI float bf_hi(unsigned u) { return __uint_as_float(u & 0xffff0000u); }
DI float bf2f(u16 v) { return __uint_as_float((unsigned)v << 16); }
DI float dot2(unsigned a, unsigned b, float c) { return __builtin_amdgcn_fdot2_f32_bf16(__builtin_bit_cast(bf2_t, a), __builtin_bit_cast(bf2_t, b), c, false); }
DI bf16x4 tr_read(lds_cptr p) { return __builtin_amdgcn_ds_read_tr16_b64_v4i16((__attribute__((address_space(3))) bf16x4*)p); }
DI bf16x8 cat8(bf16x4 a, bf16x4 b) { return __builtin_shufflevector(a, b, 0, 1, 2, 3, 4, 5, 6, 7); }
DI void glds16(const void* g, lds_ptr l) {
    unsigned keep;
    asm volatile("s_mov_b32 %0, m0\n\ts_mov_b32 m0, %2\n\ts_nop 0\n\tglobal_load_lds_dwordx4 %1, off\n\ts_mov_b32 m0, %0"
                 : "=&s"(keep) : "v"(g), "s"((unsigned)(uintptr_t)l) : "memory");
}
DI float xor32(float v) { return __shfl_xor(v, 32); }
DI float add32(float v) {
    auto r = __builtin_amdgcn_permlane32_swap(__float_as_uint(v), __float_as_uint(v), false, false);
    return __uint_as_float(r[0]) + __uint_as_float(r[1]);
}
DI float add16(float v) {
    auto r = __builtin_amdgcn_permlane16_swap(__float_as_uint(v), __float_as_uint(v), false, false);
    return __uint_as_float(r[0]) + __uint_as_float(r[1]);
}
DI float max16x(float v) { auto r = __builtin_amdgcn_permlane16_swap(__float_as_uint(v), __float_as_uint(v), false, false); return fmaxf(__uint_as_float(r[0]), __uint_as_float(r[1])); }
DI float max32x(float v) { auto r = __builtin_amdgcn_permlane32_swap(__float_as_uint(v), __float_as_uint(v), false, false); return fmaxf(__uint_as_float(r[0]), __uint_as_float(r[1])); }
template <int CTRL> DI float dppf(float v) { return __int_as_float(__builtin_amdgcn_update_dpp(0, __float_as_int(v), CTRL, 0xf, 0xf, false)); }
DI float rowmax16(float v) {
    v = fmaxf(v, dppf<0x128>(v)); v = fmaxf(v, dppf<0x124>(v)); v = fmaxf(v, dppf<0x122>(v)); v = fmaxf(v, dppf<0x121>(v)); return v;
}
DI float wavemax(float v) { return max32x(max16x(rowmax16(v))); }

constexpr size_t OFF_Q = 0;
constexpr size_t OFF_K1 = OFF_Q + (size_t)2048 * 32 * 48;
constexpr size_t OFF_K2 = OFF_K1 + 2048 * 32;
constexpr size_t OFF_V = OFF_K2 + 2048 * 8;
constexpr size_t OFF_KN = OFF_V + 128 * 640;
constexpr size_t OFF_QN = OFF_KN + 512;
constexpr size_t OFF_CS = OFF_QN + 4096;
constexpr size_t OFF_CONST = OFF_CS + 4096;
constexpr size_t OFF_CNT = OFF_CONST + 256;
constexpr size_t OFF_XG = OFF_CNT + 48 * 256;
constexpr size_t OFF_XS = OFF_XG + 131072;
constexpr size_t OFF_H = OFF_XS + 131072 + 256;
constexpr size_t OFF_U = OFF_H + (size_t)S_ * D_ * 2;
constexpr size_t OFF_X2 = OFF_U + (size_t)D_ * F_ * 2;
constexpr size_t OFF_O = OFF_X2 + (size_t)S_ * D_ * 2;
constexpr size_t OFF_W1F = OFF_O + (size_t)S_ * D_ * 2;
constexpr size_t OFF_W2F = OFF_W1F + (size_t)S_ * F_ * 2;
constexpr size_t OFF_WQF = OFF_W2F + (size_t)S_ * F_ * 2;
constexpr size_t OFF_WOF = OFF_WQF + (size_t)44 * 20 * 1024;
static_assert(OFF_H % 1024 == 0, "fragment buffers are 1 KiB aligned");
constexpr size_t WS_NEED = OFF_WOF + (size_t)800 * 1024;
constexpr int NPREP = 1728 + 1728 + 200 + 200;

DI float rbf(float v) { return bf_lo(pk2(v, 0.f)); }
DI float ln_sd(float var) { return rbf(__builtin_amdgcn_sqrtf(rbf(rbf(var) + LN_EPS))); }
DI void ln2_scalars(float mu, float var, float& shift, float& scale) {
    const float m1 = rbf(mu), sd1 = ln_sd(var), i1 = 1.0f / sd1;
    const float m2 = rbf((mu - m1) * i1), sd2 = ln_sd(var * i1 * i1);
    shift = m1 + m2 * sd1; scale = i1 / sd2;
}
DI void chan8(const f32x4 (&st)[4], float& mean, float& var) {
    float m = 0.f, m2 = 0.f;
#pragma unroll
    for (int i = 0; i < 4; ++i) { m += st[i][0] + st[i][2]; m2 += st[i][1] + st[i][3]; }
    m *= 0.125f;
#pragma unroll
    for (int i = 0; i < 4; ++i) { const float d0 = st[i][0] - m, d1 = st[i][2] - m; m2 += 80.f * (d0 * d0 + d1 * d1); }
    mean = m; var = m2 * (1.f / 640.f);
}

template <bool FAST>
DI void qkv_finish(char* __restrict__ ws, char* L, int tb, int cg, int w, int lane, f32x4 (&acc)[4][5], f32x4 (&acck)[2],
                   const f32x4 (&csq)[5], const f32x4& cs0, const f32x4& cs1, const f32x4 (&stq)[4], const f32x4 (&stk)[4],
                   const float (&sm)[4], const float (&sq)[4], const float (&cs)[5], const float (&csk)[2]) {
    const int n16 = lane & 15, g = lane >> 4, mt0 = cg >> 1, isv = cg & 1;
    constexpr int R_OFF = 0, PK_OFF = 61440, ST_OFF = 69632, CS_OFF = 71680;
#pragma unroll
    for (int j = 1; j < 4; ++j) {
        const int o = (w + j) & 3;
#pragma unroll
        for (int nt = 0; nt < 5; ++nt) *(f32x4*)(L + R_OFF + (((o * 3 + (j - 1)) * 5 + nt) * 64 + lane) * 16) = acc[j][nt];
    }
    *(f32x4*)(L + PK_OFF + ((w * 2 + 0) * 64 + lane) * 16) = acck[0];
    *(f32x4*)(L + PK_OFF + ((w * 2 + 1) * 64 + lane) * 16) = acck[1];
    if (!FAST && g == 0) {
#pragma unroll
        for (int i = 0; i < 4; ++i) *(f32x2*)(L + ST_OFF + (w * 64 + ((i + w) & 3) * 16 + n16) * 8) = f32x2{sm[i], sq[i]};
#pragma unroll
        for (int nt = 0; nt < 5; ++nt) *(float*)(L + CS_OFF + w * 448 + (nt * 16 + n16) * 4) = cs[nt];
#pragma unroll
        for (int nt = 0; nt < 2; ++nt) *(float*)(L + CS_OFF + w * 448 + (80 + nt * 16 + n16) * 4) = csk[nt];
    }
    char* qrow = ws + OFF_Q + (size_t)(tb * 8 + cg) * 12288 + (16 * w + n16) * 48;
    int qoff[5];
#pragma unroll
    for (int nt = 0; nt < 5; ++nt) { const int G = 4 * nt + g, hl = G / 5; qoff[nt] = hl * 3072 + (G - 5 * hl) * 8; PIN(qoff[nt]); }
    RAW_BARRIER();
    auto stats = [&](int tl, const f32x4 (&st)[4], float& mean, float& rstd) {
        float var;
        if (FAST) chan8(st, mean, var);
        else {
            float s = 0.f, s2 = 0.f;
#pragma unroll
            for (int ww = 0; ww < 4; ++ww) { f32x2 v = *(const f32x2*)(L + ST_OFF + (ww * 64 + tl) * 8); s += v[0]; s2 += v[1]; }
            mean = s * (1.f / 640.f);
            var = fmaxf(s2 * (1.f / 640.f) - mean * mean, 0.f);
        }
        mean = rbf(mean);
        rstd = 1.0f / ln_sd(var);
    };
    {
        f32x4 rr[3][5], cc[4][5];
#pragma unroll
        for (int j = 0; j < 3; ++j)
#pragma unroll
            for (int nt = 0; nt < 5; ++nt) rr[j][nt] = *(const f32x4*)(L + R_OFF + (((w * 3 + j) * 5 + nt) * 64 + lane) * 16);
        if (!FAST) {
#pragma unroll
            for (int ww = 0; ww < 4; ++ww)
#pragma unroll
                for (int nt = 0; nt < 5; ++nt) cc[ww][nt] = *(const f32x4*)(L + CS_OFF + ww * 448 + (nt * 16 + 4 * g) * 4);
        }
        float mean, rstd; stats(16 * w + n16, stq, mean, rstd);
        SBAR();
        const float sc = rstd * QSCALE;
        float n2[5];
#pragma unroll
        for (int nt = 0; nt < 5; ++nt) {
            const f32x4 a = (acc[0][nt] + rr[0][nt]) + (rr[1][nt] + rr[2][nt]);
            f32x4 s;
            if (FAST) s = csq[nt];
            else {
                s = (cc[0][nt] + cc[1][nt]) + (cc[2][nt] + cc[3][nt]);
                if (tb == 0 && w == 0 && n16 == 0) *(f32x4*)(ws + OFF_CS + (80 * cg + 16 * nt + 4 * g) * 4) = s;
            }
            const unsigned u0 = pk2((a[0] - mean * s[0]) * sc, (a[1] - mean * s[1]) * sc);
            const unsigned u1 = pk2((a[2] - mean * s[2]) * sc, (a[3] - mean * s[3]) * sc);
            *(u32x2*)(qrow + qoff[nt]) = u32x2{u0, u1};
            n2[nt] = dot2(u1, u1, dot2(u0, u0, 0.f));
        }
        float hn[4];
        hn[0] = n2[0] + (g == 0 ? n2[1] : 0.f);
        hn[1] = (g > 0 ? n2[1] : 0.f) + (g < 2 ? n2[2] : 0.f);
        hn[2] = (g > 1 ? n2[2] : 0.f) + (g < 3 ? n2[3] : 0.f);
        hn[3] = (g == 3 ? n2[3] : 0.f) + n2[4];
#pragma unroll
        for (int hh = 0; hh < 4; ++hh) hn[hh] = add32(add16(hn[hh]));
        const float n2a = (g & 1) ? hn[1] : hn[0], n2b = (g & 1) ? hn[3] : hn[2];
        const unsigned qb = pk2(__builtin_amdgcn_sqrtf((g & 2) ? n2b : n2a), 0.f);
        *(u32x2*)(qrow + g * 3072 + 40) = u32x2{qb, 0u};
        float m = bf_lo(qb);
        if (!(m == m)) m = INFINITY;
        m = wavemax(m);
        if (lane == 0) *(float*)(ws + OFF_QN + ((tb * 8 + cg) * 4 + w) * 4) = m;
    }
    if (w == 3) {
        const int t16 = tb * 4 + mt0;
        float mean, rstd; stats(mt0 * 16 + n16, stk, mean, rstd);
        f32x4 a0 = {0.f, 0.f, 0.f, 0.f}, a1 = a0, s0 = a0, s1 = a0;
#pragma unroll
        for (int ww = 0; ww < 4; ++ww) {
            a0 += *(const f32x4*)(L + PK_OFF + ((ww * 2 + 0) * 64 + lane) * 16);
            a1 += *(const f32x4*)(L + PK_OFF + ((ww * 2 + 1) * 64 + n16) * 16);
            if (!FAST) {
                s0 += *(const f32x4*)(L + CS_OFF + ww * 448 + (80 + 4 * g) * 4);
                s1 += *(const f32x4*)(L + CS_OFF + ww * 448 + 96 * 4);
            }
        }
        if (FAST) { s0 = cs0; s1 = cs1; }
        else if (tb == 0 && cg < 2 && n16 == 0) {
            *(f32x4*)(ws + OFF_CS + (640 + 32 * isv + 4 * g) * 4) = s0;
            if (g == 0) *(f32x4*)(ws + OFF_CS + (640 + 32 * isv + 16) * 4) = s1;
        }
        const unsigned u0 = pk2((a0[0] - mean * s0[0]) * rstd, (a0[1] - mean * s0[1]) * rstd);
        const unsigned u1 = pk2((a0[2] - mean * s0[2]) * rstd, (a0[3] - mean * s0[3]) * rstd);
        const unsigned u2 = pk2((a1[0] - mean * s1[0]) * rstd, (a1[1] - mean * s1[1]) * rstd);
        const unsigned u3 = pk2((a1[2] - mean * s1[2]) * rstd, (a1[3] - mean * s1[3]) * rstd);
        if (!isv) {
            const int key = t16 * 16 + n16;
            *(u32x2*)(ws + OFF_K1 + key * 32 + g * 8) = u32x2{u0, u1};
            if (g == 0) *(u32x2*)(ws + OFF_K2 + key * 8) = u32x2{u2, u3};
            float n2 = dot2(u1, u1, dot2(u0, u0, 0.f));
            n2 = add32(add16(n2));
            n2 = dot2(u3, u3, dot2(u2, u2, n2));
            if (!(n2 == n2)) n2 = INFINITY;
            n2 = rowmax16(n2);
            if (lane == 0) *(float*)(ws + OFF_KN + t16 * 4) = n2;
        } else {
            u16* vb = (u16*)(ws + OFF_V + t16 * 640 + (n16 >> 3) * 320) + (n16 & 7);
            vb[(4 * g + 0) * 8] = (u16)u0; vb[(4 * g + 1) * 8] = (u16)(u0 >> 16);
            vb[(4 * g + 2) * 8] = (u16)u1; vb[(4 * g + 3) * 8] = (u16)(u1 >> 16);
            if (g == 0) { vb[16 * 8] = (u16)u2; vb[17 * 8] = (u16)(u2 >> 16); vb[18 * 8] = (u16)u3; vb[19 * 8] = (u16)(u3 >> 16); }
        }
    }
    if (w == 0) *(u32x2*)(ws + OFF_XG + ((size_t)(tb * 64 + lane) * 8 + cg) * 8) = u32x2{0u, 0u};
}

constexpr int QKV_LDS = 163840;
__global__ __launch_bounds__(256, 1) void k_qkv(const u16* __restrict__ x, const u16* __restrict__ wq, const u16* __restrict__ wk,
                                                const u16* __restrict__ wv, char* __restrict__ ws) {
    extern __shared__ __attribute__((aligned(16))) char lds_raw[];
    lds_ptr lds = (lds_ptr)lds_raw;
    char* L = lds_raw;
    const int tid = threadIdx.x, lane = tid & 63, w = __builtin_amdgcn_readfirstlane(tid >> 6);
    const int b = blockIdx.x, slot = b >> 3, tb = ((b & 7) >> 1) * 8 + (slot >> 2), cg = (b & 1) * 4 + (slot & 3);
    const int mt0 = cg >> 1, isv = cg & 1;
    const int n16 = lane & 15, g = lane >> 4;
    unsigned code_touch = 0u;
    if (w == 2) {
        uint64_t a[5];
        KADDR(a[0], "_Z6k_attnILb1EEvPcPKtS2_S2_S2_S2_S2_"); KADDR(a[1], "_Z6k_attnILb0EEvPcPKtS2_S2_S2_S2_S2_");
        KADDR(a[2], "_Z5k_mlpILb1EEvPcPKtS2_S2_Pt"); KADDR(a[3], "_Z5k_mlpILb0EEvPcPKtS2_S2_Pt"); KADDR(a[4], "_Z5k_endILi0EEvv");
        uint64_t lo = a[0], hi = a[0];
#pragma unroll
        for (int i = 1; i < 5; ++i) { lo = a[i] < lo ? a[i] : lo; hi = a[i] > hi ? a[i] : hi; }
        const uint64_t pa = lo + (uint64_t)(slot * 64 + lane) * 128;
        if (pa < hi) code_touch = *(const __attribute__((address_space(1))) unsigned*)pa;
    }
    SBAR();
    f32x4 acc[4][5], acck[2];
#pragma unroll
    for (int i = 0; i < 4; ++i)
#pragma unroll
        for (int j = 0; j < 5; ++j) acc[i][j] = f32x4{0.f, 0.f, 0.f, 0.f};
    acck[0] = f32x4{0.f, 0.f, 0.f, 0.f}; acck[1] = acck[0];
    f32x4 csq[5], cs0, cs1, stq[4], stk[4];
    float sm[4] = {0.f, 0.f, 0.f, 0.f}, sq[4] = {0.f, 0.f, 0.f, 0.f};
    float cs[5] = {0.f, 0.f, 0.f, 0.f, 0.f}, csk[2] = {0.f, 0.f};
    {
        lds_ptr ra = lds + w * 40960, rb = ra + 20480;
        const char* xs = (const char*)(x + (size_t)(tb * 64) * 640 + 160 * w);
#pragma unroll
        for (int j = 0; j < 5; ++j) {
            const int c = 64 * j + lane;
            const unsigned xo = (unsigned)(c / 20) * 1280u + (unsigned)(c % 20) * 16u;
#pragma unroll
            for (int i = 0; i < 4; ++i) glds16(xs + (size_t)i * 20480 + xo, ra + i * 5120 + j * 1024);
        }
        const char* src = (const char*)(wq + (size_t)(160 * w) * 640 + 80 * cg);
        const unsigned loff = (unsigned)(lane / 10) * 1280u + (unsigned)(lane % 10) * 16u;
        const char* src2 = (const char*)((isv ? wv : wk) + (size_t)(160 * w) * 20);
#pragma unroll
        for (int i = 0; i < 16; ++i) glds16(src + (size_t)i * 7680 + loff, rb + i * 960);
#pragma unroll
        for (int i = 0; i < 3; ++i) glds16(src2 + i * 1024 + lane * 16, rb + 15488 + i * 1024);
        if (lane < 48) glds16(src2 + 3 * 1024 + lane * 16, rb + 15488 + 3 * 1024);
        VMCNT(20);
        bf16x8 xf[4][5], xk[5];
        {
            const char* xl = L + w * 40960 + n16 * 320 + g * 16;
#pragma unroll
            for (int i = 0; i < 4; ++i)
#pragma unroll
                for (int ks = 0; ks < 5; ++ks) xf[i][ks] = *(const bf16x8*)(xl + ((i + w) & 3) * 5120 + ks * 64);
#pragma unroll
            for (int ks = 0; ks < 5; ++ks) xk[ks] = *(const bf16x8*)(xl + mt0 * 5120 + ks * 64);
        }
        asm volatile("s_waitcnt lgkmcnt(0)" ::: "memory");
        SBAR();
#pragma unroll
        for (int i = 0; i < 10; ++i) glds16(src + (size_t)(16 + i) * 7680 + loff, ra + i * 960);
        if (lane < 40) glds16(src + (size_t)26 * 7680 + loff, ra + 10 * 960);
#pragma unroll
        for (int i = 0; i < 2; ++i) glds16(src2 + 3840 + i * 1024 + lane * 16, ra + 10240 + i * 1024);
        if (lane < 32) glds16(src2 + 3840 + 2 * 1024 + lane * 16, ra + 10240 + 2 * 1024);
        const unsigned ONES = 0x3f803f80u;
#pragma unroll
        for (int i = 0; i < 4; ++i)
#pragma unroll
            for (int ks = 0; ks < 5; ++ks) {
                u32x4 u = __builtin_bit_cast(u32x4, xf[i][ks]);
#pragma unroll
                for (int e = 0; e < 4; ++e) { sm[i] = dot2(u[e], ONES, sm[i]); sq[i] = dot2(u[e], u[e], sq[i]); }
            }
        SBAR();
        VMCNT(14);
        const int q4 = (lane & 15) >> 2, p4 = lane & 3;
        lds_cptr wb0 = (lds_cptr)rb + (8 * g + q4) * 160 + p4 * 8, wb1 = (lds_cptr)ra + (8 * g + q4) * 160 + p4 * 8 - 3 * 5120;
        lds_cptr kb0 = (lds_cptr)rb + 15488 + (8 * g + q4) * 40 + p4 * 8, kb1 = (lds_cptr)ra + 10240 + (8 * g + q4) * 40 + p4 * 8 - 3 * 1280;
        bf16x8 wf[2][7];
#define QKV_TR(ks, s_) do { lds_cptr wb = (ks) < 3 ? wb0 : wb1, kb = (ks) < 3 ? kb0 : kb1; \
        _Pragma("unroll") for (int nt = 0; nt < 5; ++nt) wf[s_][nt] = cat8(tr_read(wb + (ks) * 5120 + nt * 32), tr_read(wb + (ks) * 5120 + 640 + nt * 32)); \
        _Pragma("unroll") for (int nt = 0; nt < 2; ++nt) wf[s_][5 + nt] = cat8(tr_read(kb + (ks) * 1280 + nt * 32), tr_read(kb + (ks) * 1280 + 160 + nt * 32)); SBAR(); } while (0)
        QKV_TR(0, 0);
#pragma unroll
        for (int ks = 0; ks < 5; ++ks) {
            if (ks == 2) {
                VMCNT(0); SBAR();
            }
            if (ks < 4) QKV_TR(ks + 1, (ks + 1) & 1);
#pragma unroll
            for (int nt = 0; nt < 5; ++nt) {
                u32x4 u = __builtin_bit_cast(u32x4, wf[ks & 1][nt]);
#pragma unroll
                for (int e = 0; e < 4; ++e) cs[nt] = dot2(u[e], ONES, cs[nt]);
#pragma unroll
                for (int i = 0; i < 4; ++i) acc[i][nt] = MFMA16_32(wf[ks & 1][nt], xf[i][ks], acc[i][nt]);
            }
#pragma unroll
            for (int nt = 0; nt < 2; ++nt) {
                u32x4 u = __builtin_bit_cast(u32x4, wf[ks & 1][5 + nt]);
#pragma unroll
                for (int e = 0; e < 4; ++e) csk[nt] = dot2(u[e], ONES, csk[nt]);
                acck[nt] = MFMA16_32(wf[ks & 1][5 + nt], xk[ks], acck[nt]);
            }
            SBAR();
        }
#pragma unroll
        for (int i = 0; i < 4; ++i) { sm[i] = add32(add16(sm[i])); sq[i] = add32(add16(sq[i])); }
#pragma unroll
        for (int i = 0; i < 5; ++i) cs[i] = add32(add16(cs[i]));
#pragma unroll
        for (int i = 0; i < 2; ++i) csk[i] = add32(add16(csk[i]));
    }
    RAW_BARRIER();
    qkv_finish<false>(ws, L, tb, cg, w, lane, acc, acck, csq, cs0, cs1, stq, stk, sm, sq, cs, csk);
    PIN(code_touch);
    if (b == 0 && tid < 4) {
        const unsigned v = (tid & 2) ? 0x3f803f80u : 0u;
        *(u32x4*)(ws + OFF_CONST + tid * 16) = u32x4{v, v, v, v};
    }
}

constexpr int PJ0 = 0, PJ1 = 4, PJD = 3;
constexpr int PBW = 2688, A_STG = 32 * PBW, ATT_LDS = A_STG + 32768;
template <bool PREP>
__global__ __launch_bounds__(512, 2) void k_attn(char* __restrict__ ws, const u16* __restrict__ w1, const u16* __restrict__ w2,
                                                 const u16* __restrict__ wo, const u16* __restrict__ wq,
                                                 const u16* __restrict__ wk, const u16* __restrict__ wv) {
    extern __shared__ __attribute__((aligned(16))) char lds_raw[];
    char* L = lds_raw;
    const int tid = threadIdx.x, lane = tid & 63, w = __builtin_amdgcn_readfirstlane(tid >> 6);
    const int b = blockIdx.x, slot = b >> 3, tb = ((b & 7) >> 1) * 8 + (slot >> 2), cg = (b & 1) * 4 + (slot & 3);
    const int r32 = lane & 31, h = lane >> 5, n16 = lane & 15, g = lane >> 4;
    const int cgp = w & 1, kq = w >> 1;
    float kn0 = *(const float*)(ws + OFF_KN + lane * 4), kn1 = *(const float*)(ws + OFF_KN + 256 + lane * 4);
    float qmax = *(const float*)(ws + OFF_QN + ((tb * 8 + cg) * 4 + (lane & 3)) * 4);
    bf16x8 q1[4]; bf16x4 q2[4];
    {
        const char* qg = ws + OFF_Q + (size_t)(tb * 8 + cg) * 12288 + cgp * 6144 + r32 * 48;
#pragma unroll
        for (int j = 0; j < 4; ++j) { q1[j] = *(const bf16x8*)(qg + j * 1536 + 16 * h); q2[j] = *(const bf16x4*)(qg + j * 1536 + 32 + 8 * h); }
    }
    SBAR();
    const int kr = (r32 & 19) | ((r32 & 4) << 1) | ((r32 & 8) >> 1);
    const unsigned ko1 = (unsigned)OFF_K1 + (unsigned)(512 * kq + kr) * 32u + 16u * h;
    const unsigned ko2 = h ? (unsigned)OFF_CONST + 32u : (unsigned)OFF_K2 + (unsigned)(512 * kq + kr) * 8u;
    const unsigned ks2 = h ? 0u : 256u;
    const unsigned vo = r32 < 20 ? (unsigned)OFF_V + (unsigned)(32 * kq) * 640u + (unsigned)(h * 20 + r32) * 16u
                                 : (unsigned)OFF_CONST + (r32 == 20 ? 32u : 0u);
    const unsigned vs = r32 < 20 ? 640u : 0u;
    bf16x8 k1[3], v0[3], v1[3]; bf16x4 k2[3];
#define K_LOAD(t) do { k1[(t) % 3] = *(const bf16x8*)(ws + ko1 + (t) * 1024); k2[(t) % 3] = *(const bf16x4*)(ws + (ko2 + (t) * ks2)); } while (0)
#define V_LOAD(t) do { v0[(t) % 3] = *(const bf16x8*)(ws + (vo + (2 * (t)) * vs)); v1[(t) % 3] = *(const bf16x8*)(ws + (vo + (2 * (t) + 1) * vs)); } while (0)
    K_LOAD(0); V_LOAD(0); SBAR(); K_LOAD(1); V_LOAD(1); SBAR();
    PIN(kn0); PIN(kn1);
    const float kmax2 = wavemax(fmaxf(kn0, kn1));
    qmax = rowmax16(qmax);
    const float kb = __builtin_amdgcn_sqrtf(kmax2);
    const bool safe = __builtin_amdgcn_readfirstlane((int)(qmax * kb <= SAFE_BOUND)) != 0;
    const f32x16 zero16 = {0.f, 0.f, 0.f, 0.f, 0.f, 0.f, 0.f, 0.f, 0.f, 0.f, 0.f, 0.f, 0.f, 0.f, 0.f, 0.f};
    if (safe) {
        if (h) {
#pragma unroll
            for (int j = 0; j < 4; ++j) q2[j][0] = (short)pk2(-bf2f((u16)q2[j][0]) * kb, 0.f);
        }
    } else {
        float m[4];
#pragma unroll
        for (int j = 0; j < 4; ++j) { m[j] = -INFINITY; if (h) q2[j] = bf16x4{0, 0, 0, 0}; }
        for (int t = 0; t < 16; ++t) {
            const bf16x8 ka = *(const bf16x8*)(ws + ko1 + t * 1024);
            const bf16x4 kc = *(const bf16x4*)(ws + (ko2 + t * ks2));
#pragma unroll
            for (int j = 0; j < 4; ++j) {
                f32x16 s = MFMA32_16(ka, q1[j], zero16);
                SBAR(); asm volatile("s_nop 15" ::: "memory"); SBAR();
                s = MFMA32_8(kc, q2[j], s);
#pragma unroll
                for (int e = 0; e < 16; ++e) m[j] = fmaxf(m[j], s[e]);
                SBAR();
            }
        }
#pragma unroll
        for (int j = 0; j < 4; ++j) {
            const float mm = fmaxf(m[j], xor32(m[j]));
            if (h == 0) *(float*)(L + ((w * 4 + j) * 32 + r32) * 4) = mm;
        }
        RAW_BARRIER();
#pragma unroll
        for (int j = 0; j < 4; ++j) {
            float mm = -INFINITY;
#pragma unroll
            for (int qq = 0; qq < 4; ++qq) mm = fmaxf(mm, *(const float*)(L + (((2 * qq + cgp) * 4 + j) * 32 + r32) * 4));
            const unsigned hi = __float_as_uint(mm) & 0xffff0000u;
            const float r1 = mm - __uint_as_float(hi);
            const unsigned mid = __float_as_uint(r1) & 0xffff0000u;
            const float r2 = r1 - __uint_as_float(mid);
            const unsigned lo = __float_as_uint(r2) & 0xffff0000u;
            if (h) q2[j] = __builtin_bit_cast(bf16x4, u32x2{hi ^ 0x80000000u, ((mid ^ 0x80000000u) >> 16) | (lo ^ 0x80000000u)});
        }
        RAW_BARRIER();
    }
    f32x16 o[4], sa, sb;
#pragma unroll
    for (int j = 0; j < 4; ++j) o[j] = zero16;
    u32x4 p1prev = {0u, 0u, 0u, 0u};
    sa = MFMA32_16(k1[0], q1[0], zero16);
    SBAR(); asm volatile("s_nop 15" ::: "memory"); SBAR();
    sa = MFMA32_8(k2[0], q2[0], sa);
    SBAR(); asm volatile("s_nop 15" ::: "memory"); SBAR();
    u32x4 prep[4]; char* prep_dst = nullptr; int prep_st = 0;
#pragma unroll
    for (int t = 0; t < 16; ++t) {
#pragma unroll
        for (int j = 0; j < 4; ++j) {
            const int it = 4 * t + j;
            f32x16& cur = (it & 1) ? sb : sa; f32x16& nxt = (it & 1) ? sa : sb;
            const int tn = j < 3 ? t : t + 1, jn = (j + 1) & 3;
            const int tp = j > 0 ? t : t - 1, jp = (j + 3) & 3;
            u32x4 p0, p1;
            if (j == 0 && t + 2 < 16) K_LOAD(t + 2);
            if (j == 1 && t + 2 < 16) V_LOAD(t + 2);
            if (PREP && j == 2 && (t == PJ0 || t == PJ1)) {
                int id = (t == PJ1 ? 2048 : 0) + b * 8 + w; if (id >= NPREP) id -= 2048;
                const u16* src; int ld;
                if (id < 1728) { const int ks = id / 27, c4 = id - ks * 27; src = w1 + (size_t)(32 * ks) * F_ + 64 * c4; ld = F_;
                                 prep_dst = ws + OFF_W1F + (size_t)(4 * c4 * 64 + ks) * 1024; prep_st = 64 * 1024; }
                else if (id < 3456) { const int jj = id - 1728, ks = jj >> 5, c4 = jj & 31; src = w2 + (size_t)(32 * ks) * S_ + 64 * c4; ld = S_;
                                      prep_dst = ws + OFF_W2F + (size_t)(4 * c4 * 54 + ks) * 1024; prep_st = 54 * 1024; }
                else { int jj = id - 3456; const bool isq = jj >= 200; if (isq) jj -= 200; const int ks = jj / 10, c4 = jj - ks * 10;
                       src = (isq ? wq : wo) + (size_t)(32 * ks) * 640 + 64 * c4; ld = 640;
                       prep_dst = ws + (isq ? OFF_WQF : OFF_WOF) + (size_t)(4 * c4 * 20 + ks) * 1024; prep_st = 20 * 1024; }
                src += (size_t)(lane >> 3) * ld + (lane & 7) * 8;
#pragma unroll
                for (int i = 0; i < 4; ++i) prep[i] = *(const u32x4*)(src + (size_t)(8 * i) * ld);
            }
            if (PREP && j == 2 && t == 9) {
                const int id = (b * 8 + w) % 80, ks = id >> 2, ct = id & 3; int cn = 16 * (ct & 1) + n16; if (cn >= 20) cn = 0;
                const u16* sp = ((ct & 2) ? wv : wk) + (size_t)(32 * ks + 8 * g) * 20 + cn;
                if (b < 10) {
#pragma unroll
                    for (int e = 0; e < 8; ++e) prep[e >> 2][e & 3] = sp[e * 20];
                }
                prep_dst = ws + OFF_WQF + (size_t)((40 + ct) * 20 + ks) * 1024;
            }
            if (PREP && j == 2 && t == 12 && b < 10)
                *(u32x4*)(prep_dst + lane * 16) = u32x4{prep[0][0] | (prep[0][1] << 16), prep[0][2] | (prep[0][3] << 16),
                                                        prep[1][0] | (prep[1][1] << 16), prep[1][2] | (prep[1][3] << 16)};
            if (PREP && j == 2 && (t == PJ0 + PJD || t == PJ1 + PJD)) {
#pragma unroll
                for (int i = 0; i < 4; ++i) *(u32x4*)(L + A_STG + w * 4096 + i * 1024 + lane * 16) = prep[i];
            }
            if (PREP && j == 3 && (t == PJ0 + PJD || t == PJ1 + PJD)) {
                lds_cptr tp_ = (lds_cptr)(lds_ptr)L + A_STG + w * 4096 + (8 * g + (n16 >> 2)) * 128 + (lane & 3) * 8;
#pragma unroll
                for (int i = 0; i < 4; ++i) prep[i] = __builtin_bit_cast(u32x4, cat8(tr_read(tp_ + i * 32), tr_read(tp_ + 512 + i * 32)));
            }
            if (PREP && j == 0 && (t == PJ0 + PJD + 1 || t == PJ1 + PJD + 1)) {
#pragma unroll
                for (int i = 0; i < 4; ++i) PREP_STORE((u32x4*)(prep_dst + (size_t)i * prep_st + lane * 16), prep[i]);
            }
            SBAR();
            if (it < 63) nxt = MFMA32_16(k1[tn % 3], q1[jn], zero16);
            SBAR();
            p0[0] = pk2(EX2(cur[0]), EX2(cur[1])); p0[1] = pk2(EX2(cur[2]), EX2(cur[3]));
            PIN(p0); SBAR();
            if (it > 0) o[jp] = MFMA32_16(v1[(tp + 3) % 3], __builtin_bit_cast(bf16x8, p1prev), o[jp]);
            SBAR();
            p0[2] = pk2(EX2(cur[4]), EX2(cur[5])); p0[3] = pk2(EX2(cur[6]), EX2(cur[7]));
            PIN(p0); SBAR();
            if (it < 63) nxt = MFMA32_8(k2[tn % 3], q2[jn], nxt);
            SBAR();
            p1[0] = pk2(EX2(cur[8]), EX2(cur[9])); p1[1] = pk2(EX2(cur[10]), EX2(cur[11]));
            PIN(p1); SBAR();
            o[j] = MFMA32_16(v0[t % 3], __builtin_bit_cast(bf16x8, p0), o[j]);
            SBAR();
            p1[2] = pk2(EX2(cur[12]), EX2(cur[13])); p1[3] = pk2(EX2(cur[14]), EX2(cur[15]));
            PIN(p1); SBAR();
            p1prev = p1;
        }
    }
    o[3] = MFMA32_16(v1[15 % 3], __builtin_bit_cast(bf16x8, p1prev), o[3]);
#pragma unroll
    for (int j = 0; j < 4; ++j) {
        char* pb = L + (kq * 8 + cgp * 4 + j) * PBW;
        *(f32x4*)(pb + lane * 16) = f32x4{o[j][0], o[j][1], o[j][2], o[j][3]};
        *(f32x4*)(pb + 1024 + lane * 16) = f32x4{o[j][4], o[j][5], o[j][6], o[j][7]};
        if (h == 0) *(f32x4*)(pb + 2048 + r32 * 16) = f32x4{o[j][8], o[j][9], o[j][10], o[j][11]};
        else *(float*)(pb + 2560 + r32 * 4) = o[j][8];
    }
    RAW_BARRIER();
    {
        const char* pb = L + w * PBW;
        f32x4 a0 = {0.f, 0.f, 0.f, 0.f}, a1 = a0, a2 = a0; float l = 0.f;
#pragma unroll
        for (int qq = 0; qq < 4; ++qq) {
            a0 += *(const f32x4*)(pb + qq * 8 * PBW + lane * 16);
            a1 += *(const f32x4*)(pb + qq * 8 * PBW + 1024 + lane * 16);
            a2 += *(const f32x4*)(pb + qq * 8 * PBW + 2048 + r32 * 16);
            l += *(const float*)(pb + qq * 8 * PBW + 2560 + r32 * 4);
        }
        const float inv = __builtin_amdgcn_rcpf(l);
        const int tl = 32 * (w & 1) + r32, hd0 = 80 * cg + 20 * (w >> 1);
        char* ob = ws + OFF_O + (size_t)(tb * 4 + (tl >> 4)) * 20480 + (tl & 15) * 16;
        auto st = [&](int hd, const f32x4& a) {
            *(u32x2*)(ob + (hd >> 5) * 1024 + ((hd & 31) >> 3) * 256 + (hd & 7) * 2) = u32x2{pk2(a[0] * inv, a[1] * inv), pk2(a[2] * inv, a[3] * inv)};
        };
        st(hd0 + 4 * h, a0); st(hd0 + 8 + 4 * h, a1);
        if (h == 0) st(hd0 + 16, a2);
    }
    if (b == 0 && tid < 48) *(unsigned*)(ws + OFF_CNT + tid * 256) = 0u;
}

typedef __attribute__((address_space(1))) unsigned gu32;
constexpr int AUXL = 16;
#define LD_SC1(off) __builtin_bit_cast(bf16x8, __builtin_amdgcn_raw_buffer_load_b128(rs, (int)(off), 0, 16))
DI void edge_publish(gu32* cnt) {
    RAW_BARRIER();
    if (threadIdx.x == 0) __hip_atomic_fetch_add(cnt, 1u, __ATOMIC_RELAXED, __HIP_MEMORY_SCOPE_AGENT);
}
DI void edge_wait(gu32* cnt, unsigned want) {
    if (threadIdx.x == 0) {
        for (int spin = 0; spin < (1 << 20); ++spin) {
            if (__hip_atomic_load(cnt, __ATOMIC_RELAXED, __HIP_MEMORY_SCOPE_AGENT) >= want) break;
            __builtin_amdgcn_s_sleep(1);
        }
        __builtin_amdgcn_fence(__ATOMIC_ACQUIRE, "wavefront");
    }
    RAW_BARRIER();
}
constexpr int W1_A = 6, W2_FIRST = 7;
constexpr int MLP_LDS = 73728;
#define WO_LD(ks) do { _Pragma("unroll") for (int i = 0; i < 4; ++i) xf[ks][i] = xb[(((i + w) & 3) * 20 + (ks)) * 64]; \
        _Pragma("unroll") for (int nt = 0; nt < 5; ++nt) wf[ks][nt] = wb[(nt * 20 + (ks)) * 64]; SBAR(); } while (0)
template <bool R1>
__global__ __launch_bounds__(256, 1) void k_mlp(char* __restrict__ ws, const u16* __restrict__ xin, const u16* __restrict__ b1,
                                                const u16* __restrict__ b2, u16* __restrict__ out) {
    extern __shared__ __attribute__((aligned(16))) char lds_raw[];
    char* L = lds_raw;
    const int tid = threadIdx.x, lane = tid & 63, w = __builtin_amdgcn_readfirstlane(tid >> 6);
    const int b = blockIdx.x, slot = b >> 3, tb = ((b & 7) >> 1) * 8 + (slot >> 2), cg = (b & 1) * 4 + (slot & 3);
    const int ot1 = ((b & 7) >> 1) * 7 + (slot >> 2);
    const bool act1 = slot < 28 && ot1 < 27;
    const int n16 = lane & 15, g = lane >> 4;
    const __amdgpu_buffer_rsrc_t rs = __builtin_amdgcn_make_buffer_rsrc((void*)ws, 0, (int)WS_NEED, 0x00020000);
    gu32* cnt = (gu32*)(ws + OFF_CNT);
    f32x4 acc[4][5];
#define ACC_ZERO() do { _Pragma("unroll") for (int i_ = 0; i_ < 4; ++i_) _Pragma("unroll") for (int j_ = 0; j_ < 5; ++j_) acc[i_][j_] = f32x4{0.f, 0.f, 0.f, 0.f}; } while (0)
#define ACC_REDUCE() do { \
    _Pragma("unroll") for (int j_ = 1; j_ < 4; ++j_) { const int o_ = (w + j_) & 3; \
        _Pragma("unroll") for (int i_ = 0; i_ < 5; ++i_) *(f32x4*)(L + (((o_ * 3 + (j_ - 1)) * 5 + i_) * 64 + lane) * 16) = acc[j_][i_]; } \
    RAW_BARRIER(); \
    _Pragma("unroll") for (int j_ = 0; j_ < 3; ++j_) _Pragma("unroll") for (int i_ = 0; i_ < 5; ++i_) \
        acc[1 + j_][i_] = *(const f32x4*)(L + (((w * 3 + j_) * 5 + i_) * 64 + lane) * 16); \
    SBAR(); \
    _Pragma("unroll") for (int i_ = 0; i_ < 5; ++i_) acc[0][i_] = (acc[0][i_] + acc[1][i_]) + (acc[2][i_] + acc[3][i_]); } while (0)
    const int tok = tb * 64 + 16 * w + n16;
    bf16x8 wr[16][4];
    int ro1[4], ro2[4];
#pragma unroll
    for (int j = 0; j < 4; ++j) { ro1[j] = ((j + w) & 3) * 64 * 64; ro2[j] = ((j + w) & 3) * 54 * 64; }
    const bf16x8* pw1 = (const bf16x8*)(ws + OFF_W1F) + ((size_t)(4 * (act1 ? ot1 : 0)) * 64 + 16 * w) * 64 + lane;
    const int k2 = w < 2 ? 14 * w : 28 + 13 * (w - 2);
    const bf16x8* pw2 = (const bf16x8*)(ws + OFF_W2F) + ((size_t)(4 * tb) * 54 + k2) * 64 + lane;
#define W1_LD(lo, hi) do { _Pragma("unroll") for (int s_ = (lo); s_ < (hi); ++s_) _Pragma("unroll") for (int j_ = 0; j_ < 4; ++j_) wr[s_][j_] = pw1[ro1[j_] + s_ * 64]; SBAR(); } while (0)
#define W2_LD(lo, hi) do { _Pragma("unroll") for (int s_ = (lo); s_ < (hi); ++s_) _Pragma("unroll") for (int j_ = 0; j_ < 4; ++j_) wr[s_][j_] = pw2[ro2[j_] + s_ * 64]; SBAR(); } while (0)

    u32x2 x1o[5], bb; float bs;
    {
        ACC_ZERO();
        u32x2 xi[5];
        const bf16x8* xb = (const bf16x8*)(ws + OFF_O) + (size_t)(tb * 4 * 20 + 5 * w) * 64 + lane;
        const bf16x8* wb = (const bf16x8*)(ws + OFF_WOF) + (size_t)(5 * cg * 20 + 5 * w) * 64 + lane;
        bf16x8 xf[5][4], wf[5][5];
        WO_LD(0); WO_LD(1); WO_LD(2); WO_LD(3);
#pragma unroll
        for (int ks = 0; ks < 5; ++ks) {
#pragma unroll
            for (int nt = 0; nt < 5; ++nt)
#pragma unroll
                for (int i = 0; i < 4; ++i) acc[i][nt] = MFMA16_32(wf[ks][nt], xf[ks][i], acc[i][nt]);
            SBAR();
            if (ks == 0) {
                WO_LD(4);
#pragma unroll
                for (int nt = 0; nt < 5; ++nt) {
                    const int d = 80 * cg + 16 * nt + 4 * g;
                    if (!R1) xi[nt] = *(const u32x2*)(xin + ((size_t)((tok >> 4) * 20 + (d >> 5)) * 64 + ((d & 31) >> 3) * 16 + n16) * 8 + (d & 7));
                    else xi[nt] = *(const u32x2*)(xin + (size_t)tok * 640 + d);
                }
                bb = *(const u32x2*)(b1 + 64 * (act1 ? ot1 : 0) + 16 * w + 4 * g);
                bs = bf2f(b2[tok]);
                SBAR();
            }
            if (ks == 1) W1_LD(0, 2);
            if (ks == 2) W1_LD(2, 4);
            if (ks == 3) W1_LD(4, 6);
            if (ks == 4) W1_LD(6, W1_A);
        }
        ACC_REDUCE();
        float xv[20], s = 0.f;
#pragma unroll
        for (int nt = 0; nt < 5; ++nt) {
            const unsigned u0 = pk2(bf_lo(xi[nt][0]) + acc[0][nt][0], bf_hi(xi[nt][0]) + acc[0][nt][1]);
            const unsigned u1 = pk2(bf_lo(xi[nt][1]) + acc[0][nt][2], bf_hi(xi[nt][1]) + acc[0][nt][3]);
            x1o[nt] = u32x2{u0, u1};
            xv[4 * nt] = bf_lo(u0); xv[4 * nt + 1] = bf_hi(u0); xv[4 * nt + 2] = bf_lo(u1); xv[4 * nt + 3] = bf_hi(u1);
            s += (xv[4 * nt] + xv[4 * nt + 1]) + (xv[4 * nt + 2] + xv[4 * nt + 3]);
        }
        const float mw = add32(add16(s)) * (1.f / 80.f);
        float s2 = 0.f;
#pragma unroll
        for (int i = 0; i < 20; ++i) { const float dd = xv[i] - mw; s2 += dd * dd; }
        s2 = add32(add16(s2));
        gu64* gp = (gu64*)(ws + OFF_XG) + (size_t)tok * 8;
        if (g == 0) __hip_atomic_store(gp + cg, ((unsigned long long)(__float_as_uint(s2) | 0x80000000u) << 32) | __float_as_uint(mw),
                                       __ATOMIC_RELAXED, __HIP_MEMORY_SCOPE_AGENT);
        SBAR();
        unsigned long long v0, v1;
        for (int spin = 0; spin < (1 << 20); ++spin) {
            v0 = __hip_atomic_load(gp + 2 * g, __ATOMIC_RELAXED, __HIP_MEMORY_SCOPE_AGENT);
            v1 = __hip_atomic_load(gp + 2 * g + 1, __ATOMIC_RELAXED, __HIP_MEMORY_SCOPE_AGENT);
            if (__all((long long)(v0 & v1) < 0)) break;
            __builtin_amdgcn_s_sleep(4);
        }
        const float m0 = __uint_as_float((unsigned)v0), m1 = __uint_as_float((unsigned)v1);
        const float mean = add32(add16(m0 + m1)) * 0.125f;
        const float d0 = m0 - mean, d1 = m1 - mean;
        const float var = add32(add16(__uint_as_float((unsigned)(v0 >> 32) & 0x7fffffffu) + __uint_as_float((unsigned)(v1 >> 32) & 0x7fffffffu) +
                                      80.f * (d0 * d0 + d1 * d1))) * (1.f / 640.f);
        float shf, r2; ln2_scalars(mean, var, shf, r2);
        char* hb = L + w * 15360;
#pragma unroll
        for (int nt = 0; nt < 5; ++nt)
            *(u32x2*)(hb + n16 * 176 + (16 * nt + 4 * g) * 2) =
                u32x2{pk2((xv[4 * nt] - shf) * r2, (xv[4 * nt + 1] - shf) * r2), pk2((xv[4 * nt + 2] - shf) * r2, (xv[4 * nt + 3] - shf) * r2)};
        asm volatile("s_waitcnt lgkmcnt(0)" ::: "memory");
        const int q4 = (lane & 15) >> 2, p4 = lane & 3;
        lds_cptr hr = (lds_cptr)(lds_ptr)L + w * 15360 + (8 * (g & 1) + q4) * 176 + p4 * 8 + (g >> 1) * 32;
        const int s0 = tb * 64 + 16 * w + 8 * (g & 1);
        const int hoff = (int)OFF_H + ((5 * cg + (g >> 1)) * 64 + (s0 >> 5)) * 1024 + (((s0 & 31) >> 3) * 16 + n16) * 16;
        const bf16x8 f0 = cat8(tr_read(hr), tr_read(hr + 704));
        const bf16x8 f1 = cat8(tr_read(hr + 64), tr_read(hr + 704 + 64));
        const bf16x8 f2 = cat8(tr_read(hr + (g < 2 ? 128 : 0)), tr_read(hr + 704 + (g < 2 ? 128 : 0)));
        __builtin_amdgcn_raw_buffer_store_b128(__builtin_bit_cast(u32x4, f0), rs, hoff, 0, 16);
        __builtin_amdgcn_raw_buffer_store_b128(__builtin_bit_cast(u32x4, f1), rs, hoff + 2 * 65536, 0, 16);
        if (g < 2) __builtin_amdgcn_raw_buffer_store_b128(__builtin_bit_cast(u32x4, f2), rs, hoff + 4 * 65536, 0, 16);
        SBAR();
        W1_LD(W1_A, W1_A + 4);
        VMCNT(16);
        edge_publish(cnt + cg * 64);
    }
    if (act1) {
        W1_LD(W1_A + 4, 16);
        edge_wait(cnt + cg * 64, 32);
        ACC_ZERO();
        const int ho = (int)OFF_H + ((5 * cg) * 64 + 16 * w) * 1024 + lane * 16;
        constexpr int PF = 6;
        bf16x8 rf[PF][5];
#pragma unroll
        for (int s = 0; s < PF; ++s) {
#pragma unroll
            for (int i = 0; i < 5; ++i) rf[s][i] = LD_SC1(ho + i * 65536 + s * 1024);
            SBAR();
        }
#pragma unroll
        for (int s = 0; s < 16; ++s) {
#pragma unroll
            for (int j = 0; j < 4; ++j)
#pragma unroll
                for (int i = 0; i < 5; ++i) acc[j][i] = MFMA16_32(wr[s][j], rf[s % PF][i], acc[j][i]);
            SBAR();
            if (s + PF < 16) {
#pragma unroll
                for (int i = 0; i < 5; ++i) rf[s % PF][i] = LD_SC1(ho + i * 65536 + (s + PF) * 1024);
                SBAR();
            }
            if (s >= W2_FIRST && s - W2_FIRST < 9) W2_LD(s - W2_FIRST, s - W2_FIRST + 1);
        }
        ACC_REDUCE();
        const int f = 64 * ot1 + 16 * w + 4 * g;
        const float bv[4] = {bf_lo(bb[0]), bf_hi(bb[0]), bf_lo(bb[1]), bf_hi(bb[1])};
        const int uo = (int)OFF_U + ((5 * cg) * 54 + (f >> 5)) * 1024 + (((f & 31) >> 3) * 16 + n16) * 16 + (f & 7) * 2;
#pragma unroll
        for (int i = 0; i < 5; ++i) {
            float r[4];
#pragma unroll
            for (int e = 0; e < 4; ++e) {
                const float u = acc[0][i][e] + bv[e];
                const float z = u * (1.f + 0.044715f * u * u) * (-2.f * 0.7978845608028654f * 1.4426950408889634f);
                r[e] = u * __builtin_amdgcn_rcpf(1.f + EX2(z));
            }
            __builtin_amdgcn_raw_buffer_store_b64(u32x2{pk2(r[0], r[1]), pk2(r[2], r[3])}, rs, uo + i * 54 * 1024, 0, 16);
        }
        SBAR();
        W2_LD(9, 13);
        VMCNT(16);
        edge_publish(cnt + (8 + cg) * 64);
    } else {
        W2_LD(0, 13);
    }
    {
        if (w < 2) W2_LD(13, 14);
        edge_wait(cnt + (8 + cg) * 64, 27);
        ACC_ZERO();
        const int uo = (int)OFF_U + ((5 * cg) * 54 + k2) * 1024 + lane * 16;
        constexpr int PF = 6;
        bf16x8 rf[PF][5];
#pragma unroll
        for (int s = 0; s < PF; ++s) {
#pragma unroll
            for (int i = 0; i < 5; ++i) rf[s][i] = LD_SC1(uo + i * 54 * 1024 + s * 1024);
            SBAR();
        }
#pragma unroll
        for (int s = 0; s < 14; ++s) {
            if (s < 13 || w < 2) {
#pragma unroll
                for (int j = 0; j < 4; ++j)
#pragma unroll
                    for (int i = 0; i < 5; ++i) acc[j][i] = MFMA16_32(rf[s % PF][i], wr[s][j], acc[j][i]);
            }
            SBAR();
            if (s + PF < 13 || (s + PF == 13 && w < 2)) {
#pragma unroll
                for (int i = 0; i < 5; ++i) rf[s % PF][i] = LD_SC1(uo + i * 54 * 1024 + (s + PF) * 1024);
                SBAR();
            }
        }
        ACC_REDUCE();
        float xv[20], sum = 0.f;
#pragma unroll
        for (int i = 0; i < 5; ++i) {
            const int d = 80 * cg + 16 * i + 4 * g;
            const unsigned u0 = pk2(bf_lo(x1o[i][0]) + acc[0][i][0] + bs, bf_hi(x1o[i][0]) + acc[0][i][1] + bs);
            const unsigned u1 = pk2(bf_lo(x1o[i][1]) + acc[0][i][2] + bs, bf_hi(x1o[i][1]) + acc[0][i][3] + bs);
            if (R1) __builtin_amdgcn_raw_buffer_store_b64(u32x2{u0, u1}, rs,
                        (int)OFF_X2 + ((4 * tb + w) * 20 + (d >> 5)) * 1024 + (((d & 31) >> 3) * 16 + n16) * 16 + (d & 7) * 2, 0, 16);
            else *(u32x2*)(out + (size_t)tok * 640 + d) = u32x2{u0, u1};
            xv[4 * i] = bf_lo(u0); xv[4 * i + 1] = bf_hi(u0); xv[4 * i + 2] = bf_lo(u1); xv[4 * i + 3] = bf_hi(u1);
            sum += (xv[4 * i] + xv[4 * i + 1]) + (xv[4 * i + 2] + xv[4 * i + 3]);
        }
        if (R1) {
            const float mw = add32(add16(sum)) * (1.f / 80.f);
            float s2 = 0.f;
#pragma unroll
            for (int i = 0; i < 20; ++i) { const float dd = xv[i] - mw; s2 += dd * dd; }
            s2 = add32(add16(s2));
            if (g == 0) __builtin_amdgcn_raw_buffer_store_b64(u32x2{__float_as_uint(mw), __float_as_uint(s2)}, rs, (int)OFF_XS + ((tb * 8 + cg) * 64 + 16 * w + n16) * 8, 0, 16);
        }
    }
    if (R1) {
        const int mt0 = cg >> 1, isv = cg & 1;
        const bf16x8* wb = (const bf16x8*)(ws + OFF_WQF) + (size_t)(5 * cg * 20 + 5 * w) * 64 + lane;
        const bf16x8* kb = (const bf16x8*)(ws + OFF_WQF) + (size_t)((40 + 2 * isv) * 20 + 5 * w) * 64 + lane;
        bf16x8 wf[5][5], wkf[5][2], xf[5][4], xk[5];
        SBAR();
#pragma unroll
        for (int i = 0; i < 16; ++i) wf[i / 5][i % 5] = wb[((i % 5) * 20 + i / 5) * 64];
        SBAR();
        VMCNT(16);
        edge_publish(cnt + (16 + tb) * 64);
#pragma unroll
        for (int i = 16; i < 25; ++i) wf[i / 5][i % 5] = wb[((i % 5) * 20 + i / 5) * 64];
#pragma unroll
        for (int ks = 0; ks < 5; ++ks) { wkf[ks][0] = kb[ks * 64]; wkf[ks][1] = kb[(20 + ks) * 64]; }
        f32x4 csq[5], cs0, cs1, stq[4], stk[4], acck[2];
        {
            const float* csp = (const float*)(ws + OFF_CS);
#pragma unroll
            for (int nt = 0; nt < 5; ++nt) csq[nt] = *(const f32x4*)(csp + 80 * cg + 16 * nt + 4 * g);
            cs0 = *(const f32x4*)(csp + 640 + 32 * isv + 4 * g); cs1 = *(const f32x4*)(csp + 640 + 32 * isv + 16);
        }
        SBAR();
        edge_wait(cnt + (16 + tb) * 64, 8);
        const int xo = (int)OFF_X2 + (tb * 4 * 20 + 5 * w) * 1024 + lane * 16;
#pragma unroll
        for (int ks = 0; ks < 5; ++ks) {
#pragma unroll
            for (int i = 0; i < 4; ++i) xf[ks][i] = LD_SC1(xo + (((i + w) & 3) * 20 + ks) * 1024);
            xk[ks] = LD_SC1(xo + (mt0 * 20 + ks) * 1024);
            SBAR();
        }
#pragma unroll
        for (int i = 0; i < 4; ++i) {
            const u32x2 a0 = __builtin_amdgcn_raw_buffer_load_b64(rs, (int)OFF_XS + ((tb * 8 + 2 * i) * 64 + 16 * w + n16) * 8, 0, AUXL);
            const u32x2 a1 = __builtin_amdgcn_raw_buffer_load_b64(rs, (int)OFF_XS + ((tb * 8 + 2 * i + 1) * 64 + 16 * w + n16) * 8, 0, AUXL);
            const u32x2 c0 = __builtin_amdgcn_raw_buffer_load_b64(rs, (int)OFF_XS + ((tb * 8 + 2 * i) * 64 + 16 * mt0 + n16) * 8, 0, AUXL);
            const u32x2 c1 = __builtin_amdgcn_raw_buffer_load_b64(rs, (int)OFF_XS + ((tb * 8 + 2 * i + 1) * 64 + 16 * mt0 + n16) * 8, 0, AUXL);
            stq[i] = __builtin_bit_cast(f32x4, u32x4{a0[0], a0[1], a1[0], a1[1]});
            stk[i] = __builtin_bit_cast(f32x4, u32x4{c0[0], c0[1], c1[0], c1[1]});
        }
        SBAR();
        ACC_ZERO();
        acck[0] = f32x4{0.f, 0.f, 0.f, 0.f}; acck[1] = acck[0];
#pragma unroll
        for (int ks = 0; ks < 5; ++ks) {
#pragma unroll
            for (int nt = 0; nt < 5; ++nt)
#pragma unroll
                for (int i = 0; i < 4; ++i) acc[i][nt] = MFMA16_32(wf[ks][nt], xf[ks][i], acc[i][nt]);
            acck[0] = MFMA16_32(wkf[ks][0], xk[ks], acck[0]);
            acck[1] = MFMA16_32(wkf[ks][1], xk[ks], acck[1]);
            SBAR();
        }
        const float z4[4] = {0.f, 0.f, 0.f, 0.f}, z5[5] = {0.f, 0.f, 0.f, 0.f, 0.f}, z2[2] = {0.f, 0.f};
        qkv_finish<true>(ws, L, tb, cg, w, lane, acc, acck, csq, cs0, cs1, stq, stk, z4, z4, z5, z2);
    }
}

template <int N> __global__ void k_end() {}

extern "C" void kernel_launch(void* const* d_in, const int* in_sizes, int n_in, void* d_out, int out_size, void* d_ws,
                              size_t ws_size, hipStream_t stream) {
    const u16* x = (const u16*)d_in[0];
    const u16* wq = (const u16*)d_in[1];
    const u16* wk = (const u16*)d_in[2];
    const u16* wv = (const u16*)d_in[3];
    const u16* wo = (const u16*)d_in[4];
    const u16* w1 = (const u16*)d_in[6];
    const u16* b1 = (const u16*)d_in[7];
    const u16* w2 = (const u16*)d_in[8];
    const u16* b2 = (const u16*)d_in[9];
    char* ws = (char*)d_ws;
    u16* x2 = (u16*)(ws + OFF_X2);
    (void)hipFuncSetAttribute((const void*)k_qkv, hipFuncAttributeMaxDynamicSharedMemorySize, QKV_LDS);
    (void)hipFuncSetAttribute((const void*)k_attn<true>, hipFuncAttributeMaxDynamicSharedMemorySize, ATT_LDS);
    (void)hipFuncSetAttribute((const void*)k_attn<false>, hipFuncAttributeMaxDynamicSharedMemorySize, ATT_LDS);
    (void)hipFuncSetAttribute((const void*)k_mlp<true>, hipFuncAttributeMaxDynamicSharedMemorySize, MLP_LDS);
    (void)hipFuncSetAttribute((const void*)k_mlp<false>, hipFuncAttributeMaxDynamicSharedMemorySize, MLP_LDS);
    (void)hipFuncSetAttribute((const void*)k_end<0>, hipFuncAttributeMaxDynamicSharedMemorySize, 0);
    k_qkv<<<256, 256, QKV_LDS, stream>>>(x, wq, wk, wv, ws);
    k_attn<true><<<256, 512, ATT_LDS, stream>>>(ws, w1, w2, wo, wq, wk, wv);
    k_mlp<true><<<256, 256, MLP_LDS, stream>>>(ws, x, b1, b2, nullptr);
    k_attn<false><<<256, 512, A_STG, stream>>>(ws, w1, w2, wo, wq, wk, wv);
    k_mlp<false><<<256, 256, MLP_LDS, stream>>>(ws, x2, b1, b2, (u16*)d_out);
}
```

```cpp
#include <hip/hip_runtime.h>
#include <hip/hip_bf16.h>
#include <stdint.h>


typedef unsigned short u16;
typedef short bf16x8 __attribute__((ext_vector_type(8)));
typedef short bf16x4 __attribute__((ext_vector_type(4)));
typedef float f32x16 __attribute__((ext_vector_type(16)));
typedef float f32x4 __attribute__((ext_vector_type(4)));
typedef float f32x2 __attribute__((ext_vector_type(2)));
typedef unsigned u32x4 __attribute__((ext_vector_type(4)));
typedef unsigned u32x2 __attribute__((ext_vector_type(2)));
typedef __bf16 bf2_t __attribute__((ext_vector_type(2)));
typedef __attribute__((address_space(3))) char* lds_ptr;
typedef __attribute__((address_space(3))) const char* lds_cptr;
typedef __attribute__((address_space(1))) unsigned long long gu64;

#define DI __device__ __forceinline__
#define SBAR() __builtin_amdgcn_sched_barrier(0)
#define PIN(x) asm volatile("" : "+v"(x))
#define MFMA32_16(a, b, c) __builtin_amdgcn_mfma_f32_32x32x16_bf16(a, b, c, 0, 0, 0)
#define MFMA32_8(a, b, c) __builtin_amdgcn_mfma_f32_32x32x8bf16_1k(a, b, c, 0, 0, 0)
#define MFMA16_32(a, b, c) __builtin_amdgcn_mfma_f32_16x16x32_bf16(a, b, c, 0, 0, 0)
#define EX2(x) __builtin_amdgcn_exp2f(x)
#define RAW_BARRIER() asm volatile("s_waitcnt lgkmcnt(0)\n\ts_barrier" ::: "memory")
#define VMCNT(n) asm volatile("s_waitcnt vmcnt(%0)" :: "i"(n) : "memory")

#define PREP_STORE(p, v) asm volatile("global_store_dwordx4 %0, %1, off sc1\n\ts_nop 1" :: "v"(p), "v"(v) : "memory")
#define KADDR(dst, sym) asm volatile("s_getpc_b64 s[40:41]\n\ts_add_u32 s40, s40, " sym "@rel32@lo+4\n\ts_addc_u32 s41, s41, " sym "@rel32@hi+12\n\ts_mov_b64 %0, s[40:41]" : "=s"(dst) :: "s40", "s41", "scc")
constexpr int S_ = 2048, D_ = 640, F_ = 1728;
constexpr float LN_EPS = 1e-5f;
constexpr float QSCALE = 0.22360679774997896f * 1.4426950408889634f;
constexpr float SAFE_BOUND = 48.0f;

DI unsigned pk2(float a, float b) { f32x2 v = {a, b}; bf2_t r = __builtin_convertvector(v, bf2_t); return __builtin_bit_cast(unsigned, r); }
DI float bf_lo(unsigned u) { return __uint_as_float(u << 16); }
DI float bf_hi(unsigned u) { return __uint_as_float(u & 0xffff0000u); }
DI float bf2f(u16 v) { return __uint_as_float((unsigned)v << 16); }
DI float dot2(unsigned a, unsigned b, float c) { return __builtin_amdgcn_fdot2_f32_bf16(__builtin_bit_cast(bf2_t, a), __builtin_bit_cast(bf2_t, b), c, false); }
DI bf16x4 tr_read(lds_cptr p) { return __builtin_amdgcn_ds_read_tr16_b64_v4i16((__attribute__((address_space(3))) bf16x4*)p); }
DI bf16x8 cat8(bf16x4 a, bf16x4 b) { return __builtin_shufflevector(a, b, 0, 1, 2, 3, 4, 5, 6, 7); }
DI void glds16(const void* g, lds_ptr l) {
    unsigned keep;
    asm volatile("s_mov_b32 %0, m0\n\ts_mov_b32 m0, %2\n\ts_nop 0\n\tglobal_load_lds_dwordx4 %1, off\n\ts_mov_b32 m0, %0"
                 : "=&s"(keep) : "v"(g), "s"(__builtin_amdgcn_readfirstlane((unsigned)(uintptr_t)l)) : "memory");
}
DI float xor32(float v) { return __shfl_xor(v, 32); }
DI float add32(float v) {
    auto r = __builtin_amdgcn_permlane32_swap(__float_as_uint(v), __float_as_uint(v), false, false);
    return __uint_as_float(r[0]) + __uint_as_float(r[1]);
}
DI float add16(float v) {
    auto r = __builtin_amdgcn_permlane16_swap(__float_as_uint(v), __float_as_uint(v), false, false);
    return __uint_as_float(r[0]) + __uint_as_float(r[1]);
}
DI float max16x(float v) { auto r = __builtin_amdgcn_permlane16_swap(__float_as_uint(v), __float_as_uint(v), false, false); return fmaxf(__uint_as_float(r[0]), __uint_as_float(r[1])); }
DI float max32x(float v) { auto r = __builtin_amdgcn_permlane32_swap(__float_as_uint(v), __float_as_uint(v), false, false); return fmaxf(__uint_as_float(r[0]), __uint_as_float(r[1])); }
template <int CTRL> DI float dppf(float v) { return __int_as_float(__builtin_amdgcn_update_dpp(0, __float_as_int(v), CTRL, 0xf, 0xf, false)); }
DI float rowmax16(float v) {
    v = fmaxf(v, dppf<0x128>(v)); v = fmaxf(v, dppf<0x124>(v)); v = fmaxf(v, dppf<0x122>(v)); v = fmaxf(v, dppf<0x121>(v)); return v;
}
DI float wavemax(float v) { return max32x(max16x(rowmax16(v))); }

constexpr size_t OFF_Q = 0;
constexpr size_t OFF_K1 = OFF_Q + (size_t)2048 * 32 * 48;
constexpr size_t OFF_K2 = OFF_K1 + 2048 * 32;
constexpr size_t OFF_V = OFF_K2 + 2048 * 8;
constexpr size_t OFF_KN = OFF_V + 128 * 640;
constexpr size_t OFF_QN = OFF_KN + 512;
constexpr size_t OFF_CS = OFF_QN + 4096;
constexpr size_t OFF_LNS = OFF_CS + 4096;
constexpr size_t OFF_CONST = OFF_LNS + 16384;
constexpr size_t OFF_CNT = OFF_CONST + 256;
constexpr size_t OFF_XG = OFF_CNT + 48 * 256;
constexpr size_t OFF_XS = OFF_XG + 131072;
constexpr size_t OFF_H = OFF_XS + 131072 + 256;
constexpr size_t OFF_U = OFF_H + (size_t)S_ * D_ * 2;
constexpr size_t OFF_X2 = OFF_U + (size_t)D_ * F_ * 2;
constexpr size_t OFF_O = OFF_X2 + (size_t)S_ * D_ * 2;
constexpr size_t OFF_W1F = OFF_O + (size_t)S_ * D_ * 2;
constexpr size_t OFF_W2F = OFF_W1F + (size_t)S_ * F_ * 2;
constexpr size_t OFF_WQF = OFF_W2F + (size_t)S_ * F_ * 2;
constexpr size_t OFF_WOF = OFF_WQF + (size_t)44 * 20 * 1024;
static_assert(OFF_H % 1024 == 0, "fragment buffers are 1 KiB aligned");
constexpr size_t WS_NEED = OFF_WOF + (size_t)800 * 1024;
constexpr int NPREP = 1728 + 1728 + 200 + 200;

DI float rbf(float v) { return bf_lo(pk2(v, 0.f)); }
DI float ln_sd(float var) { return rbf(__builtin_amdgcn_sqrtf(rbf(rbf(var) + LN_EPS))); }
DI void ln2_scalars(float mu, float var, float& shift, float& scale) {
    const float m1 = rbf(mu), sd1 = ln_sd(var), i1 = 1.0f / sd1;
    const float m2 = rbf((mu - m1) * i1), sd2 = ln_sd(var * i1 * i1);
    shift = m1 + m2 * sd1; scale = i1 / sd2;
}
DI void chan8(const f32x4 (&st)[4], float& mean, float& var) {
    float m = 0.f, m2 = 0.f;
#pragma unroll
    for (int i = 0; i < 4; ++i) { m += st[i][0] + st[i][2]; m2 += st[i][1] + st[i][3]; }
    m *= 0.125f;
#pragma unroll
    for (int i = 0; i < 4; ++i) { const float d0 = st[i][0] - m, d1 = st[i][2] - m; m2 += 80.f * (d0 * d0 + d1 * d1); }
    mean = m; var = m2 * (1.f / 640.f);
}

DI void kv_store(char* __restrict__ ws, int t16, int isv, int lane, const f32x4& a0, const f32x4& a1, const f32x4& s0, const f32x4& s1,
                 float mean, float rstd) {
    const int n16 = lane & 15, g = lane >> 4;
    const unsigned u0 = pk2((a0[0] - mean * s0[0]) * rstd, (a0[1] - mean * s0[1]) * rstd);
    const unsigned u1 = pk2((a0[2] - mean * s0[2]) * rstd, (a0[3] - mean * s0[3]) * rstd);
    const unsigned u2 = pk2((a1[0] - mean * s1[0]) * rstd, (a1[1] - mean * s1[1]) * rstd);
    const unsigned u3 = pk2((a1[2] - mean * s1[2]) * rstd, (a1[3] - mean * s1[3]) * rstd);
    if (!isv) {
        const int key = t16 * 16 + n16;
        *(u32x2*)(ws + OFF_K1 + key * 32 + g * 8) = u32x2{u0, u1};
        if (g == 0) *(u32x2*)(ws + OFF_K2 + key * 8) = u32x2{u2, u3};
        float n2 = dot2(u1, u1, dot2(u0, u0, 0.f));
        n2 = add32(add16(n2));
        n2 = dot2(u3, u3, dot2(u2, u2, n2));
        if (!(n2 == n2)) n2 = INFINITY;
        n2 = rowmax16(n2);
        if (lane == 0) *(float*)(ws + OFF_KN + t16 * 4) = n2;
    } else {
        u16* vb = (u16*)(ws + OFF_V + t16 * 640 + (n16 >> 3) * 320) + (n16 & 7);
        vb[(4 * g + 0) * 8] = (u16)u0; vb[(4 * g + 1) * 8] = (u16)(u0 >> 16);
        vb[(4 * g + 2) * 8] = (u16)u1; vb[(4 * g + 3) * 8] = (u16)(u1 >> 16);
        if (g == 0) { vb[16 * 8] = (u16)u2; vb[17 * 8] = (u16)(u2 >> 16); vb[18 * 8] = (u16)u3; vb[19 * 8] = (u16)(u3 >> 16); }
    }
}
DI void qkv_finish(char* __restrict__ ws, char* L, int tb, int cg, int w, int lane, f32x4 (&acc)[4][5], f32x4 (&acck)[2],
                   const f32x4 (&csq)[5], const f32x4& cs0, const f32x4& cs1, const f32x4 (&stq)[4], const f32x4 (&stk)[4]) {
    const int n16 = lane & 15, g = lane >> 4, mt0 = cg >> 1, isv = cg & 1;
    constexpr int R_OFF = 0, PK_OFF = 61440;
#pragma unroll
    for (int j = 1; j < 4; ++j) {
        const int o = (w + j) & 3;
#pragma unroll
        for (int nt = 0; nt < 5; ++nt) *(f32x4*)(L + R_OFF + (((o * 3 + (j - 1)) * 5 + nt) * 64 + lane) * 16) = acc[j][nt];
    }
    *(f32x4*)(L + PK_OFF + ((w * 2 + 0) * 64 + lane) * 16) = acck[0];
    *(f32x4*)(L + PK_OFF + ((w * 2 + 1) * 64 + lane) * 16) = acck[1];
    char* qrow = ws + OFF_Q + (size_t)(tb * 8 + cg) * 12288 + (16 * w + n16) * 48;
    int qoff[5];
#pragma unroll
    for (int nt = 0; nt < 5; ++nt) { const int G = 4 * nt + g, hl = G / 5; qoff[nt] = hl * 3072 + (G - 5 * hl) * 8; PIN(qoff[nt]); }
    RAW_BARRIER();
    {
        f32x4 rr[3][5];
#pragma unroll
        for (int j = 0; j < 3; ++j)
#pragma unroll
            for (int nt = 0; nt < 5; ++nt) rr[j][nt] = *(const f32x4*)(L + R_OFF + (((w * 3 + j) * 5 + nt) * 64 + lane) * 16);
        float mean, var; chan8(stq, mean, var);
        mean = rbf(mean);
        const float sc = QSCALE / ln_sd(var);
        SBAR();
        float n2[5];
#pragma unroll
        for (int nt = 0; nt < 5; ++nt) {
            const f32x4 a = (acc[0][nt] + rr[0][nt]) + (rr[1][nt] + rr[2][nt]), s = csq[nt];
            const unsigned u0 = pk2((a[0] - mean * s[0]) * sc, (a[1] - mean * s[1]) * sc);
            const unsigned u1 = pk2((a[2] - mean * s[2]) * sc, (a[3] - mean * s[3]) * sc);
            *(u32x2*)(qrow + qoff[nt]) = u32x2{u0, u1};
            n2[nt] = dot2(u1, u1, dot2(u0, u0, 0.f));
        }
        float hn[4];
        hn[0] = n2[0] + (g == 0 ? n2[1] : 0.f);
        hn[1] = (g > 0 ? n2[1] : 0.f) + (g < 2 ? n2[2] : 0.f);
        hn[2] = (g > 1 ? n2[2] : 0.f) + (g < 3 ? n2[3] : 0.f);
        hn[3] = (g == 3 ? n2[3] : 0.f) + n2[4];
#pragma unroll
        for (int hh = 0; hh < 4; ++hh) hn[hh] = add32(add16(hn[hh]));
        const float n2a = (g & 1) ? hn[1] : hn[0], n2b = (g & 1) ? hn[3] : hn[2];
        const unsigned qb = pk2(__builtin_amdgcn_sqrtf((g & 2) ? n2b : n2a), 0.f);
        *(u32x2*)(qrow + g * 3072 + 40) = u32x2{qb, 0u};
        float m = bf_lo(qb);
        if (!(m == m)) m = INFINITY;
        m = wavemax(m);
        if (lane == 0) *(float*)(ws + OFF_QN + ((tb * 8 + cg) * 4 + w) * 4) = m;
    }
    if (w == 3) {
        float mean, var; chan8(stk, mean, var);
        mean = rbf(mean);
        const float rstd = 1.0f / ln_sd(var);
        f32x4 a0 = {0.f, 0.f, 0.f, 0.f}, a1 = a0;
#pragma unroll
        for (int ww = 0; ww < 4; ++ww) {
            a0 += *(const f32x4*)(L + PK_OFF + ((ww * 2 + 0) * 64 + lane) * 16);
            a1 += *(const f32x4*)(L + PK_OFF + ((ww * 2 + 1) * 64 + n16) * 16);
        }
        kv_store(ws, tb * 4 + mt0, isv, lane, a0, a1, cs0, cs1, mean, rstd);
    }
    if (w == 0) *(u32x2*)(ws + OFF_XG + ((size_t)(tb * 64 + lane) * 8 + cg) * 8) = u32x2{0u, 0u};
}

__global__ __launch_bounds__(256, 1) void k_kv(const u16* __restrict__ x, const u16* __restrict__ wk, const u16* __restrict__ wv,
                                               char* __restrict__ ws) {
    __shared__ __attribute__((aligned(16))) char L[4 * 6528 + 8192 + 512 + 512];
    lds_ptr lds = (lds_ptr)L;
    const int tid = threadIdx.x, lane = tid & 63, w = __builtin_amdgcn_readfirstlane(tid >> 6);
    const int b = blockIdx.x, t16 = b >> 1, isv = b & 1;
    const int n16 = lane & 15, g = lane >> 4;
    unsigned code_touch = 0u;
    if (w == 2) {
        uint64_t a[5];
        KADDR(a[0], "_Z6k_attnILb1EEvPcPKtS2_S2_S2_S2_S2_S2_"); KADDR(a[1], "_Z6k_attnILb0EEvPcPKtS2_S2_S2_S2_S2_S2_");
        KADDR(a[2], "_Z5k_mlpILb1EEvPcPKtS2_S2_Pt"); KADDR(a[3], "_Z5k_mlpILb0EEvPcPKtS2_S2_Pt"); KADDR(a[4], "_Z5k_endILi0EEvv");
        uint64_t lo = a[0], hi = a[0];
#pragma unroll
        for (int i = 1; i < 5; ++i) { lo = a[i] < lo ? a[i] : lo; hi = a[i] > hi ? a[i] : hi; }
        const uint64_t pa = lo + (uint64_t)((b >> 3) * 64 + lane) * 128;
        if (pa < hi) code_touch = *(const __attribute__((address_space(1))) unsigned*)pa;
    }
    SBAR();
    {
        const char* src = (const char*)((isv ? wv : wk) + (size_t)(160 * w) * 20);
#pragma unroll
        for (int i = 0; i < 6; ++i) glds16(src + i * 1024 + lane * 16, lds + w * 6528 + i * 1024);
        if (lane < 16) glds16(src + 6 * 1024 + lane * 16, lds + w * 6528 + 6 * 1024);
    }
    bf16x8 xk[5];
    const u16* xr = x + (size_t)(t16 * 16 + n16) * 640 + 160 * w + 8 * g;
#pragma unroll
    for (int ks = 0; ks < 5; ++ks) xk[ks] = *(const bf16x8*)(xr + 32 * ks);
    const unsigned ONES = 0x3f803f80u;
    float sm = 0.f, sq = 0.f, csk[2] = {0.f, 0.f};
#pragma unroll
    for (int ks = 0; ks < 5; ++ks) {
        const u32x4 u = __builtin_bit_cast(u32x4, xk[ks]);
#pragma unroll
        for (int e = 0; e < 4; ++e) { sm = dot2(u[e], ONES, sm); sq = dot2(u[e], u[e], sq); }
    }
    VMCNT(0);
    f32x4 acck[2] = {{0.f, 0.f, 0.f, 0.f}, {0.f, 0.f, 0.f, 0.f}};
    lds_cptr kb = (lds_cptr)lds + w * 6528 + (8 * g + (n16 >> 2)) * 40 + (lane & 3) * 8;
#pragma unroll
    for (int ks = 0; ks < 5; ++ks)
#pragma unroll
        for (int nt = 0; nt < 2; ++nt) {
            const bf16x8 a = cat8(tr_read(kb + ks * 1280 + nt * 32), tr_read(kb + ks * 1280 + 160 + nt * 32));
            const u32x4 u = __builtin_bit_cast(u32x4, a);
#pragma unroll
            for (int e = 0; e < 4; ++e) csk[nt] = dot2(u[e], ONES, csk[nt]);
            acck[nt] = MFMA16_32(a, xk[ks], acck[nt]);
        }
    sm = add32(add16(sm)); sq = add32(add16(sq));
    csk[0] = add32(add16(csk[0])); csk[1] = add32(add16(csk[1]));
    constexpr int PK = 4 * 6528, ST = PK + 8192, CS = ST + 512;
    *(f32x4*)(L + PK + ((w * 2 + 0) * 64 + lane) * 16) = acck[0];
    *(f32x4*)(L + PK + ((w * 2 + 1) * 64 + lane) * 16) = acck[1];
    if (g == 0) {
        *(f32x2*)(L + ST + (w * 16 + n16) * 8) = f32x2{sm, sq};
        *(float*)(L + CS + (w * 32 + n16) * 4) = csk[0];
        *(float*)(L + CS + (w * 32 + 16 + n16) * 4) = csk[1];
    }
    RAW_BARRIER();
    if (w == 0) {
        f32x4 a0 = {0.f, 0.f, 0.f, 0.f}, a1 = a0, s0 = a0, s1 = a0; float s = 0.f, s2 = 0.f;
#pragma unroll
        for (int ww = 0; ww < 4; ++ww) {
            a0 += *(const f32x4*)(L + PK + ((ww * 2 + 0) * 64 + lane) * 16);
            a1 += *(const f32x4*)(L + PK + ((ww * 2 + 1) * 64 + n16) * 16);
            s0 += *(const f32x4*)(L + CS + (ww * 32 + 4 * g) * 4);
            s1 += *(const f32x4*)(L + CS + (ww * 32 + 16) * 4);
            const f32x2 v = *(const f32x2*)(L + ST + (ww * 16 + n16) * 8); s += v[0]; s2 += v[1];
        }
        float mean = s * (1.f / 640.f);
        const float var = fmaxf(s2 * (1.f / 640.f) - mean * mean, 0.f);
        mean = rbf(mean);
        const float rstd = 1.0f / ln_sd(var);
        if (!isv && g == 0) *(f32x2*)(ws + OFF_LNS + (t16 * 16 + n16) * 8) = f32x2{mean, rstd};
        if (t16 == 0 && n16 == 0) {
            *(f32x4*)(ws + OFF_CS + (640 + 32 * isv + 4 * g) * 4) = s0;
            if (g == 0) *(f32x4*)(ws + OFF_CS + (640 + 32 * isv + 16) * 4) = s1;
        }
        kv_store(ws, t16, isv, lane, a0, a1, s0, s1, mean, rstd);
    } else if (w == 1) {
        if (lane < 32) *(u32x4*)(ws + OFF_XG + b * 512 + lane * 16) = u32x4{0u, 0u, 0u, 0u};
        if (b == 0 && lane >= 60) {
            const unsigned v = (lane & 2) ? 0x3f803f80u : 0u;
            *(u32x4*)(ws + OFF_CONST + (lane & 3) * 16) = u32x4{v, v, v, v};
        }
    }
    PIN(code_touch);
}

constexpr int PJ0 = 0, PJ1 = 4, PJD = 3;
constexpr int PBW = 2688, A_STG = 32 * PBW, ATT_LDS = 163840;
template <bool PREP>
__global__ __launch_bounds__(512, 2) void k_attn(char* __restrict__ ws, const u16* __restrict__ w1, const u16* __restrict__ w2,
                                                 const u16* __restrict__ wo, const u16* __restrict__ wq,
                                                 const u16* __restrict__ wk, const u16* __restrict__ wv,
                                                 const u16* __restrict__ x) {
    extern __shared__ __attribute__((aligned(16))) char lds_raw[];
    char* L = lds_raw;
    const int tid = threadIdx.x, lane = tid & 63, w = __builtin_amdgcn_readfirstlane(tid >> 6);
    const int b = blockIdx.x, slot = b >> 3, tb = ((b & 7) >> 1) * 8 + (slot >> 2), cg = (b & 1) * 4 + (slot & 3);
    const int r32 = lane & 31, h = lane >> 5, n16 = lane & 15, g = lane >> 4;
    const int cgp = w & 1, kq = w >> 1;
    float kn0 = *(const float*)(ws + OFF_KN + lane * 4), kn1 = *(const float*)(ws + OFF_KN + 256 + lane * 4);
    float qmax = 0.f;
    bf16x8 q1[4]; bf16x4 q2[4];
    f32x2 lns = {0.f, 0.f};
    if (!PREP) {
        qmax = *(const float*)(ws + OFF_QN + ((tb * 8 + cg) * 4 + (lane & 3)) * 4);
        const char* qg = ws + OFF_Q + (size_t)(tb * 8 + cg) * 12288 + cgp * 6144 + r32 * 48;
#pragma unroll
        for (int j = 0; j < 4; ++j) { q1[j] = *(const bf16x8*)(qg + j * 1536 + 16 * h); q2[j] = *(const bf16x4*)(qg + j * 1536 + 32 + 8 * h); }
    } else {
        lns = *(const f32x2*)(ws + OFF_LNS + (tb * 64 + 16 * kq + n16) * 8);
    }
    SBAR();
    const int kr = (r32 & 19) | ((r32 & 4) << 1) | ((r32 & 8) >> 1);
    const unsigned ko1 = (unsigned)OFF_K1 + (unsigned)(512 * kq + kr) * 32u + 16u * h;
    const unsigned ko2 = h ? (unsigned)OFF_CONST + 32u : (unsigned)OFF_K2 + (unsigned)(512 * kq + kr) * 8u;
    const unsigned ks2 = h ? 0u : 256u;
    const unsigned vo = r32 < 20 ? (unsigned)OFF_V + (unsigned)(32 * kq) * 640u + (unsigned)(h * 20 + r32) * 16u
                                 : (unsigned)OFF_CONST + (r32 == 20 ? 32u : 0u);
    const unsigned vs = r32 < 20 ? 640u : 0u;
    bf16x8 k1[3], v0[3], v1[3]; bf16x4 k2[3];
#define K_LOAD(t) do { k1[(t) % 3] = *(const bf16x8*)(ws + ko1 + (t) * 1024); k2[(t) % 3] = *(const bf16x4*)(ws + (ko2 + (t) * ks2)); } while (0)
#define V_LOAD(t) do { v0[(t) % 3] = *(const bf16x8*)(ws + (vo + (2 * (t)) * vs)); v1[(t) % 3] = *(const bf16x8*)(ws + (vo + (2 * (t) + 1) * vs)); } while (0)
    K_LOAD(0); V_LOAD(0); SBAR(); K_LOAD(1); V_LOAD(1); SBAR();
    if (PREP) {
        lds_ptr lds = (lds_ptr)lds_raw;
        const char* xs = (const char*)(x + (size_t)(tb * 64 + 32 * cgp) * 640 + 160 * kq);
        const char* wsrc = (const char*)(wq + (size_t)(160 * kq + 64 * cgp) * 640 + 80 * cg);
#pragma unroll
        for (int j = 0; j < 5; ++j) {
            const int c = 64 * j + lane;
            const unsigned xo = (unsigned)(c / 20) * 1280u + (unsigned)(c % 20) * 16u;
#pragma unroll
            for (int i = 0; i < 2; ++i) glds16(xs + (size_t)i * 20480 + xo, lds + w * 10240 + i * 5120 + j * 1024);
        }
        unsigned wo_[5];
#pragma unroll
        for (int j = 0; j < 5; ++j) {
            const int c = 64 * j + lane;
            wo_[j] = (unsigned)(c / 10) * 1280u + (unsigned)(c % 10) * 16u;
#pragma unroll
            for (int i = 0; i < 2; ++i) glds16(wsrc + (size_t)i * 40960 + wo_[j], lds + 81920 + w * 10240 + i * 5120 + j * 1024);
        }
        VMCNT(10);
        bf16x8 xf[2][5];
#pragma unroll
        for (int i = 0; i < 2; ++i)
#pragma unroll
            for (int ks = 0; ks < 5; ++ks) xf[i][ks] = *(const bf16x8*)(L + w * 10240 + i * 5120 + n16 * 320 + g * 16 + ks * 64);
        asm volatile("s_waitcnt lgkmcnt(0)" ::: "memory");
        SBAR();
        {
            const char* w4 = (const char*)(wq + (size_t)(160 * kq + 128 + 16 * cgp) * 640 + 80 * cg);
            glds16(w4 + wo_[0], lds + w * 10240); glds16(w4 + wo_[1], lds + w * 10240 + 1024);
            if (lane < 32) glds16(w4 + wo_[2], lds + w * 10240 + 2048);
        }
        VMCNT(3);
        RAW_BARRIER();
        f32x4 qa[2][5]; float cs[5] = {0.f, 0.f, 0.f, 0.f, 0.f};
        const unsigned m0 = cgp ? 0u : 0x3f803f80u, m1 = cgp ? 0x3f803f80u : 0u;
        lds_cptr wb = (lds_cptr)lds + 81920 + kq * 20480 + (8 * g + (n16 >> 2)) * 160 + (lane & 3) * 8;
        lds_cptr wb4 = (lds_cptr)lds + (2 * kq + (g >> 1)) * 10240 + (8 * (g & 1) + (n16 >> 2)) * 160 + (lane & 3) * 8;
#pragma unroll
        for (int ks = 0; ks < 5; ++ks) {
            if (ks == 4) { VMCNT(0); RAW_BARRIER(); }
            bf16x8 a[5];
#pragma unroll
            for (int nt = 0; nt < 5; ++nt)
                a[nt] = ks < 4 ? cat8(tr_read(wb + ks * 5120 + nt * 32), tr_read(wb + ks * 5120 + 640 + nt * 32))
                               : cat8(tr_read(wb4 + nt * 32), tr_read(wb4 + 640 + nt * 32));
#pragma unroll
            for (int nt = 0; nt < 5; ++nt) {
                const u32x4 u = __builtin_bit_cast(u32x4, a[nt]);
                cs[nt] = dot2(u[0], m0, dot2(u[1], m0, dot2(u[2], m1, dot2(u[3], m1, cs[nt]))));
#pragma unroll
                for (int i = 0; i < 2; ++i) qa[i][nt] = MFMA16_32(a[nt], xf[i][ks], (ks == 0 ? f32x4{0.f, 0.f, 0.f, 0.f} : qa[i][nt]));
            }
            SBAR();
        }
#pragma unroll
        for (int nt = 0; nt < 5; ++nt) cs[nt] = add32(add16(cs[nt]));
        RAW_BARRIER();
        constexpr int QP = 0, QC = 81920, QI = QC + 2560, QN2 = QI + 12288;
#pragma unroll
        for (int i = 0; i < 2; ++i)
#pragma unroll
            for (int nt = 0; nt < 5; ++nt) *(f32x4*)(L + QP + (((kq * 4 + 2 * cgp + i) * 5 + nt) * 64 + lane) * 16) = qa[i][nt];
        if (g == 0) {
#pragma unroll
            for (int nt = 0; nt < 5; ++nt) *(float*)(L + QC + (w * 80 + nt * 16 + n16) * 4) = cs[nt];
        }
        RAW_BARRIER();
        {
            const float sc = lns[1] * QSCALE;
#pragma unroll
            for (int i = 0; i < 3; ++i) {
                const int nt = cgp ? (i < 2 ? 3 + i : 4) : i;
                f32x4 a = {0.f, 0.f, 0.f, 0.f}, c = a;
#pragma unroll
                for (int qq = 0; qq < 4; ++qq) a += *(const f32x4*)(L + QP + (((qq * 4 + kq) * 5 + nt) * 64 + lane) * 16);
#pragma unroll
                for (int ww = 0; ww < 8; ++ww) c += *(const f32x4*)(L + QC + (ww * 80 + nt * 16 + 4 * g) * 4);
                if (tb == 0 && kq == 0 && n16 == 0) *(f32x4*)(ws + OFF_CS + (80 * cg + 16 * nt + 4 * g) * 4) = c;
                const unsigned u0 = pk2((a[0] - lns[0] * c[0]) * sc, (a[1] - lns[0] * c[1]) * sc);
                const unsigned u1 = pk2((a[2] - lns[0] * c[2]) * sc, (a[3] - lns[0] * c[3]) * sc);
                const int G = 4 * nt + g, hl = G / 5;
                *(u32x2*)(L + QI + hl * 3072 + (16 * kq + n16) * 48 + (G - 5 * hl) * 8) = u32x2{u0, u1};
                *(float*)(L + QN2 + ((16 * kq + n16) * 20 + G) * 4) = dot2(u1, u1, dot2(u0, u0, 0.f));
            }
        }
        RAW_BARRIER();
        float qn[4], qsel0, qsel1;
        {
            f32x4 v[5];
#pragma unroll
            for (int i = 0; i < 5; ++i) v[i] = *(const f32x4*)(L + QN2 + lane * 80 + i * 16);
            qn[0] = (v[0][0] + v[0][1]) + (v[0][2] + v[0][3]) + v[1][0];
            qn[1] = (v[1][1] + v[1][2]) + (v[1][3] + v[2][0]) + v[2][1];
            qn[2] = (v[2][2] + v[2][3]) + (v[3][0] + v[3][1]) + v[3][2];
            qn[3] = (v[3][3] + v[4][0]) + (v[4][1] + v[4][2]) + v[4][3];
#pragma unroll
            for (int i = 0; i < 4; ++i) { qn[i] = __builtin_amdgcn_sqrtf(qn[i]); if (!(qn[i] == qn[i])) qn[i] = INFINITY; }
            qmax = wavemax(fmaxf(fmaxf(qn[0], qn[1]), fmaxf(qn[2], qn[3])));
            const float t0 = qn[0], t1 = qn[1], t2 = qn[2], t3 = qn[3];
            qsel0 = cgp ? t2 : t0; qsel1 = cgp ? t3 : t1;
            PIN(qsel0); PIN(qsel1);
        }
#pragma unroll
        for (int j = 0; j < 4; ++j) {
            const char* qg = L + QI + (2 * cgp + (j >> 1)) * 3072 + (32 * (j & 1) + r32) * 48;
            q1[j] = *(const bf16x8*)(qg + 16 * h);
            q2[j] = *(const bf16x4*)(qg + 32);
            const float own = (j >> 1) ? qsel1 : qsel0;
            const float oth = __shfl_xor(own, 32);
            if (h) q2[j] = __builtin_bit_cast(bf16x4, u32x2{pk2((j & 1) ? own : oth, 0.f), 0u});
        }
        RAW_BARRIER();
    }
    PIN(kn0); PIN(kn1);
    const float kmax2 = wavemax(fmaxf(kn0, kn1));
    qmax = rowmax16(qmax);
    const float kb = __builtin_amdgcn_sqrtf(kmax2);
    const bool safe = __builtin_amdgcn_readfirstlane((int)(qmax * kb <= SAFE_BOUND)) != 0;
    const f32x16 zero16 = {0.f, 0.f, 0.f, 0.f, 0.f, 0.f, 0.f, 0.f, 0.f, 0.f, 0.f, 0.f, 0.f, 0.f, 0.f, 0.f};
    if (safe) {
        if (h) {
#pragma unroll
            for (int j = 0; j < 4; ++j) q2[j][0] = (short)pk2(-bf2f((u16)q2[j][0]) * kb, 0.f);
        }
    } else {
        float m[4];
#pragma unroll
        for (int j = 0; j < 4; ++j) { m[j] = -INFINITY; if (h) q2[j] = bf16x4{0, 0, 0, 0}; }
        for (int t = 0; t < 16; ++t) {
            const bf16x8 ka = *(const bf16x8*)(ws + ko1 + t * 1024);
            const bf16x4 kc = *(const bf16x4*)(ws + (ko2 + t * ks2));
#pragma unroll
            for (int j = 0; j < 4; ++j) {
                f32x16 s = MFMA32_16(ka, q1[j], zero16);
                SBAR(); asm volatile("s_nop 15" ::: "memory"); SBAR();
                s = MFMA32_8(kc, q2[j], s);
#pragma unroll
                for (int e = 0; e < 16; ++e) m[j] = fmaxf(m[j], s[e]);
                SBAR();
            }
        }
#pragma unroll
        for (int j = 0; j < 4; ++j) {
            const float mm = fmaxf(m[j], xor32(m[j]));
            if (h == 0) *(float*)(L + ((w * 4 + j) * 32 + r32) * 4) = mm;
        }
        RAW_BARRIER();
#pragma unroll
        for (int j = 0; j < 4; ++j) {
            float mm = -INFINITY;
#pragma unroll
            for (int qq = 0; qq < 4; ++qq) mm = fmaxf(mm, *(const float*)(L + (((2 * qq + cgp) * 4 + j) * 32 + r32) * 4));
            const unsigned hi = __float_as_uint(mm) & 0xffff0000u;
            const float r1 = mm - __uint_as_float(hi);
            const unsigned mid = __float_as_uint(r1) & 0xffff0000u;
            const float r2 = r1 - __uint_as_float(mid);
            const unsigned lo = __float_as_uint(r2) & 0xffff0000u;
            if (h) q2[j] = __builtin_bit_cast(bf16x4, u32x2{hi ^ 0x80000000u, ((mid ^ 0x80000000u) >> 16) | (lo ^ 0x80000000u)});
        }
        RAW_BARRIER();
    }
    f32x16 o[4], sa, sb;
#pragma unroll
    for (int j = 0; j < 4; ++j) o[j] = zero16;
    u32x4 p1prev = {0u, 0u, 0u, 0u};
    sa = MFMA32_16(k1[0], q1[0], zero16);
    SBAR(); asm volatile("s_nop 15" ::: "memory"); SBAR();
    sa = MFMA32_8(k2[0], q2[0], sa);
    SBAR(); asm volatile("s_nop 15" ::: "memory"); SBAR();
    u32x4 prep[4]; char* prep_dst = nullptr; int prep_st = 0;
#pragma unroll
    for (int t = 0; t < 16; ++t) {
#pragma unroll
        for (int j = 0; j < 4; ++j) {
            const int it = 4 * t + j;
            f32x16& cur = (it & 1) ? sb : sa; f32x16& nxt = (it & 1) ? sa : sb;
            const int tn = j < 3 ? t : t + 1, jn = (j + 1) & 3;
            const int tp = j > 0 ? t : t - 1, jp = (j + 3) & 3;
            u32x4 p0, p1;
            if (j == 0 && t + 2 < 16) K_LOAD(t + 2);
            if (j == 1 && t + 2 < 16) V_LOAD(t + 2);
            if (PREP && j == 2 && (t == PJ0 || t == PJ1)) {
                int id = (t == PJ1 ? 2048 : 0) + b * 8 + w; if (id >= NPREP) id -= 2048;
                const u16* src; int ld;
                if (id < 1728) { const int ks = id / 27, c4 = id - ks * 27; src = w1 + (size_t)(32 * ks) * F_ + 64 * c4; ld = F_;
                                 prep_dst = ws + OFF_W1F + (size_t)(4 * c4 * 64 + ks) * 1024; prep_st = 64 * 1024; }
                else if (id < 3456) { const int jj = id - 1728, ks = jj >> 5, c4 = jj & 31; src = w2 + (size_t)(32 * ks) * S_ + 64 * c4; ld = S_;
                                      prep_dst = ws + OFF_W2F + (size_t)(4 * c4 * 54 + ks) * 1024; prep_st = 54 * 1024; }
                else { int jj = id - 3456; const bool isq = jj >= 200; if (isq) jj -= 200; const int ks = jj / 10, c4 = jj - ks * 10;
                       src = (isq ? wq : wo) + (size_t)(32 * ks) * 640 + 64 * c4; ld = 640;
                       prep_dst = ws + (isq ? OFF_WQF : OFF_WOF) + (size_t)(4 * c4 * 20 + ks) * 1024; prep_st = 20 * 1024; }
                src += (size_t)(lane >> 3) * ld + (lane & 7) * 8;
#pragma unroll
                for (int i = 0; i < 4; ++i) prep[i] = *(const u32x4*)(src + (size_t)(8 * i) * ld);
            }
            if (PREP && j == 2 && t == 9) {
                const int id = (b * 8 + w) % 80, ks = id >> 2, ct = id & 3; int cn = 16 * (ct & 1) + n16; if (cn >= 20) cn = 0;
                const u16* sp = ((ct & 2) ? wv : wk) + (size_t)(32 * ks + 8 * g) * 20 + cn;
                if (b < 10) {
#pragma unroll
                    for (int e = 0; e < 8; ++e) prep[e >> 2][e & 3] = sp[e * 20];
                }
                prep_dst = ws + OFF_WQF + (size_t)((40 + ct) * 20 + ks) * 1024;
            }
            if (PREP && j == 2 && t == 12 && b < 10)
                *(u32x4*)(prep_dst + lane * 16) = u32x4{prep[0][0] | (prep[0][1] << 16), prep[0][2] | (prep[0][3] << 16),
                                                        prep[1][0] | (prep[1][1] << 16), prep[1][2] | (prep[1][3] << 16)};
            if (PREP && j == 2 && (t == PJ0 + PJD || t == PJ1 + PJD)) {
#pragma unroll
                for (int i = 0; i < 4; ++i) *(u32x4*)(L + A_STG + w * 4096 + i * 1024 + lane * 16) = prep[i];
            }
            if (PREP && j == 3 && (t == PJ0 + PJD || t == PJ1 + PJD)) {
                lds_cptr tp_ = (lds_cptr)(lds_ptr)L + A_STG + w * 4096 + (8 * g + (n16 >> 2)) * 128 + (lane & 3) * 8;
#pragma unroll
                for (int i = 0; i < 4; ++i) prep[i] = __builtin_bit_cast(u32x4, cat8(tr_read(tp_ + i * 32), tr_read(tp_ + 512 + i * 32)));
            }
            if (PREP && j == 0 && (t == PJ0 + PJD + 1 || t == PJ1 + PJD + 1)) {
#pragma unroll
                for (int i = 0; i < 4; ++i) PREP_STORE((u32x4*)(prep_dst + (size_t)i * prep_st + lane * 16), prep[i]);
            }
            SBAR();
            if (it < 63) nxt = MFMA32_16(k1[tn % 3], q1[jn], zero16);
            SBAR();
            p0[0] = pk2(EX2(cur[0]), EX2(cur[1])); p0[1] = pk2(EX2(cur[2]), EX2(cur[3]));
            PIN(p0); SBAR();
            if (it > 0) o[jp] = MFMA32_16(v1[(tp + 3) % 3], __builtin_bit_cast(bf16x8, p1prev), o[jp]);
            SBAR();
            p0[2] = pk2(EX2(cur[4]), EX2(cur[5])); p0[3] = pk2(EX2(cur[6]), EX2(cur[7]));
            PIN(p0); SBAR();
            if (it < 63) nxt = MFMA32_8(k2[tn % 3], q2[jn], nxt);
            SBAR();
            p1[0] = pk2(EX2(cur[8]), EX2(cur[9])); p1[1] = pk2(EX2(cur[10]), EX2(cur[11]));
            PIN(p1); SBAR();
            o[j] = MFMA32_16(v0[t % 3], __builtin_bit_cast(bf16x8, p0), o[j]);
            SBAR();
            p1[2] = pk2(EX2(cur[12]), EX2(cur[13])); p1[3] = pk2(EX2(cur[14]), EX2(cur[15]));
            PIN(p1); SBAR();
            p1prev = p1;
        }
    }
    o[3] = MFMA32_16(v1[15 % 3], __builtin_bit_cast(bf16x8, p1prev), o[3]);
#pragma unroll
    for (int j = 0; j < 4; ++j) {
        char* pb = L + (kq * 8 + cgp * 4 + j) * PBW;
        *(f32x4*)(pb + lane * 16) = f32x4{o[j][0], o[j][1], o[j][2], o[j][3]};
        *(f32x4*)(pb + 1024 + lane * 16) = f32x4{o[j][4], o[j][5], o[j][6], o[j][7]};
        if (h == 0) *(f32x4*)(pb + 2048 + r32 * 16) = f32x4{o[j][8], o[j][9], o[j][10], o[j][11]};
        else *(float*)(pb + 2560 + r32 * 4) = o[j][8];
    }
    RAW_BARRIER();
    {
        const char* pb = L + w * PBW;
        f32x4 a0 = {0.f, 0.f, 0.f, 0.f}, a1 = a0, a2 = a0; float l = 0.f;
#pragma unroll
        for (int qq = 0; qq < 4; ++qq) {
            a0 += *(const f32x4*)(pb + qq * 8 * PBW + lane * 16);
            a1 += *(const f32x4*)(pb + qq * 8 * PBW + 1024 + lane * 16);
            a2 += *(const f32x4*)(pb + qq * 8 * PBW + 2048 + r32 * 16);
            l += *(const float*)(pb + qq * 8 * PBW + 2560 + r32 * 4);
        }
        const float inv = __builtin_amdgcn_rcpf(l);
        const int tl = 32 * (w & 1) + r32, hd0 = 80 * cg + 20 * (w >> 1);
        char* ob = ws + OFF_O + (size_t)(tb * 4 + (tl >> 4)) * 20480 + (tl & 15) * 16;
        auto st = [&](int hd, const f32x4& a) {
            *(u32x2*)(ob + (hd >> 5) * 1024 + ((hd & 31) >> 3) * 256 + (hd & 7) * 2) = u32x2{pk2(a[0] * inv, a[1] * inv), pk2(a[2] * inv, a[3] * inv)};
        };
        st(hd0 + 4 * h, a0); st(hd0 + 8 + 4 * h, a1);
        if (h == 0) st(hd0 + 16, a2);
    }
    if (b == 0 && tid < 48) *(unsigned*)(ws + OFF_CNT + tid * 256) = 0u;
}

typedef __attribute__((address_space(1))) unsigned gu32;
constexpr int AUXL = 16;
#define LD_SC1(off) __builtin_bit_cast(bf16x8, __builtin_amdgcn_raw_buffer_load_b128(rs, (int)(off), 0, 16))
DI void edge_publish(gu32* cnt) {
    RAW_BARRIER();
    if (threadIdx.x == 0) __hip_atomic_fetch_add(cnt, 1u, __ATOMIC_RELAXED, __HIP_MEMORY_SCOPE_AGENT);
}
DI void edge_wait(gu32* cnt, unsigned want) {
    if (threadIdx.x == 0) {
        for (int spin = 0; spin < (1 << 20); ++spin) {
            if (__hip_atomic_load(cnt, __ATOMIC_RELAXED, __HIP_MEMORY_SCOPE_AGENT) >= want) break;
            __builtin_amdgcn_s_sleep(1);
        }
        __builtin_amdgcn_fence(__ATOMIC_ACQUIRE, "wavefront");
    }
    RAW_BARRIER();
}
constexpr int W1_A = 6, W2_FIRST = 7;
constexpr int MLP_LDS = 73728;
#define WO_LD(ks) do { _Pragma("unroll") for (int i = 0; i < 4; ++i) xf[ks][i] = xb[(((i + w) & 3) * 20 + (ks)) * 64]; \
        _Pragma("unroll") for (int nt = 0; nt < 5; ++nt) wf[ks][nt] = wb[(nt * 20 + (ks)) * 64]; SBAR(); } while (0)
template <bool R1>
__global__ __launch_bounds__(256, 1) void k_mlp(char* __restrict__ ws, const u16* __restrict__ xin, const u16* __restrict__ b1,
                                                const u16* __restrict__ b2, u16* __restrict__ out) {
    extern __shared__ __attribute__((aligned(16))) char lds_raw[];
    char* L = lds_raw;
    const int tid = threadIdx.x, lane = tid & 63, w = __builtin_amdgcn_readfirstlane(tid >> 6);
    const int b = blockIdx.x, slot = b >> 3, tb = ((b & 7) >> 1) * 8 + (slot >> 2), cg = (b & 1) * 4 + (slot & 3);
    const int ot1 = ((b & 7) >> 1) * 7 + (slot >> 2);
    const bool act1 = slot < 28 && ot1 < 27;
    const int n16 = lane & 15, g = lane >> 4;
    const __amdgpu_buffer_rsrc_t rs = __builtin_amdgcn_make_buffer_rsrc((void*)ws, 0, (int)WS_NEED, 0x00020000);
    gu32* cnt = (gu32*)(ws + OFF_CNT);
    f32x4 acc[4][5];
#define ACC_ZERO() do { _Pragma("unroll") for (int i_ = 0; i_ < 4; ++i_) _Pragma("unroll") for (int j_ = 0; j_ < 5; ++j_) acc[i_][j_] = f32x4{0.f, 0.f, 0.f, 0.f}; } while (0)
#define ACC_REDUCE() do { \
    _Pragma("unroll") for (int j_ = 1; j_ < 4; ++j_) { const int o_ = (w + j_) & 3; \
        _Pragma("unroll") for (int i_ = 0; i_ < 5; ++i_) *(f32x4*)(L + (((o_ * 3 + (j_ - 1)) * 5 + i_) * 64 + lane) * 16) = acc[j_][i_]; } \
    RAW_BARRIER(); \
    _Pragma("unroll") for (int j_ = 0; j_ < 3; ++j_) _Pragma("unroll") for (int i_ = 0; i_ < 5; ++i_) \
        acc[1 + j_][i_] = *(const f32x4*)(L + (((w * 3 + j_) * 5 + i_) * 64 + lane) * 16); \
    SBAR(); \
    _Pragma("unroll") for (int i_ = 0; i_ < 5; ++i_) acc[0][i_] = (acc[0][i_] + acc[1][i_]) + (acc[2][i_] + acc[3][i_]); } while (0)
    const int tok = tb * 64 + 16 * w + n16;
    bf16x8 wr[16][4];
    int ro1[4], ro2[4];
#pragma unroll
    for (int j = 0; j < 4; ++j) { ro1[j] = ((j + w) & 3) * 64 * 64; ro2[j] = ((j + w) & 3) * 54 * 64; }
    const bf16x8* pw1 = (const bf16x8*)(ws + OFF_W1F) + ((size_t)(4 * (act1 ? ot1 : 0)) * 64 + 16 * w) * 64 + lane;
    const int k2 = w < 2 ? 14 * w : 28 + 13 * (w - 2);
    const bf16x8* pw2 = (const bf16x8*)(ws + OFF_W2F) + ((size_t)(4 * tb) * 54 + k2) * 64 + lane;
#define W1_LD(lo, hi) do { _Pragma("unroll") for (int s_ = (lo); s_ < (hi); ++s_) _Pragma("unroll") for (int j_ = 0; j_ < 4; ++j_) wr[s_][j_] = pw1[ro1[j_] + s_ * 64]; SBAR(); } while (0)
#define W2_LD(lo, hi) do { _Pragma("unroll") for (int s_ = (lo); s_ < (hi); ++s_) _Pragma("unroll") for (int j_ = 0; j_ < 4; ++j_) wr[s_][j_] = pw2[ro2[j_] + s_ * 64]; SBAR(); } while (0)

    u32x2 x1o[5], bb; float bs;
    {
        ACC_ZERO();
        u32x2 xi[5];
        const bf16x8* xb = (const bf16x8*)(ws + OFF_O) + (size_t)(tb * 4 * 20 + 5 * w) * 64 + lane;
        const bf16x8* wb = (const bf16x8*)(ws + OFF_WOF) + (size_t)(5 * cg * 20 + 5 * w) * 64 + lane;
        bf16x8 xf[5][4], wf[5][5];
        WO_LD(0); WO_LD(1); WO_LD(2); WO_LD(3);
#pragma unroll
        for (int ks = 0; ks < 5; ++ks) {
#pragma unroll
            for (int nt = 0; nt < 5; ++nt)
#pragma unroll
                for (int i = 0; i < 4; ++i) acc[i][nt] = MFMA16_32(wf[ks][nt], xf[ks][i], acc[i][nt]);
            SBAR();
            if (ks == 0) {
                WO_LD(4);
#pragma unroll
                for (int nt = 0; nt < 5; ++nt) {
                    const int d = 80 * cg + 16 * nt + 4 * g;
                    if (!R1) xi[nt] = *(const u32x2*)(xin + ((size_t)((tok >> 4) * 20 + (d >> 5)) * 64 + ((d & 31) >> 3) * 16 + n16) * 8 + (d & 7));
                    else xi[nt] = *(const u32x2*)(xin + (size_t)tok * 640 + d);
                }
                bb = *(const u32x2*)(b1 + 64 * (act1 ? ot1 : 0) + 16 * w + 4 * g);
                bs = bf2f(b2[tok]);
                SBAR();
            }
            if (ks == 1) W1_LD(0, 2);
            if (ks == 2) W1_LD(2, 4);
            if (ks == 3) W1_LD(4, 6);
            if (ks == 4) W1_LD(6, W1_A);
        }
        ACC_REDUCE();
        float xv[20], s = 0.f;
#pragma unroll
        for (int nt = 0; nt < 5; ++nt) {
            const unsigned u0 = pk2(bf_lo(xi[nt][0]) + acc[0][nt][0], bf_hi(xi[nt][0]) + acc[0][nt][1]);
            const unsigned u1 = pk2(bf_lo(xi[nt][1]) + acc[0][nt][2], bf_hi(xi[nt][1]) + acc[0][nt][3]);
            x1o[nt] = u32x2{u0, u1};
            xv[4 * nt] = bf_lo(u0); xv[4 * nt + 1] = bf_hi(u0); xv[4 * nt + 2] = bf_lo(u1); xv[4 * nt + 3] = bf_hi(u1);
            s += (xv[4 * nt] + xv[4 * nt + 1]) + (xv[4 * nt + 2] + xv[4 * nt + 3]);
        }
        const float mw = add32(add16(s)) * (1.f / 80.f);
        float s2 = 0.f;
#pragma unroll
        for (int i = 0; i < 20; ++i) { const float dd = xv[i] - mw; s2 += dd * dd; }
        s2 = add32(add16(s2));
        gu64* gp = (gu64*)(ws + OFF_XG) + (size_t)tok * 8;
        if (g == 0) __hip_atomic_store(gp + cg, ((unsigned long long)(__float_as_uint(s2) | 0x80000000u) << 32) | __float_as_uint(mw),
                                       __ATOMIC_RELAXED, __HIP_MEMORY_SCOPE_AGENT);
        SBAR();
        unsigned long long v0, v1;
        for (int spin = 0; spin < (1 << 20); ++spin) {
            v0 = __hip_atomic_load(gp + 2 * g, __ATOMIC_RELAXED, __HIP_MEMORY_SCOPE_AGENT);
            v1 = __hip_atomic_load(gp + 2 * g + 1, __ATOMIC_RELAXED, __HIP_MEMORY_SCOPE_AGENT);
            if (__all((long long)(v0 & v1) < 0)) break;
            __builtin_amdgcn_s_sleep(4);
        }
        const float m0 = __uint_as_float((unsigned)v0), m1 = __uint_as_float((unsigned)v1);
        const float mean = add32(add16(m0 + m1)) * 0.125f;
        const float d0 = m0 - mean, d1 = m1 - mean;
        const float var = add32(add16(__uint_as_float((unsigned)(v0 >> 32) & 0x7fffffffu) + __uint_as_float((unsigned)(v1 >> 32) & 0x7fffffffu) +
                                      80.f * (d0 * d0 + d1 * d1))) * (1.f / 640.f);
        float shf, r2; ln2_scalars(mean, var, shf, r2);
        char* hb = L + w * 15360;
#pragma unroll
        for (int nt = 0; nt < 5; ++nt)
            *(u32x2*)(hb + n16 * 176 + (16 * nt + 4 * g) * 2) =
                u32x2{pk2((xv[4 * nt] - shf) * r2, (xv[4 * nt + 1] - shf) * r2), pk2((xv[4 * nt + 2] - shf) * r2, (xv[4 * nt + 3] - shf) * r2)};
        asm volatile("s_waitcnt lgkmcnt(0)" ::: "memory");
        const int q4 = (lane & 15) >> 2, p4 = lane & 3;
        lds_cptr hr = (lds_cptr)(lds_ptr)L + w * 15360 + (8 * (g & 1) + q4) * 176 + p4 * 8 + (g >> 1) * 32;
        const int s0 = tb * 64 + 16 * w + 8 * (g & 1);
        const int hoff = (int)OFF_H + ((5 * cg + (g >> 1)) * 64 + (s0 >> 5)) * 1024 + (((s0 & 31) >> 3) * 16 + n16) * 16;
        const bf16x8 f0 = cat8(tr_read(hr), tr_read(hr + 704));
        const bf16x8 f1 = cat8(tr_read(hr + 64), tr_read(hr + 704 + 64));
        const bf16x8 f2 = cat8(tr_read(hr + (g < 2 ? 128 : 0)), tr_read(hr + 704 + (g < 2 ? 128 : 0)));
        __builtin_amdgcn_raw_buffer_store_b128(__builtin_bit_cast(u32x4, f0), rs, hoff, 0, 16);
        __builtin_amdgcn_raw_buffer_store_b128(__builtin_bit_cast(u32x4, f1), rs, hoff + 2 * 65536, 0, 16);
        if (g < 2) __builtin_amdgcn_raw_buffer_store_b128(__builtin_bit_cast(u32x4, f2), rs, hoff + 4 * 65536, 0, 16);
        SBAR();
        W1_LD(W1_A, W1_A + 4);
        VMCNT(16);
        edge_publish(cnt + cg * 64);
    }
    if (act1) {
        W1_LD(W1_A + 4, 16);
        edge_wait(cnt + cg * 64, 32);
        ACC_ZERO();
        const int ho = (int)OFF_H + ((5 * cg) * 64 + 16 * w) * 1024 + lane * 16;
        constexpr int PF = 6;
        bf16x8 rf[PF][5];
#pragma unroll
        for (int s = 0; s < PF; ++s) {
#pragma unroll
            for (int i = 0; i < 5; ++i) rf[s][i] = LD_SC1(ho + i * 65536 + s * 1024);
            SBAR();
        }
#pragma unroll
        for (int s = 0; s < 16; ++s) {
#pragma unroll
            for (int j = 0; j < 4; ++j)
#pragma unroll
                for (int i = 0; i < 5; ++i) acc[j][i] = MFMA16_32(wr[s][j], rf[s % PF][i], acc[j][i]);
            SBAR();
            if (s + PF < 16) {
#pragma unroll
                for (int i = 0; i < 5; ++i) rf[s % PF][i] = LD_SC1(ho + i * 65536 + (s + PF) * 1024);
                SBAR();
            }
            if (s >= W2_FIRST && s - W2_FIRST < 9) W2_LD(s - W2_FIRST, s - W2_FIRST + 1);
        }
        ACC_REDUCE();
        const int f = 64 * ot1 + 16 * w + 4 * g;
        const float bv[4] = {bf_lo(bb[0]), bf_hi(bb[0]), bf_lo(bb[1]), bf_hi(bb[1])};
        const int uo = (int)OFF_U + ((5 * cg) * 54 + (f >> 5)) * 1024 + (((f & 31) >> 3) * 16 + n16) * 16 + (f & 7) * 2;
#pragma unroll
        for (int i = 0; i < 5; ++i) {
            float r[4];
#pragma unroll
            for (int e = 0; e < 4; ++e) {
                const float u = acc[0][i][e] + bv[e];
                const float z = u * (1.f + 0.044715f * u * u) * (-2.f * 0.7978845608028654f * 1.4426950408889634f);
                r[e] = u * __builtin_amdgcn_rcpf(1.f + EX2(z));
            }
            __builtin_amdgcn_raw_buffer_store_b64(u32x2{pk2(r[0], r[1]), pk2(r[2], r[3])}, rs, uo + i * 54 * 1024, 0, 16);
        }
        SBAR();
        W2_LD(9, 13);
        VMCNT(16);
        edge_publish(cnt + (8 + cg) * 64);
    } else {
        W2_LD(0, 13);
    }
    {
        if (w < 2) W2_LD(13, 14);
        edge_wait(cnt + (8 + cg) * 64, 27);
        ACC_ZERO();
        const int uo = (int)OFF_U + ((5 * cg) * 54 + k2) * 1024 + lane * 16;
        constexpr int PF = 6;
        bf16x8 rf[PF][5];
#pragma unroll
        for (int s = 0; s < PF; ++s) {
#pragma unroll
            for (int i = 0; i < 5; ++i) rf[s][i] = LD_SC1(uo + i * 54 * 1024 + s * 1024);
            SBAR();
        }
#pragma unroll
        for (int s = 0; s < 14; ++s) {
            if (s < 13 || w < 2) {
#pragma unroll
                for (int j = 0; j < 4; ++j)
#pragma unroll
                    for (int i = 0; i < 5; ++i) acc[j][i] = MFMA16_32(rf[s % PF][i], wr[s][j], acc[j][i]);
            }
            SBAR();
            if (s + PF < 13 || (s + PF == 13 && w < 2)) {
#pragma unroll
                for (int i = 0; i < 5; ++i) rf[s % PF][i] = LD_SC1(uo + i * 54 * 1024 + (s + PF) * 1024);
                SBAR();
            }
        }
        ACC_REDUCE();
        float xv[20], sum = 0.f;
#pragma unroll
        for (int i = 0; i < 5; ++i) {
            const int d = 80 * cg + 16 * i + 4 * g;
            const unsigned u0 = pk2(bf_lo(x1o[i][0]) + acc[0][i][0] + bs, bf_hi(x1o[i][0]) + acc[0][i][1] + bs);
            const unsigned u1 = pk2(bf_lo(x1o[i][1]) + acc[0][i][2] + bs, bf_hi(x1o[i][1]) + acc[0][i][3] + bs);
            if (R1) __builtin_amdgcn_raw_buffer_store_b64(u32x2{u0, u1}, rs,
                        (int)OFF_X2 + ((4 * tb + w) * 20 + (d >> 5)) * 1024 + (((d & 31) >> 3) * 16 + n16) * 16 + (d & 7) * 2, 0, 16);
            else *(u32x2*)(out + (size_t)tok * 640 + d) = u32x2{u0, u1};
            xv[4 * i] = bf_lo(u0); xv[4 * i + 1] = bf_hi(u0); xv[4 * i + 2] = bf_lo(u1); xv[4 * i + 3] = bf_hi(u1);
            sum += (xv[4 * i] + xv[4 * i + 1]) + (xv[4 * i + 2] + xv[4 * i + 3]);
        }
        if (R1) {
            const float mw = add32(add16(sum)) * (1.f / 80.f);
            float s2 = 0.f;
#pragma unroll
            for (int i = 0; i < 20; ++i) { const float dd = xv[i] - mw; s2 += dd * dd; }
            s2 = add32(add16(s2));
            if (g == 0) __builtin_amdgcn_raw_buffer_store_b64(u32x2{__float_as_uint(mw), __float_as_uint(s2)}, rs, (int)OFF_XS + ((tb * 8 + cg) * 64 + 16 * w + n16) * 8, 0, 16);
        }
    }
    if (R1) {
        const int mt0 = cg >> 1, isv = cg & 1;
        const bf16x8* wb = (const bf16x8*)(ws + OFF_WQF) + (size_t)(5 * cg * 20 + 5 * w) * 64 + lane;
        const bf16x8* kb = (const bf16x8*)(ws + OFF_WQF) + (size_t)((40 + 2 * isv) * 20 + 5 * w) * 64 + lane;
        bf16x8 wf[5][5], wkf[5][2], xf[5][4], xk[5];
        SBAR();
#pragma unroll
        for (int i = 0; i < 16; ++i) wf[i / 5][i % 5] = wb[((i % 5) * 20 + i / 5) * 64];
        SBAR();
        VMCNT(16);
        edge_publish(cnt + (16 + tb) * 64);
#pragma unroll
        for (int i = 16; i < 25; ++i) wf[i / 5][i % 5] = wb[((i % 5) * 20 + i / 5) * 64];
#pragma unroll
        for (int ks = 0; ks < 5; ++ks) { wkf[ks][0] = kb[ks * 64]; wkf[ks][1] = kb[(20 + ks) * 64]; }
        f32x4 csq[5], cs0, cs1, stq[4], stk[4], acck[2];
        {
            const float* csp = (const float*)(ws + OFF_CS);
#pragma unroll
            for (int nt = 0; nt < 5; ++nt) csq[nt] = *(const f32x4*)(csp + 80 * cg + 16 * nt + 4 * g);
            cs0 = *(const f32x4*)(csp + 640 + 32 * isv + 4 * g); cs1 = *(const f32x4*)(csp + 640 + 32 * isv + 16);
        }
        SBAR();
        edge_wait(cnt + (16 + tb) * 64, 8);
        const int xo = (int)OFF_X2 + (tb * 4 * 20 + 5 * w) * 1024 + lane * 16;
#pragma unroll
        for (int ks = 0; ks < 5; ++ks) {
#pragma unroll
            for (int i = 0; i < 4; ++i) xf[ks][i] = LD_SC1(xo + (((i + w) & 3) * 20 + ks) * 1024);
            xk[ks] = LD_SC1(xo + (mt0 * 20 + ks) * 1024);
            SBAR();
        }
#pragma unroll
        for (int i = 0; i < 4; ++i) {
            const u32x2 a0 = __builtin_amdgcn_raw_buffer_load_b64(rs, (int)OFF_XS + ((tb * 8 + 2 * i) * 64 + 16 * w + n16) * 8, 0, AUXL);
            const u32x2 a1 = __builtin_amdgcn_raw_buffer_load_b64(rs, (int)OFF_XS + ((tb * 8 + 2 * i + 1) * 64 + 16 * w + n16) * 8, 0, AUXL);
            const u32x2 c0 = __builtin_amdgcn_raw_buffer_load_b64(rs, (int)OFF_XS + ((tb * 8 + 2 * i) * 64 + 16 * mt0 + n16) * 8, 0, AUXL);
            const u32x2 c1 = __builtin_amdgcn_raw_buffer_load_b64(rs, (int)OFF_XS + ((tb * 8 + 2 * i + 1) * 64 + 16 * mt0 + n16) * 8, 0, AUXL);
            stq[i] = __builtin_bit_cast(f32x4, u32x4{a0[0], a0[1], a1[0], a1[1]});
            stk[i] = __builtin_bit_cast(f32x4, u32x4{c0[0], c0[1], c1[0], c1[1]});
        }
        SBAR();
        ACC_ZERO();
        acck[0] = f32x4{0.f, 0.f, 0.f, 0.f}; acck[1] = acck[0];
#pragma unroll
        for (int ks = 0; ks < 5; ++ks) {
#pragma unroll
            for (int nt = 0; nt < 5; ++nt)
#pragma unroll
                for (int i = 0; i < 4; ++i) acc[i][nt] = MFMA16_32(wf[ks][nt], xf[ks][i], acc[i][nt]);
            acck[0] = MFMA16_32(wkf[ks][0], xk[ks], acck[0]);
            acck[1] = MFMA16_32(wkf[ks][1], xk[ks], acck[1]);
            SBAR();
        }
        qkv_finish(ws, L, tb, cg, w, lane, acc, acck, csq, cs0, cs1, stq, stk);
    }
}

template <int N> __global__ void k_end() {}

extern "C" void kernel_launch(void* const* d_in, const int* in_sizes, int n_in, void* d_out, int out_size, void* d_ws,
                              size_t ws_size, hipStream_t stream) {
    const u16* x = (const u16*)d_in[0];
    const u16* wq = (const u16*)d_in[1];
    const u16* wk = (const u16*)d_in[2];
    const u16* wv = (const u16*)d_in[3];
    const u16* wo = (const u16*)d_in[4];
    const u16* w1 = (const u16*)d_in[6];
    const u16* b1 = (const u16*)d_in[7];
    const u16* w2 = (const u16*)d_in[8];
    const u16* b2 = (const u16*)d_in[9];
    char* ws = (char*)d_ws;
    u16* x2 = (u16*)(ws + OFF_X2);
    (void)hipFuncSetAttribute((const void*)k_attn<true>, hipFuncAttributeMaxDynamicSharedMemorySize, ATT_LDS);
    (void)hipFuncSetAttribute((const void*)k_attn<false>, hipFuncAttributeMaxDynamicSharedMemorySize, ATT_LDS);
    (void)hipFuncSetAttribute((const void*)k_mlp<true>, hipFuncAttributeMaxDynamicSharedMemorySize, MLP_LDS);
    (void)hipFuncSetAttribute((const void*)k_mlp<false>, hipFuncAttributeMaxDynamicSharedMemorySize, MLP_LDS);
    (void)hipFuncSetAttribute((const void*)k_end<0>, hipFuncAttributeMaxDynamicSharedMemorySize, 0);
    k_kv<<<256, 256, 0, stream>>>(x, wk, wv, ws);
    k_attn<true><<<256, 512, ATT_LDS, stream>>>(ws, w1, w2, wo, wq, wk, wv, x);
    k_mlp<true><<<256, 256, MLP_LDS, stream>>>(ws, x, b1, b2, nullptr);
    k_attn<false><<<256, 512, A_STG, stream>>>(ws, w1, w2, wo, wq, wk, wv, x);
    k_mlp<false><<<256, 256, MLP_LDS, stream>>>(ws, x2, b1, b2, (u16*)d_out);
}
```

```cpp
#include <hip/hip_runtime.h>
#include <hip/hip_bf16.h>
#include <stdint.h>


typedef unsigned short u16;
typedef short bf16x8 __attribute__((ext_vector_type(8)));
typedef short bf16x4 __attribute__((ext_vector_type(4)));
typedef float f32x16 __attribute__((ext_vector_type(16)));
typedef float f32x4 __attribute__((ext_vector_type(4)));
typedef float f32x2 __attribute__((ext_vector_type(2)));
typedef unsigned u32x4 __attribute__((ext_vector_type(4)));
typedef unsigned u32x2 __attribute__((ext_vector_type(2)));
typedef __bf16 bf2_t __attribute__((ext_vector_type(2)));
typedef __attribute__((address_space(3))) char* lds_ptr;
typedef __attribute__((address_space(3))) const char* lds_cptr;
typedef __attribute__((address_space(1))) unsigned long long gu64;

#define DI __device__ __forceinline__
#define SBAR() __builtin_amdgcn_sched_barrier(0)
#define PIN(x) asm volatile("" : "+v"(x))
#define MFMA32_16(a, b, c) __builtin_amdgcn_mfma_f32_32x32x16_bf16(a, b, c, 0, 0, 0)
#define MFMA32_8(a, b, c) __builtin_amdgcn_mfma_f32_32x32x8bf16_1k(a, b, c, 0, 0, 0)
#define MFMA16_32(a, b, c) __builtin_amdgcn_mfma_f32_16x16x32_bf16(a, b, c, 0, 0, 0)
#define EX2(x) __builtin_amdgcn_exp2f(x)
#define RAW_BARRIER() asm volatile("s_waitcnt lgkmcnt(0)\n\ts_barrier" ::: "memory")
#define VMCNT(n) asm volatile("s_waitcnt vmcnt(%0)" :: "i"(n) : "memory")

#define PREP_STORE(p, v) asm volatile("global_store_dwordx4 %0, %1, off sc1\n\ts_nop 1" :: "v"(p), "v"(v) : "memory")
#define KADDR(dst, sym) asm volatile("s_getpc_b64 s[40:41]\n\ts_add_u32 s40, s40, " sym "@rel32@lo+4\n\ts_addc_u32 s41, s41, " sym "@rel32@hi+12\n\ts_mov_b64 %0, s[40:41]" : "=s"(dst) :: "s40", "s41", "scc")
constexpr int S_ = 2048, D_ = 640, F_ = 1728;
constexpr float LN_EPS = 1e-5f;
constexpr float QSCALE = 0.22360679774997896f * 1.4426950408889634f;
constexpr float SAFE_BOUND = 48.0f;

DI unsigned pk2(float a, float b) { f32x2 v = {a, b}; bf2_t r = __builtin_convertvector(v, bf2_t); return __builtin_bit_cast(unsigned, r); }
DI float bf_lo(unsigned u) { return __uint_as_float(u << 16); }
DI float bf_hi(unsigned u) { return __uint_as_float(u & 0xffff0000u); }
DI float bf2f(u16 v) { return __uint_as_float((unsigned)v << 16); }
DI float dot2(unsigned a, unsigned b, float c) { return __builtin_amdgcn_fdot2_f32_bf16(__builtin_bit_cast(bf2_t, a), __builtin_bit_cast(bf2_t, b), c, false); }
DI bf16x4 tr_read(lds_cptr p) { return __builtin_amdgcn_ds_read_tr16_b64_v4i16((__attribute__((address_space(3))) bf16x4*)p); }
DI bf16x8 cat8(bf16x4 a, bf16x4 b) { return __builtin_shufflevector(a, b, 0, 1, 2, 3, 4, 5, 6, 7); }
DI void glds16(const void* g, lds_ptr l) {
    unsigned keep;
    asm volatile("s_mov_b32 %0, m0\n\ts_mov_b32 m0, %2\n\ts_nop 0\n\tglobal_load_lds_dwordx4 %1, off\n\ts_mov_b32 m0, %0"
                 : "=&s"(keep) : "v"(g), "s"((unsigned)(uintptr_t)l) : "memory");
}
DI float xor32(float v) { return __shfl_xor(v, 32); }
DI float add32(float v) {
    auto r = __builtin_amdgcn_permlane32_swap(__float_as_uint(v), __float_as_uint(v), false, false);
    return __uint_as_float(r[0]) + __uint_as_float(r[1]);
}
DI float add16(float v) {
    auto r = __builtin_amdgcn_permlane16_swap(__float_as_uint(v), __float_as_uint(v), false, false);
    return __uint_as_float(r[0]) + __uint_as_float(r[1]);
}
DI float max16x(float v) { auto r = __builtin_amdgcn_permlane16_swap(__float_as_uint(v), __float_as_uint(v), false, false); return fmaxf(__uint_as_float(r[0]), __uint_as_float(r[1])); }
DI float max32x(float v) { auto r = __builtin_amdgcn_permlane32_swap(__float_as_uint(v), __float_as_uint(v), false, false); return fmaxf(__uint_as_float(r[0]), __uint_as_float(r[1])); }
template <int CTRL> DI float dppf(float v) { return __int_as_float(__builtin_amdgcn_update_dpp(0, __float_as_int(v), CTRL, 0xf, 0xf, false)); }
DI float rowmax16(float v) {
    v = fmaxf(v, dppf<0x128>(v)); v = fmaxf(v, dppf<0x124>(v)); v = fmaxf(v, dppf<0x122>(v)); v = fmaxf(v, dppf<0x121>(v)); return v;
}
DI float wavemax(float v) { return max32x(max16x(rowmax16(v))); }

constexpr size_t OFF_Q = 0;
constexpr size_t OFF_K1 = OFF_Q + (size_t)2048 * 32 * 48;
constexpr size_t OFF_K2 = OFF_K1 + 2048 * 32;
constexpr size_t OFF_V = OFF_K2 + 2048 * 8;
constexpr size_t OFF_KN = OFF_V + 128 * 640;
constexpr size_t OFF_QN = OFF_KN + 512;
constexpr size_t OFF_CS = OFF_QN + 4096;
constexpr size_t OFF_CONST = OFF_CS + 4096;
constexpr size_t OFF_CNT = OFF_CONST + 256;
constexpr size_t OFF_XG = OFF_CNT + 48 * 256;
constexpr size_t OFF_XS = OFF_XG + 131072;
constexpr size_t OFF_H = OFF_XS + 131072 + 256;
constexpr size_t OFF_U = OFF_H + (size_t)S_ * D_ * 2;
constexpr size_t OFF_X2 = OFF_U + (size_t)D_ * F_ * 2;
constexpr size_t OFF_O = OFF_X2 + (size_t)S_ * D_ * 2;
constexpr size_t OFF_W1F = OFF_O + (size_t)S_ * D_ * 2;
constexpr size_t OFF_W2F = OFF_W1F + (size_t)S_ * F_ * 2;
constexpr size_t OFF_WQF = OFF_W2F + (size_t)S_ * F_ * 2;
constexpr size_t OFF_WOF = OFF_WQF + (size_t)44 * 20 * 1024;
constexpr size_t WS_NEED = OFF_WOF + (size_t)800 * 1024;
constexpr int NPREP = 1728 + 1728 + 200 + 200;

DI float rbf(float v) { return bf_lo(pk2(v, 0.f)); }
DI float ln_sd(float var) { return rbf(__builtin_amdgcn_sqrtf(rbf(rbf(var) + LN_EPS))); }
DI void ln2_scalars(float mu, float var, float& shift, float& scale) {
    const float m1 = rbf(mu), sd1 = ln_sd(var), i1 = 1.0f / sd1;
    const float m2 = rbf((mu - m1) * i1), sd2 = ln_sd(var * i1 * i1);
    shift = m1 + m2 * sd1; scale = i1 / sd2;
}
DI void chan8(const f32x4 (&st)[4], float& mean, float& var) {
    float m = 0.f, m2 = 0.f;
#pragma unroll
    for (int i = 0; i < 4; ++i) { m += st[i][0] + st[i][2]; m2 += st[i][1] + st[i][3]; }
    m *= 0.125f;
#pragma unroll
    for (int i = 0; i < 4; ++i) { const float d0 = st[i][0] - m, d1 = st[i][2] - m; m2 += 80.f * (d0 * d0 + d1 * d1); }
    mean = m; var = m2 * (1.f / 640.f);
}

template <bool FAST>
DI void qkv_finish(char* __restrict__ ws, char* L, int tb, int cg, int w, int lane, f32x4 (&acc)[4][5], f32x4 (&acck)[2],
                   const f32x4 (&csq)[5], const f32x4& cs0, const f32x4& cs1, const f32x4 (&stq)[4], const f32x4 (&stk)[4],
                   const float (&sm)[4], const float (&sq)[4], const float (&cs)[5], const float (&csk)[2]) {
    const int n16 = lane & 15, g = lane >> 4, mt0 = cg >> 1, isv = cg & 1;
    constexpr int R_OFF = 0, PK_OFF = 61440, ST_OFF = 69632, CS_OFF = 71680;
#pragma unroll
    for (int j = 1; j < 4; ++j) {
        const int o = (w + j) & 3;
#pragma unroll
        for (int nt = 0; nt < 5; ++nt) *(f32x4*)(L + R_OFF + (((o * 3 + (j - 1)) * 5 + nt) * 64 + lane) * 16) = acc[j][nt];
    }
    *(f32x4*)(L + PK_OFF + ((w * 2 + 0) * 64 + lane) * 16) = acck[0];
    *(f32x4*)(L + PK_OFF + ((w * 2 + 1) * 64 + lane) * 16) = acck[1];
    if (!FAST && g == 0) {
#pragma unroll
        for (int i = 0; i < 4; ++i) *(f32x2*)(L + ST_OFF + (w * 64 + ((i + w) & 3) * 16 + n16) * 8) = f32x2{sm[i], sq[i]};
#pragma unroll
        for (int nt = 0; nt < 5; ++nt) *(float*)(L + CS_OFF + w * 448 + (nt * 16 + n16) * 4) = cs[nt];
#pragma unroll
        for (int nt = 0; nt < 2; ++nt) *(float*)(L + CS_OFF + w * 448 + (80 + nt * 16 + n16) * 4) = csk[nt];
    }
    char* qrow = ws + OFF_Q + (size_t)(tb * 8 + cg) * 12288 + (16 * w + n16) * 48;
    int qoff[5];
#pragma unroll
    for (int nt = 0; nt < 5; ++nt) { const int G = 4 * nt + g, hl = G / 5; qoff[nt] = hl * 3072 + (G - 5 * hl) * 8; PIN(qoff[nt]); }
    RAW_BARRIER();
    auto stats = [&](int tl, const f32x4 (&st)[4], float& mean, float& rstd) {
        float var;
        if (FAST) chan8(st, mean, var);
        else {
            float s = 0.f, s2 = 0.f;
#pragma unroll
            for (int ww = 0; ww < 4; ++ww) { f32x2 v = *(const f32x2*)(L + ST_OFF + (ww * 64 + tl) * 8); s += v[0]; s2 += v[1]; }
            mean = s * (1.f / 640.f);
            var = fmaxf(s2 * (1.f / 640.f) - mean * mean, 0.f);
        }
        mean = rbf(mean);
        rstd = 1.0f / ln_sd(var);
    };
    {
        f32x4 rr[3][5], cc[4][5];
#pragma unroll
        for (int j = 0; j < 3; ++j)
#pragma unroll
            for (int nt = 0; nt < 5; ++nt) rr[j][nt] = *(const f32x4*)(L + R_OFF + (((w * 3 + j) * 5 + nt) * 64 + lane) * 16);
        if (!FAST) {
#pragma unroll
            for (int ww = 0; ww < 4; ++ww)
#pragma unroll
                for (int nt = 0; nt < 5; ++nt) cc[ww][nt] = *(const f32x4*)(L + CS_OFF + ww * 448 + (nt * 16 + 4 * g) * 4);
        }
        float mean, rstd; stats(16 * w + n16, stq, mean, rstd);
        SBAR();
        const float sc = rstd * QSCALE;
        float n2[5];
#pragma unroll
        for (int nt = 0; nt < 5; ++nt) {
            const f32x4 a = (acc[0][nt] + rr[0][nt]) + (rr[1][nt] + rr[2][nt]);
            f32x4 s;
            if (FAST) s = csq[nt];
            else {
                s = (cc[0][nt] + cc[1][nt]) + (cc[2][nt] + cc[3][nt]);
                if (tb == 0 && w == 0 && n16 == 0) *(f32x4*)(ws + OFF_CS + (80 * cg + 16 * nt + 4 * g) * 4) = s;
            }
            const unsigned u0 = pk2((a[0] - mean * s[0]) * sc, (a[1] - mean * s[1]) * sc);
            const unsigned u1 = pk2((a[2] - mean * s[2]) * sc, (a[3] - mean * s[3]) * sc);
            *(u32x2*)(qrow + qoff[nt]) = u32x2{u0, u1};
            n2[nt] = dot2(u1, u1, dot2(u0, u0, 0.f));
        }
        float hn[4];
        hn[0] = n2[0] + (g == 0 ? n2[1] : 0.f);
        hn[1] = (g > 0 ? n2[1] : 0.f) + (g < 2 ? n2[2] : 0.f);
        hn[2] = (g > 1 ? n2[2] : 0.f) + (g < 3 ? n2[3] : 0.f);
        hn[3] = (g == 3 ? n2[3] : 0.f) + n2[4];
#pragma unroll
        for (int hh = 0; hh < 4; ++hh) hn[hh] = add32(add16(hn[hh]));
        const float n2a = (g & 1) ? hn[1] : hn[0], n2b = (g & 1) ? hn[3] : hn[2];
        const unsigned qb = pk2(__builtin_amdgcn_sqrtf((g & 2) ? n2b : n2a), 0.f);
        *(u32x2*)(qrow + g * 3072 + 40) = u32x2{qb, 0u};
        float m = bf_lo(qb);
        if (!(m == m)) m = INFINITY;
        m = wavemax(m);
        if (lane == 0) *(float*)(ws + OFF_QN + ((tb * 8 + cg) * 4 + w) * 4) = m;
    }
    if (w == 3) {
        const int t16 = tb * 4 + mt0;
        float mean, rstd; stats(mt0 * 16 + n16, stk, mean, rstd);
        f32x4 a0 = {0.f, 0.f, 0.f, 0.f}, a1 = a0, s0 = a0, s1 = a0;
#pragma unroll
        for (int ww = 0; ww < 4; ++ww) {
            a0 += *(const f32x4*)(L + PK_OFF + ((ww * 2 + 0) * 64 + lane) * 16);
            a1 += *(const f32x4*)(L + PK_OFF + ((ww * 2 + 1) * 64 + n16) * 16);
            if (!FAST) {
                s0 += *(const f32x4*)(L + CS_OFF + ww * 448 + (80 + 4 * g) * 4);
                s1 += *(const f32x4*)(L + CS_OFF + ww * 448 + 96 * 4);
            }
        }
        if (FAST) { s0 = cs0; s1 = cs1; }
        else if (tb == 0 && cg < 2 && n16 == 0) {
            *(f32x4*)(ws + OFF_CS + (640 + 32 * isv + 4 * g) * 4) = s0;
            if (g == 0) *(f32x4*)(ws + OFF_CS + (640 + 32 * isv + 16) * 4) = s1;
        }
        const unsigned u0 = pk2((a0[0] - mean * s0[0]) * rstd, (a0[1] - mean * s0[1]) * rstd);
        const unsigned u1 = pk2((a0[2] - mean * s0[2]) * rstd, (a0[3] - mean * s0[3]) * rstd);
        const unsigned u2 = pk2((a1[0] - mean * s1[0]) * rstd, (a1[1] - mean * s1[1]) * rstd);
        const unsigned u3 = pk2((a1[2] - mean * s1[2]) * rstd, (a1[3] - mean * s1[3]) * rstd);
        if (!isv) {
            const int key = t16 * 16 + n16;
            *(u32x2*)(ws + OFF_K1 + key * 32 + g * 8) = u32x2{u0, u1};
            if (g == 0) *(u32x2*)(ws + OFF_K2 + key * 8) = u32x2{u2, u3};
            float n2 = dot2(u1, u1, dot2(u0, u0, 0.f));
            n2 = add32(add16(n2));
            n2 = dot2(u3, u3, dot2(u2, u2, n2));
            if (!(n2 == n2)) n2 = INFINITY;
            n2 = rowmax16(n2);
            if (lane == 0) *(float*)(ws + OFF_KN + t16 * 4) = n2;
        } else {
            u16* vb = (u16*)(ws + OFF_V + t16 * 640 + (n16 >> 3) * 320) + (n16 & 7);
            vb[(4 * g + 0) * 8] = (u16)u0; vb[(4 * g + 1) * 8] = (u16)(u0 >> 16);
            vb[(4 * g + 2) * 8] = (u16)u1; vb[(4 * g + 3) * 8] = (u16)(u1 >> 16);
            if (g == 0) { vb[16 * 8] = (u16)u2; vb[17 * 8] = (u16)(u2 >> 16); vb[18 * 8] = (u16)u3; vb[19 * 8] = (u16)(u3 >> 16); }
        }
    }
    if (w == 0) *(u32x2*)(ws + OFF_XG + ((size_t)(tb * 64 + lane) * 8 + cg) * 8) = u32x2{0u, 0u};
}

constexpr int QKV_LDS = 163840;
__global__ __launch_bounds__(256, 1) void k_qkv(const u16* __restrict__ x, const u16* __restrict__ wq, const u16* __restrict__ wk,
                                                const u16* __restrict__ wv, char* __restrict__ ws) {
    extern __shared__ __attribute__((aligned(16))) char lds_raw[];
    lds_ptr lds = (lds_ptr)lds_raw;
    char* L = lds_raw;
    const int tid = threadIdx.x, lane = tid & 63, w = __builtin_amdgcn_readfirstlane(tid >> 6);
    const int b = blockIdx.x, slot = b >> 3, tb = ((b & 7) >> 1) * 8 + (slot >> 2), cg = (b & 1) * 4 + (slot & 3);
    const int mt0 = cg >> 1, isv = cg & 1;
    const int n16 = lane & 15, g = lane >> 4;
    unsigned code_touch = 0u;
    if (w == 2) {
        uint64_t a[5];
        KADDR(a[0], "_Z6k_attnILb1EEvPcPKtS2_S2_S2_S2_S2_"); KADDR(a[1], "_Z6k_attnILb0EEvPcPKtS2_S2_S2_S2_S2_");
        KADDR(a[2], "_Z5k_mlpILb1EEvPcPKtS2_S2_Pt"); KADDR(a[3], "_Z5k_mlpILb0EEvPcPKtS2_S2_Pt"); KADDR(a[4], "_Z5k_endILi0EEvv");
        uint64_t lo = a[0], hi = a[0];
#pragma unroll
        for (int i = 1; i < 5; ++i) { lo = a[i] < lo ? a[i] : lo; hi = a[i] > hi ? a[i] : hi; }
        const uint64_t pa = lo + (uint64_t)(slot * 64 + lane) * 128;
        if (pa < hi) code_touch = *(const __attribute__((address_space(1))) unsigned*)pa;
    }
    SBAR();
    f32x4 acc[4][5], acck[2];
#pragma unroll
    for (int i = 0; i < 4; ++i)
#pragma unroll
        for (int j = 0; j < 5; ++j) acc[i][j] = f32x4{0.f, 0.f, 0.f, 0.f};
    acck[0] = f32x4{0.f, 0.f, 0.f, 0.f}; acck[1] = acck[0];
    f32x4 csq[5], cs0, cs1, stq[4], stk[4];
    float sm[4] = {0.f, 0.f, 0.f, 0.f}, sq[4] = {0.f, 0.f, 0.f, 0.f};
    float cs[5] = {0.f, 0.f, 0.f, 0.f, 0.f}, csk[2] = {0.f, 0.f};
    {
        lds_ptr ra = lds + w * 40960, rb = ra + 20480;
        const char* xs = (const char*)(x + (size_t)(tb * 64) * 640 + 160 * w);
#pragma unroll
        for (int j = 0; j < 5; ++j) {
            const int c = 64 * j + lane;
            const unsigned xo = (unsigned)(c / 20) * 1280u + (unsigned)(c % 20) * 16u;
#pragma unroll
            for (int i = 0; i < 4; ++i) glds16(xs + (size_t)i * 20480 + xo, ra + i * 5120 + j * 1024);
        }
        const char* src = (const char*)(wq + (size_t)(160 * w) * 640 + 80 * cg);
        const unsigned loff = (unsigned)(lane / 10) * 1280u + (unsigned)(lane % 10) * 16u;
        const char* src2 = (const char*)((isv ? wv : wk) + (size_t)(160 * w) * 20);
#pragma unroll
        for (int i = 0; i < 16; ++i) glds16(src + (size_t)i * 7680 + loff, rb + i * 960);
#pragma unroll
        for (int i = 0; i < 3; ++i) glds16(src2 + i * 1024 + lane * 16, rb + 15488 + i * 1024);
        if (lane < 48) glds16(src2 + 3 * 1024 + lane * 16, rb + 15488 + 3 * 1024);
        VMCNT(20);
        bf16x8 xf[4][5], xk[5];
        {
            const char* xl = L + w * 40960 + n16 * 320 + g * 16;
#pragma unroll
            for (int i = 0; i < 4; ++i)
#pragma unroll
                for (int ks = 0; ks < 5; ++ks) xf[i][ks] = *(const bf16x8*)(xl + ((i + w) & 3) * 5120 + ks * 64);
#pragma unroll
            for (int ks = 0; ks < 5; ++ks) xk[ks] = *(const bf16x8*)(xl + mt0 * 5120 + ks * 64);
        }
        asm volatile("s_waitcnt lgkmcnt(0)" ::: "memory");
        SBAR();
#pragma unroll
        for (int i = 0; i < 10; ++i) glds16(src + (size_t)(16 + i) * 7680 + loff, ra + i * 960);
        if (lane < 40) glds16(src + (size_t)26 * 7680 + loff, ra + 10 * 960);
#pragma unroll
        for (int i = 0; i < 2; ++i) glds16(src2 + 3840 + i * 1024 + lane * 16, ra + 10240 + i * 1024);
        if (lane < 32) glds16(src2 + 3840 + 2 * 1024 + lane * 16, ra + 10240 + 2 * 1024);
        const unsigned ONES = 0x3f803f80u;
#pragma unroll
        for (int i = 0; i < 4; ++i)
#pragma unroll
            for (int ks = 0; ks < 5; ++ks) {
                u32x4 u = __builtin_bit_cast(u32x4, xf[i][ks]);
#pragma unroll
                for (int e = 0; e < 4; ++e) { sm[i] = dot2(u[e], ONES, sm[i]); sq[i] = dot2(u[e], u[e], sq[i]); }
            }
        SBAR();
        VMCNT(14);
        const int q4 = (lane & 15) >> 2, p4 = lane & 3;
        lds_cptr wb0 = (lds_cptr)rb + (8 * g + q4) * 160 + p4 * 8, wb1 = (lds_cptr)ra + (8 * g + q4) * 160 + p4 * 8 - 3 * 5120;
        lds_cptr kb0 = (lds_cptr)rb + 15488 + (8 * g + q4) * 40 + p4 * 8, kb1 = (lds_cptr)ra + 10240 + (8 * g + q4) * 40 + p4 * 8 - 3 * 1280;
        bf16x8 wf[2][7];
#define QKV_TR(ks, s_) do { lds_cptr wb = (ks) < 3 ? wb0 : wb1, kb = (ks) < 3 ? kb0 : kb1; \
        _Pragma("unroll") for (int nt = 0; nt < 5; ++nt) wf[s_][nt] = cat8(tr_read(wb + (ks) * 5120 + nt * 32), tr_read(wb + (ks) * 5120 + 640 + nt * 32)); \
        _Pragma("unroll") for (int nt = 0; nt < 2; ++nt) wf[s_][5 + nt] = cat8(tr_read(kb + (ks) * 1280 + nt * 32), tr_read(kb + (ks) * 1280 + 160 + nt * 32)); SBAR(); } while (0)
        QKV_TR(0, 0);
#pragma unroll
        for (int ks = 0; ks < 5; ++ks) {
            if (ks == 2) {
                VMCNT(0); SBAR();
            }
            if (ks < 4) QKV_TR(ks + 1, (ks + 1) & 1);
#pragma unroll
            for (int nt = 0; nt < 5; ++nt) {
                u32x4 u = __builtin_bit_cast(u32x4, wf[ks & 1][nt]);
#pragma unroll
                for (int e = 0; e < 4; ++e) cs[nt] = dot2(u[e], ONES, cs[nt]);
#pragma unroll
                for (int i = 0; i < 4; ++i) acc[i][nt] = MFMA16_32(wf[ks & 1][nt], xf[i][ks], acc[i][nt]);
            }
#pragma unroll
            for (int nt = 0; nt < 2; ++nt) {
                u32x4 u = __builtin_bit_cast(u32x4, wf[ks & 1][5 + nt]);
#pragma unroll
                for (int e = 0; e < 4; ++e) csk[nt] = dot2(u[e], ONES, csk[nt]);
                acck[nt] = MFMA16_32(wf[ks & 1][5 + nt], xk[ks], acck[nt]);
            }
            SBAR();
        }
#pragma unroll
        for (int i = 0; i < 4; ++i) { sm[i] = add32(add16(sm[i])); sq[i] = add32(add16(sq[i])); }
#pragma unroll
        for (int i = 0; i < 5; ++i) cs[i] = add32(add16(cs[i]));
#pragma unroll
        for (int i = 0; i < 2; ++i) csk[i] = add32(add16(csk[i]));
    }
    RAW_BARRIER();
    qkv_finish<false>(ws, L, tb, cg, w, lane, acc, acck, csq, cs0, cs1, stq, stk, sm, sq, cs, csk);
    PIN(code_touch);
    if (b == 0 && tid < 4) {
        const unsigned v = (tid & 2) ? 0x3f803f80u : 0u;
        *(u32x4*)(ws + OFF_CONST + tid * 16) = u32x4{v, v, v, v};
    }
}

constexpr int PJ0 = 0, PJ1 = 4, PJD = 3;
constexpr int PBW = 2688, A_STG = 32 * PBW, ATT_LDS = A_STG + 32768;
template <bool PREP>
__global__ __launch_bounds__(512, 2) void k_attn(char* __restrict__ ws, const u16* __restrict__ w1, const u16* __restrict__ w2,
                                                 const u16* __restrict__ wo, const u16* __restrict__ wq,
                                                 const u16* __restrict__ wk, const u16* __restrict__ wv) {
    extern __shared__ __attribute__((aligned(16))) char lds_raw[];
    char* L = lds_raw;
    const int tid = threadIdx.x, lane = tid & 63, w = __builtin_amdgcn_readfirstlane(tid >> 6);
    const int b = blockIdx.x, slot = b >> 3, tb = ((b & 7) >> 1) * 8 + (slot >> 2), cg = (b & 1) * 4 + (slot & 3);
    const int r32 = lane & 31, h = lane >> 5, n16 = lane & 15, g = lane >> 4;
    const int cgp = w & 1, kq = w >> 1;
    float kn0 = *(const float*)(ws + OFF_KN + lane * 4), kn1 = *(const float*)(ws + OFF_KN + 256 + lane * 4);
    float qmax = *(const float*)(ws + OFF_QN + ((tb * 8 + cg) * 4 + (lane & 3)) * 4);
    bf16x8 q1[4]; bf16x4 q2[4];
    {
        const char* qg = ws + OFF_Q + (size_t)(tb * 8 + cg) * 12288 + cgp * 6144 + r32 * 48;
#pragma unroll
        for (int j = 0; j < 4; ++j) { q1[j] = *(const bf16x8*)(qg + j * 1536 + 16 * h); q2[j] = *(const bf16x4*)(qg + j * 1536 + 32 + 8 * h); }
    }
    SBAR();
    const int kr = (r32 & 19) | ((r32 & 4) << 1) | ((r32 & 8) >> 1);
    const unsigned ko1 = (unsigned)OFF_K1 + (unsigned)(512 * kq + kr) * 32u + 16u * h;
    const unsigned ko2 = h ? (unsigned)OFF_CONST + 32u : (unsigned)OFF_K2 + (unsigned)(512 * kq + kr) * 8u;
    const unsigned ks2 = h ? 0u : 256u;
    const unsigned vo = r32 < 20 ? (unsigned)OFF_V + (unsigned)(32 * kq) * 640u + (unsigned)(h * 20 + r32) * 16u
                                 : (unsigned)OFF_CONST + (r32 == 20 ? 32u : 0u);
    const unsigned vs = r32 < 20 ? 640u : 0u;
    bf16x8 k1[3], v0[3], v1[3]; bf16x4 k2[3];
#define K_LOAD(t) do { k1[(t) % 3] = *(const bf16x8*)(ws + ko1 + (t) * 1024); k2[(t) % 3] = *(const bf16x4*)(ws + (ko2 + (t) * ks2)); } while (0)
#define V_LOAD(t) do { v0[(t) % 3] = *(const bf16x8*)(ws + (vo + (2 * (t)) * vs)); v1[(t) % 3] = *(const bf16x8*)(ws + (vo + (2 * (t) + 1) * vs)); } while (0)
    K_LOAD(0); V_LOAD(0); SBAR(); K_LOAD(1); V_LOAD(1); SBAR();
    PIN(kn0); PIN(kn1);
    const float kmax2 = wavemax(fmaxf(kn0, kn1));
    qmax = rowmax16(qmax);
    const float kb = __builtin_amdgcn_sqrtf(kmax2);
    const bool safe = __builtin_amdgcn_readfirstlane((int)(qmax * kb <= SAFE_BOUND)) != 0;
    const f32x16 zero16 = {0.f, 0.f, 0.f, 0.f, 0.f, 0.f, 0.f, 0.f, 0.f, 0.f, 0.f, 0.f, 0.f, 0.f, 0.f, 0.f};
    if (safe) {
        if (h) {
#pragma unroll
            for (int j = 0; j < 4; ++j) q2[j][0] = (short)pk2(-bf2f((u16)q2[j][0]) * kb, 0.f);
        }
    } else {
        float m[4];
#pragma unroll
        for (int j = 0; j < 4; ++j) { m[j] = -INFINITY; if (h) q2[j] = bf16x4{0, 0, 0, 0}; }
        for (int t = 0; t < 16; ++t) {
            const bf16x8 ka = *(const bf16x8*)(ws + ko1 + t * 1024);
            const bf16x4 kc = *(const bf16x4*)(ws + (ko2 + t * ks2));
#pragma unroll
            for (int j = 0; j < 4; ++j) {
                f32x16 s = MFMA32_16(ka, q1[j], zero16);
                SBAR(); asm volatile("s_nop 15" ::: "memory"); SBAR();
                s = MFMA32_8(kc, q2[j], s);
#pragma unroll
                for (int e = 0; e < 16; ++e) m[j] = fmaxf(m[j], s[e]);
                SBAR();
            }
        }
#pragma unroll
        for (int j = 0; j < 4; ++j) {
            const float mm = fmaxf(m[j], xor32(m[j]));
            if (h == 0) *(float*)(L + ((w * 4 + j) * 32 + r32) * 4) = mm;
        }
        RAW_BARRIER();
#pragma unroll
        for (int j = 0; j < 4; ++j) {
            float mm = -INFINITY;
#pragma unroll
            for (int qq = 0; qq < 4; ++qq) mm = fmaxf(mm, *(const float*)(L + (((2 * qq + cgp) * 4 + j) * 32 + r32) * 4));
            const unsigned hi = __float_as_uint(mm) & 0xffff0000u;
            const float r1 = mm - __uint_as_float(hi);
            const unsigned mid = __float_as_uint(r1) & 0xffff0000u;
            const float r2 = r1 - __uint_as_float(mid);
            const unsigned lo = __float_as_uint(r2) & 0xffff0000u;
            if (h) q2[j] = __builtin_bit_cast(bf16x4, u32x2{hi ^ 0x80000000u, ((mid ^ 0x80000000u) >> 16) | (lo ^ 0x80000000u)});
        }
        RAW_BARRIER();
    }
    f32x16 o[4], sa, sb;
#pragma unroll
    for (int j = 0; j < 4; ++j) o[j] = zero16;
    u32x4 p1prev = {0u, 0u, 0u, 0u};
    sa = MFMA32_16(k1[0], q1[0], zero16);
    SBAR(); asm volatile("s_nop 15" ::: "memory"); SBAR();
    sa = MFMA32_8(k2[0], q2[0], sa);
    SBAR(); asm volatile("s_nop 15" ::: "memory"); SBAR();
    u32x4 prep[4]; char* prep_dst = nullptr; int prep_st = 0;
#pragma unroll
    for (int t = 0; t < 16; ++t) {
#pragma unroll
        for (int j = 0; j < 4; ++j) {
            const int it = 4 * t + j;
            f32x16& cur = (it & 1) ? sb : sa; f32x16& nxt = (it & 1) ? sa : sb;
            const int tn = j < 3 ? t : t + 1, jn = (j + 1) & 3;
            const int tp = j > 0 ? t : t - 1, jp = (j + 3) & 3;
            u32x4 p0, p1;
            if (j == 0 && t + 2 < 16) K_LOAD(t + 2);
            if (j == 1 && t + 2 < 16) V_LOAD(t + 2);
            if (PREP && j == 2 && (t == PJ0 || t == PJ1)) {
                int id = (t == PJ1 ? 2048 : 0) + b * 8 + w; if (id >= NPREP) id -= 2048;
                const u16* src; int ld;
                if (id < 1728) { const int ks = id / 27, c4 = id - ks * 27; src = w1 + (size_t)(32 * ks) * F_ + 64 * c4; ld = F_;
                                 prep_dst = ws + OFF_W1F + (size_t)(4 * c4 * 64 + ks) * 1024; prep_st = 64 * 1024; }
                else if (id < 3456) { const int jj = id - 1728, ks = jj >> 5, c4 = jj & 31; src = w2 + (size_t)(32 * ks) * S_ + 64 * c4; ld = S_;
                                      prep_dst = ws + OFF_W2F + (size_t)(4 * c4 * 54 + ks) * 1024; prep_st = 54 * 1024; }
                else { int jj = id - 3456; const bool isq = jj >= 200; if (isq) jj -= 200; const int ks = jj / 10, c4 = jj - ks * 10;
                       src = (isq ? wq : wo) + (size_t)(32 * ks) * 640 + 64 * c4; ld = 640;
                       prep_dst = ws + (isq ? OFF_WQF : OFF_WOF) + (size_t)(4 * c4 * 20 + ks) * 1024; prep_st = 20 * 1024; }
                src += (size_t)(lane >> 3) * ld + (lane & 7) * 8;
#pragma unroll
                for (int i = 0; i < 4; ++i) prep[i] = *(const u32x4*)(src + (size_t)(8 * i) * ld);
            }
            if (PREP && j == 2 && t == 9) {
                const int id = (b * 8 + w) % 80, ks = id >> 2, ct = id & 3; int cn = 16 * (ct & 1) + n16; if (cn >= 20) cn = 0;
                const u16* sp = ((ct & 2) ? wv : wk) + (size_t)(32 * ks + 8 * g) * 20 + cn;
                if (b < 10) {
#pragma unroll
                    for (int e = 0; e < 8; ++e) prep[e >> 2][e & 3] = sp[e * 20];
                }
                prep_dst = ws + OFF_WQF + (size_t)((40 + ct) * 20 + ks) * 1024;
            }
            if (PREP && j == 2 && t == 12 && b < 10)
                *(u32x4*)(prep_dst + lane * 16) = u32x4{prep[0][0] | (prep[0][1] << 16), prep[0][2] | (prep[0][3] << 16),
                                                        prep[1][0] | (prep[1][1] << 16), prep[1][2] | (prep[1][3] << 16)};
            if (PREP && j == 2 && (t == PJ0 + PJD || t == PJ1 + PJD)) {
#pragma unroll
                for (int i = 0; i < 4; ++i) *(u32x4*)(L + A_STG + w * 4096 + i * 1024 + lane * 16) = prep[i];
            }
            if (PREP && j == 3 && (t == PJ0 + PJD || t == PJ1 + PJD)) {
                lds_cptr tp_ = (lds_cptr)(lds_ptr)L + A_STG + w * 4096 + (8 * g + (n16 >> 2)) * 128 + (lane & 3) * 8;
#pragma unroll
                for (int i = 0; i < 4; ++i) prep[i] = __builtin_bit_cast(u32x4, cat8(tr_read(tp_ + i * 32), tr_read(tp_ + 512 + i * 32)));
            }
            if (PREP && j == 0 && (t == PJ0 + PJD + 1 || t == PJ1 + PJD + 1)) {
#pragma unroll
                for (int i = 0; i < 4; ++i) PREP_STORE((u32x4*)(prep_dst + (size_t)i * prep_st + lane * 16), prep[i]);
            }
            SBAR();
            if (it < 63) nxt = MFMA32_16(k1[tn % 3], q1[jn], zero16);
            SBAR();
            p0[0] = pk2(EX2(cur[0]), EX2(cur[1])); p0[1] = pk2(EX2(cur[2]), EX2(cur[3]));
            PIN(p0); SBAR();
            if (it > 0) o[jp] = MFMA32_16(v1[(tp + 3) % 3], __builtin_bit_cast(bf16x8, p1prev), o[jp]);
            SBAR();
            p0[2] = pk2(EX2(cur[4]), EX2(cur[5])); p0[3] = pk2(EX2(cur[6]), EX2(cur[7]));
            PIN(p0); SBAR();
            if (it < 63) nxt = MFMA32_8(k2[tn % 3], q2[jn], nxt);
            SBAR();
            p1[0] = pk2(EX2(cur[8]), EX2(cur[9])); p1[1] = pk2(EX2(cur[10]), EX2(cur[11]));
            PIN(p1); SBAR();
            o[j] = MFMA32_16(v0[t % 3], __builtin_bit_cast(bf16x8, p0), o[j]);
            SBAR();
            p1[2] = pk2(EX2(cur[12]), EX2(cur[13])); p1[3] = pk2(EX2(cur[14]), EX2(cur[15]));
            PIN(p1); SBAR();
            p1prev = p1;
        }
    }
    o[3] = MFMA32_16(v1[15 % 3], __builtin_bit_cast(bf16x8, p1prev), o[3]);
#pragma unroll
    for (int j = 0; j < 4; ++j) {
        char* pb = L + (kq * 8 + cgp * 4 + j) * PBW;
        *(f32x4*)(pb + lane * 16) = f32x4{o[j][0], o[j][1], o[j][2], o[j][3]};
        *(f32x4*)(pb + 1024 + lane * 16) = f32x4{o[j][4], o[j][5], o[j][6], o[j][7]};
        if (h == 0) *(f32x4*)(pb + 2048 + r32 * 16) = f32x4{o[j][8], o[j][9], o[j][10], o[j][11]};
        else *(float*)(pb + 2560 + r32 * 4) = o[j][8];
    }
    RAW_BARRIER();
    {
        const char* pb = L + w * PBW;
        f32x4 a0 = {0.f, 0.f, 0.f, 0.f}, a1 = a0, a2 = a0; float l = 0.f;
#pragma unroll
        for (int qq = 0; qq < 4; ++qq) {
            a0 += *(const f32x4*)(pb + qq * 8 * PBW + lane * 16);
            a1 += *(const f32x4*)(pb + qq * 8 * PBW + 1024 + lane * 16);
            a2 += *(const f32x4*)(pb + qq * 8 * PBW + 2048 + r32 * 16);
            l += *(const float*)(pb + qq * 8 * PBW + 2560 + r32 * 4);
        }
        const float inv = __builtin_amdgcn_rcpf(l);
        const int tl = 32 * (w & 1) + r32, hd0 = 80 * cg + 20 * (w >> 1);
        char* ob = ws + OFF_O + (size_t)(tb * 4 + (tl >> 4)) * 20480 + (tl & 15) * 16;
        auto st = [&](int hd, const f32x4& a) {
            *(u32x2*)(ob + (hd >> 5) * 1024 + ((hd & 31) >> 3) * 256 + (hd & 7) * 2) = u32x2{pk2(a[0] * inv, a[1] * inv), pk2(a[2] * inv, a[3] * inv)};
        };
        st(hd0 + 4 * h, a0); st(hd0 + 8 + 4 * h, a1);
        if (h == 0) st(hd0 + 16, a2);
    }
    if (b == 0 && tid < 48) *(unsigned*)(ws + OFF_CNT + tid * 256) = 0u;
}

typedef __attribute__((address_space(1))) unsigned gu32;
constexpr int AUXL = 16;
#define LD_SC1(off) __builtin_bit_cast(bf16x8, __builtin_amdgcn_raw_buffer_load_b128(rs, (int)(off), 0, 16))
DI void edge_publish(gu32* cnt) {
    RAW_BARRIER();
    if (threadIdx.x == 0) __hip_atomic_fetch_add(cnt, 1u, __ATOMIC_RELAXED, __HIP_MEMORY_SCOPE_AGENT);
}
DI void edge_wait(gu32* cnt, unsigned want) {
    if (threadIdx.x == 0) {
        for (int spin = 0; spin < (1 << 20); ++spin) {
            if (__hip_atomic_load(cnt, __ATOMIC_RELAXED, __HIP_MEMORY_SCOPE_AGENT) >= want) break;
            __builtin_amdgcn_s_sleep(1);
        }
        __builtin_amdgcn_fence(__ATOMIC_ACQUIRE, "wavefront");
    }
    RAW_BARRIER();
}
constexpr int W1_A = 6, W2_FIRST = 7;
constexpr int MLP_LDS = 73728;
#define WO_LD(ks) do { _Pragma("unroll") for (int i = 0; i < 4; ++i) xf[ks][i] = xb[(((i + w) & 3) * 20 + (ks)) * 64]; \
        _Pragma("unroll") for (int nt = 0; nt < 5; ++nt) wf[ks][nt] = wb[(nt * 20 + (ks)) * 64]; SBAR(); } while (0)
template <bool R1>
__global__ __launch_bounds__(256, 1) void k_mlp(char* __restrict__ ws, const u16* __restrict__ xin, const u16* __restrict__ b1,
                                                const u16* __restrict__ b2, u16* __restrict__ out) {
    extern __shared__ __attribute__((aligned(16))) char lds_raw[];
    char* L = lds_raw;
    const int tid = threadIdx.x, lane = tid & 63, w = __builtin_amdgcn_readfirstlane(tid >> 6);
    const int b = blockIdx.x, slot = b >> 3, tb = ((b & 7) >> 1) * 8 + (slot >> 2), cg = (b & 1) * 4 + (slot & 3);
    const int ot1 = ((b & 7) >> 1) * 7 + (slot >> 2);
    const bool act1 = slot < 28 && ot1 < 27;
    const int n16 = lane & 15, g = lane >> 4;
    const __amdgpu_buffer_rsrc_t rs = __builtin_amdgcn_make_buffer_rsrc((void*)ws, 0, (int)WS_NEED, 0x00020000);
    gu32* cnt = (gu32*)(ws + OFF_CNT);
    f32x4 acc[4][5];
#define ACC_ZERO() do { _Pragma("unroll") for (int i_ = 0; i_ < 4; ++i_) _Pragma("unroll") for (int j_ = 0; j_ < 5; ++j_) acc[i_][j_] = f32x4{0.f, 0.f, 0.f, 0.f}; } while (0)
#define ACC_REDUCE() do { \
    _Pragma("unroll") for (int j_ = 1; j_ < 4; ++j_) { const int o_ = (w + j_) & 3; \
        _Pragma("unroll") for (int i_ = 0; i_ < 5; ++i_) *(f32x4*)(L + (((o_ * 3 + (j_ - 1)) * 5 + i_) * 64 + lane) * 16) = acc[j_][i_]; } \
    RAW_BARRIER(); \
    _Pragma("unroll") for (int j_ = 0; j_ < 3; ++j_) _Pragma("unroll") for (int i_ = 0; i_ < 5; ++i_) \
        acc[1 + j_][i_] = *(const f32x4*)(L + (((w * 3 + j_) * 5 + i_) * 64 + lane) * 16); \
    SBAR(); \
    _Pragma("unroll") for (int i_ = 0; i_ < 5; ++i_) acc[0][i_] = (acc[0][i_] + acc[1][i_]) + (acc[2][i_] + acc[3][i_]); } while (0)
    const int tok = tb * 64 + 16 * w + n16;
    bf16x8 wr[16][4];
    int ro1[4], ro2[4];
#pragma unroll
    for (int j = 0; j < 4; ++j) { ro1[j] = ((j + w) & 3) * 64 * 64; ro2[j] = ((j + w) & 3) * 54 * 64; }
    const bf16x8* pw1 = (const bf16x8*)(ws + OFF_W1F) + ((size_t)(4 * (act1 ? ot1 : 0)) * 64 + 16 * w) * 64 + lane;
    const int k2 = w < 2 ? 14 * w : 28 + 13 * (w - 2);
    const bf16x8* pw2 = (const bf16x8*)(ws + OFF_W2F) + ((size_t)(4 * tb) * 54 + k2) * 64 + lane;
#define W1_LD(lo, hi) do { _Pragma("unroll") for (int s_ = (lo); s_ < (hi); ++s_) _Pragma("unroll") for (int j_ = 0; j_ < 4; ++j_) wr[s_][j_] = pw1[ro1[j_] + s_ * 64]; SBAR(); } while (0)
#define W2_LD(lo, hi) do { _Pragma("unroll") for (int s_ = (lo); s_ < (hi); ++s_) _Pragma("unroll") for (int j_ = 0; j_ < 4; ++j_) wr[s_][j_] = pw2[ro2[j_] + s_ * 64]; SBAR(); } while (0)

    u32x2 x1o[5], bb; float bs;
    {
        ACC_ZERO();
        u32x2 xi[5];
        const bf16x8* xb = (const bf16x8*)(ws + OFF_O) + (size_t)(tb * 4 * 20 + 5 * w) * 64 + lane;
        const bf16x8* wb = (const bf16x8*)(ws + OFF_WOF) + (size_t)(5 * cg * 20 + 5 * w) * 64 + lane;
        bf16x8 xf[5][4], wf[5][5];
        WO_LD(0); WO_LD(1); WO_LD(2); WO_LD(3);
#pragma unroll
        for (int ks = 0; ks < 5; ++ks) {
#pragma unroll
            for (int nt = 0; nt < 5; ++nt)
#pragma unroll
                for (int i = 0; i < 4; ++i) acc[i][nt] = MFMA16_32(wf[ks][nt], xf[ks][i], acc[i][nt]);
            SBAR();
            if (ks == 0) {
                WO_LD(4);
#pragma unroll
                for (int nt = 0; nt < 5; ++nt) {
                    const int d = 80 * cg + 16 * nt + 4 * g;
                    if (!R1) xi[nt] = *(const u32x2*)(xin + ((size_t)((tok >> 4) * 20 + (d >> 5)) * 64 + ((d & 31) >> 3) * 16 + n16) * 8 + (d & 7));
                    else xi[nt] = *(const u32x2*)(xin + (size_t)tok * 640 + d);
                }
                bb = *(const u32x2*)(b1 + 64 * (act1 ? ot1 : 0) + 16 * w + 4 * g);
                bs = bf2f(b2[tok]);
                SBAR();
            }
            if (ks == 1) W1_LD(0, 2);
            if (ks == 2) W1_LD(2, 4);
            if (ks == 3) W1_LD(4, 6);
            if (ks == 4) W1_LD(6, W1_A);
        }
        ACC_REDUCE();
        float xv[20], s = 0.f;
#pragma unroll
        for (int nt = 0; nt < 5; ++nt) {
            const unsigned u0 = pk2(bf_lo(xi[nt][0]) + acc[0][nt][0], bf_hi(xi[nt][0]) + acc[0][nt][1]);
            const unsigned u1 = pk2(bf_lo(xi[nt][1]) + acc[0][nt][2], bf_hi(xi[nt][1]) + acc[0][nt][3]);
            x1o[nt] = u32x2{u0, u1};
            xv[4 * nt] = bf_lo(u0); xv[4 * nt + 1] = bf_hi(u0); xv[4 * nt + 2] = bf_lo(u1); xv[4 * nt + 3] = bf_hi(u1);
            s += (xv[4 * nt] + xv[4 * nt + 1]) + (xv[4 * nt + 2] + xv[4 * nt + 3]);
        }
        const float mw = add32(add16(s)) * (1.f / 80.f);
        float s2 = 0.f;
#pragma unroll
        for (int i = 0; i < 20; ++i) { const float dd = xv[i] - mw; s2 += dd * dd; }
        s2 = add32(add16(s2));
        gu64* gp = (gu64*)(ws + OFF_XG) + (size_t)tok * 8;
        if (g == 0) __hip_atomic_store(gp + cg, ((unsigned long long)(__float_as_uint(s2) | 0x80000000u) << 32) | __float_as_uint(mw),
                                       __ATOMIC_RELAXED, __HIP_MEMORY_SCOPE_AGENT);
        SBAR();
        unsigned long long v0, v1;
        for (int spin = 0; spin < (1 << 20); ++spin) {
            v0 = __hip_atomic_load(gp + 2 * g, __ATOMIC_RELAXED, __HIP_MEMORY_SCOPE_AGENT);
            v1 = __hip_atomic_load(gp + 2 * g + 1, __ATOMIC_RELAXED, __HIP_MEMORY_SCOPE_AGENT);
            if (__all((long long)(v0 & v1) < 0)) break;
            __builtin_amdgcn_s_sleep(4);
        }
        const float m0 = __uint_as_float((unsigned)v0), m1 = __uint_as_float((unsigned)v1);
        const float mean = add32(add16(m0 + m1)) * 0.125f;
        const float d0 = m0 - mean, d1 = m1 - mean;
        const float var = add32(add16(__uint_as_float((unsigned)(v0 >> 32) & 0x7fffffffu) + __uint_as_float((unsigned)(v1 >> 32) & 0x7fffffffu) +
                                      80.f * (d0 * d0 + d1 * d1))) * (1.f / 640.f);
        float shf, r2; ln2_scalars(mean, var, shf, r2);
        char* hb = L + w * 15360;
#pragma unroll
        for (int nt = 0; nt < 5; ++nt)
            *(u32x2*)(hb + n16 * 176 + (16 * nt + 4 * g) * 2) =
                u32x2{pk2((xv[4 * nt] - shf) * r2, (xv[4 * nt + 1] - shf) * r2), pk2((xv[4 * nt + 2] - shf) * r2, (xv[4 * nt + 3] - shf) * r2)};
        asm volatile("s_waitcnt lgkmcnt(0)" ::: "memory");
        const int q4 = (lane & 15) >> 2, p4 = lane & 3;
        lds_cptr hr = (lds_cptr)(lds_ptr)L + w * 15360 + (8 * (g & 1) + q4) * 176 + p4 * 8 + (g >> 1) * 32;
        const int s0 = tb * 64 + 16 * w + 8 * (g & 1);
        const int hoff = (int)OFF_H + ((5 * cg + (g >> 1)) * 64 + (s0 >> 5)) * 1024 + (((s0 & 31) >> 3) * 16 + n16) * 16;
        const bf16x8 f0 = cat8(tr_read(hr), tr_read(hr + 704));
        const bf16x8 f1 = cat8(tr_read(hr + 64), tr_read(hr + 704 + 64));
        const bf16x8 f2 = cat8(tr_read(hr + (g < 2 ? 128 : 0)), tr_read(hr + 704 + (g < 2 ? 128 : 0)));
        __builtin_amdgcn_raw_buffer_store_b128(__builtin_bit_cast(u32x4, f0), rs, hoff, 0, 16);
        __builtin_amdgcn_raw_buffer_store_b128(__builtin_bit_cast(u32x4, f1), rs, hoff + 2 * 65536, 0, 16);
        if (g < 2) __builtin_amdgcn_raw_buffer_store_b128(__builtin_bit_cast(u32x4, f2), rs, hoff + 4 * 65536, 0, 16);
        SBAR();
        W1_LD(W1_A, W1_A + 4);
        VMCNT(16);
        edge_publish(cnt + cg * 64);
    }
    if (act1) {
        W1_LD(W1_A + 4, 16);
        edge_wait(cnt + cg * 64, 32);
        ACC_ZERO();
        const int ho = (int)OFF_H + ((5 * cg) * 64 + 16 * w) * 1024 + lane * 16;
        constexpr int PF = 6;
        bf16x8 rf[PF][5];
#pragma unroll
        for (int s = 0; s < PF; ++s) {
#pragma unroll
            for (int i = 0; i < 5; ++i) rf[s][i] = LD_SC1(ho + i * 65536 + s * 1024);
            SBAR();
        }
#pragma unroll
        for (int s = 0; s < 16; ++s) {
#pragma unroll
            for (int j = 0; j < 4; ++j)
#pragma unroll
                for (int i = 0; i < 5; ++i) acc[j][i] = MFMA16_32(wr[s][j], rf[s % PF][i], acc[j][i]);
            SBAR();
            if (s + PF < 16) {
#pragma unroll
                for (int i = 0; i < 5; ++i) rf[s % PF][i] = LD_SC1(ho + i * 65536 + (s + PF) * 1024);
                SBAR();
            }
            if (s >= W2_FIRST && s - W2_FIRST < 9) W2_LD(s - W2_FIRST, s - W2_FIRST + 1);
        }
        ACC_REDUCE();
        const int f = 64 * ot1 + 16 * w + 4 * g;
        const float bv[4] = {bf_lo(bb[0]), bf_hi(bb[0]), bf_lo(bb[1]), bf_hi(bb[1])};
        const int uo = (int)OFF_U + ((5 * cg) * 54 + (f >> 5)) * 1024 + (((f & 31) >> 3) * 16 + n16) * 16 + (f & 7) * 2;
#pragma unroll
        for (int i = 0; i < 5; ++i) {
            float r[4];
#pragma unroll
            for (int e = 0; e < 4; ++e) {
                const float u = acc[0][i][e] + bv[e];
                const float z = u * (1.f + 0.044715f * u * u) * (-2.f * 0.7978845608028654f * 1.4426950408889634f);
                r[e] = u * __builtin_amdgcn_rcpf(1.f + EX2(z));
            }
            __builtin_amdgcn_raw_buffer_store_b64(u32x2{pk2(r[0], r[1]), pk2(r[2], r[3])}, rs, uo + i * 54 * 1024, 0, 16);
        }
        SBAR();
        W2_LD(9, 13);
        VMCNT(16);
        edge_publish(cnt + (8 + cg) * 64);
    } else {
        W2_LD(0, 13);
    }
    {
        if (w < 2) W2_LD(13, 14);
        edge_wait(cnt + (8 + cg) * 64, 27);
        ACC_ZERO();
        const int uo = (int)OFF_U + ((5 * cg) * 54 + k2) * 1024 + lane * 16;
        constexpr int PF = 6;
        bf16x8 rf[PF][5];
#pragma unroll
        for (int s = 0; s < PF; ++s) {
#pragma unroll
            for (int i = 0; i < 5; ++i) rf[s][i] = LD_SC1(uo + i * 54 * 1024 + s * 1024);
            SBAR();
        }
#pragma unroll
        for (int s = 0; s < 14; ++s) {
            if (s < 13 || w < 2) {
#pragma unroll
                for (int j = 0; j < 4; ++j)
#pragma unroll
                    for (int i = 0; i < 5; ++i) acc[j][i] = MFMA16_32(rf[s % PF][i], wr[s][j], acc[j][i]);
            }
            SBAR();
            if (s + PF < 13 || (s + PF == 13 && w < 2)) {
#pragma unroll
                for (int i = 0; i < 5; ++i) rf[s % PF][i] = LD_SC1(uo + i * 54 * 1024 + (s + PF) * 1024);
                SBAR();
            }
        }
        ACC_REDUCE();
        float xv[20], sum = 0.f;
#pragma unroll
        for (int i = 0; i < 5; ++i) {
            const int d = 80 * cg + 16 * i + 4 * g;
            const unsigned u0 = pk2(bf_lo(x1o[i][0]) + acc[0][i][0] + bs, bf_hi(x1o[i][0]) + acc[0][i][1] + bs);
            const unsigned u1 = pk2(bf_lo(x1o[i][1]) + acc[0][i][2] + bs, bf_hi(x1o[i][1]) + acc[0][i][3] + bs);
            if (R1) __builtin_amdgcn_raw_buffer_store_b64(u32x2{u0, u1}, rs,
                        (int)OFF_X2 + ((4 * tb + w) * 20 + (d >> 5)) * 1024 + (((d & 31) >> 3) * 16 + n16) * 16 + (d & 7) * 2, 0, 16);
            else *(u32x2*)(out + (size_t)tok * 640 + d) = u32x2{u0, u1};
            xv[4 * i] = bf_lo(u0); xv[4 * i + 1] = bf_hi(u0); xv[4 * i + 2] = bf_lo(u1); xv[4 * i + 3] = bf_hi(u1);
            sum += (xv[4 * i] + xv[4 * i + 1]) + (xv[4 * i + 2] + xv[4 * i + 3]);
        }
        if (R1) {
            const float mw = add32(add16(sum)) * (1.f / 80.f);
            float s2 = 0.f;
#pragma unroll
            for (int i = 0; i < 20; ++i) { const float dd = xv[i] - mw; s2 += dd * dd; }
            s2 = add32(add16(s2));
            if (g == 0) __builtin_amdgcn_raw_buffer_store_b64(u32x2{__float_as_uint(mw), __float_as_uint(s2)}, rs, (int)OFF_XS + ((tb * 8 + cg) * 64 + 16 * w + n16) * 8, 0, 16);
        }
    }
    if (R1) {
        const int mt0 = cg >> 1, isv = cg & 1;
        const bf16x8* wb = (const bf16x8*)(ws + OFF_WQF) + (size_t)(5 * cg * 20 + 5 * w) * 64 + lane;
        const bf16x8* kb = (const bf16x8*)(ws + OFF_WQF) + (size_t)((40 + 2 * isv) * 20 + 5 * w) * 64 + lane;
        bf16x8 wf[5][5], wkf[5][2], xf[5][4], xk[5];
        SBAR();
#pragma unroll
        for (int i = 0; i < 16; ++i) wf[i / 5][i % 5] = wb[((i % 5) * 20 + i / 5) * 64];
        SBAR();
        VMCNT(16);
        edge_publish(cnt + (16 + tb) * 64);
#pragma unroll
        for (int i = 16; i < 25; ++i) wf[i / 5][i % 5] = wb[((i % 5) * 20 + i / 5) * 64];
#pragma unroll
        for (int ks = 0; ks < 5; ++ks) { wkf[ks][0] = kb[ks * 64]; wkf[ks][1] = kb[(20 + ks) * 64]; }
        f32x4 csq[5], cs0, cs1, stq[4], stk[4], acck[2];
        {
            const float* csp = (const float*)(ws + OFF_CS);
#pragma unroll
            for (int nt = 0; nt < 5; ++nt) csq[nt] = *(const f32x4*)(csp + 80 * cg + 16 * nt + 4 * g);
            cs0 = *(const f32x4*)(csp + 640 + 32 * isv + 4 * g); cs1 = *(const f32x4*)(csp + 640 + 32 * isv + 16);
        }
        SBAR();
        edge_wait(cnt + (16 + tb) * 64, 8);
        const int xo = (int)OFF_X2 + (tb * 4 * 20 + 5 * w) * 1024 + lane * 16;
#pragma unroll
        for (int ks = 0; ks < 5; ++ks) {
#pragma unroll
            for (int i = 0; i < 4; ++i) xf[ks][i] = LD_SC1(xo + (((i + w) & 3) * 20 + ks) * 1024);
            xk[ks] = LD_SC1(xo + (mt0 * 20 + ks) * 1024);
            SBAR();
        }
#pragma unroll
        for (int i = 0; i < 4; ++i) {
            const u32x2 a0 = __builtin_amdgcn_raw_buffer_load_b64(rs, (int)OFF_XS + ((tb * 8 + 2 * i) * 64 + 16 * w + n16) * 8, 0, AUXL);
            const u32x2 a1 = __builtin_amdgcn_raw_buffer_load_b64(rs, (int)OFF_XS + ((tb * 8 + 2 * i + 1) * 64 + 16 * w + n16) * 8, 0, AUXL);
            const u32x2 c0 = __builtin_amdgcn_raw_buffer_load_b64(rs, (int)OFF_XS + ((tb * 8 + 2 * i) * 64 + 16 * mt0 + n16) * 8, 0, AUXL);
            const u32x2 c1 = __builtin_amdgcn_raw_buffer_load_b64(rs, (int)OFF_XS + ((tb * 8 + 2 * i + 1) * 64 + 16 * mt0 + n16) * 8, 0, AUXL);
            stq[i] = __builtin_bit_cast(f32x4, u32x4{a0[0], a0[1], a1[0], a1[1]});
            stk[i] = __builtin_bit_cast(f32x4, u32x4{c0[0], c0[1], c1[0], c1[1]});
        }
        SBAR();
        ACC_ZERO();
        acck[0] = f32x4{0.f, 0.f, 0.f, 0.f}; acck[1] = acck[0];
#pragma unroll
        for (int ks = 0; ks < 5; ++ks) {
#pragma unroll
            for (int nt = 0; nt < 5; ++nt)
#pragma unroll
                for (int i = 0; i < 4; ++i) acc[i][nt] = MFMA16_32(wf[ks][nt], xf[ks][i], acc[i][nt]);
            acck[0] = MFMA16_32(wkf[ks][0], xk[ks], acck[0]);
            acck[1] = MFMA16_32(wkf[ks][1], xk[ks], acck[1]);
            SBAR();
        }
        const float z4[4] = {0.f, 0.f, 0.f, 0.f}, z5[5] = {0.f, 0.f, 0.f, 0.f, 0.f}, z2[2] = {0.f, 0.f};
        qkv_finish<true>(ws, L, tb, cg, w, lane, acc, acck, csq, cs0, cs1, stq, stk, z4, z4, z5, z2);
    }
}

template <int N> __global__ void k_end() {}

extern "C" void kernel_launch(void* const* d_in, const int* in_sizes, int n_in, void* d_out, int out_size, void* d_ws,
                              size_t ws_size, hipStream_t stream) {
    const u16* x = (const u16*)d_in[0];
    const u16* wq = (const u16*)d_in[1];
    const u16* wk = (const u16*)d_in[2];
    const u16* wv = (const u16*)d_in[3];
    const u16* wo = (const u16*)d_in[4];
    const u16* w1 = (const u16*)d_in[6];
    const u16* b1 = (const u16*)d_in[7];
    const u16* w2 = (const u16*)d_in[8];
    const u16* b2 = (const u16*)d_in[9];
    char* ws = (char*)d_ws;
    u16* x2 = (u16*)(ws + OFF_X2);
    (void)hipFuncSetAttribute((const void*)k_qkv, hipFuncAttributeMaxDynamicSharedMemorySize, QKV_LDS);
    (void)hipFuncSetAttribute((const void*)k_attn<true>, hipFuncAttributeMaxDynamicSharedMemorySize, ATT_LDS);
    (void)hipFuncSetAttribute((const void*)k_attn<false>, hipFuncAttributeMaxDynamicSharedMemorySize, ATT_LDS);
    (void)hipFuncSetAttribute((const void*)k_mlp<true>, hipFuncAttributeMaxDynamicSharedMemorySize, MLP_LDS);
    (void)hipFuncSetAttribute((const void*)k_mlp<false>, hipFuncAttributeMaxDynamicSharedMemorySize, MLP_LDS);
    (void)hipFuncSetAttribute((const void*)k_end<0>, hipFuncAttributeMaxDynamicSharedMemorySize, 0);
    k_qkv<<<256, 256, QKV_LDS, stream>>>(x, wq, wk, wv, ws);
    k_attn<true><<<256, 512, ATT_LDS, stream>>>(ws, w1, w2, wo, wq, wk, wv);
    k_mlp<true><<<256, 256, MLP_LDS, stream>>>(ws, x, b1, b2, nullptr);
    k_attn<false><<<256, 512, A_STG, stream>>>(ws, w1, w2, wo, wq, wk, wv);
    k_mlp<false><<<256, 256, MLP_LDS, stream>>>(ws, x2, b1, b2, (u16*)d_out);
}
```
